```python
import torch

def _softmax(x, axis=-1):
    return torch.softmax(x, dim=axis)
def _gelu(x):
    return torch.nn.functional.gelu(x, approximate="tanh")
def _silu(x):
    return torch.nn.functional.silu(x)
def _sigmoid(x):
    return torch.sigmoid(x)
def _layernorm(x, axis=-1, eps=1e-5):
    m = x.mean(dim=axis, keepdim=True)
    v = x.var(dim=axis, keepdim=True, unbiased=False)
    return (x - m) / torch.sqrt(v + eps)
def _scatter_add(tgt, idx, src, axis=0):
    return tgt.clone().index_add_(axis, idx, src)
def _segment_sum(src, seg, n):
    out = torch.zeros((n,) + src.shape[1:], dtype=src.dtype)
    return out.index_add_(0, seg, src)
def _pad(x, pw):
    flat = tuple(v for pair in reversed(pw) for v in pair)
    return torch.nn.functional.pad(x, flat)

kernel_name = 'concat_heads__ssm_conv_s5__compressed_kv_cla__prompt'

def setup_inputs():

    _w = (lambda a: (lambda t: t.to(torch.bfloat16) if t.is_floating_point() else t)(torch.as_tensor(a)))
    def _A_setup():
        B, S, D = 1, 2048, 640
        P = D
        Lambda_re = -0.5 * (lambda _x: torch.exp(torch.as_tensor(_x)))((lambda *s: torch.randn(*s, dtype=torch.bfloat16) * 0.2)(P))
        Lambda_im = (lambda *s: torch.randn(*s, dtype=torch.bfloat16) * 0.4)(P) * 1.0
        B_re = (lambda *s: torch.randn(*s, dtype=torch.bfloat16) * 0.2)(P, D) * (D ** -0.5)
        B_im = (lambda *s: torch.randn(*s, dtype=torch.bfloat16) * 0.2)(P, D) * (D ** -0.5)
        C_re = (lambda *s: torch.randn(*s, dtype=torch.bfloat16) * 0.2)(D, P) * (P ** -0.5)
        C_im = (lambda *s: torch.randn(*s, dtype=torch.bfloat16) * 0.2)(D, P) * (P ** -0.5)
        log_dt = (lambda *s: torch.randn(*s, dtype=torch.bfloat16) * 0.2)(P) * 0.3 - 2.0
        Dskip = (lambda *s: torch.randn(*s, dtype=torch.bfloat16) * 0.05)(D)
        return {
            "x": _w((lambda *s: torch.randn(*s, dtype=torch.bfloat16) * 0.05)(B, S, D)),
            "Lambda_re": _w(Lambda_re),
            "Lambda_im": _w(Lambda_im),
            "B_re": _w(B_re),
            "B_im": _w(B_im),
            "C_re": _w(C_re),
            "C_im": _w(C_im),
            "log_dt": _w(log_dt),
            "Dskip": _w(Dskip),
        }
    _A_in = _A_setup()

    def _B_setup():
        B, H, Sq, Sk, D, M = 1, 32, 2048, 2048, 128, 640
        Dc = 4 * D
        return {
            "x": _w((lambda *s: torch.randn(*s, dtype=torch.bfloat16) * 0.8)(B, Sq, M)),
            "c_kv": _w((lambda *s: torch.randn(*s, dtype=torch.bfloat16) * 0.2)(B, Sk, Dc)),
            "w_q": _w((lambda *s: torch.randn(*s, dtype=torch.bfloat16) * 3.2)(M, H, D) * (M ** -0.5)),
            "w_uk": _w((lambda *s: torch.randn(*s, dtype=torch.bfloat16) * 3.2)(Dc, H, D) * (Dc ** -0.5)),
            "w_uv": _w((lambda *s: torch.randn(*s, dtype=torch.bfloat16) * 0.2)(Dc, H, D) * (Dc ** -0.5)),
            "w_o": _w((lambda *s: torch.randn(*s, dtype=torch.bfloat16) * 0.2)(H, D, M) * ((H * D) ** -0.5)),
        }
    _B_in = _B_setup()

    return {
        "A_x": _A_in["x"],
        "A_Lambda_re": _A_in["Lambda_re"],
        "A_Lambda_im": _A_in["Lambda_im"],
        "A_B_re": _A_in["B_re"],
        "A_B_im": _A_in["B_im"],
        "A_C_re": _A_in["C_re"],
        "A_C_im": _A_in["C_im"],
        "A_log_dt": _A_in["log_dt"],
        "A_Dskip": _A_in["Dskip"],
        "B_x": _B_in["x"],
        "B_c_kv": _B_in["c_kv"],
        "B_w_q": _B_in["w_q"],
        "B_w_uk": _B_in["w_uk"],
        "B_w_uv": _B_in["w_uv"],
        "B_w_o": _B_in["w_o"],
    }

def reference(A_x, A_Lambda_re, A_Lambda_im, A_B_re, A_B_im, A_C_re, A_C_im, A_log_dt, A_Dskip, B_x, B_c_kv, B_w_q, B_w_uk, B_w_uv, B_w_o):
    def _A_ref(x, Lambda_re, Lambda_im, B_re, B_im, C_re, C_im, log_dt, Dskip):
        Bsz, L, H = x.shape
        Lam = Lambda_re + 1j * Lambda_im
        Bmat = B_re + 1j * B_im
        Cmat = C_re + 1j * C_im
        dt = torch.exp(log_dt)
        Lam_bar = torch.exp(dt * Lam)
        B_bar = ((Lam_bar - 1.0) / Lam)[:, None] * Bmat
        Bu = torch.einsum("bld,pd->blp", x + 0j, B_bar)
        h = torch.zeros_like(Bu[:, 0, :])
        hs = []
        for t in range(L):
            h = Lam_bar[None, :] * h + Bu[:, t, :]
            hs.append(h)
        hseq = torch.stack(hs, 1)
        y = 2.0 * torch.einsum("blp,dp->bld", hseq, Cmat).real + x * Dskip[None, None, :]
        return y

    def _B_ref(x, c_kv, w_q, w_uk, w_uv, w_o):
        B, Sq, M = x.shape[0], x.shape[1], x.shape[2]
        Sk = c_kv.shape[1]
        H, D = w_q.shape[1], w_q.shape[2]
        q = torch.einsum("bqm,mhd->bhqd", x, w_q)
        k = torch.einsum("bkc,chd->bhkd", c_kv, w_uk)
        v = torch.einsum("bkc,chd->bhkd", c_kv, w_uv)
        scores = torch.einsum("bhqd,bhkd->bhqk", q, k) * (D ** -0.5)
        off = Sk - Sq
        qi = torch.arange(Sq)[:, None]
        kj = torch.arange(Sk)[None, :]
        cmask = kj <= (qi + off)
        scores = torch.where(cmask[None, None], scores, -1e9)
        p = _softmax(scores, axis=-1)
        ctx = torch.einsum("bhqk,bhkd->bhqd", p, v)
        return torch.einsum("bhqd,hdm->bqm", ctx, w_o)

    a = _A_ref(A_x, A_Lambda_re, A_Lambda_im, A_B_re, A_B_im, A_C_re, A_C_im, A_log_dt, A_Dskip)
    b = _B_ref(B_x, B_c_kv, B_w_q, B_w_uk, B_w_uv, B_w_o)
    return torch.concatenate([a, b], axis=-1)
```

```cpp
#include <hip/hip_runtime.h>
#include <hip/hip_bf16.h>
#include <stdint.h>
typedef unsigned short u16;
__device__ __forceinline__ float bf2f(u16 v){ return __uint_as_float(((unsigned)v)<<16); }
__device__ __forceinline__ u16 f2bf(float f){ __hip_bfloat16 b = __float2bfloat16(f); return *(u16*)&b; }
__device__ __forceinline__ float rbf(float f){ return bf2f(f2bf(f)); }

__global__ void gemm_kn(const u16* A, const u16* B, u16* C, int M, int N, int K){
  __shared__ float As[32][33], Bs[32][33];
  int tx=threadIdx.x, ty=threadIdx.y; int row=blockIdx.y*32+ty, col=blockIdx.x*32+tx; float acc=0;
  for(int k0=0;k0<K;k0+=32){
    As[ty][tx]=bf2f(A[(size_t)row*K+k0+tx]); Bs[ty][tx]=bf2f(B[(size_t)(k0+ty)*N+col]);
    __syncthreads();
    for(int k=0;k<32;k++) acc+=As[ty][k]*Bs[k][tx];
    __syncthreads();
  }
  C[(size_t)row*N+col]=f2bf(acc);
}
__global__ void gemm_kn_out(const u16* A, const u16* B, float* C, int M, int N, int K, int ldc){
  __shared__ float As[32][33], Bs[32][33];
  int tx=threadIdx.x, ty=threadIdx.y; int row=blockIdx.y*32+ty, col=blockIdx.x*32+tx; float acc=0;
  for(int k0=0;k0<K;k0+=32){
    As[ty][tx]=bf2f(A[(size_t)row*K+k0+tx]); Bs[ty][tx]=bf2f(B[(size_t)(k0+ty)*N+col]);
    __syncthreads();
    for(int k=0;k<32;k++) acc+=As[ty][k]*Bs[k][tx];
    __syncthreads();
  }
  C[(size_t)row*ldc+col]=rbf(acc);
}
__global__ void gemm_nt_f32(const u16* A, const u16* Bt, float* C, int M, int N, int K){
  __shared__ float As[32][33], Bs[32][33];
  int tx=threadIdx.x, ty=threadIdx.y; int row=blockIdx.y*32+ty, col=blockIdx.x*32+tx; float acc=0;
  for(int k0=0;k0<K;k0+=32){
    As[ty][tx]=bf2f(A[(size_t)row*K+k0+tx]); Bs[ty][tx]=bf2f(Bt[(size_t)(blockIdx.x*32+ty)*K+k0+tx]);
    __syncthreads();
    for(int k=0;k<32;k++) acc+=As[ty][k]*Bs[tx][k];
    __syncthreads();
  }
  C[(size_t)row*N+col]=acc;
}
__global__ void scan_k(const u16* Lre,const u16* Lim,const u16* ldt, float* bre, float* bim, int L, int P){
  int p=blockIdx.x*blockDim.x+threadIdx.x; if(p>=P) return;
  float lr=bf2f(Lre[p]), li=bf2f(Lim[p]); float dt=rbf(expf(bf2f(ldt[p])));
  float a=dt*lr,b=dt*li; float e=expf(a); float ar=e*cosf(b), ai=e*sinf(b);
  float nr=ar-1.f, ni=ai; float den=lr*lr+li*li; float cr=(nr*lr+ni*li)/den, ci=(ni*lr-nr*li)/den;
  float hr=0,hi=0;
  for(int t=0;t<L;t++){ float xr=bre[(size_t)t*P+p], xi=bim[(size_t)t*P+p]; float ur=cr*xr-ci*xi, ui=cr*xi+ci*xr;
    float nhr=ar*hr-ai*hi+ur, nhi=ar*hi+ai*hr+ui; hr=nhr; hi=nhi; bre[(size_t)t*P+p]=hr; bim[(size_t)t*P+p]=hi; }
}
__global__ void yk(const float* hr,const float* hi,const u16* Cre,const u16* Cim,const u16* x,const u16* Dk,float* out,int L,int P,int ldc){
  __shared__ float As[32][33], Bs[32][33], A2[32][33], B2[32][33];
  int tx=threadIdx.x, ty=threadIdx.y; int row=blockIdx.y*32+ty, col=blockIdx.x*32+tx; float acc=0;
  for(int k0=0;k0<P;k0+=32){
    As[ty][tx]=rbf(hr[(size_t)row*P+k0+tx]); A2[ty][tx]=rbf(hi[(size_t)row*P+k0+tx]);
    Bs[ty][tx]=bf2f(Cre[(size_t)(blockIdx.x*32+ty)*P+k0+tx]); B2[ty][tx]=bf2f(Cim[(size_t)(blockIdx.x*32+ty)*P+k0+tx]);
    __syncthreads();
    for(int k=0;k<32;k++) acc+=As[ty][k]*Bs[tx][k]-A2[ty][k]*B2[tx][k];
    __syncthreads();
  }
  out[(size_t)row*ldc+col]=2.f*acc+rbf(bf2f(x[(size_t)row*P+col])*bf2f(Dk[col]));
}
__global__ void attn(const u16* Q,const u16* K,const u16* V,u16* O,int S){
  int q=blockIdx.x,h=blockIdx.y,t=threadIdx.x; __shared__ float qs[128]; __shared__ float sc[2048]; __shared__ float red[128];
  qs[t]=bf2f(Q[(size_t)q*4096+h*128+t]); __syncthreads();
  float mx=-1e30f;
  for(int k=t;k<=q;k+=128){ float s=0; const u16* kp=K+(size_t)k*4096+h*128; for(int d=0;d<128;d++) s+=qs[d]*bf2f(kp[d]); s*=0.08838834764831845f; sc[k]=s; mx=fmaxf(mx,s);}
  red[t]=mx; __syncthreads(); for(int o=64;o>0;o>>=1){ if(t<o) red[t]=fmaxf(red[t],red[t+o]); __syncthreads(); } mx=red[0]; __syncthreads();
  float sum=0; for(int k=t;k<=q;k+=128){ float e=expf(sc[k]-mx); sc[k]=e; sum+=e; }
  red[t]=sum; __syncthreads(); for(int o=64;o>0;o>>=1){ if(t<o) red[t]+=red[t+o]; __syncthreads(); } sum=red[0];
  float acc=0; for(int k=0;k<=q;k++) acc+=rbf(sc[k])*bf2f(V[(size_t)k*4096+h*128+t]);
  O[(size_t)q*4096+h*128+t]=f2bf(acc/sum);
}
extern "C" void kernel_launch(void* const* d_in, const int* in_sizes, int n_in, void* d_out, int out_size, void* d_ws, size_t ws_size, hipStream_t stream) {
  const u16* Ax=(const u16*)d_in[0]; const u16* Lre=(const u16*)d_in[1]; const u16* Lim=(const u16*)d_in[2];
  const u16* Bre=(const u16*)d_in[3]; const u16* Bim=(const u16*)d_in[4]; const u16* Cre=(const u16*)d_in[5]; const u16* Cim=(const u16*)d_in[6];
  const u16* ldt=(const u16*)d_in[7]; const u16* Dk=(const u16*)d_in[8]; const u16* Bx=(const u16*)d_in[9]; const u16* ckv=(const u16*)d_in[10];
  const u16* wq=(const u16*)d_in[11]; const u16* wuk=(const u16*)d_in[12]; const u16* wuv=(const u16*)d_in[13]; const u16* wo=(const u16*)d_in[14];
  float* out=(float*)d_out; char* ws=(char*)d_ws;
  u16* Q=(u16*)ws; u16* K=(u16*)(ws+(16<<20)); u16* V=(u16*)(ws+(32<<20)); u16* O=(u16*)(ws+(48<<20));
  float* hr=(float*)(ws+(64<<20)); float* hi=(float*)(ws+(72<<20));
  dim3 b(32,32);
  gemm_kn<<<dim3(128,64),b,0,stream>>>(Bx,wq,Q,2048,4096,640);
  gemm_kn<<<dim3(128,64),b,0,stream>>>(ckv,wuk,K,2048,4096,512);
  gemm_kn<<<dim3(128,64),b,0,stream>>>(ckv,wuv,V,2048,4096,512);
  attn<<<dim3(2048,32),128,0,stream>>>(Q,K,V,O,2048);
  gemm_kn_out<<<dim3(20,64),b,0,stream>>>(O,wo,out+640,2048,640,4096,1280);
  gemm_nt_f32<<<dim3(20,64),b,0,stream>>>(Ax,Bre,hr,2048,640,640);
  gemm_nt_f32<<<dim3(20,64),b,0,stream>>>(Ax,Bim,hi,2048,640,640);
  scan_k<<<10,64,0,stream>>>(Lre,Lim,ldt,hr,hi,2048,640);
  yk<<<dim3(20,64),b,0,stream>>>(hr,hi,Cre,Cim,Ax,Dk,out,2048,640,1280);
}
```

```cpp
#include <hip/hip_runtime.h>
#include <stdint.h>
typedef unsigned short u16;
typedef short bf16x8 __attribute__((ext_vector_type(8)));
typedef float f32x4 __attribute__((ext_vector_type(4)));
typedef unsigned u32x4 __attribute__((ext_vector_type(4)));
typedef __bf16 bf16x2_t __attribute__((ext_vector_type(2)));
typedef float f32x2_t __attribute__((ext_vector_type(2)));
constexpr size_t WS_KV = 1048576, WS_CTX = 34603008, PSTR = 1048576;
__device__ __forceinline__ float bf2f(u16 v) { return __uint_as_float(((unsigned)v) << 16); }
__device__ __forceinline__ unsigned cvtpk(float lo, float hi) { f32x2_t v = {lo, hi}; bf16x2_t b = __builtin_convertvector(v, bf16x2_t); return __builtin_bit_cast(unsigned, b); }
__device__ __forceinline__ void attn_slow16(const u16* bx, const u16* wq, const char* KVh, char* ctx, int qb16, int head, int lane) {
  const int r = lane & 15, g = lane >> 4, qrow = 16 * qb16 + r;
  bf16x8 q[4];
  #pragma unroll
  for (int ds = 0; ds < 4; ++ds) {
    float a[8] = {0.f, 0.f, 0.f, 0.f, 0.f, 0.f, 0.f, 0.f};
    const u16* wp = wq + head * 128 + 32 * ds + 4 * g;
    for (int m = 0; m < 640; ++m) { const float xv = bf2f(bx[(size_t)qrow * 640 + m]);
      #pragma unroll
      for (int j = 0; j < 8; ++j) a[j] = fmaf(xv, bf2f(wp[(size_t)m * 4096 + 16 * (j >> 2) + (j & 3)]), a[j]); }
    const float c = 0.12751743f;
    u32x4 w4 = {cvtpk(a[0] * c, a[1] * c), cvtpk(a[2] * c, a[3] * c), cvtpk(a[4] * c, a[5] * c), cvtpk(a[6] * c, a[7] * c)};
    q[ds] = __builtin_bit_cast(bf16x8, w4);
  }
  float m = -INFINITY, l = 0.f; f32x4 o[8];
  #pragma unroll
  for (int db = 0; db < 8; ++db) o[db] = (f32x4){0.f, 0.f, 0.f, 0.f};
  const int ngrp = (16 * qb16 + 15) / 32 + 1;
  for (int u = 0; u < ngrp; ++u) {
    const char* T = KVh + (size_t)(u >> 1) * 32768; const int ks = u & 1;
    f32x4 s0 = {0.f, 0.f, 0.f, 0.f}, s1 = s0;
    #pragma unroll
    for (int ds = 0; ds < 4; ++ds) {
      s0 = __builtin_amdgcn_mfma_f32_16x16x32_bf16(*(const bf16x8*)(T + ((2 * ks) * 4 + ds) * 1024 + lane * 16), q[ds], s0, 0, 0, 0);
      s1 = __builtin_amdgcn_mfma_f32_16x16x32_bf16(*(const bf16x8*)(T + ((2 * ks + 1) * 4 + ds) * 1024 + lane * 16), q[ds], s1, 0, 0, 0); }
    float mx = -INFINITY;
    #pragma unroll
    for (int j = 0; j < 4; ++j) { const int k0 = 32 * u + 4 * g + j; if (k0 > qrow) s0[j] = -INFINITY; if (k0 + 16 > qrow) s1[j] = -INFINITY; mx = fmaxf(mx, fmaxf(s0[j], s1[j])); }
    mx = fmaxf(mx, __shfl_xor(mx, 16)); mx = fmaxf(mx, __shfl_xor(mx, 32));
    const float mn = fmaxf(m, mx), al = exp2f(m - mn); m = mn;
    float p[8];
    #pragma unroll
    for (int j = 0; j < 4; ++j) { p[j] = exp2f(s0[j] - mn); p[4 + j] = exp2f(s1[j] - mn); }
    u32x4 pw = {cvtpk(p[0], p[1]), cvtpk(p[2], p[3]), cvtpk(p[4], p[5]), cvtpk(p[6], p[7])};
    l = l * al + (((p[0] + p[1]) + (p[2] + p[3])) + ((p[4] + p[5]) + (p[6] + p[7])));
    #pragma unroll
    for (int db = 0; db < 8; ++db) { o[db] *= al; o[db] = __builtin_amdgcn_mfma_f32_16x16x32_bf16(*(const bf16x8*)(T + 16384 + (db * 2 + ks) * 1024 + lane * 16), __builtin_bit_cast(bf16x8, pw), o[db], 0, 0, 0); }
  }
  l += __shfl_xor(l, 16); l += __shfl_xor(l, 32);
  const float rl = 1.0f / l;
  #pragma unroll
  for (int ds = 0; ds < 4; ++ds) { const f32x4 a = o[2 * ds] * rl, b = o[2 * ds + 1] * rl;
    u32x4 w4 = {cvtpk(a[0], a[1]), cvtpk(a[2], a[3]), cvtpk(b[0], b[1]), cvtpk(b[2], b[3])};
    *(u32x4*)(ctx + (size_t)(qb16 >> 3) * PSTR + (head * 2 + (ds >> 1)) * 16384 + (qb16 & 7) * 2048 + (ds & 1) * 1024 + lane * 16) = w4; }
}

__global__ __launch_bounds__(256, 1) void fused_kv_q_attn(const char* ckv, const char* wuk, const char* wuv, char* ws, const char* bx, const char* wq,
                                                         const char* ax, const char* bre, const char* bim, const u16* lre, const u16* lim, const u16* ldt, const char* wo, const char* cre, const char* cim) {
  extern __shared__ __attribute__((aligned(16))) char lds[];
  const int bid = blockIdx.x; const int w = __builtin_amdgcn_readfirstlane(threadIdx.x >> 6); const unsigned ldsb = (unsigned)(size_t)lds;
  unsigned fa, fb, v_lre, v_lim, v_ldt;
  const unsigned long long ka = (unsigned long long)__builtin_amdgcn_kernarg_segment_ptr();
  asm volatile(
R"(s_load_dwordx16 s[16:31],%5,0x0
s_load_dwordx2 s[34:35],%5,0x40
s_mov_b32 s56,%7
s_mov_b32 s57,%8
s_and_b32 s63,%6,7
s_lshr_b32 s64,%6,3
v_mbcnt_lo_u32_b32 v254,-1,0
v_mbcnt_hi_u32_b32 v254,-1,v254
v_lshlrev_b32 v255,4,v254
s_waitcnt lgkmcnt(0)
s_and_b32 s58,s64,7
s_lshr_b32 s59,s64,3
s_cmp_ge_u32 s58,3
s_cselect_b32 s65,1,0
s_cselect_b32 s60,5,3
s_cselect_b32 s61,3,0
s_mul_i32 s59,s59,s60
s_add_u32 s73,s59,s58
s_sub_u32 s73,s73,s61
s_cmp_ge_u32 s73,10
s_cselect_b32 s58,1,0
s_lshl_b32 s75,s63,1
s_add_u32 s75,s75,s58
s_xor_b32 s75,s75,0
s_mul_i32 s58,s58,10
s_sub_u32 s76,s73,s58
s_cmp_eq_u32 s65,0
s_cbranch_scc1 L_A1_%=
s_load_dwordx4 s[92:95],%5,0x48
s_load_dwordx2 s[96:97],%5,0x58
s_lshl_b32 s58,s76,7
v_lshl_add_u32 v249,v254,1,s58
s_waitcnt lgkmcnt(0)
global_load_ushort %[lre],v249,s[92:93]
global_load_ushort %[lim],v249,s[94:95]
global_load_ushort %[ldt],v249,s[96:97]
s_mov_b64 s[36:37],s[28:29]
s_mov_b32 s38,0x280000
s_mov_b32 s39,0x20000
s_mov_b64 s[40:41],s[30:31]
s_mov_b32 s42,0xc8000
s_mov_b32 s43,0x20000
s_mov_b64 s[44:45],s[34:35]
s_sub_u32 s44,s44,0x800
s_subb_u32 s45,s45,0
s_mov_b32 s46,0xc8800
s_mov_b32 s47,0x20000
v_and_b32 v246,7,v254
v_lshrrev_b32 v247,4,v254
v_mov_b32 v248,v247
v_xor_b32 v248,v248,v246
v_lshlrev_b32 v248,4,v248
v_lshrrev_b32 v253,5,v254
v_lshlrev_b32 v253,4,v253
v_bfe_u32 v252,v254,3,2
v_add_u32 v253,v253,v252
v_mul_u32_u24 v253,1280,v253
v_add3_u32 v0,v253,v248,0
v_or_b32 v248,4,v247
v_xor_b32 v248,v248,v246
v_lshlrev_b32 v248,4,v248
v_lshrrev_b32 v253,5,v254
v_lshlrev_b32 v253,4,v253
v_bfe_u32 v252,v254,3,2
v_add_u32 v253,v253,v252
v_mul_u32_u24 v253,1280,v253
v_add3_u32 v1,v253,v248,0
v_add_u32 v1,39936,v1
v_mov_b32 v248,v247
v_xor_b32 v248,v248,v246
v_lshlrev_b32 v248,4,v248
v_lshrrev_b32 v253,5,v254
v_lshlrev_b32 v253,4,v253
v_bfe_u32 v252,v254,3,2
v_add_u32 v253,v253,v252
v_mul_u32_u24 v253,1280,v253
v_add3_u32 v2,v253,v248,0
v_add_u32 v2,3072,v2
v_or_b32 v248,4,v247
v_xor_b32 v248,v248,v246
v_lshlrev_b32 v248,4,v248
v_lshrrev_b32 v253,5,v254
v_lshlrev_b32 v253,4,v253
v_bfe_u32 v252,v254,3,2
v_add_u32 v253,v253,v252
v_mul_u32_u24 v253,1280,v253
v_add3_u32 v3,v253,v248,0
v_add_u32 v3,43008,v3
v_and_b32 v246,7,v254
v_lshrrev_b32 v247,4,v254
v_mov_b32 v248,v247
v_xor_b32 v248,v248,v246
v_lshlrev_b32 v248,4,v248
v_lshrrev_b32 v253,3,v254
v_mul_u32_u24 v253,1280,v253
v_add3_u32 v4,v253,v248,0
v_or_b32 v248,4,v247
v_xor_b32 v248,v248,v246
v_lshlrev_b32 v248,4,v248
v_lshrrev_b32 v253,3,v254
v_mul_u32_u24 v253,1280,v253
v_add3_u32 v5,v253,v248,0
v_add_u32 v5,10240,v5
v_mov_b32 v248,v247
v_xor_b32 v248,v248,v246
v_lshlrev_b32 v248,4,v248
v_lshrrev_b32 v253,3,v254
v_mul_u32_u24 v253,1280,v253
v_add3_u32 v6,v253,v248,0
v_or_b32 v248,4,v247
v_xor_b32 v248,v248,v246
v_lshlrev_b32 v248,4,v248
v_lshrrev_b32 v253,3,v254
v_mul_u32_u24 v253,1280,v253
v_add3_u32 v7,v253,v248,0
v_add_u32 v7,10240,v7
v_subrev_u32 v5,0x400,v5
v_subrev_u32 v7,0x400,v7
s_lshl_b32 s58,s75,7
s_lshr_b32 s59,s56,1
s_lshl_b32 s59,s59,6
s_add_u32 s58,s58,s59
s_and_b32 s59,s56,1
s_lshl_b32 s59,s59,3
s_add_u32 s58,s58,s59
s_mul_i32 s66,s58,1280
s_add_u32 s66,s66,0
s_lshl_b32 s58,s76,6
s_lshl_b32 s59,s56,4
s_add_u32 s58,s58,s59
s_mul_i32 s68,s58,1280
s_add_u32 s68,s68,0
s_lshl_b32 s58,s56,12
s_add_u32 s70,s57,s58
s_add_u32 m0,s70,32768
s_nop 0
buffer_load_dwordx4 v0,s[36:39],s66 offen offset:0 lds
buffer_load_dwordx4 v1,s[36:39],s66 offen offset:1024 lds
buffer_load_dwordx4 v2,s[36:39],s66 offen offset:2048 lds
buffer_load_dwordx4 v3,s[36:39],s66 offen offset:3072 lds
s_add_u32 s66,s66,128
s_add_u32 m0,s70,49152
s_nop 0
buffer_load_dwordx4 v4,s[40:43],s68 offen offset:0 lds
buffer_load_dwordx4 v5,s[40:43],s68 offen offset:1024 lds
buffer_load_dwordx4 v6,s[44:47],s68 offen offset:2048 lds
buffer_load_dwordx4 v7,s[44:47],s68 offen offset:3072 lds
s_add_u32 s68,s68,128
s_add_u32 m0,s70,98304
s_nop 0
buffer_load_dwordx4 v0,s[36:39],s66 offen offset:0 lds
buffer_load_dwordx4 v1,s[36:39],s66 offen offset:1024 lds
buffer_load_dwordx4 v2,s[36:39],s66 offen offset:2048 lds
buffer_load_dwordx4 v3,s[36:39],s66 offen offset:3072 lds
s_add_u32 s66,s66,128
s_add_u32 m0,s70,114688
s_nop 0
buffer_load_dwordx4 v4,s[40:43],s68 offen offset:0 lds
buffer_load_dwordx4 v5,s[40:43],s68 offen offset:1024 lds
buffer_load_dwordx4 v6,s[44:47],s68 offen offset:2048 lds
buffer_load_dwordx4 v7,s[44:47],s68 offen offset:3072 lds
s_add_u32 s68,s68,128
s_add_u32 m0,s70,0
s_nop 0
buffer_load_dwordx4 v0,s[36:39],s66 offen offset:0 lds
buffer_load_dwordx4 v1,s[36:39],s66 offen offset:1024 lds
buffer_load_dwordx4 v2,s[36:39],s66 offen offset:2048 lds
buffer_load_dwordx4 v3,s[36:39],s66 offen offset:3072 lds
s_add_u32 s66,s66,128
s_add_u32 m0,s70,16384
s_nop 0
buffer_load_dwordx4 v4,s[40:43],s68 offen offset:0 lds
buffer_load_dwordx4 v5,s[40:43],s68 offen offset:1024 lds
buffer_load_dwordx4 v6,s[44:47],s68 offen offset:2048 lds
buffer_load_dwordx4 v7,s[44:47],s68 offen offset:3072 lds
s_add_u32 s68,s68,128
s_add_u32 m0,s70,65536
s_nop 0
buffer_load_dwordx4 v0,s[36:39],s66 offen offset:0 lds
buffer_load_dwordx4 v1,s[36:39],s66 offen offset:1024 lds
buffer_load_dwordx4 v2,s[36:39],s66 offen offset:2048 lds
buffer_load_dwordx4 v3,s[36:39],s66 offen offset:3072 lds
s_add_u32 s66,s66,128
s_add_u32 m0,s70,81920
s_nop 0
buffer_load_dwordx4 v4,s[40:43],s68 offen offset:0 lds
buffer_load_dwordx4 v5,s[40:43],s68 offen offset:1024 lds
buffer_load_dwordx4 v6,s[44:47],s68 offen offset:2048 lds
buffer_load_dwordx4 v7,s[44:47],s68 offen offset:3072 lds
s_add_u32 s68,s68,128
s_lshl_b32 s58,s73,2
s_add_u32 s58,s58,s56
v_lshlrev_b32 v243,7,v254
s_mov_b64 s[48:49],s[34:35]
s_mov_b32 s50,0xc8000
s_mov_b32 s51,0x20000
s_lshl_b32 s59,s58,13
buffer_load_dword v244,v243,s[40:43],s59 offen
s_add_u32 s60,s59,655360
buffer_load_dword v244,v243,s[40:43],s60 offen
buffer_load_dword v244,v243,s[48:51],s59 offen
buffer_load_dword v244,v243,s[48:51],s60 offen
s_mul_i32 s60,s63,327680
s_lshr_b32 s59,s59,1
s_add_u32 s60,s60,s59
v_min_u32 v246,0xf80,v243
buffer_load_dword v244,v246,s[36:39],s60 offen
s_waitcnt vmcnt(37)
s_branch L_A1E_%=
L_A1_%=:
s_lshr_b32 s74,s64,4
s_cmp_lg_u32 s74,0
s_cselect_b64 s[48:49],s[20:21],s[18:19]
s_mov_b32 s50,0x400000
s_mov_b32 s51,0x20000
s_mov_b64 s[52:53],s[16:17]
s_mov_b32 s54,0x200000
s_mov_b32 s55,0x20000
v_and_b32 v246,7,v254
v_lshrrev_b32 v247,4,v254
v_lshrrev_b32 v253,3,v254
v_mul_u32_u24 v253,8192,v253
v_and_b32 v248,1,v247
v_lshlrev_b32 v248,1,v248
v_xor_b32 v248,v248,v246
v_lshl_add_u32 v230,v248,4,v253
v_and_b32 v248,1,v247
v_lshlrev_b32 v248,1,v248
v_or_b32 v248,4,v248
v_xor_b32 v248,v248,v246
v_lshl_add_u32 v231,v248,4,v253
v_add_u32 v231,64512,v231
v_and_b32 v248,1,v247
v_lshlrev_b32 v248,1,v248
v_xor_b32 v248,v248,v246
v_lshl_add_u32 v232,v248,4,v253
v_add_u32 v232,129024,v232
v_and_b32 v248,1,v247
v_lshlrev_b32 v248,1,v248
v_or_b32 v248,4,v248
v_xor_b32 v248,v248,v246
v_lshl_add_u32 v233,v248,4,v253
v_add_u32 v233,193536,v233
v_and_b32 v246,7,v254
v_lshrrev_b32 v247,4,v254
v_mov_b32 v248,v247
v_xor_b32 v248,v248,v246
v_lshlrev_b32 v248,4,v248
v_lshrrev_b32 v253,3,v254
v_mul_u32_u24 v253,1024,v253
v_add3_u32 v234,v253,v248,0
v_or_b32 v248,4,v247
v_xor_b32 v248,v248,v246
v_lshlrev_b32 v248,4,v248
v_lshrrev_b32 v253,3,v254
v_mul_u32_u24 v253,1024,v253
v_add3_u32 v235,v253,v248,0
v_add_u32 v235,7168,v235
v_mov_b32 v248,v247
v_xor_b32 v248,v248,v246
v_lshlrev_b32 v248,4,v248
v_lshrrev_b32 v253,3,v254
v_mul_u32_u24 v253,1024,v253
v_add3_u32 v236,v253,v248,0
v_add_u32 v236,14336,v236
v_or_b32 v248,4,v247
v_xor_b32 v248,v248,v246
v_lshlrev_b32 v248,4,v248
v_lshrrev_b32 v253,3,v254
v_mul_u32_u24 v253,1024,v253
v_add3_u32 v237,v253,v248,0
v_add_u32 v237,21504,v237
s_bfe_u32 s60,s64,0x10003
s_lshl_b32 s60,s60,1
s_lshl_b32 s61,s63,2
s_add_u32 s72,s60,s61
s_lshl_b32 s60,s72,8
s_lshl_b32 s61,s56,7
s_add_u32 s79,s60,s61
s_add_u32 s80,s79,262144
s_and_b32 s60,s56,1
s_add_u32 s72,s72,s60
s_lshl_b32 s72,s72,20
s_and_b32 s61,s64,7
s_lshl_b32 s60,s61,17
s_add_u32 s72,s72,s60
s_lshl_b32 s60,s61,18
s_lshl_b32 s61,s56,16
s_add_u32 s81,s60,s61
s_add_u32 s82,s81,32768
s_lshl_b32 s60,s56,13
s_add_u32 s83,s57,s60
s_add_u32 s84,s83,0x10000
s_lshr_b32 s60,s56,1
s_lshl_b32 s60,s60,16
s_add_u32 s72,s72,s60
s_lshl_b32 s60,s74,14
s_add_u32 s72,s72,s60
s_add_u32 m0,s83,0
s_nop 0
buffer_load_dwordx4 v230,s[48:51],s79 offen offset:0 lds
buffer_load_dwordx4 v231,s[48:51],s79 offen offset:1024 lds
buffer_load_dwordx4 v232,s[48:51],s79 offen offset:2048 lds
buffer_load_dwordx4 v233,s[48:51],s79 offen offset:3072 lds
s_add_u32 m0,s83,4096
s_nop 0
buffer_load_dwordx4 v230,s[48:51],s80 offen offset:0 lds
buffer_load_dwordx4 v231,s[48:51],s80 offen offset:1024 lds
buffer_load_dwordx4 v232,s[48:51],s80 offen offset:2048 lds
buffer_load_dwordx4 v233,s[48:51],s80 offen offset:3072 lds
s_add_u32 s79,s79,524288
s_add_u32 s80,s80,524288
s_add_u32 m0,s84,0
s_nop 0
buffer_load_dwordx4 v234,s[52:55],s81 offen offset:0 lds
buffer_load_dwordx4 v235,s[52:55],s81 offen offset:1024 lds
buffer_load_dwordx4 v236,s[52:55],s81 offen offset:2048 lds
buffer_load_dwordx4 v237,s[52:55],s81 offen offset:3072 lds
s_add_u32 m0,s84,4096
s_nop 0
buffer_load_dwordx4 v234,s[52:55],s82 offen offset:0 lds
buffer_load_dwordx4 v235,s[52:55],s82 offen offset:1024 lds
buffer_load_dwordx4 v236,s[52:55],s82 offen offset:2048 lds
buffer_load_dwordx4 v237,s[52:55],s82 offen offset:3072 lds
s_add_u32 s81,s81,128
s_add_u32 s82,s82,128
s_add_u32 m0,s83,32768
s_nop 0
buffer_load_dwordx4 v230,s[48:51],s79 offen offset:0 lds
buffer_load_dwordx4 v231,s[48:51],s79 offen offset:1024 lds
buffer_load_dwordx4 v232,s[48:51],s79 offen offset:2048 lds
buffer_load_dwordx4 v233,s[48:51],s79 offen offset:3072 lds
s_add_u32 m0,s83,36864
s_nop 0
buffer_load_dwordx4 v230,s[48:51],s80 offen offset:0 lds
buffer_load_dwordx4 v231,s[48:51],s80 offen offset:1024 lds
buffer_load_dwordx4 v232,s[48:51],s80 offen offset:2048 lds
buffer_load_dwordx4 v233,s[48:51],s80 offen offset:3072 lds
s_add_u32 s79,s79,524288
s_add_u32 s80,s80,524288
s_add_u32 m0,s84,32768
s_nop 0
buffer_load_dwordx4 v234,s[52:55],s81 offen offset:0 lds
buffer_load_dwordx4 v235,s[52:55],s81 offen offset:1024 lds
buffer_load_dwordx4 v236,s[52:55],s81 offen offset:2048 lds
buffer_load_dwordx4 v237,s[52:55],s81 offen offset:3072 lds
s_add_u32 m0,s84,36864
s_nop 0
buffer_load_dwordx4 v234,s[52:55],s82 offen offset:0 lds
buffer_load_dwordx4 v235,s[52:55],s82 offen offset:1024 lds
buffer_load_dwordx4 v236,s[52:55],s82 offen offset:2048 lds
buffer_load_dwordx4 v237,s[52:55],s82 offen offset:3072 lds
s_add_u32 s81,s81,128
s_add_u32 s82,s82,128
L_A1E_%=:
)"
    : "=&s"(fa), "=&s"(fb), [lre] "=&v"(v_lre), [lim] "=&v"(v_lim), [ldt] "=&v"(v_ldt) : "s"(ka), "s"(bid), "s"(w), "s"(ldsb) : "s16","s17","s18","s19","s20","s21","s22","s23","s24","s25","s26","s27","s28","s29","s30","s31","s34","s35","s36","s37","s38","s39","s40","s41","s42","s43","s44","s45","s46","s47","s48","s49","s50","s51","s52","s53","s54","s55","s56","s57","s58","s59","s60","s61","s62","s63","s64","s65","s66","s67","s68","s69","s70","s71","s72","s73","s74","s75","s76","s77","s78","s79","s80","s81","s82","s83","s84","s85","s86","s87","s88","s89","s90","s91","s92","s93","s94","s95","s96","s97","s98","s99","vcc","scc","memory","v0","v1","v2","v3","v4","v5","v6","v7","v8","v9","v10","v11","v12","v13","v14","v15","v224","v225","v226","v227","v228","v229","v230","v231","v232","v233","v234","v235","v236","v237","v238","v239","v240","v241","v242","v243","v244","v245","v246","v247","v248","v249","v250","v251","v252","v253","v254","v255");
  {
    const int sl = bid >> 3;
    if ((sl & 7) >= 3 && threadIdx.x < 64) {
      const float lr = bf2f((u16)v_lre), li = bf2f((u16)v_lim);
      const float dt = bf2f((u16)(cvtpk(expf(bf2f((u16)v_ldt)), 0.f) & 0xffffu));
      const float a = dt * lr, b = dt * li, e = expf(a); float sn, cs; sincosf(b, &sn, &cs);
      const float ar = e * cs, ai = e * sn;
      const double nr = (double)ar - 1.0, ni = ai, den = (double)lr * lr + (double)li * li;
      const double cr = (nr * lr + ni * li) / den, ci = (ni * lr - nr * li) / den;
      double pr = ar, pi = ai, t;
      for (int k = 0; k < 4; ++k) { t = pr * pr - pi * pi; pi = 2.0 * pr * pi; pr = t; }
      float* C = (float*)(lds + 152 * 1024) + threadIdx.x;
      C[0] = ar; C[64] = ai; C[128] = (float)(2.0 * cr); C[192] = (float)(2.0 * ci); C[256] = (float)pr; C[320] = (float)pi;
      f32x2_t* PW = (f32x2_t*)(lds + 154 * 1024) + threadIdx.x;
      double wr = 1.0, wi = 0.0;
      for (int k = 0; k < 8; ++k) { PW[64 * k] = (f32x2_t){(float)wr, (float)wi}; t = wr * pr - wi * pi; wi = wr * pi + wi * pr; wr = t; }
      C[384] = (float)wr; C[448] = (float)wi;
    } else if ((sl & 7) >= 3) {
      const u32x4 z = {0u, 0u, 0u, 0u};
      for (int k = threadIdx.x - 64; k < 480; k += 192) *(u32x4*)(lds + 128 * 1024 + k * 16) = z;
      for (int k = threadIdx.x - 64; k < 224; k += 192) *(u32x4*)(lds + 144 * 1024 + (k / 56) * 1920 + (k % 56) * 16) = z;
    }
  }
  asm volatile(
R"(s_load_dwordx16 s[16:31],%2,0x0
s_load_dwordx2 s[34:35],%2,0x40
s_mov_b32 s56,%4
s_mov_b32 s57,%5
s_and_b32 s63,%3,7
s_lshr_b32 s64,%3,3
v_mbcnt_lo_u32_b32 v254,-1,0
v_mbcnt_hi_u32_b32 v254,-1,v254
v_lshlrev_b32 v255,4,v254
s_waitcnt lgkmcnt(0)
s_and_b32 s58,s64,7
s_lshr_b32 s59,s64,3
s_cmp_ge_u32 s58,3
s_cselect_b32 s65,1,0
s_cselect_b32 s60,5,3
s_cselect_b32 s61,3,0
s_mul_i32 s59,s59,s60
s_add_u32 s73,s59,s58
s_sub_u32 s73,s73,s61
s_cmp_ge_u32 s73,10
s_cselect_b32 s58,1,0
s_lshl_b32 s75,s63,1
s_add_u32 s75,s75,s58
s_xor_b32 s75,s75,0
s_mul_i32 s58,s58,10
s_sub_u32 s76,s73,s58
s_lshr_b32 s58,s64,3
s_lshl_b32 s77,s63,2
s_add_u32 s77,s77,s58
s_and_b32 s78,s64,7
s_bfe_u32 s58,s64,0x10003
s_lshl_b32 s59,s63,1
s_add_u32 s58,s58,s59
s_lshl_b32 s92,s58,8
s_lshl_b32 s58,s65,7
s_add_u32 s92,s92,s58
s_lshr_b32 s58,s77,1
s_lshl_b32 s99,s58,8
s_lshl_b32 s94,s76,8
s_add_u32 s94,s94,4096
s_cmp_eq_u32 s65,0
s_cbranch_scc1 L_NB_%=
s_mov_b64 s[36:37],s[28:29]
s_mov_b32 s38,0x280000
s_mov_b32 s39,0x20000
s_mov_b64 s[40:41],s[30:31]
s_mov_b32 s42,0xc8000
s_mov_b32 s43,0x20000
s_mov_b64 s[44:45],s[34:35]
s_sub_u32 s44,s44,0x800
s_subb_u32 s45,s45,0
s_mov_b32 s46,0xc8800
s_mov_b32 s47,0x20000
v_and_b32 v246,7,v254
v_lshrrev_b32 v247,4,v254
v_mov_b32 v248,v247
v_xor_b32 v248,v248,v246
v_lshlrev_b32 v248,4,v248
v_lshrrev_b32 v253,5,v254
v_lshlrev_b32 v253,4,v253
v_bfe_u32 v252,v254,3,2
v_add_u32 v253,v253,v252
v_mul_u32_u24 v253,1280,v253
v_add3_u32 v0,v253,v248,0
v_or_b32 v248,4,v247
v_xor_b32 v248,v248,v246
v_lshlrev_b32 v248,4,v248
v_lshrrev_b32 v253,5,v254
v_lshlrev_b32 v253,4,v253
v_bfe_u32 v252,v254,3,2
v_add_u32 v253,v253,v252
v_mul_u32_u24 v253,1280,v253
v_add3_u32 v1,v253,v248,0
v_add_u32 v1,39936,v1
v_mov_b32 v248,v247
v_xor_b32 v248,v248,v246
v_lshlrev_b32 v248,4,v248
v_lshrrev_b32 v253,5,v254
v_lshlrev_b32 v253,4,v253
v_bfe_u32 v252,v254,3,2
v_add_u32 v253,v253,v252
v_mul_u32_u24 v253,1280,v253
v_add3_u32 v2,v253,v248,0
v_add_u32 v2,3072,v2
v_or_b32 v248,4,v247
v_xor_b32 v248,v248,v246
v_lshlrev_b32 v248,4,v248
v_lshrrev_b32 v253,5,v254
v_lshlrev_b32 v253,4,v253
v_bfe_u32 v252,v254,3,2
v_add_u32 v253,v253,v252
v_mul_u32_u24 v253,1280,v253
v_add3_u32 v3,v253,v248,0
v_add_u32 v3,43008,v3
v_and_b32 v246,7,v254
v_lshrrev_b32 v247,4,v254
v_mov_b32 v248,v247
v_xor_b32 v248,v248,v246
v_lshlrev_b32 v248,4,v248
v_lshrrev_b32 v253,3,v254
v_mul_u32_u24 v253,1280,v253
v_add3_u32 v4,v253,v248,0
v_or_b32 v248,4,v247
v_xor_b32 v248,v248,v246
v_lshlrev_b32 v248,4,v248
v_lshrrev_b32 v253,3,v254
v_mul_u32_u24 v253,1280,v253
v_add3_u32 v5,v253,v248,0
v_add_u32 v5,10240,v5
v_mov_b32 v248,v247
v_xor_b32 v248,v248,v246
v_lshlrev_b32 v248,4,v248
v_lshrrev_b32 v253,3,v254
v_mul_u32_u24 v253,1280,v253
v_add3_u32 v6,v253,v248,0
v_or_b32 v248,4,v247
v_xor_b32 v248,v248,v246
v_lshlrev_b32 v248,4,v248
v_lshrrev_b32 v253,3,v254
v_mul_u32_u24 v253,1280,v253
v_add3_u32 v7,v253,v248,0
v_add_u32 v7,10240,v7
v_subrev_u32 v5,0x400,v5
v_subrev_u32 v7,0x400,v7
s_lshl_b32 s58,s75,7
s_lshr_b32 s59,s56,1
s_lshl_b32 s59,s59,6
s_add_u32 s58,s58,s59
s_and_b32 s59,s56,1
s_lshl_b32 s59,s59,3
s_add_u32 s58,s58,s59
s_mul_i32 s66,s58,1280
s_add_u32 s66,s66,512
s_lshl_b32 s58,s76,6
s_lshl_b32 s59,s56,4
s_add_u32 s58,s58,s59
s_mul_i32 s68,s58,1280
s_add_u32 s68,s68,512
s_lshl_b32 s58,s56,12
s_add_u32 s70,s57,s58
s_lshr_b32 s58,s56,1
s_lshl_b32 s58,s58,13
s_add_u32 s58,s58,s57
v_lshrrev_b32 v246,4,v254
v_and_b32 v247,15,v254
v_lshrrev_b32 v248,1,v247
v_or_b32 v249,0,v246
v_xor_b32 v249,v249,v248
v_lshlrev_b32 v249,4,v249
v_lshl_add_u32 v249,v247,7,v249
v_add_u32 v8,s58,v249
v_or_b32 v249,4,v246
v_xor_b32 v249,v249,v248
v_lshlrev_b32 v249,4,v249
v_lshl_add_u32 v249,v247,7,v249
v_add_u32 v9,s58,v249
s_and_b32 s58,s56,1
s_lshl_b32 s58,s58,13
s_add_u32 s58,s58,s57
s_add_u32 s58,s58,0x4000
v_lshrrev_b32 v246,4,v254
v_and_b32 v247,15,v254
v_lshrrev_b32 v248,1,v247
v_or_b32 v249,0,v246
v_xor_b32 v249,v249,v248
v_lshlrev_b32 v249,4,v249
v_lshl_add_u32 v249,v247,7,v249
v_add_u32 v10,s58,v249
v_or_b32 v249,4,v246
v_xor_b32 v249,v249,v248
v_lshlrev_b32 v249,4,v249
v_lshl_add_u32 v249,v247,7,v249
v_add_u32 v11,s58,v249
v_add_u32 v12,0x10000,v8
v_add_u32 v14,0x10000,v10
v_add_u32 v13,0x10000,v9
v_add_u32 v15,0x10000,v11
s_waitcnt vmcnt(20) lgkmcnt(0)
s_barrier
ds_read_b128 a[0:3],v8 offset:32768
ds_read_b128 a[4:7],v9 offset:32768
ds_read_b128 a[8:11],v8 offset:34816
ds_read_b128 a[12:15],v9 offset:34816
ds_read_b128 a[16:19],v8 offset:36864
ds_read_b128 a[20:23],v9 offset:36864
ds_read_b128 a[24:27],v8 offset:38912
ds_read_b128 a[28:31],v9 offset:38912
ds_read_b128 a[64:67],v10 offset:32768
ds_read_b128 a[68:71],v11 offset:32768
ds_read_b128 a[72:75],v10 offset:34816
ds_read_b128 a[76:79],v11 offset:34816
s_waitcnt lgkmcnt(0)
s_barrier
v_mfma_f32_16x16x32_bf16 v[160:163],a[0:3],a[64:67],0
v_mfma_f32_16x16x32_bf16 v[160:163],a[4:7],a[68:71],v[160:163]
ds_read_b128 a[80:83],v10 offset:36864
v_mfma_f32_16x16x32_bf16 v[164:167],a[8:11],a[64:67],0
ds_read_b128 a[84:87],v11 offset:36864
ds_read_b128 a[32:35],v12 offset:32768
v_mfma_f32_16x16x32_bf16 v[164:167],a[12:15],a[68:71],v[164:167]
ds_read_b128 a[36:39],v13 offset:32768
ds_read_b128 a[40:43],v12 offset:34816
v_mfma_f32_16x16x32_bf16 v[168:171],a[16:19],a[64:67],0
ds_read_b128 a[44:47],v13 offset:34816
s_lshr_b32 s74,s64,4
v_mfma_f32_16x16x32_bf16 v[168:171],a[20:23],a[68:71],v[168:171]
ds_read_b128 a[48:51],v12 offset:36864
ds_read_b128 a[52:55],v13 offset:36864
v_mfma_f32_16x16x32_bf16 v[172:175],a[24:27],a[64:67],0
ds_read_b128 a[56:59],v12 offset:38912
v_mfma_f32_16x16x32_bf16 v[172:175],a[28:31],a[68:71],v[172:175]
ds_read_b128 a[60:63],v13 offset:38912
v_mfma_f32_16x16x32_bf16 v[176:179],a[0:3],a[72:75],0
ds_read_b128 a[88:91],v10 offset:38912
ds_read_b128 a[92:95],v11 offset:38912
v_mfma_f32_16x16x32_bf16 v[176:179],a[4:7],a[76:79],v[176:179]
s_add_u32 m0,s70,32768
s_nop 0
buffer_load_dwordx4 v0,s[36:39],s66 offen offset:0 lds
v_mfma_f32_16x16x32_bf16 v[180:183],a[8:11],a[72:75],0
s_cmp_lg_u32 s74,0
s_cselect_b64 s[48:49],s[20:21],s[18:19]
v_mfma_f32_16x16x32_bf16 v[180:183],a[12:15],a[76:79],v[180:183]
s_mov_b32 s50,0x400000
s_mov_b32 s51,0x20000
v_mfma_f32_16x16x32_bf16 v[184:187],a[16:19],a[72:75],0
s_mov_b64 s[52:53],s[16:17]
s_mov_b32 s54,0x200000
v_mfma_f32_16x16x32_bf16 v[184:187],a[20:23],a[76:79],v[184:187]
buffer_load_dwordx4 v1,s[36:39],s66 offen offset:1024 lds
s_mov_b32 s55,0x20000
v_mfma_f32_16x16x32_bf16 v[188:191],a[24:27],a[72:75],0
v_and_b32 v246,7,v254
v_lshrrev_b32 v247,4,v254
v_mfma_f32_16x16x32_bf16 v[188:191],a[28:31],a[76:79],v[188:191]
buffer_load_dwordx4 v2,s[36:39],s66 offen offset:2048 lds
buffer_load_dwordx4 v3,s[36:39],s66 offen offset:3072 lds
s_add_u32 s66,s66,128
s_waitcnt vmcnt(16) lgkmcnt(0)
s_barrier
v_mfma_f32_16x16x32_bf16 v[192:195],a[0:3],a[80:83],0
s_add_u32 m0,s70,49152
s_nop 0
buffer_load_dwordx4 v4,s[40:43],s68 offen offset:0 lds
v_mfma_f32_16x16x32_bf16 v[192:195],a[4:7],a[84:87],v[192:195]
ds_read_b128 a[64:67],v14 offset:32768
v_lshrrev_b32 v253,3,v254
v_mfma_f32_16x16x32_bf16 v[196:199],a[8:11],a[80:83],0
ds_read_b128 a[68:71],v15 offset:32768
v_mul_u32_u24 v253,8192,v253
v_mfma_f32_16x16x32_bf16 v[196:199],a[12:15],a[84:87],v[196:199]
v_and_b32 v248,1,v247
v_lshlrev_b32 v248,1,v248
v_mfma_f32_16x16x32_bf16 v[200:203],a[16:19],a[80:83],0
buffer_load_dwordx4 v5,s[40:43],s68 offen offset:1024 lds
v_xor_b32 v248,v248,v246
v_mfma_f32_16x16x32_bf16 v[200:203],a[20:23],a[84:87],v[200:203]
v_lshl_add_u32 v230,v248,4,v253
v_and_b32 v248,1,v247
v_mfma_f32_16x16x32_bf16 v[204:207],a[24:27],a[80:83],0
v_lshlrev_b32 v248,1,v248
v_or_b32 v248,4,v248
v_mfma_f32_16x16x32_bf16 v[204:207],a[28:31],a[84:87],v[204:207]
v_xor_b32 v248,v248,v246
v_lshl_add_u32 v231,v248,4,v253
v_mfma_f32_16x16x32_bf16 v[208:211],a[0:3],a[88:91],0
buffer_load_dwordx4 v6,s[44:47],s68 offen offset:2048 lds
v_add_u32 v231,64512,v231
v_mfma_f32_16x16x32_bf16 v[208:211],a[4:7],a[92:95],v[208:211]
ds_read_b128 a[72:75],v14 offset:34816
v_and_b32 v248,1,v247
v_mfma_f32_16x16x32_bf16 v[212:215],a[8:11],a[88:91],0
ds_read_b128 a[76:79],v15 offset:34816
v_lshlrev_b32 v248,1,v248
v_mfma_f32_16x16x32_bf16 v[212:215],a[12:15],a[92:95],v[212:215]
v_xor_b32 v248,v248,v246
v_lshl_add_u32 v232,v248,4,v253
v_mfma_f32_16x16x32_bf16 v[216:219],a[16:19],a[88:91],0
buffer_load_dwordx4 v7,s[44:47],s68 offen offset:3072 lds
v_add_u32 v232,129024,v232
v_mfma_f32_16x16x32_bf16 v[216:219],a[20:23],a[92:95],v[216:219]
v_and_b32 v248,1,v247
v_lshlrev_b32 v248,1,v248
v_mfma_f32_16x16x32_bf16 v[220:223],a[24:27],a[88:91],0
v_or_b32 v248,4,v248
v_xor_b32 v248,v248,v246
v_mfma_f32_16x16x32_bf16 v[220:223],a[28:31],a[92:95],v[220:223]
s_add_u32 s68,s68,128
v_lshl_add_u32 v233,v248,4,v253
s_waitcnt lgkmcnt(3)
v_mfma_f32_16x16x32_bf16 v[160:163],a[32:35],a[64:67],v[160:163]
v_add_u32 v233,193536,v233
v_and_b32 v246,7,v254
s_waitcnt lgkmcnt(2)
v_mfma_f32_16x16x32_bf16 v[160:163],a[36:39],a[68:71],v[160:163]
ds_read_b128 a[80:83],v14 offset:36864
v_lshrrev_b32 v247,4,v254
v_mfma_f32_16x16x32_bf16 v[164:167],a[40:43],a[64:67],v[164:167]
ds_read_b128 a[84:87],v15 offset:36864
ds_read_b128 a[0:3],v8 offset:0
v_mfma_f32_16x16x32_bf16 v[164:167],a[44:47],a[68:71],v[164:167]
ds_read_b128 a[4:7],v9 offset:0
ds_read_b128 a[8:11],v8 offset:2048
v_mfma_f32_16x16x32_bf16 v[168:171],a[48:51],a[64:67],v[168:171]
ds_read_b128 a[12:15],v9 offset:2048
v_mov_b32 v248,v247
v_mfma_f32_16x16x32_bf16 v[168:171],a[52:55],a[68:71],v[168:171]
ds_read_b128 a[16:19],v8 offset:4096
ds_read_b128 a[20:23],v9 offset:4096
v_mfma_f32_16x16x32_bf16 v[172:175],a[56:59],a[64:67],v[172:175]
ds_read_b128 a[24:27],v8 offset:6144
v_xor_b32 v248,v248,v246
v_mfma_f32_16x16x32_bf16 v[172:175],a[60:63],a[68:71],v[172:175]
ds_read_b128 a[28:31],v9 offset:6144
v_lshlrev_b32 v248,4,v248
s_waitcnt lgkmcnt(11)
v_mfma_f32_16x16x32_bf16 v[176:179],a[32:35],a[72:75],v[176:179]
ds_read_b128 a[88:91],v14 offset:38912
ds_read_b128 a[92:95],v15 offset:38912
s_waitcnt lgkmcnt(12)
v_mfma_f32_16x16x32_bf16 v[176:179],a[36:39],a[76:79],v[176:179]
s_add_u32 m0,s70,98304
s_nop 0
buffer_load_dwordx4 v0,s[36:39],s66 offen offset:0 lds
v_mfma_f32_16x16x32_bf16 v[180:183],a[40:43],a[72:75],v[180:183]
v_lshrrev_b32 v253,3,v254
v_mul_u32_u24 v253,1024,v253
v_mfma_f32_16x16x32_bf16 v[180:183],a[44:47],a[76:79],v[180:183]
v_add3_u32 v234,v253,v248,0
v_or_b32 v248,4,v247
v_mfma_f32_16x16x32_bf16 v[184:187],a[48:51],a[72:75],v[184:187]
v_xor_b32 v248,v248,v246
v_lshlrev_b32 v248,4,v248
v_mfma_f32_16x16x32_bf16 v[184:187],a[52:55],a[76:79],v[184:187]
buffer_load_dwordx4 v1,s[36:39],s66 offen offset:1024 lds
v_lshrrev_b32 v253,3,v254
v_mfma_f32_16x16x32_bf16 v[188:191],a[56:59],a[72:75],v[188:191]
v_mul_u32_u24 v253,1024,v253
v_add3_u32 v235,v253,v248,0
v_mfma_f32_16x16x32_bf16 v[188:191],a[60:63],a[76:79],v[188:191]
buffer_load_dwordx4 v2,s[36:39],s66 offen offset:2048 lds
buffer_load_dwordx4 v3,s[36:39],s66 offen offset:3072 lds
s_add_u32 s66,s66,128
s_waitcnt vmcnt(16) lgkmcnt(0)
s_barrier
v_mfma_f32_16x16x32_bf16 v[192:195],a[32:35],a[80:83],v[192:195]
s_add_u32 m0,s70,114688
s_nop 0
buffer_load_dwordx4 v4,s[40:43],s68 offen offset:0 lds
v_mfma_f32_16x16x32_bf16 v[192:195],a[36:39],a[84:87],v[192:195]
ds_read_b128 a[64:67],v10 offset:0
v_add_u32 v235,7168,v235
v_mfma_f32_16x16x32_bf16 v[196:199],a[40:43],a[80:83],v[196:199]
ds_read_b128 a[68:71],v11 offset:0
v_mov_b32 v248,v247
v_mfma_f32_16x16x32_bf16 v[196:199],a[44:47],a[84:87],v[196:199]
v_xor_b32 v248,v248,v246
v_lshlrev_b32 v248,4,v248
v_mfma_f32_16x16x32_bf16 v[200:203],a[48:51],a[80:83],v[200:203]
buffer_load_dwordx4 v5,s[40:43],s68 offen offset:1024 lds
v_lshrrev_b32 v253,3,v254
v_mfma_f32_16x16x32_bf16 v[200:203],a[52:55],a[84:87],v[200:203]
v_mul_u32_u24 v253,1024,v253
v_add3_u32 v236,v253,v248,0
v_mfma_f32_16x16x32_bf16 v[204:207],a[56:59],a[80:83],v[204:207]
v_add_u32 v236,14336,v236
v_or_b32 v248,4,v247
v_mfma_f32_16x16x32_bf16 v[204:207],a[60:63],a[84:87],v[204:207]
v_xor_b32 v248,v248,v246
v_lshlrev_b32 v248,4,v248
v_mfma_f32_16x16x32_bf16 v[208:211],a[32:35],a[88:91],v[208:211]
buffer_load_dwordx4 v6,s[44:47],s68 offen offset:2048 lds
v_lshrrev_b32 v253,3,v254
v_mfma_f32_16x16x32_bf16 v[208:211],a[36:39],a[92:95],v[208:211]
ds_read_b128 a[72:75],v10 offset:2048
v_mul_u32_u24 v253,1024,v253
v_mfma_f32_16x16x32_bf16 v[212:215],a[40:43],a[88:91],v[212:215]
ds_read_b128 a[76:79],v11 offset:2048
v_add3_u32 v237,v253,v248,0
v_mfma_f32_16x16x32_bf16 v[212:215],a[44:47],a[92:95],v[212:215]
v_add_u32 v237,21504,v237
s_bfe_u32 s60,s64,0x10003
v_mfma_f32_16x16x32_bf16 v[216:219],a[48:51],a[88:91],v[216:219]
buffer_load_dwordx4 v7,s[44:47],s68 offen offset:3072 lds
s_lshl_b32 s60,s60,1
v_mfma_f32_16x16x32_bf16 v[216:219],a[52:55],a[92:95],v[216:219]
s_lshl_b32 s61,s63,2
s_add_u32 s72,s60,s61
v_mfma_f32_16x16x32_bf16 v[220:223],a[56:59],a[88:91],v[220:223]
s_lshl_b32 s60,s72,8
s_lshl_b32 s61,s56,7
v_mfma_f32_16x16x32_bf16 v[220:223],a[60:63],a[92:95],v[220:223]
s_add_u32 s68,s68,128
s_add_u32 s79,s60,s61
s_waitcnt lgkmcnt(3)
v_mfma_f32_16x16x32_bf16 v[160:163],a[0:3],a[64:67],v[160:163]
s_add_u32 s80,s79,262144
s_and_b32 s60,s56,1
s_waitcnt lgkmcnt(2)
v_mfma_f32_16x16x32_bf16 v[160:163],a[4:7],a[68:71],v[160:163]
)"
R"(ds_read_b128 a[80:83],v10 offset:4096
s_add_u32 s72,s72,s60
v_mfma_f32_16x16x32_bf16 v[164:167],a[8:11],a[64:67],v[164:167]
ds_read_b128 a[84:87],v11 offset:4096
ds_read_b128 a[32:35],v12 offset:0
v_mfma_f32_16x16x32_bf16 v[164:167],a[12:15],a[68:71],v[164:167]
ds_read_b128 a[36:39],v13 offset:0
ds_read_b128 a[40:43],v12 offset:2048
v_mfma_f32_16x16x32_bf16 v[168:171],a[16:19],a[64:67],v[168:171]
ds_read_b128 a[44:47],v13 offset:2048
s_lshl_b32 s72,s72,20
v_mfma_f32_16x16x32_bf16 v[168:171],a[20:23],a[68:71],v[168:171]
ds_read_b128 a[48:51],v12 offset:4096
ds_read_b128 a[52:55],v13 offset:4096
v_mfma_f32_16x16x32_bf16 v[172:175],a[24:27],a[64:67],v[172:175]
ds_read_b128 a[56:59],v12 offset:6144
s_and_b32 s61,s64,7
v_mfma_f32_16x16x32_bf16 v[172:175],a[28:31],a[68:71],v[172:175]
ds_read_b128 a[60:63],v13 offset:6144
s_lshl_b32 s60,s61,17
s_waitcnt lgkmcnt(11)
v_mfma_f32_16x16x32_bf16 v[176:179],a[0:3],a[72:75],v[176:179]
ds_read_b128 a[88:91],v10 offset:6144
ds_read_b128 a[92:95],v11 offset:6144
s_waitcnt lgkmcnt(12)
v_mfma_f32_16x16x32_bf16 v[176:179],a[4:7],a[76:79],v[176:179]
s_add_u32 m0,s70,0
s_nop 0
buffer_load_dwordx4 v0,s[36:39],s66 offen offset:0 lds
v_mfma_f32_16x16x32_bf16 v[180:183],a[8:11],a[72:75],v[180:183]
s_add_u32 s72,s72,s60
s_lshl_b32 s60,s61,18
v_mfma_f32_16x16x32_bf16 v[180:183],a[12:15],a[76:79],v[180:183]
s_lshl_b32 s61,s56,16
s_add_u32 s81,s60,s61
v_mfma_f32_16x16x32_bf16 v[184:187],a[16:19],a[72:75],v[184:187]
s_add_u32 s82,s81,32768
s_lshl_b32 s60,s56,13
v_mfma_f32_16x16x32_bf16 v[184:187],a[20:23],a[76:79],v[184:187]
buffer_load_dwordx4 v1,s[36:39],s66 offen offset:1024 lds
s_add_u32 s83,s57,s60
v_mfma_f32_16x16x32_bf16 v[188:191],a[24:27],a[72:75],v[188:191]
s_add_u32 s84,s83,0x10000
s_lshr_b32 s60,s56,1
v_mfma_f32_16x16x32_bf16 v[188:191],a[28:31],a[76:79],v[188:191]
buffer_load_dwordx4 v2,s[36:39],s66 offen offset:2048 lds
buffer_load_dwordx4 v3,s[36:39],s66 offen offset:3072 lds
s_add_u32 s66,s66,128
s_waitcnt vmcnt(16) lgkmcnt(0)
s_barrier
v_mfma_f32_16x16x32_bf16 v[192:195],a[0:3],a[80:83],v[192:195]
s_add_u32 m0,s70,16384
s_nop 0
buffer_load_dwordx4 v4,s[40:43],s68 offen offset:0 lds
v_mfma_f32_16x16x32_bf16 v[192:195],a[4:7],a[84:87],v[192:195]
ds_read_b128 a[64:67],v14 offset:0
s_lshl_b32 s60,s60,16
v_mfma_f32_16x16x32_bf16 v[196:199],a[8:11],a[80:83],v[196:199]
ds_read_b128 a[68:71],v15 offset:0
s_add_u32 s72,s72,s60
v_mfma_f32_16x16x32_bf16 v[196:199],a[12:15],a[84:87],v[196:199]
s_lshl_b32 s60,s74,14
s_add_u32 s72,s72,s60
v_mfma_f32_16x16x32_bf16 v[200:203],a[16:19],a[80:83],v[200:203]
buffer_load_dwordx4 v5,s[40:43],s68 offen offset:1024 lds
s_and_b32 s60,s56,1
v_mfma_f32_16x16x32_bf16 v[200:203],a[20:23],a[84:87],v[200:203]
s_lshl_b32 s60,s60,14
s_add_u32 s60,s60,s57
v_mfma_f32_16x16x32_bf16 v[204:207],a[24:27],a[80:83],v[204:207]
v_lshrrev_b32 v246,4,v254
v_bfe_u32 v247,v254,2,2
v_mfma_f32_16x16x32_bf16 v[204:207],a[28:31],a[84:87],v[204:207]
v_and_b32 v248,3,v254
v_lshrrev_b32 v249,1,v247
v_mfma_f32_16x16x32_bf16 v[208:211],a[0:3],a[88:91],v[208:211]
buffer_load_dwordx4 v6,s[44:47],s68 offen offset:2048 lds
v_and_b32 v250,1,v246
v_mfma_f32_16x16x32_bf16 v[208:211],a[4:7],a[92:95],v[208:211]
ds_read_b128 a[72:75],v14 offset:2048
v_lshl_or_b32 v249,v250,1,v249
v_mfma_f32_16x16x32_bf16 v[212:215],a[8:11],a[88:91],v[212:215]
ds_read_b128 a[76:79],v15 offset:2048
v_lshl_add_u32 v247,v246,3,v247
v_mfma_f32_16x16x32_bf16 v[212:215],a[12:15],a[92:95],v[212:215]
v_lshlrev_b32 v247,7,v247
v_lshl_add_u32 v247,v248,3,v247
v_mfma_f32_16x16x32_bf16 v[216:219],a[16:19],a[88:91],v[216:219]
buffer_load_dwordx4 v7,s[44:47],s68 offen offset:3072 lds
v_add_u32 v247,s60,v247
v_mfma_f32_16x16x32_bf16 v[216:219],a[20:23],a[92:95],v[216:219]
v_xor_b32 v250,0,v249
v_lshl_add_u32 v224,v250,5,v247
v_mfma_f32_16x16x32_bf16 v[220:223],a[24:27],a[88:91],v[220:223]
v_xor_b32 v250,1,v249
v_lshl_add_u32 v225,v250,5,v247
v_mfma_f32_16x16x32_bf16 v[220:223],a[28:31],a[92:95],v[220:223]
s_add_u32 s68,s68,128
v_xor_b32 v250,2,v249
s_waitcnt lgkmcnt(3)
v_mfma_f32_16x16x32_bf16 v[160:163],a[32:35],a[64:67],v[160:163]
v_lshl_add_u32 v226,v250,5,v247
v_xor_b32 v250,3,v249
s_waitcnt lgkmcnt(2)
v_mfma_f32_16x16x32_bf16 v[160:163],a[36:39],a[68:71],v[160:163]
ds_read_b128 a[80:83],v14 offset:4096
v_lshl_add_u32 v227,v250,5,v247
v_mfma_f32_16x16x32_bf16 v[164:167],a[40:43],a[64:67],v[164:167]
ds_read_b128 a[84:87],v15 offset:4096
ds_read_b128 a[0:3],v8 offset:32768
v_mfma_f32_16x16x32_bf16 v[164:167],a[44:47],a[68:71],v[164:167]
ds_read_b128 a[4:7],v9 offset:32768
ds_read_b128 a[8:11],v8 offset:34816
v_mfma_f32_16x16x32_bf16 v[168:171],a[48:51],a[64:67],v[168:171]
ds_read_b128 a[12:15],v9 offset:34816
s_lshr_b32 s60,s56,1
v_mfma_f32_16x16x32_bf16 v[168:171],a[52:55],a[68:71],v[168:171]
ds_read_b128 a[16:19],v8 offset:36864
ds_read_b128 a[20:23],v9 offset:36864
v_mfma_f32_16x16x32_bf16 v[172:175],a[56:59],a[64:67],v[172:175]
ds_read_b128 a[24:27],v8 offset:38912
s_lshl_b32 s60,s60,14
v_mfma_f32_16x16x32_bf16 v[172:175],a[60:63],a[68:71],v[172:175]
ds_read_b128 a[28:31],v9 offset:38912
s_add_u32 s60,s60,s57
s_waitcnt lgkmcnt(11)
v_mfma_f32_16x16x32_bf16 v[176:179],a[32:35],a[72:75],v[176:179]
ds_read_b128 a[88:91],v14 offset:6144
ds_read_b128 a[92:95],v15 offset:6144
s_waitcnt lgkmcnt(12)
v_mfma_f32_16x16x32_bf16 v[176:179],a[36:39],a[76:79],v[176:179]
s_add_u32 m0,s70,65536
s_nop 0
buffer_load_dwordx4 v0,s[36:39],s66 offen offset:0 lds
v_mfma_f32_16x16x32_bf16 v[180:183],a[40:43],a[72:75],v[180:183]
s_add_u32 s60,s60,0x10000
v_lshrrev_b32 v246,4,v254
v_mfma_f32_16x16x32_bf16 v[180:183],a[44:47],a[76:79],v[180:183]
v_and_b32 v247,15,v254
v_lshrrev_b32 v248,1,v247
v_mfma_f32_16x16x32_bf16 v[184:187],a[48:51],a[72:75],v[184:187]
v_or_b32 v249,0,v246
v_xor_b32 v249,v249,v248
v_mfma_f32_16x16x32_bf16 v[184:187],a[52:55],a[76:79],v[184:187]
buffer_load_dwordx4 v1,s[36:39],s66 offen offset:1024 lds
v_lshlrev_b32 v249,4,v249
v_mfma_f32_16x16x32_bf16 v[188:191],a[56:59],a[72:75],v[188:191]
v_lshl_add_u32 v249,v247,7,v249
v_add_u32 v228,s60,v249
v_mfma_f32_16x16x32_bf16 v[188:191],a[60:63],a[76:79],v[188:191]
buffer_load_dwordx4 v2,s[36:39],s66 offen offset:2048 lds
buffer_load_dwordx4 v3,s[36:39],s66 offen offset:3072 lds
s_add_u32 s66,s66,128
s_waitcnt vmcnt(16) lgkmcnt(0)
s_barrier
v_mfma_f32_16x16x32_bf16 v[192:195],a[32:35],a[80:83],v[192:195]
s_add_u32 m0,s70,81920
s_nop 0
buffer_load_dwordx4 v4,s[40:43],s68 offen offset:0 lds
v_mfma_f32_16x16x32_bf16 v[192:195],a[36:39],a[84:87],v[192:195]
ds_read_b128 a[64:67],v10 offset:32768
v_or_b32 v249,4,v246
v_mfma_f32_16x16x32_bf16 v[196:199],a[40:43],a[80:83],v[196:199]
ds_read_b128 a[68:71],v11 offset:32768
v_xor_b32 v249,v249,v248
v_mfma_f32_16x16x32_bf16 v[196:199],a[44:47],a[84:87],v[196:199]
v_lshlrev_b32 v249,4,v249
v_lshl_add_u32 v249,v247,7,v249
v_mfma_f32_16x16x32_bf16 v[200:203],a[48:51],a[80:83],v[200:203]
buffer_load_dwordx4 v5,s[40:43],s68 offen offset:1024 lds
v_add_u32 v229,s60,v249
v_mfma_f32_16x16x32_bf16 v[200:203],a[52:55],a[84:87],v[200:203]
v_mfma_f32_16x16x32_bf16 v[204:207],a[56:59],a[80:83],v[204:207]
v_mfma_f32_16x16x32_bf16 v[204:207],a[60:63],a[84:87],v[204:207]
v_mfma_f32_16x16x32_bf16 v[208:211],a[32:35],a[88:91],v[208:211]
buffer_load_dwordx4 v6,s[44:47],s68 offen offset:2048 lds
v_mfma_f32_16x16x32_bf16 v[208:211],a[36:39],a[92:95],v[208:211]
ds_read_b128 a[72:75],v10 offset:34816
v_mfma_f32_16x16x32_bf16 v[212:215],a[40:43],a[88:91],v[212:215]
ds_read_b128 a[76:79],v11 offset:34816
v_mfma_f32_16x16x32_bf16 v[212:215],a[44:47],a[92:95],v[212:215]
v_mfma_f32_16x16x32_bf16 v[216:219],a[48:51],a[88:91],v[216:219]
buffer_load_dwordx4 v7,s[44:47],s68 offen offset:3072 lds
v_mfma_f32_16x16x32_bf16 v[216:219],a[52:55],a[92:95],v[216:219]
v_mfma_f32_16x16x32_bf16 v[220:223],a[56:59],a[88:91],v[220:223]
v_mfma_f32_16x16x32_bf16 v[220:223],a[60:63],a[92:95],v[220:223]
s_add_u32 s68,s68,128
s_waitcnt lgkmcnt(3)
v_mfma_f32_16x16x32_bf16 v[160:163],a[0:3],a[64:67],v[160:163]
s_waitcnt lgkmcnt(2)
v_mfma_f32_16x16x32_bf16 v[160:163],a[4:7],a[68:71],v[160:163]
ds_read_b128 a[80:83],v10 offset:36864
v_mfma_f32_16x16x32_bf16 v[164:167],a[8:11],a[64:67],v[164:167]
ds_read_b128 a[84:87],v11 offset:36864
ds_read_b128 a[32:35],v12 offset:32768
v_mfma_f32_16x16x32_bf16 v[164:167],a[12:15],a[68:71],v[164:167]
ds_read_b128 a[36:39],v13 offset:32768
ds_read_b128 a[40:43],v12 offset:34816
v_mfma_f32_16x16x32_bf16 v[168:171],a[16:19],a[64:67],v[168:171]
ds_read_b128 a[44:47],v13 offset:34816
v_mfma_f32_16x16x32_bf16 v[168:171],a[20:23],a[68:71],v[168:171]
ds_read_b128 a[48:51],v12 offset:36864
ds_read_b128 a[52:55],v13 offset:36864
v_mfma_f32_16x16x32_bf16 v[172:175],a[24:27],a[64:67],v[172:175]
ds_read_b128 a[56:59],v12 offset:38912
v_mfma_f32_16x16x32_bf16 v[172:175],a[28:31],a[68:71],v[172:175]
ds_read_b128 a[60:63],v13 offset:38912
s_waitcnt lgkmcnt(11)
v_mfma_f32_16x16x32_bf16 v[176:179],a[0:3],a[72:75],v[176:179]
ds_read_b128 a[88:91],v10 offset:38912
ds_read_b128 a[92:95],v11 offset:38912
s_waitcnt lgkmcnt(12)
v_mfma_f32_16x16x32_bf16 v[176:179],a[4:7],a[76:79],v[176:179]
s_add_u32 m0,s70,32768
s_nop 0
buffer_load_dwordx4 v0,s[36:39],s66 offen offset:0 lds
v_mfma_f32_16x16x32_bf16 v[180:183],a[8:11],a[72:75],v[180:183]
v_mfma_f32_16x16x32_bf16 v[180:183],a[12:15],a[76:79],v[180:183]
v_mfma_f32_16x16x32_bf16 v[184:187],a[16:19],a[72:75],v[184:187]
v_mfma_f32_16x16x32_bf16 v[184:187],a[20:23],a[76:79],v[184:187]
buffer_load_dwordx4 v1,s[36:39],s66 offen offset:1024 lds
v_mfma_f32_16x16x32_bf16 v[188:191],a[24:27],a[72:75],v[188:191]
v_mfma_f32_16x16x32_bf16 v[188:191],a[28:31],a[76:79],v[188:191]
buffer_load_dwordx4 v2,s[36:39],s66 offen offset:2048 lds
buffer_load_dwordx4 v3,s[36:39],s66 offen offset:3072 lds
s_add_u32 s66,s66,128
s_waitcnt vmcnt(16) lgkmcnt(0)
s_barrier
v_mfma_f32_16x16x32_bf16 v[192:195],a[0:3],a[80:83],v[192:195]
s_add_u32 m0,s70,49152
s_nop 0
buffer_load_dwordx4 v4,s[40:43],s68 offen offset:0 lds
v_mfma_f32_16x16x32_bf16 v[192:195],a[4:7],a[84:87],v[192:195]
ds_read_b128 a[64:67],v14 offset:32768
v_mfma_f32_16x16x32_bf16 v[196:199],a[8:11],a[80:83],v[196:199]
ds_read_b128 a[68:71],v15 offset:32768
v_mfma_f32_16x16x32_bf16 v[196:199],a[12:15],a[84:87],v[196:199]
v_mfma_f32_16x16x32_bf16 v[200:203],a[16:19],a[80:83],v[200:203]
buffer_load_dwordx4 v5,s[40:43],s68 offen offset:1024 lds
v_mfma_f32_16x16x32_bf16 v[200:203],a[20:23],a[84:87],v[200:203]
v_mfma_f32_16x16x32_bf16 v[204:207],a[24:27],a[80:83],v[204:207]
v_mfma_f32_16x16x32_bf16 v[204:207],a[28:31],a[84:87],v[204:207]
v_mfma_f32_16x16x32_bf16 v[208:211],a[0:3],a[88:91],v[208:211]
buffer_load_dwordx4 v6,s[44:47],s68 offen offset:2048 lds
v_mfma_f32_16x16x32_bf16 v[208:211],a[4:7],a[92:95],v[208:211]
ds_read_b128 a[72:75],v14 offset:34816
v_mfma_f32_16x16x32_bf16 v[212:215],a[8:11],a[88:91],v[212:215]
ds_read_b128 a[76:79],v15 offset:34816
v_mfma_f32_16x16x32_bf16 v[212:215],a[12:15],a[92:95],v[212:215]
v_mfma_f32_16x16x32_bf16 v[216:219],a[16:19],a[88:91],v[216:219]
buffer_load_dwordx4 v7,s[44:47],s68 offen offset:3072 lds
v_mfma_f32_16x16x32_bf16 v[216:219],a[20:23],a[92:95],v[216:219]
v_mfma_f32_16x16x32_bf16 v[220:223],a[24:27],a[88:91],v[220:223]
v_mfma_f32_16x16x32_bf16 v[220:223],a[28:31],a[92:95],v[220:223]
s_add_u32 s68,s68,128
s_waitcnt lgkmcnt(3)
v_mfma_f32_16x16x32_bf16 v[160:163],a[32:35],a[64:67],v[160:163]
s_waitcnt lgkmcnt(2)
v_mfma_f32_16x16x32_bf16 v[160:163],a[36:39],a[68:71],v[160:163]
ds_read_b128 a[80:83],v14 offset:36864
v_mfma_f32_16x16x32_bf16 v[164:167],a[40:43],a[64:67],v[164:167]
ds_read_b128 a[84:87],v15 offset:36864
ds_read_b128 a[0:3],v8 offset:0
v_mfma_f32_16x16x32_bf16 v[164:167],a[44:47],a[68:71],v[164:167]
ds_read_b128 a[4:7],v9 offset:0
ds_read_b128 a[8:11],v8 offset:2048
v_mfma_f32_16x16x32_bf16 v[168:171],a[48:51],a[64:67],v[168:171]
ds_read_b128 a[12:15],v9 offset:2048
v_mfma_f32_16x16x32_bf16 v[168:171],a[52:55],a[68:71],v[168:171]
ds_read_b128 a[16:19],v8 offset:4096
ds_read_b128 a[20:23],v9 offset:4096
v_mfma_f32_16x16x32_bf16 v[172:175],a[56:59],a[64:67],v[172:175]
ds_read_b128 a[24:27],v8 offset:6144
v_mfma_f32_16x16x32_bf16 v[172:175],a[60:63],a[68:71],v[172:175]
ds_read_b128 a[28:31],v9 offset:6144
s_waitcnt lgkmcnt(11)
v_mfma_f32_16x16x32_bf16 v[176:179],a[32:35],a[72:75],v[176:179]
ds_read_b128 a[88:91],v14 offset:38912
ds_read_b128 a[92:95],v15 offset:38912
s_waitcnt lgkmcnt(12)
v_mfma_f32_16x16x32_bf16 v[176:179],a[36:39],a[76:79],v[176:179]
s_add_u32 m0,s70,98304
s_nop 0
buffer_load_dwordx4 v0,s[36:39],s66 offen offset:0 lds
v_mfma_f32_16x16x32_bf16 v[180:183],a[40:43],a[72:75],v[180:183]
v_mfma_f32_16x16x32_bf16 v[180:183],a[44:47],a[76:79],v[180:183]
v_mfma_f32_16x16x32_bf16 v[184:187],a[48:51],a[72:75],v[184:187]
v_mfma_f32_16x16x32_bf16 v[184:187],a[52:55],a[76:79],v[184:187]
buffer_load_dwordx4 v1,s[36:39],s66 offen offset:1024 lds
v_mfma_f32_16x16x32_bf16 v[188:191],a[56:59],a[72:75],v[188:191]
v_mfma_f32_16x16x32_bf16 v[188:191],a[60:63],a[76:79],v[188:191]
buffer_load_dwordx4 v2,s[36:39],s66 offen offset:2048 lds
buffer_load_dwordx4 v3,s[36:39],s66 offen offset:3072 lds
s_add_u32 s66,s66,128
s_waitcnt vmcnt(16) lgkmcnt(0)
s_barrier
v_mfma_f32_16x16x32_bf16 v[192:195],a[32:35],a[80:83],v[192:195]
s_add_u32 m0,s70,114688
s_nop 0
buffer_load_dwordx4 v4,s[40:43],s68 offen offset:0 lds
v_mfma_f32_16x16x32_bf16 v[192:195],a[36:39],a[84:87],v[192:195]
ds_read_b128 a[64:67],v10 offset:0
v_mfma_f32_16x16x32_bf16 v[196:199],a[40:43],a[80:83],v[196:199]
ds_read_b128 a[68:71],v11 offset:0
v_mfma_f32_16x16x32_bf16 v[196:199],a[44:47],a[84:87],v[196:199]
v_mfma_f32_16x16x32_bf16 v[200:203],a[48:51],a[80:83],v[200:203]
buffer_load_dwordx4 v5,s[40:43],s68 offen offset:1024 lds
v_mfma_f32_16x16x32_bf16 v[200:203],a[52:55],a[84:87],v[200:203]
v_mfma_f32_16x16x32_bf16 v[204:207],a[56:59],a[80:83],v[204:207]
v_mfma_f32_16x16x32_bf16 v[204:207],a[60:63],a[84:87],v[204:207]
v_mfma_f32_16x16x32_bf16 v[208:211],a[32:35],a[88:91],v[208:211]
buffer_load_dwordx4 v6,s[44:47],s68 offen offset:2048 lds
v_mfma_f32_16x16x32_bf16 v[208:211],a[36:39],a[92:95],v[208:211]
ds_read_b128 a[72:75],v10 offset:2048
v_mfma_f32_16x16x32_bf16 v[212:215],a[40:43],a[88:91],v[212:215]
ds_read_b128 a[76:79],v11 offset:2048
v_mfma_f32_16x16x32_bf16 v[212:215],a[44:47],a[92:95],v[212:215]
v_mfma_f32_16x16x32_bf16 v[216:219],a[48:51],a[88:91],v[216:219]
buffer_load_dwordx4 v7,s[44:47],s68 offen offset:3072 lds
v_mfma_f32_16x16x32_bf16 v[216:219],a[52:55],a[92:95],v[216:219]
v_mfma_f32_16x16x32_bf16 v[220:223],a[56:59],a[88:91],v[220:223]
v_mfma_f32_16x16x32_bf16 v[220:223],a[60:63],a[92:95],v[220:223]
s_add_u32 s68,s68,128
s_waitcnt lgkmcnt(3)
v_mfma_f32_16x16x32_bf16 v[160:163],a[0:3],a[64:67],v[160:163]
s_waitcnt lgkmcnt(2)
v_mfma_f32_16x16x32_bf16 v[160:163],a[4:7],a[68:71],v[160:163]
ds_read_b128 a[80:83],v10 offset:4096
v_mfma_f32_16x16x32_bf16 v[164:167],a[8:11],a[64:67],v[164:167]
ds_read_b128 a[84:87],v11 offset:4096
ds_read_b128 a[32:35],v12 offset:0
v_mfma_f32_16x16x32_bf16 v[164:167],a[12:15],a[68:71],v[164:167]
ds_read_b128 a[36:39],v13 offset:0
ds_read_b128 a[40:43],v12 offset:2048
v_mfma_f32_16x16x32_bf16 v[168:171],a[16:19],a[64:67],v[168:171]
ds_read_b128 a[44:47],v13 offset:2048
v_mfma_f32_16x16x32_bf16 v[168:171],a[20:23],a[68:71],v[168:171]
ds_read_b128 a[48:51],v12 offset:4096
ds_read_b128 a[52:55],v13 offset:4096
v_mfma_f32_16x16x32_bf16 v[172:175],a[24:27],a[64:67],v[172:175]
ds_read_b128 a[56:59],v12 offset:6144
v_mfma_f32_16x16x32_bf16 v[172:175],a[28:31],a[68:71],v[172:175]
ds_read_b128 a[60:63],v13 offset:6144
s_waitcnt lgkmcnt(11)
v_mfma_f32_16x16x32_bf16 v[176:179],a[0:3],a[72:75],v[176:179]
ds_read_b128 a[88:91],v10 offset:6144
ds_read_b128 a[92:95],v11 offset:6144
s_waitcnt lgkmcnt(12)
v_mfma_f32_16x16x32_bf16 v[176:179],a[4:7],a[76:79],v[176:179]
v_mfma_f32_16x16x32_bf16 v[180:183],a[8:11],a[72:75],v[180:183]
v_mfma_f32_16x16x32_bf16 v[180:183],a[12:15],a[76:79],v[180:183]
v_mfma_f32_16x16x32_bf16 v[184:187],a[16:19],a[72:75],v[184:187]
v_mfma_f32_16x16x32_bf16 v[184:187],a[20:23],a[76:79],v[184:187]
v_mfma_f32_16x16x32_bf16 v[188:191],a[24:27],a[72:75],v[188:191]
v_mfma_f32_16x16x32_bf16 v[188:191],a[28:31],a[76:79],v[188:191]
s_waitcnt vmcnt(12) lgkmcnt(0)
s_barrier
v_mfma_f32_16x16x32_bf16 v[192:195],a[0:3],a[80:83],v[192:195]
v_mfma_f32_16x16x32_bf16 v[192:195],a[4:7],a[84:87],v[192:195]
ds_read_b128 a[64:67],v14 offset:0
v_mfma_f32_16x16x32_bf16 v[196:199],a[8:11],a[80:83],v[196:199]
ds_read_b128 a[68:71],v15 offset:0
v_mfma_f32_16x16x32_bf16 v[196:199],a[12:15],a[84:87],v[196:199]
v_mfma_f32_16x16x32_bf16 v[200:203],a[16:19],a[80:83],v[200:203]
v_mfma_f32_16x16x32_bf16 v[200:203],a[20:23],a[84:87],v[200:203]
v_mfma_f32_16x16x32_bf16 v[204:207],a[24:27],a[80:83],v[204:207]
v_mfma_f32_16x16x32_bf16 v[204:207],a[28:31],a[84:87],v[204:207]
v_mfma_f32_16x16x32_bf16 v[208:211],a[0:3],a[88:91],v[208:211]
v_mfma_f32_16x16x32_bf16 v[208:211],a[4:7],a[92:95],v[208:211]
ds_read_b128 a[72:75],v14 offset:2048
v_mfma_f32_16x16x32_bf16 v[212:215],a[8:11],a[88:91],v[212:215]
ds_read_b128 a[76:79],v15 offset:2048
v_mfma_f32_16x16x32_bf16 v[212:215],a[12:15],a[92:95],v[212:215]
v_mfma_f32_16x16x32_bf16 v[216:219],a[16:19],a[88:91],v[216:219]
v_mfma_f32_16x16x32_bf16 v[216:219],a[20:23],a[92:95],v[216:219]
v_mfma_f32_16x16x32_bf16 v[220:223],a[24:27],a[88:91],v[220:223]
v_mfma_f32_16x16x32_bf16 v[220:223],a[28:31],a[92:95],v[220:223]
s_waitcnt lgkmcnt(3)
v_mfma_f32_16x16x32_bf16 v[160:163],a[32:35],a[64:67],v[160:163]
s_waitcnt lgkmcnt(2)
v_mfma_f32_16x16x32_bf16 v[160:163],a[36:39],a[68:71],v[160:163]
ds_read_b128 a[80:83],v14 offset:4096
v_mfma_f32_16x16x32_bf16 v[164:167],a[40:43],a[64:67],v[164:167]
ds_read_b128 a[84:87],v15 offset:4096
ds_read_b128 a[0:3],v8 offset:32768
v_mfma_f32_16x16x32_bf16 v[164:167],a[44:47],a[68:71],v[164:167]
ds_read_b128 a[4:7],v9 offset:32768
ds_read_b128 a[8:11],v8 offset:34816
v_mfma_f32_16x16x32_bf16 v[168:171],a[48:51],a[64:67],v[168:171]
ds_read_b128 a[12:15],v9 offset:34816
v_mfma_f32_16x16x32_bf16 v[168:171],a[52:55],a[68:71],v[168:171]
ds_read_b128 a[16:19],v8 offset:36864
ds_read_b128 a[20:23],v9 offset:36864
v_mfma_f32_16x16x32_bf16 v[172:175],a[56:59],a[64:67],v[172:175]
ds_read_b128 a[24:27],v8 offset:38912
v_mfma_f32_16x16x32_bf16 v[172:175],a[60:63],a[68:71],v[172:175]
ds_read_b128 a[28:31],v9 offset:38912
s_waitcnt lgkmcnt(11)
v_mfma_f32_16x16x32_bf16 v[176:179],a[32:35],a[72:75],v[176:179]
ds_read_b128 a[88:91],v14 offset:6144
)"
R"(ds_read_b128 a[92:95],v15 offset:6144
s_waitcnt lgkmcnt(12)
v_mfma_f32_16x16x32_bf16 v[176:179],a[36:39],a[76:79],v[176:179]
v_mfma_f32_16x16x32_bf16 v[180:183],a[40:43],a[72:75],v[180:183]
v_mfma_f32_16x16x32_bf16 v[180:183],a[44:47],a[76:79],v[180:183]
v_mfma_f32_16x16x32_bf16 v[184:187],a[48:51],a[72:75],v[184:187]
v_mfma_f32_16x16x32_bf16 v[184:187],a[52:55],a[76:79],v[184:187]
v_mfma_f32_16x16x32_bf16 v[188:191],a[56:59],a[72:75],v[188:191]
v_mfma_f32_16x16x32_bf16 v[188:191],a[60:63],a[76:79],v[188:191]
s_waitcnt vmcnt(4) lgkmcnt(0)
s_barrier
v_mfma_f32_16x16x32_bf16 v[192:195],a[32:35],a[80:83],v[192:195]
s_add_u32 m0,s83,0
s_nop 0
buffer_load_dwordx4 v230,s[48:51],s79 offen offset:0 lds
v_mfma_f32_16x16x32_bf16 v[192:195],a[36:39],a[84:87],v[192:195]
ds_read_b128 a[64:67],v10 offset:32768
v_mfma_f32_16x16x32_bf16 v[196:199],a[40:43],a[80:83],v[196:199]
ds_read_b128 a[68:71],v11 offset:32768
v_mfma_f32_16x16x32_bf16 v[196:199],a[44:47],a[84:87],v[196:199]
v_mfma_f32_16x16x32_bf16 v[200:203],a[48:51],a[80:83],v[200:203]
buffer_load_dwordx4 v231,s[48:51],s79 offen offset:1024 lds
v_mfma_f32_16x16x32_bf16 v[200:203],a[52:55],a[84:87],v[200:203]
v_mfma_f32_16x16x32_bf16 v[204:207],a[56:59],a[80:83],v[204:207]
v_mfma_f32_16x16x32_bf16 v[204:207],a[60:63],a[84:87],v[204:207]
v_mfma_f32_16x16x32_bf16 v[208:211],a[32:35],a[88:91],v[208:211]
buffer_load_dwordx4 v232,s[48:51],s79 offen offset:2048 lds
v_mfma_f32_16x16x32_bf16 v[208:211],a[36:39],a[92:95],v[208:211]
ds_read_b128 a[72:75],v10 offset:34816
v_mfma_f32_16x16x32_bf16 v[212:215],a[40:43],a[88:91],v[212:215]
ds_read_b128 a[76:79],v11 offset:34816
v_mfma_f32_16x16x32_bf16 v[212:215],a[44:47],a[92:95],v[212:215]
v_mfma_f32_16x16x32_bf16 v[216:219],a[48:51],a[88:91],v[216:219]
buffer_load_dwordx4 v233,s[48:51],s79 offen offset:3072 lds
v_mfma_f32_16x16x32_bf16 v[216:219],a[52:55],a[92:95],v[216:219]
v_mfma_f32_16x16x32_bf16 v[220:223],a[56:59],a[88:91],v[220:223]
v_mfma_f32_16x16x32_bf16 v[220:223],a[60:63],a[92:95],v[220:223]
s_waitcnt lgkmcnt(3)
v_mfma_f32_16x16x32_bf16 v[160:163],a[0:3],a[64:67],v[160:163]
s_add_u32 m0,s83,4096
s_nop 0
buffer_load_dwordx4 v230,s[48:51],s80 offen offset:0 lds
s_waitcnt lgkmcnt(2)
v_mfma_f32_16x16x32_bf16 v[160:163],a[4:7],a[68:71],v[160:163]
ds_read_b128 a[80:83],v10 offset:36864
v_mfma_f32_16x16x32_bf16 v[164:167],a[8:11],a[64:67],v[164:167]
ds_read_b128 a[84:87],v11 offset:36864
ds_read_b128 a[32:35],v12 offset:32768
v_mfma_f32_16x16x32_bf16 v[164:167],a[12:15],a[68:71],v[164:167]
ds_read_b128 a[36:39],v13 offset:32768
ds_read_b128 a[40:43],v12 offset:34816
v_mfma_f32_16x16x32_bf16 v[168:171],a[16:19],a[64:67],v[168:171]
ds_read_b128 a[44:47],v13 offset:34816
buffer_load_dwordx4 v231,s[48:51],s80 offen offset:1024 lds
v_mfma_f32_16x16x32_bf16 v[168:171],a[20:23],a[68:71],v[168:171]
ds_read_b128 a[48:51],v12 offset:36864
ds_read_b128 a[52:55],v13 offset:36864
v_mfma_f32_16x16x32_bf16 v[172:175],a[24:27],a[64:67],v[172:175]
ds_read_b128 a[56:59],v12 offset:38912
v_mfma_f32_16x16x32_bf16 v[172:175],a[28:31],a[68:71],v[172:175]
ds_read_b128 a[60:63],v13 offset:38912
s_waitcnt lgkmcnt(11)
v_mfma_f32_16x16x32_bf16 v[176:179],a[0:3],a[72:75],v[176:179]
ds_read_b128 a[88:91],v10 offset:38912
ds_read_b128 a[92:95],v11 offset:38912
s_waitcnt lgkmcnt(12)
v_mfma_f32_16x16x32_bf16 v[176:179],a[4:7],a[76:79],v[176:179]
buffer_load_dwordx4 v232,s[48:51],s80 offen offset:2048 lds
v_mfma_f32_16x16x32_bf16 v[180:183],a[8:11],a[72:75],v[180:183]
v_mfma_f32_16x16x32_bf16 v[180:183],a[12:15],a[76:79],v[180:183]
v_mfma_f32_16x16x32_bf16 v[184:187],a[16:19],a[72:75],v[184:187]
v_mfma_f32_16x16x32_bf16 v[184:187],a[20:23],a[76:79],v[184:187]
buffer_load_dwordx4 v233,s[48:51],s80 offen offset:3072 lds
v_mfma_f32_16x16x32_bf16 v[188:191],a[24:27],a[72:75],v[188:191]
v_mfma_f32_16x16x32_bf16 v[188:191],a[28:31],a[76:79],v[188:191]
s_waitcnt vmcnt(8) lgkmcnt(0)
s_barrier
v_mfma_f32_16x16x32_bf16 v[192:195],a[0:3],a[80:83],v[192:195]
v_mfma_f32_16x16x32_bf16 v[192:195],a[4:7],a[84:87],v[192:195]
ds_read_b128 a[64:67],v14 offset:32768
s_add_u32 s79,s79,524288
v_mfma_f32_16x16x32_bf16 v[196:199],a[8:11],a[80:83],v[196:199]
ds_read_b128 a[68:71],v15 offset:32768
s_add_u32 s80,s80,524288
v_mfma_f32_16x16x32_bf16 v[196:199],a[12:15],a[84:87],v[196:199]
s_add_u32 m0,s84,0
s_nop 0
buffer_load_dwordx4 v234,s[52:55],s81 offen offset:0 lds
v_mfma_f32_16x16x32_bf16 v[200:203],a[16:19],a[80:83],v[200:203]
v_mfma_f32_16x16x32_bf16 v[200:203],a[20:23],a[84:87],v[200:203]
v_mfma_f32_16x16x32_bf16 v[204:207],a[24:27],a[80:83],v[204:207]
v_mfma_f32_16x16x32_bf16 v[204:207],a[28:31],a[84:87],v[204:207]
buffer_load_dwordx4 v235,s[52:55],s81 offen offset:1024 lds
v_mfma_f32_16x16x32_bf16 v[208:211],a[0:3],a[88:91],v[208:211]
v_mfma_f32_16x16x32_bf16 v[208:211],a[4:7],a[92:95],v[208:211]
ds_read_b128 a[72:75],v14 offset:34816
v_mfma_f32_16x16x32_bf16 v[212:215],a[8:11],a[88:91],v[212:215]
ds_read_b128 a[76:79],v15 offset:34816
v_mfma_f32_16x16x32_bf16 v[212:215],a[12:15],a[92:95],v[212:215]
buffer_load_dwordx4 v236,s[52:55],s81 offen offset:2048 lds
v_mfma_f32_16x16x32_bf16 v[216:219],a[16:19],a[88:91],v[216:219]
v_mfma_f32_16x16x32_bf16 v[216:219],a[20:23],a[92:95],v[216:219]
v_mfma_f32_16x16x32_bf16 v[220:223],a[24:27],a[88:91],v[220:223]
v_mfma_f32_16x16x32_bf16 v[220:223],a[28:31],a[92:95],v[220:223]
buffer_load_dwordx4 v237,s[52:55],s81 offen offset:3072 lds
s_waitcnt lgkmcnt(3)
v_mfma_f32_16x16x32_bf16 v[160:163],a[32:35],a[64:67],v[160:163]
s_waitcnt lgkmcnt(2)
v_mfma_f32_16x16x32_bf16 v[160:163],a[36:39],a[68:71],v[160:163]
ds_read_b128 a[80:83],v14 offset:36864
v_mfma_f32_16x16x32_bf16 v[164:167],a[40:43],a[64:67],v[164:167]
ds_read_b128 a[84:87],v15 offset:36864
v_mfma_f32_16x16x32_bf16 v[164:167],a[44:47],a[68:71],v[164:167]
s_add_u32 m0,s84,4096
s_nop 0
buffer_load_dwordx4 v234,s[52:55],s82 offen offset:0 lds
v_mfma_f32_16x16x32_bf16 v[168:171],a[48:51],a[64:67],v[168:171]
v_mfma_f32_16x16x32_bf16 v[168:171],a[52:55],a[68:71],v[168:171]
v_mfma_f32_16x16x32_bf16 v[172:175],a[56:59],a[64:67],v[172:175]
v_mfma_f32_16x16x32_bf16 v[172:175],a[60:63],a[68:71],v[172:175]
buffer_load_dwordx4 v235,s[52:55],s82 offen offset:1024 lds
s_waitcnt lgkmcnt(3)
v_mfma_f32_16x16x32_bf16 v[176:179],a[32:35],a[72:75],v[176:179]
ds_read_b128 a[88:91],v14 offset:38912
ds_read_b128 a[92:95],v15 offset:38912
s_waitcnt lgkmcnt(4)
v_mfma_f32_16x16x32_bf16 v[176:179],a[36:39],a[76:79],v[176:179]
v_mfma_f32_16x16x32_bf16 v[180:183],a[40:43],a[72:75],v[180:183]
v_mfma_f32_16x16x32_bf16 v[180:183],a[44:47],a[76:79],v[180:183]
buffer_load_dwordx4 v236,s[52:55],s82 offen offset:2048 lds
v_mfma_f32_16x16x32_bf16 v[184:187],a[48:51],a[72:75],v[184:187]
v_mfma_f32_16x16x32_bf16 v[184:187],a[52:55],a[76:79],v[184:187]
v_mfma_f32_16x16x32_bf16 v[188:191],a[56:59],a[72:75],v[188:191]
v_mfma_f32_16x16x32_bf16 v[188:191],a[60:63],a[76:79],v[188:191]
buffer_load_dwordx4 v237,s[52:55],s82 offen offset:3072 lds
s_waitcnt lgkmcnt(3)
v_mfma_f32_16x16x32_bf16 v[192:195],a[32:35],a[80:83],v[192:195]
s_waitcnt lgkmcnt(2)
v_mfma_f32_16x16x32_bf16 v[192:195],a[36:39],a[84:87],v[192:195]
v_mfma_f32_16x16x32_bf16 v[196:199],a[40:43],a[80:83],v[196:199]
v_mfma_f32_16x16x32_bf16 v[196:199],a[44:47],a[84:87],v[196:199]
s_add_u32 s81,s81,128
s_add_u32 s82,s82,128
v_mfma_f32_16x16x32_bf16 v[200:203],a[48:51],a[80:83],v[200:203]
v_mfma_f32_16x16x32_bf16 v[200:203],a[52:55],a[84:87],v[200:203]
v_mfma_f32_16x16x32_bf16 v[204:207],a[56:59],a[80:83],v[204:207]
v_mfma_f32_16x16x32_bf16 v[204:207],a[60:63],a[84:87],v[204:207]
s_waitcnt lgkmcnt(1)
v_mfma_f32_16x16x32_bf16 v[208:211],a[32:35],a[88:91],v[208:211]
s_waitcnt lgkmcnt(0)
v_mfma_f32_16x16x32_bf16 v[208:211],a[36:39],a[92:95],v[208:211]
v_mfma_f32_16x16x32_bf16 v[212:215],a[40:43],a[88:91],v[212:215]
v_mfma_f32_16x16x32_bf16 v[212:215],a[44:47],a[92:95],v[212:215]
v_mfma_f32_16x16x32_bf16 v[216:219],a[48:51],a[88:91],v[216:219]
v_mfma_f32_16x16x32_bf16 v[216:219],a[52:55],a[92:95],v[216:219]
v_mfma_f32_16x16x32_bf16 v[220:223],a[56:59],a[88:91],v[220:223]
v_mfma_f32_16x16x32_bf16 v[220:223],a[60:63],a[92:95],v[220:223]
s_waitcnt lgkmcnt(0)
s_barrier
s_add_u32 m0,s83,32768
s_nop 0
buffer_load_dwordx4 v230,s[48:51],s79 offen offset:0 lds
buffer_load_dwordx4 v231,s[48:51],s79 offen offset:1024 lds
buffer_load_dwordx4 v232,s[48:51],s79 offen offset:2048 lds
buffer_load_dwordx4 v233,s[48:51],s79 offen offset:3072 lds
s_add_u32 m0,s83,36864
s_nop 0
buffer_load_dwordx4 v230,s[48:51],s80 offen offset:0 lds
buffer_load_dwordx4 v231,s[48:51],s80 offen offset:1024 lds
buffer_load_dwordx4 v232,s[48:51],s80 offen offset:2048 lds
buffer_load_dwordx4 v233,s[48:51],s80 offen offset:3072 lds
s_add_u32 s79,s79,524288
s_add_u32 s80,s80,524288
s_add_u32 m0,s84,32768
s_nop 0
buffer_load_dwordx4 v234,s[52:55],s81 offen offset:0 lds
buffer_load_dwordx4 v235,s[52:55],s81 offen offset:1024 lds
buffer_load_dwordx4 v236,s[52:55],s81 offen offset:2048 lds
buffer_load_dwordx4 v237,s[52:55],s81 offen offset:3072 lds
s_add_u32 m0,s84,36864
s_nop 0
buffer_load_dwordx4 v234,s[52:55],s82 offen offset:0 lds
buffer_load_dwordx4 v235,s[52:55],s82 offen offset:1024 lds
buffer_load_dwordx4 v236,s[52:55],s82 offen offset:2048 lds
buffer_load_dwordx4 v237,s[52:55],s82 offen offset:3072 lds
s_add_u32 s81,s81,128
s_add_u32 s82,s82,128
s_branch L_KV_%=
L_NB_%=:
s_lshr_b32 s74,s64,4
s_cmp_lg_u32 s74,0
s_cselect_b64 s[48:49],s[20:21],s[18:19]
s_mov_b32 s50,0x400000
s_mov_b32 s51,0x20000
s_mov_b64 s[52:53],s[16:17]
s_mov_b32 s54,0x200000
s_mov_b32 s55,0x20000
v_and_b32 v246,7,v254
v_lshrrev_b32 v247,4,v254
v_lshrrev_b32 v253,3,v254
v_mul_u32_u24 v253,8192,v253
v_and_b32 v248,1,v247
v_lshlrev_b32 v248,1,v248
v_xor_b32 v248,v248,v246
v_lshl_add_u32 v230,v248,4,v253
v_and_b32 v248,1,v247
v_lshlrev_b32 v248,1,v248
v_or_b32 v248,4,v248
v_xor_b32 v248,v248,v246
v_lshl_add_u32 v231,v248,4,v253
v_add_u32 v231,64512,v231
v_and_b32 v248,1,v247
v_lshlrev_b32 v248,1,v248
v_xor_b32 v248,v248,v246
v_lshl_add_u32 v232,v248,4,v253
v_add_u32 v232,129024,v232
v_and_b32 v248,1,v247
v_lshlrev_b32 v248,1,v248
v_or_b32 v248,4,v248
v_xor_b32 v248,v248,v246
v_lshl_add_u32 v233,v248,4,v253
v_add_u32 v233,193536,v233
v_and_b32 v246,7,v254
v_lshrrev_b32 v247,4,v254
v_mov_b32 v248,v247
v_xor_b32 v248,v248,v246
v_lshlrev_b32 v248,4,v248
v_lshrrev_b32 v253,3,v254
v_mul_u32_u24 v253,1024,v253
v_add3_u32 v234,v253,v248,0
v_or_b32 v248,4,v247
v_xor_b32 v248,v248,v246
v_lshlrev_b32 v248,4,v248
v_lshrrev_b32 v253,3,v254
v_mul_u32_u24 v253,1024,v253
v_add3_u32 v235,v253,v248,0
v_add_u32 v235,7168,v235
v_mov_b32 v248,v247
v_xor_b32 v248,v248,v246
v_lshlrev_b32 v248,4,v248
v_lshrrev_b32 v253,3,v254
v_mul_u32_u24 v253,1024,v253
v_add3_u32 v236,v253,v248,0
v_add_u32 v236,14336,v236
v_or_b32 v248,4,v247
v_xor_b32 v248,v248,v246
v_lshlrev_b32 v248,4,v248
v_lshrrev_b32 v253,3,v254
v_mul_u32_u24 v253,1024,v253
v_add3_u32 v237,v253,v248,0
v_add_u32 v237,21504,v237
s_bfe_u32 s60,s64,0x10003
s_lshl_b32 s60,s60,1
s_lshl_b32 s61,s63,2
s_add_u32 s72,s60,s61
s_lshl_b32 s60,s72,8
s_lshl_b32 s61,s56,7
s_add_u32 s79,s60,s61
s_add_u32 s79,s79,1048576
s_add_u32 s80,s79,262144
s_and_b32 s60,s56,1
s_add_u32 s72,s72,s60
s_lshl_b32 s72,s72,20
s_and_b32 s61,s64,7
s_lshl_b32 s60,s61,17
s_add_u32 s72,s72,s60
s_lshl_b32 s60,s61,18
s_lshl_b32 s61,s56,16
s_add_u32 s81,s60,s61
s_add_u32 s81,s81,256
s_add_u32 s82,s81,32768
s_lshl_b32 s60,s56,13
s_add_u32 s83,s57,s60
s_add_u32 s84,s83,0x10000
s_lshr_b32 s60,s56,1
s_lshl_b32 s60,s60,16
s_add_u32 s72,s72,s60
s_lshl_b32 s60,s74,14
s_add_u32 s72,s72,s60
s_and_b32 s60,s56,1
s_lshl_b32 s60,s60,14
s_add_u32 s60,s60,s57
v_lshrrev_b32 v246,4,v254
v_bfe_u32 v247,v254,2,2
v_and_b32 v248,3,v254
v_lshrrev_b32 v249,1,v247
v_and_b32 v250,1,v246
v_lshl_or_b32 v249,v250,1,v249
v_lshl_add_u32 v247,v246,3,v247
v_lshlrev_b32 v247,7,v247
v_lshl_add_u32 v247,v248,3,v247
v_add_u32 v247,s60,v247
v_xor_b32 v250,0,v249
v_lshl_add_u32 v224,v250,5,v247
v_xor_b32 v250,1,v249
v_lshl_add_u32 v225,v250,5,v247
v_xor_b32 v250,2,v249
v_lshl_add_u32 v226,v250,5,v247
v_xor_b32 v250,3,v249
v_lshl_add_u32 v227,v250,5,v247
s_lshr_b32 s60,s56,1
s_lshl_b32 s60,s60,14
s_add_u32 s60,s60,s57
s_add_u32 s60,s60,0x10000
v_lshrrev_b32 v246,4,v254
v_and_b32 v247,15,v254
v_lshrrev_b32 v248,1,v247
v_or_b32 v249,0,v246
v_xor_b32 v249,v249,v248
v_lshlrev_b32 v249,4,v249
v_lshl_add_u32 v249,v247,7,v249
v_add_u32 v228,s60,v249
v_or_b32 v249,4,v246
v_xor_b32 v249,v249,v248
v_lshlrev_b32 v249,4,v249
v_lshl_add_u32 v249,v247,7,v249
v_add_u32 v229,s60,v249
s_lshl_b32 s58,s73,2
s_add_u32 s61,s58,s56
s_lshl_b32 s58,s61,16
s_lshl_b32 s59,s63,10
s_add_u32 s58,s58,s59
s_add_u32 s59,s58,3145728
v_lshrrev_b32 v243,3,v254
v_lshlrev_b32 v243,13,v243
v_and_b32 v246,7,v254
v_lshl_add_u32 v243,v246,7,v243
s_mov_b64 s[44:45],s[18:19]
s_mov_b32 s46,0x400000
s_mov_b32 s47,0x20000
s_mov_b64 s[36:37],s[20:21]
s_mov_b32 s38,0x400000
s_mov_b32 s39,0x20000
s_mul_i32 s61,s61,5504
s_lshl_b32 s60,s63,18
s_add_u32 s61,s61,s60
v_min_u32 v247,42,v254
v_lshlrev_b32 v247,7,v247
buffer_load_dword v244,v247,s[52:55],s61 offen
buffer_load_dword v244,v243,s[36:39],s58 offen
buffer_load_dword v244,v243,s[44:47],s58 offen
buffer_load_dword v244,v243,s[36:39],s59 offen
buffer_load_dword v244,v243,s[44:47],s59 offen
L_KV_%=:
s_cmp_lg_u32 s74,0
s_cbranch_scc1 L_KVV_%=
s_waitcnt vmcnt(16) lgkmcnt(0)
s_barrier
ds_read_b64_tr_b16 v[0:1],v224 offset:0
ds_read_b64_tr_b16 v[2:3],v224 offset:512
ds_read_b64_tr_b16 v[4:5],v224 offset:4096
ds_read_b64_tr_b16 v[6:7],v224 offset:4608
ds_read_b64_tr_b16 v[8:9],v225 offset:0
ds_read_b64_tr_b16 v[10:11],v225 offset:512
ds_read_b64_tr_b16 v[12:13],v225 offset:4096
ds_read_b64_tr_b16 v[14:15],v225 offset:4608
ds_read_b64_tr_b16 v[16:17],v226 offset:0
ds_read_b64_tr_b16 v[18:19],v226 offset:512
ds_read_b64_tr_b16 v[20:21],v226 offset:4096
ds_read_b64_tr_b16 v[22:23],v226 offset:4608
ds_read_b64_tr_b16 v[24:25],v227 offset:0
ds_read_b64_tr_b16 v[26:27],v227 offset:512
ds_read_b64_tr_b16 v[28:29],v227 offset:4096
ds_read_b64_tr_b16 v[30:31],v227 offset:4608
ds_read_b64_tr_b16 v[32:33],v224 offset:8192
ds_read_b64_tr_b16 v[34:35],v224 offset:8704
ds_read_b64_tr_b16 v[36:37],v224 offset:12288
ds_read_b64_tr_b16 v[38:39],v224 offset:12800
ds_read_b64_tr_b16 v[40:41],v225 offset:8192
ds_read_b64_tr_b16 v[42:43],v225 offset:8704
ds_read_b64_tr_b16 v[44:45],v225 offset:12288
ds_read_b64_tr_b16 v[46:47],v225 offset:12800
ds_read_b64_tr_b16 v[48:49],v226 offset:8192
ds_read_b64_tr_b16 v[50:51],v226 offset:8704
ds_read_b64_tr_b16 v[52:53],v226 offset:12288
ds_read_b64_tr_b16 v[54:55],v226 offset:12800
ds_read_b64_tr_b16 v[56:57],v227 offset:8192
ds_read_b64_tr_b16 v[58:59],v227 offset:8704
ds_read_b64_tr_b16 v[60:61],v227 offset:12288
ds_read_b64_tr_b16 v[62:63],v227 offset:12800
ds_read_b128 v[128:131],v228 offset:0
ds_read_b128 v[132:135],v229 offset:0
ds_read_b128 v[136:139],v228 offset:2048
ds_read_b128 v[140:143],v229 offset:2048
s_waitcnt lgkmcnt(3)
v_mfma_f32_16x16x32_bf16 a[0:3],v[0:3],v[128:131],0
s_waitcnt lgkmcnt(2)
v_mfma_f32_16x16x32_bf16 a[0:3],v[4:7],v[132:135],a[0:3]
ds_read_b128 v[144:147],v228 offset:4096
s_and_b32 s85,s56,1
v_mfma_f32_16x16x32_bf16 a[4:7],v[8:11],v[128:131],0
ds_read_b128 v[148:151],v229 offset:4096
s_mov_b64 s[40:41],s[22:23]
v_mfma_f32_16x16x32_bf16 a[4:7],v[12:15],v[132:135],a[4:7]
s_add_u32 s40,s40,1048576
s_addc_u32 s41,s41,0
v_mfma_f32_16x16x32_bf16 a[8:11],v[16:19],v[128:131],0
s_mov_b32 s42,0x2000000
s_mov_b32 s43,0x20000
v_mfma_f32_16x16x32_bf16 a[8:11],v[20:23],v[132:135],a[8:11]
)"
R"(s_lshl_b32 s86,s85,7
s_add_u32 s86,s86,s57
v_mfma_f32_16x16x32_bf16 a[12:15],v[24:27],v[128:131],0
s_add_u32 s86,s86,155648
v_and_b32 v241,15,v254
v_mfma_f32_16x16x32_bf16 a[12:15],v[28:31],v[132:135],a[12:15]
v_lshl_add_u32 v238,v241,2,s86
s_mul_i32 s85,s85,3840
v_mfma_f32_16x16x32_bf16 a[16:19],v[32:35],v[128:131],0
s_add_u32 s85,s85,s57
s_add_u32 s85,s85,147456
v_mfma_f32_16x16x32_bf16 a[16:19],v[36:39],v[132:135],a[16:19]
s_lshr_b32 s86,s56,1
s_lshl_b32 s86,s86,9
v_mfma_f32_16x16x32_bf16 a[20:23],v[40:43],v[128:131],0
s_add_u32 s86,s86,s85
v_lshl_add_u32 v240,v254,3,s86
v_mfma_f32_16x16x32_bf16 a[20:23],v[44:47],v[132:135],a[20:23]
ds_read_b32 v152,v238 offset:0
ds_read_b32 v153,v238 offset:256
v_mfma_f32_16x16x32_bf16 a[24:27],v[48:51],v[128:131],0
v_mfma_f32_16x16x32_bf16 a[24:27],v[52:55],v[132:135],a[24:27]
v_mfma_f32_16x16x32_bf16 a[28:31],v[56:59],v[128:131],0
v_mfma_f32_16x16x32_bf16 a[28:31],v[60:63],v[132:135],a[28:31]
s_waitcnt lgkmcnt(5)
v_mfma_f32_16x16x32_bf16 a[32:35],v[0:3],v[136:139],0
s_waitcnt lgkmcnt(4)
v_mfma_f32_16x16x32_bf16 a[32:35],v[4:7],v[140:143],a[32:35]
ds_read_b128 v[128:131],v228 offset:6144
s_waitcnt lgkmcnt(2)
v_fma_f32 v246,v152,v160,v161
v_mfma_f32_16x16x32_bf16 a[36:39],v[8:11],v[136:139],0
ds_read_b128 v[132:135],v229 offset:6144
v_fma_f32 v247,v152,v176,v177
v_mfma_f32_16x16x32_bf16 a[36:39],v[12:15],v[140:143],a[36:39]
s_waitcnt lgkmcnt(2)
v_fma_f32 v246,-v153,v176,v246
v_fma_f32 v247,v153,v160,v247
v_mfma_f32_16x16x32_bf16 a[40:43],v[16:19],v[136:139],0
v_fma_f32 v248,v152,v246,v162
v_fma_f32 v249,v152,v247,v178
v_mfma_f32_16x16x32_bf16 a[40:43],v[20:23],v[140:143],a[40:43]
v_fma_f32 v248,-v153,v247,v248
v_fma_f32 v249,v153,v246,v249
v_mfma_f32_16x16x32_bf16 a[44:47],v[24:27],v[136:139],0
v_fma_f32 v246,v152,v248,v163
v_fma_f32 v247,v152,v249,v179
v_mfma_f32_16x16x32_bf16 a[44:47],v[28:31],v[140:143],a[44:47]
v_fma_f32 v246,-v153,v249,v246
v_fma_f32 v247,v153,v248,v247
v_mfma_f32_16x16x32_bf16 a[48:51],v[32:35],v[136:139],0
v_fma_f32 v248,v152,v246,v164
v_fma_f32 v249,v152,v247,v180
v_mfma_f32_16x16x32_bf16 a[48:51],v[36:39],v[140:143],a[48:51]
v_fma_f32 v248,-v153,v247,v248
v_fma_f32 v249,v153,v246,v249
v_mfma_f32_16x16x32_bf16 a[52:55],v[40:43],v[136:139],0
v_fma_f32 v246,v152,v248,v165
v_fma_f32 v247,v152,v249,v181
v_mfma_f32_16x16x32_bf16 a[52:55],v[44:47],v[140:143],a[52:55]
v_fma_f32 v246,-v153,v249,v246
v_fma_f32 v247,v153,v248,v247
v_mfma_f32_16x16x32_bf16 a[56:59],v[48:51],v[136:139],0
v_fma_f32 v248,v152,v246,v166
v_fma_f32 v249,v152,v247,v182
v_mfma_f32_16x16x32_bf16 a[56:59],v[52:55],v[140:143],a[56:59]
v_fma_f32 v248,-v153,v247,v248
v_fma_f32 v249,v153,v246,v249
v_mfma_f32_16x16x32_bf16 a[60:63],v[56:59],v[136:139],0
v_fma_f32 v246,v152,v248,v167
v_fma_f32 v247,v152,v249,v183
v_mfma_f32_16x16x32_bf16 a[60:63],v[60:63],v[140:143],a[60:63]
v_fma_f32 v246,-v153,v249,v246
v_fma_f32 v247,v153,v248,v247
v_mfma_f32_16x16x32_bf16 a[64:67],v[0:3],v[144:147],0
v_fma_f32 v248,v152,v246,v168
v_fma_f32 v249,v152,v247,v184
v_mfma_f32_16x16x32_bf16 a[64:67],v[4:7],v[148:151],a[64:67]
ds_read_b128 v[136:139],v228 offset:8192
v_fma_f32 v248,-v153,v247,v248
v_mfma_f32_16x16x32_bf16 a[68:71],v[8:11],v[144:147],0
ds_read_b128 v[140:143],v229 offset:8192
v_fma_f32 v249,v153,v246,v249
v_mfma_f32_16x16x32_bf16 a[68:71],v[12:15],v[148:151],a[68:71]
s_waitcnt vmcnt(8) lgkmcnt(0)
s_barrier
v_mfma_f32_16x16x32_bf16 a[72:75],v[16:19],v[144:147],0
ds_read_b64_tr_b16 v[64:65],v224 offset:32768
v_fma_f32 v246,v152,v248,v169
v_mfma_f32_16x16x32_bf16 a[72:75],v[20:23],v[148:151],a[72:75]
ds_read_b64_tr_b16 v[66:67],v224 offset:33280
v_fma_f32 v247,v152,v249,v185
v_mfma_f32_16x16x32_bf16 a[76:79],v[24:27],v[144:147],0
s_add_u32 m0,s83,0
s_nop 0
buffer_load_dwordx4 v230,s[48:51],s79 offen offset:0 lds
v_mfma_f32_16x16x32_bf16 a[76:79],v[28:31],v[148:151],a[76:79]
ds_read_b64_tr_b16 v[68:69],v224 offset:36864
v_fma_f32 v246,-v153,v249,v246
v_mfma_f32_16x16x32_bf16 a[80:83],v[32:35],v[144:147],0
ds_read_b64_tr_b16 v[70:71],v224 offset:37376
v_fma_f32 v247,v153,v248,v247
v_mfma_f32_16x16x32_bf16 a[80:83],v[36:39],v[148:151],a[80:83]
v_fma_f32 v248,v152,v246,v170
v_fma_f32 v249,v152,v247,v186
v_mfma_f32_16x16x32_bf16 a[84:87],v[40:43],v[144:147],0
ds_read_b64_tr_b16 v[72:73],v225 offset:32768
buffer_load_dwordx4 v231,s[48:51],s79 offen offset:1024 lds
v_mfma_f32_16x16x32_bf16 a[84:87],v[44:47],v[148:151],a[84:87]
ds_read_b64_tr_b16 v[74:75],v225 offset:33280
v_fma_f32 v248,-v153,v247,v248
v_mfma_f32_16x16x32_bf16 a[88:91],v[48:51],v[144:147],0
v_fma_f32 v249,v153,v246,v249
v_fma_f32 v246,v152,v248,v171
v_mfma_f32_16x16x32_bf16 a[88:91],v[52:55],v[148:151],a[88:91]
ds_read_b64_tr_b16 v[76:77],v225 offset:36864
v_fma_f32 v247,v152,v249,v187
v_mfma_f32_16x16x32_bf16 a[92:95],v[56:59],v[144:147],0
buffer_load_dwordx4 v232,s[48:51],s79 offen offset:2048 lds
v_fma_f32 v246,-v153,v249,v246
v_mfma_f32_16x16x32_bf16 a[92:95],v[60:63],v[148:151],a[92:95]
ds_read_b64_tr_b16 v[78:79],v225 offset:37376
v_fma_f32 v247,v153,v248,v247
v_mfma_f32_16x16x32_bf16 a[96:99],v[0:3],v[128:131],0
ds_read_b64_tr_b16 v[80:81],v226 offset:32768
v_fma_f32 v248,v152,v246,v172
v_mfma_f32_16x16x32_bf16 a[96:99],v[4:7],v[132:135],a[96:99]
ds_read_b128 v[144:147],v228 offset:10240
v_fma_f32 v249,v152,v247,v188
v_mfma_f32_16x16x32_bf16 a[100:103],v[8:11],v[128:131],0
ds_read_b128 v[148:151],v229 offset:10240
ds_read_b64_tr_b16 v[82:83],v226 offset:33280
v_mfma_f32_16x16x32_bf16 a[100:103],v[12:15],v[132:135],a[100:103]
ds_read_b64_tr_b16 v[84:85],v226 offset:36864
buffer_load_dwordx4 v233,s[48:51],s79 offen offset:3072 lds
v_mfma_f32_16x16x32_bf16 a[104:107],v[16:19],v[128:131],0
v_fma_f32 v248,-v153,v247,v248
v_fma_f32 v249,v153,v246,v249
v_mfma_f32_16x16x32_bf16 a[104:107],v[20:23],v[132:135],a[104:107]
ds_read_b64_tr_b16 v[86:87],v226 offset:37376
v_fma_f32 v246,v152,v248,v173
v_mfma_f32_16x16x32_bf16 a[108:111],v[24:27],v[128:131],0
v_fma_f32 v247,v152,v249,v189
v_fma_f32 v246,-v153,v249,v246
v_mfma_f32_16x16x32_bf16 a[108:111],v[28:31],v[132:135],a[108:111]
ds_read_b64_tr_b16 v[88:89],v227 offset:32768
v_fma_f32 v247,v153,v248,v247
v_mfma_f32_16x16x32_bf16 a[112:115],v[32:35],v[128:131],0
ds_read_b64_tr_b16 v[90:91],v227 offset:33280
v_fma_f32 v248,v152,v246,v174
v_mfma_f32_16x16x32_bf16 a[112:115],v[36:39],v[132:135],a[112:115]
s_add_u32 m0,s83,4096
s_nop 0
buffer_load_dwordx4 v230,s[48:51],s80 offen offset:0 lds
v_mfma_f32_16x16x32_bf16 a[116:119],v[40:43],v[128:131],0
ds_read_b64_tr_b16 v[92:93],v227 offset:36864
v_fma_f32 v249,v152,v247,v190
v_mfma_f32_16x16x32_bf16 a[116:119],v[44:47],v[132:135],a[116:119]
ds_read_b64_tr_b16 v[94:95],v227 offset:37376
v_fma_f32 v248,-v153,v247,v248
v_mfma_f32_16x16x32_bf16 a[120:123],v[48:51],v[128:131],0
v_fma_f32 v249,v153,v246,v249
v_fma_f32 v246,v152,v248,v175
v_mfma_f32_16x16x32_bf16 a[120:123],v[52:55],v[132:135],a[120:123]
ds_read_b64_tr_b16 v[96:97],v224 offset:40960
buffer_load_dwordx4 v231,s[48:51],s80 offen offset:1024 lds
v_mfma_f32_16x16x32_bf16 a[124:127],v[56:59],v[128:131],0
v_fma_f32 v247,v152,v249,v191
v_fma_f32 v246,-v153,v249,v246
v_mfma_f32_16x16x32_bf16 a[124:127],v[60:63],v[132:135],a[124:127]
ds_read_b64_tr_b16 v[98:99],v224 offset:41472
v_fma_f32 v247,v153,v248,v247
v_mfma_f32_16x16x32_bf16 a[128:131],v[0:3],v[136:139],0
ds_read_b64_tr_b16 v[100:101],v224 offset:45056
ds_write_b64 v240,v[246:247] offset:896
v_mfma_f32_16x16x32_bf16 a[128:131],v[4:7],v[140:143],a[128:131]
ds_read_b128 v[128:131],v228 offset:12288
buffer_load_dwordx4 v232,s[48:51],s80 offen offset:2048 lds
v_mfma_f32_16x16x32_bf16 a[132:135],v[8:11],v[136:139],0
ds_read_b128 v[132:135],v229 offset:12288
ds_read_b64_tr_b16 v[102:103],v224 offset:45568
v_mfma_f32_16x16x32_bf16 a[132:135],v[12:15],v[140:143],a[132:135]
ds_read_b64_tr_b16 v[104:105],v225 offset:40960
ds_read_b32 v152,v238 offset:64
v_mfma_f32_16x16x32_bf16 a[136:139],v[16:19],v[136:139],0
ds_read_b32 v153,v238 offset:320
v_mfma_f32_16x16x32_bf16 a[136:139],v[20:23],v[140:143],a[136:139]
ds_read_b64_tr_b16 v[106:107],v225 offset:41472
buffer_load_dwordx4 v233,s[48:51],s80 offen offset:3072 lds
v_mfma_f32_16x16x32_bf16 a[140:143],v[24:27],v[136:139],0
v_mfma_f32_16x16x32_bf16 a[140:143],v[28:31],v[140:143],a[140:143]
ds_read_b64_tr_b16 v[108:109],v225 offset:45056
v_mfma_f32_16x16x32_bf16 a[144:147],v[32:35],v[136:139],0
ds_read_b64_tr_b16 v[110:111],v225 offset:45568
v_mfma_f32_16x16x32_bf16 a[144:147],v[36:39],v[140:143],a[144:147]
s_add_u32 s79,s79,524288
s_add_u32 s80,s80,524288
v_mfma_f32_16x16x32_bf16 a[148:151],v[40:43],v[136:139],0
ds_read_b64_tr_b16 v[112:113],v226 offset:40960
s_waitcnt lgkmcnt(5)
v_fma_f32 v246,v152,v192,v193
v_mfma_f32_16x16x32_bf16 a[148:151],v[44:47],v[140:143],a[148:151]
ds_read_b64_tr_b16 v[114:115],v226 offset:41472
v_fma_f32 v247,v152,v208,v209
v_mfma_f32_16x16x32_bf16 a[152:155],v[48:51],v[136:139],0
s_waitcnt lgkmcnt(5)
v_fma_f32 v246,-v153,v208,v246
v_fma_f32 v247,v153,v192,v247
v_mfma_f32_16x16x32_bf16 a[152:155],v[52:55],v[140:143],a[152:155]
ds_read_b64_tr_b16 v[116:117],v226 offset:45056
v_fma_f32 v248,v152,v246,v194
v_mfma_f32_16x16x32_bf16 a[156:159],v[56:59],v[136:139],0
v_fma_f32 v249,v152,v247,v210
v_fma_f32 v248,-v153,v247,v248
v_mfma_f32_16x16x32_bf16 a[156:159],v[60:63],v[140:143],a[156:159]
ds_read_b64_tr_b16 v[118:119],v226 offset:45568
v_fma_f32 v249,v153,v246,v249
v_mfma_f32_16x16x32_bf16 a[160:163],v[0:3],v[144:147],0
ds_read_b128 v[136:139],v228 offset:14336
ds_read_b128 v[140:143],v229 offset:14336
v_mfma_f32_16x16x32_bf16 a[160:163],v[4:7],v[148:151],a[160:163]
ds_read_b64_tr_b16 v[120:121],v227 offset:40960
v_fma_f32 v246,v152,v248,v195
v_mfma_f32_16x16x32_bf16 a[164:167],v[8:11],v[144:147],0
ds_read_b64_tr_b16 v[122:123],v227 offset:41472
v_fma_f32 v247,v152,v249,v211
v_mfma_f32_16x16x32_bf16 a[164:167],v[12:15],v[148:151],a[164:167]
ds_read_b64_tr_b16 v[124:125],v227 offset:45056
v_fma_f32 v246,-v153,v249,v246
v_mfma_f32_16x16x32_bf16 a[168:171],v[16:19],v[144:147],0
v_fma_f32 v247,v153,v248,v247
v_fma_f32 v248,v152,v246,v196
v_mfma_f32_16x16x32_bf16 a[168:171],v[20:23],v[148:151],a[168:171]
ds_read_b64_tr_b16 v[126:127],v227 offset:45568
v_fma_f32 v249,v152,v247,v212
v_mfma_f32_16x16x32_bf16 a[172:175],v[24:27],v[144:147],0
v_fma_f32 v248,-v153,v247,v248
v_fma_f32 v249,v153,v246,v249
v_mfma_f32_16x16x32_bf16 a[172:175],v[28:31],v[148:151],a[172:175]
v_fma_f32 v246,v152,v248,v197
v_fma_f32 v247,v152,v249,v213
v_mfma_f32_16x16x32_bf16 a[176:179],v[32:35],v[144:147],0
v_fma_f32 v246,-v153,v249,v246
v_fma_f32 v247,v153,v248,v247
v_mfma_f32_16x16x32_bf16 a[176:179],v[36:39],v[148:151],a[176:179]
v_fma_f32 v248,v152,v246,v198
v_fma_f32 v249,v152,v247,v214
v_mfma_f32_16x16x32_bf16 a[180:183],v[40:43],v[144:147],0
v_fma_f32 v248,-v153,v247,v248
v_fma_f32 v249,v153,v246,v249
v_mfma_f32_16x16x32_bf16 a[180:183],v[44:47],v[148:151],a[180:183]
v_fma_f32 v246,v152,v248,v199
v_fma_f32 v247,v152,v249,v215
v_mfma_f32_16x16x32_bf16 a[184:187],v[48:51],v[144:147],0
v_fma_f32 v246,-v153,v249,v246
v_fma_f32 v247,v153,v248,v247
v_mfma_f32_16x16x32_bf16 a[184:187],v[52:55],v[148:151],a[184:187]
v_fma_f32 v248,v152,v246,v200
v_fma_f32 v249,v152,v247,v216
v_mfma_f32_16x16x32_bf16 a[188:191],v[56:59],v[144:147],0
v_fma_f32 v248,-v153,v247,v248
v_fma_f32 v249,v153,v246,v249
v_mfma_f32_16x16x32_bf16 a[188:191],v[60:63],v[148:151],a[188:191]
s_waitcnt vmcnt(0) lgkmcnt(0)
s_barrier
v_mfma_f32_16x16x32_bf16 a[192:195],v[0:3],v[128:131],0
s_add_u32 m0,s84,0
s_nop 0
buffer_load_dwordx4 v234,s[52:55],s81 offen offset:0 lds
v_mfma_f32_16x16x32_bf16 a[192:195],v[4:7],v[132:135],a[192:195]
ds_read_b128 v[144:147],v228 offset:32768
v_fma_f32 v246,v152,v248,v201
v_mfma_f32_16x16x32_bf16 a[196:199],v[8:11],v[128:131],0
ds_read_b128 v[148:151],v229 offset:32768
v_fma_f32 v247,v152,v249,v217
v_mfma_f32_16x16x32_bf16 a[196:199],v[12:15],v[132:135],a[196:199]
v_fma_f32 v246,-v153,v249,v246
v_fma_f32 v247,v153,v248,v247
v_mfma_f32_16x16x32_bf16 a[200:203],v[16:19],v[128:131],0
buffer_load_dwordx4 v235,s[52:55],s81 offen offset:1024 lds
v_fma_f32 v248,v152,v246,v202
v_mfma_f32_16x16x32_bf16 a[200:203],v[20:23],v[132:135],a[200:203]
v_fma_f32 v249,v152,v247,v218
v_fma_f32 v248,-v153,v247,v248
v_mfma_f32_16x16x32_bf16 a[204:207],v[24:27],v[128:131],0
v_fma_f32 v249,v153,v246,v249
v_fma_f32 v246,v152,v248,v203
v_mfma_f32_16x16x32_bf16 a[204:207],v[28:31],v[132:135],a[204:207]
v_fma_f32 v247,v152,v249,v219
v_fma_f32 v246,-v153,v249,v246
v_mfma_f32_16x16x32_bf16 a[208:211],v[32:35],v[128:131],0
buffer_load_dwordx4 v236,s[52:55],s81 offen offset:2048 lds
v_fma_f32 v247,v153,v248,v247
v_mfma_f32_16x16x32_bf16 a[208:211],v[36:39],v[132:135],a[208:211]
v_fma_f32 v248,v152,v246,v204
v_fma_f32 v249,v152,v247,v220
v_mfma_f32_16x16x32_bf16 a[212:215],v[40:43],v[128:131],0
v_fma_f32 v248,-v153,v247,v248
v_fma_f32 v249,v153,v246,v249
v_mfma_f32_16x16x32_bf16 a[212:215],v[44:47],v[132:135],a[212:215]
v_fma_f32 v246,v152,v248,v205
v_fma_f32 v247,v152,v249,v221
v_mfma_f32_16x16x32_bf16 a[216:219],v[48:51],v[128:131],0
buffer_load_dwordx4 v237,s[52:55],s81 offen offset:3072 lds
v_fma_f32 v246,-v153,v249,v246
v_mfma_f32_16x16x32_bf16 a[216:219],v[52:55],v[132:135],a[216:219]
v_fma_f32 v247,v153,v248,v247
v_fma_f32 v248,v152,v246,v206
v_mfma_f32_16x16x32_bf16 a[220:223],v[56:59],v[128:131],0
v_fma_f32 v249,v152,v247,v222
v_fma_f32 v248,-v153,v247,v248
v_mfma_f32_16x16x32_bf16 a[220:223],v[60:63],v[132:135],a[220:223]
v_fma_f32 v249,v153,v246,v249
v_fma_f32 v246,v152,v248,v207
v_mfma_f32_16x16x32_bf16 a[224:227],v[0:3],v[136:139],0
s_add_u32 m0,s84,4096
s_nop 0
buffer_load_dwordx4 v234,s[52:55],s82 offen offset:0 lds
v_mfma_f32_16x16x32_bf16 a[224:227],v[4:7],v[140:143],a[224:227]
ds_read_b128 v[128:131],v228 offset:34816
v_fma_f32 v247,v152,v249,v223
v_mfma_f32_16x16x32_bf16 a[228:231],v[8:11],v[136:139],0
ds_read_b128 v[132:135],v229 offset:34816
v_fma_f32 v246,-v153,v249,v246
v_mfma_f32_16x16x32_bf16 a[228:231],v[12:15],v[140:143],a[228:231]
v_fma_f32 v247,v153,v248,v247
ds_write_b64 v240,v[246:247] offset:2816
v_mfma_f32_16x16x32_bf16 a[232:235],v[16:19],v[136:139],0
buffer_load_dwordx4 v235,s[52:55],s82 offen offset:1024 lds
v_mfma_f32_16x16x32_bf16 a[232:235],v[20:23],v[140:143],a[232:235]
v_mfma_f32_16x16x32_bf16 a[236:239],v[24:27],v[136:139],0
v_mfma_f32_16x16x32_bf16 a[236:239],v[28:31],v[140:143],a[236:239]
v_mfma_f32_16x16x32_bf16 a[240:243],v[32:35],v[136:139],0
buffer_load_dwordx4 v236,s[52:55],s82 offen offset:2048 lds
v_mfma_f32_16x16x32_bf16 a[240:243],v[36:39],v[140:143],a[240:243]
v_mfma_f32_16x16x32_bf16 a[244:247],v[40:43],v[136:139],0
v_mfma_f32_16x16x32_bf16 a[244:247],v[44:47],v[140:143],a[244:247]
v_mfma_f32_16x16x32_bf16 a[248:251],v[48:51],v[136:139],0
buffer_load_dwordx4 v237,s[52:55],s82 offen offset:3072 lds
v_mfma_f32_16x16x32_bf16 a[248:251],v[52:55],v[140:143],a[248:251]
v_mfma_f32_16x16x32_bf16 a[252:255],v[56:59],v[136:139],0
v_mfma_f32_16x16x32_bf16 a[252:255],v[60:63],v[140:143],a[252:255]
s_add_u32 s81,s81,128
s_add_u32 s82,s82,128
s_waitcnt lgkmcnt(4)
v_mfma_f32_16x16x32_bf16 a[0:3],v[64:67],v[144:147],a[0:3]
s_waitcnt lgkmcnt(3)
v_mfma_f32_16x16x32_bf16 a[0:3],v[68:71],v[148:151],a[0:3]
ds_read_b128 v[136:139],v228 offset:36864
v_mfma_f32_16x16x32_bf16 a[4:7],v[72:75],v[144:147],a[4:7]
ds_read_b128 v[140:143],v229 offset:36864
ds_read_b64_tr_b16 v[0:1],v224 offset:0
v_mfma_f32_16x16x32_bf16 a[4:7],v[76:79],v[148:151],a[4:7]
s_add_u32 m0,s83,32768
s_nop 0
buffer_load_dwordx4 v230,s[48:51],s79 offen offset:0 lds
v_mfma_f32_16x16x32_bf16 a[8:11],v[80:83],v[144:147],a[8:11]
ds_read_b64_tr_b16 v[2:3],v224 offset:512
v_mfma_f32_16x16x32_bf16 a[8:11],v[84:87],v[148:151],a[8:11]
v_mfma_f32_16x16x32_bf16 a[12:15],v[88:91],v[144:147],a[12:15]
v_mfma_f32_16x16x32_bf16 a[12:15],v[92:95],v[148:151],a[12:15]
ds_read_b64_tr_b16 v[4:5],v224 offset:4096
buffer_load_dwordx4 v231,s[48:51],s79 offen offset:1024 lds
v_mfma_f32_16x16x32_bf16 a[16:19],v[96:99],v[144:147],a[16:19]
v_mfma_f32_16x16x32_bf16 a[16:19],v[100:103],v[148:151],a[16:19]
ds_read_b64_tr_b16 v[6:7],v224 offset:4608
v_mfma_f32_16x16x32_bf16 a[20:23],v[104:107],v[144:147],a[20:23]
v_mfma_f32_16x16x32_bf16 a[20:23],v[108:111],v[148:151],a[20:23]
buffer_load_dwordx4 v232,s[48:51],s79 offen offset:2048 lds
v_mfma_f32_16x16x32_bf16 a[24:27],v[112:115],v[144:147],a[24:27]
ds_read_b64_tr_b16 v[8:9],v225 offset:0
v_mfma_f32_16x16x32_bf16 a[24:27],v[116:119],v[148:151],a[24:27]
v_mfma_f32_16x16x32_bf16 a[28:31],v[120:123],v[144:147],a[28:31]
v_mfma_f32_16x16x32_bf16 a[28:31],v[124:127],v[148:151],a[28:31]
ds_read_b64_tr_b16 v[10:11],v225 offset:512
buffer_load_dwordx4 v233,s[48:51],s79 offen offset:3072 lds
s_waitcnt lgkmcnt(10)
v_mfma_f32_16x16x32_bf16 a[32:35],v[64:67],v[128:131],a[32:35]
s_waitcnt lgkmcnt(9)
v_mfma_f32_16x16x32_bf16 a[32:35],v[68:71],v[132:135],a[32:35]
ds_read_b128 v[144:147],v228 offset:38912
ds_read_b64_tr_b16 v[12:13],v225 offset:4096
v_mfma_f32_16x16x32_bf16 a[36:39],v[72:75],v[128:131],a[36:39]
ds_read_b128 v[148:151],v229 offset:38912
v_mfma_f32_16x16x32_bf16 a[36:39],v[76:79],v[132:135],a[36:39]
s_add_u32 m0,s83,36864
s_nop 0
buffer_load_dwordx4 v230,s[48:51],s80 offen offset:0 lds
v_mfma_f32_16x16x32_bf16 a[40:43],v[80:83],v[128:131],a[40:43]
ds_read_b64_tr_b16 v[14:15],v225 offset:4608
v_mfma_f32_16x16x32_bf16 a[40:43],v[84:87],v[132:135],a[40:43]
v_mfma_f32_16x16x32_bf16 a[44:47],v[88:91],v[128:131],a[44:47]
v_mfma_f32_16x16x32_bf16 a[44:47],v[92:95],v[132:135],a[44:47]
ds_read_b64_tr_b16 v[16:17],v226 offset:0
buffer_load_dwordx4 v231,s[48:51],s80 offen offset:1024 lds
v_mfma_f32_16x16x32_bf16 a[48:51],v[96:99],v[128:131],a[48:51]
v_mfma_f32_16x16x32_bf16 a[48:51],v[100:103],v[132:135],a[48:51]
ds_read_b64_tr_b16 v[18:19],v226 offset:512
v_mfma_f32_16x16x32_bf16 a[52:55],v[104:107],v[128:131],a[52:55]
v_mfma_f32_16x16x32_bf16 a[52:55],v[108:111],v[132:135],a[52:55]
)"
R"(buffer_load_dwordx4 v232,s[48:51],s80 offen offset:2048 lds
v_mfma_f32_16x16x32_bf16 a[56:59],v[112:115],v[128:131],a[56:59]
ds_read_b64_tr_b16 v[20:21],v226 offset:4096
v_mfma_f32_16x16x32_bf16 a[56:59],v[116:119],v[132:135],a[56:59]
v_mfma_f32_16x16x32_bf16 a[60:63],v[120:123],v[128:131],a[60:63]
v_mfma_f32_16x16x32_bf16 a[60:63],v[124:127],v[132:135],a[60:63]
ds_read_b64_tr_b16 v[22:23],v226 offset:4608
buffer_load_dwordx4 v233,s[48:51],s80 offen offset:3072 lds
s_waitcnt lgkmcnt(15)
v_mfma_f32_16x16x32_bf16 a[64:67],v[64:67],v[136:139],a[64:67]
s_waitcnt lgkmcnt(14)
v_mfma_f32_16x16x32_bf16 a[64:67],v[68:71],v[140:143],a[64:67]
ds_read_b128 v[128:131],v228 offset:40960
ds_read_b64_tr_b16 v[24:25],v227 offset:0
v_mfma_f32_16x16x32_bf16 a[68:71],v[72:75],v[136:139],a[68:71]
ds_read_b128 v[132:135],v229 offset:40960
v_mfma_f32_16x16x32_bf16 a[68:71],v[76:79],v[140:143],a[68:71]
s_add_u32 s79,s79,524288
s_add_u32 s80,s80,524288
v_mfma_f32_16x16x32_bf16 a[72:75],v[80:83],v[136:139],a[72:75]
ds_read_b64_tr_b16 v[26:27],v227 offset:512
v_mfma_f32_16x16x32_bf16 a[72:75],v[84:87],v[140:143],a[72:75]
v_mfma_f32_16x16x32_bf16 a[76:79],v[88:91],v[136:139],a[76:79]
v_mfma_f32_16x16x32_bf16 a[76:79],v[92:95],v[140:143],a[76:79]
ds_read_b64_tr_b16 v[28:29],v227 offset:4096
v_mfma_f32_16x16x32_bf16 a[80:83],v[96:99],v[136:139],a[80:83]
v_mfma_f32_16x16x32_bf16 a[80:83],v[100:103],v[140:143],a[80:83]
ds_read_b64_tr_b16 v[30:31],v227 offset:4608
v_mfma_f32_16x16x32_bf16 a[84:87],v[104:107],v[136:139],a[84:87]
v_mfma_f32_16x16x32_bf16 a[84:87],v[108:111],v[140:143],a[84:87]
v_mfma_f32_16x16x32_bf16 a[88:91],v[112:115],v[136:139],a[88:91]
ds_read_b64_tr_b16 v[32:33],v224 offset:8192
v_mfma_f32_16x16x32_bf16 a[88:91],v[116:119],v[140:143],a[88:91]
v_mfma_f32_16x16x32_bf16 a[92:95],v[120:123],v[136:139],a[92:95]
v_mfma_f32_16x16x32_bf16 a[92:95],v[124:127],v[140:143],a[92:95]
ds_read_b64_tr_b16 v[34:35],v224 offset:8704
s_waitcnt lgkmcnt(15)
v_mfma_f32_16x16x32_bf16 a[96:99],v[64:67],v[144:147],a[96:99]
s_waitcnt lgkmcnt(13)
v_mfma_f32_16x16x32_bf16 a[96:99],v[68:71],v[148:151],a[96:99]
ds_read_b128 v[136:139],v228 offset:43008
ds_read_b64_tr_b16 v[36:37],v224 offset:12288
v_mfma_f32_16x16x32_bf16 a[100:103],v[72:75],v[144:147],a[100:103]
ds_read_b128 v[140:143],v229 offset:43008
s_mov_b64 s[44:45],s[26:27]
v_mfma_f32_16x16x32_bf16 a[100:103],v[76:79],v[148:151],a[100:103]
s_mov_b32 s46,0x500000
s_mov_b32 s47,0x20000
v_mfma_f32_16x16x32_bf16 a[104:107],v[80:83],v[144:147],a[104:107]
ds_read_b64_tr_b16 v[38:39],v224 offset:12800
s_mov_b64 s[88:89],s[24:25]
v_mfma_f32_16x16x32_bf16 a[104:107],v[84:87],v[148:151],a[104:107]
s_mov_b32 s90,0x280000
s_mov_b32 s91,0x20000
v_mfma_f32_16x16x32_bf16 a[108:111],v[88:91],v[144:147],a[108:111]
s_cmp_eq_u32 s65,0
s_cselect_b32 s46,0,s46
s_cselect_b32 s90,0,s90
v_mfma_f32_16x16x32_bf16 a[108:111],v[92:95],v[148:151],a[108:111]
ds_read_b64_tr_b16 v[40:41],v225 offset:8192
s_lshl_b32 s60,s73,2
v_mfma_f32_16x16x32_bf16 a[112:115],v[96:99],v[144:147],a[112:115]
s_add_u32 s61,s60,s56
s_lshl_b32 s58,s61,16
v_mfma_f32_16x16x32_bf16 a[112:115],v[100:103],v[148:151],a[112:115]
ds_read_b64_tr_b16 v[42:43],v225 offset:8704
s_lshl_b32 s60,s63,10
v_mfma_f32_16x16x32_bf16 a[116:119],v[104:107],v[144:147],a[116:119]
s_add_u32 s58,s58,s60
v_lshrrev_b32 v243,3,v254
v_mfma_f32_16x16x32_bf16 a[116:119],v[108:111],v[148:151],a[116:119]
v_lshlrev_b32 v243,13,v243
v_and_b32 v242,7,v254
v_mfma_f32_16x16x32_bf16 a[120:123],v[112:115],v[144:147],a[120:123]
ds_read_b64_tr_b16 v[44:45],v225 offset:12288
v_lshl_add_u32 v243,v242,7,v243
v_mfma_f32_16x16x32_bf16 a[120:123],v[116:119],v[148:151],a[120:123]
s_lshl_b32 s61,s61,12
s_mul_i32 s60,s63,327680
v_mfma_f32_16x16x32_bf16 a[124:127],v[120:123],v[144:147],a[124:127]
s_add_u32 s61,s61,s60
v_min_u32 v242,31,v254
v_mfma_f32_16x16x32_bf16 a[124:127],v[124:127],v[148:151],a[124:127]
ds_read_b64_tr_b16 v[46:47],v225 offset:12800
v_lshlrev_b32 v242,7,v242
s_waitcnt lgkmcnt(15)
v_mfma_f32_16x16x32_bf16 a[128:131],v[64:67],v[128:131],a[128:131]
buffer_load_dword v244,v242,s[88:91],s61 offen
buffer_load_dword v244,v243,s[44:47],s58 offen
s_waitcnt lgkmcnt(13)
v_mfma_f32_16x16x32_bf16 a[128:131],v[68:71],v[132:135],a[128:131]
ds_read_b128 v[144:147],v228 offset:45056
ds_read_b64_tr_b16 v[48:49],v226 offset:8192
v_mfma_f32_16x16x32_bf16 a[132:135],v[72:75],v[128:131],a[132:135]
ds_read_b128 v[148:151],v229 offset:45056
v_mfma_f32_16x16x32_bf16 a[132:135],v[76:79],v[132:135],a[132:135]
v_mfma_f32_16x16x32_bf16 a[136:139],v[80:83],v[128:131],a[136:139]
ds_read_b64_tr_b16 v[50:51],v226 offset:8704
v_mfma_f32_16x16x32_bf16 a[136:139],v[84:87],v[132:135],a[136:139]
v_mfma_f32_16x16x32_bf16 a[140:143],v[88:91],v[128:131],a[140:143]
v_mfma_f32_16x16x32_bf16 a[140:143],v[92:95],v[132:135],a[140:143]
ds_read_b64_tr_b16 v[52:53],v226 offset:12288
v_mfma_f32_16x16x32_bf16 a[144:147],v[96:99],v[128:131],a[144:147]
v_mfma_f32_16x16x32_bf16 a[144:147],v[100:103],v[132:135],a[144:147]
ds_read_b64_tr_b16 v[54:55],v226 offset:12800
v_mfma_f32_16x16x32_bf16 a[148:151],v[104:107],v[128:131],a[148:151]
v_mfma_f32_16x16x32_bf16 a[148:151],v[108:111],v[132:135],a[148:151]
v_mfma_f32_16x16x32_bf16 a[152:155],v[112:115],v[128:131],a[152:155]
ds_read_b64_tr_b16 v[56:57],v227 offset:8192
v_mfma_f32_16x16x32_bf16 a[152:155],v[116:119],v[132:135],a[152:155]
v_mfma_f32_16x16x32_bf16 a[156:159],v[120:123],v[128:131],a[156:159]
v_mfma_f32_16x16x32_bf16 a[156:159],v[124:127],v[132:135],a[156:159]
ds_read_b64_tr_b16 v[58:59],v227 offset:8704
s_waitcnt lgkmcnt(15)
v_mfma_f32_16x16x32_bf16 a[160:163],v[64:67],v[136:139],a[160:163]
ds_read_b128 v[128:131],v228 offset:47104
ds_read_b128 v[132:135],v229 offset:47104
s_waitcnt lgkmcnt(15)
v_mfma_f32_16x16x32_bf16 a[160:163],v[68:71],v[140:143],a[160:163]
ds_read_b64_tr_b16 v[60:61],v227 offset:12288
v_mfma_f32_16x16x32_bf16 a[164:167],v[72:75],v[136:139],a[164:167]
v_mfma_f32_16x16x32_bf16 a[164:167],v[76:79],v[140:143],a[164:167]
v_mfma_f32_16x16x32_bf16 a[168:171],v[80:83],v[136:139],a[168:171]
ds_read_b64_tr_b16 v[62:63],v227 offset:12800
v_mfma_f32_16x16x32_bf16 a[168:171],v[84:87],v[140:143],a[168:171]
v_mfma_f32_16x16x32_bf16 a[172:175],v[88:91],v[136:139],a[172:175]
v_mfma_f32_16x16x32_bf16 a[172:175],v[92:95],v[140:143],a[172:175]
v_mfma_f32_16x16x32_bf16 a[176:179],v[96:99],v[136:139],a[176:179]
v_mfma_f32_16x16x32_bf16 a[176:179],v[100:103],v[140:143],a[176:179]
v_mfma_f32_16x16x32_bf16 a[180:183],v[104:107],v[136:139],a[180:183]
v_mfma_f32_16x16x32_bf16 a[180:183],v[108:111],v[140:143],a[180:183]
v_mfma_f32_16x16x32_bf16 a[184:187],v[112:115],v[136:139],a[184:187]
v_mfma_f32_16x16x32_bf16 a[184:187],v[116:119],v[140:143],a[184:187]
v_mfma_f32_16x16x32_bf16 a[188:191],v[120:123],v[136:139],a[188:191]
v_mfma_f32_16x16x32_bf16 a[188:191],v[124:127],v[140:143],a[188:191]
s_waitcnt vmcnt(2) lgkmcnt(0)
s_barrier
v_mfma_f32_16x16x32_bf16 a[192:195],v[64:67],v[144:147],a[192:195]
s_add_u32 m0,s84,32768
s_nop 0
buffer_load_dwordx4 v234,s[52:55],s81 offen offset:0 lds
v_mfma_f32_16x16x32_bf16 a[192:195],v[68:71],v[148:151],a[192:195]
ds_read_b128 v[136:139],v228 offset:0
s_lshr_b32 s86,s56,1
v_mfma_f32_16x16x32_bf16 a[196:199],v[72:75],v[144:147],a[196:199]
ds_read_b128 v[140:143],v229 offset:0
s_lshl_b32 s86,s86,6
v_mfma_f32_16x16x32_bf16 a[196:199],v[76:79],v[148:151],a[196:199]
v_add_u32 v239,s86,v238
ds_read_b32 v152,v239 offset:1024
v_mfma_f32_16x16x32_bf16 a[200:203],v[80:83],v[144:147],a[200:203]
buffer_load_dwordx4 v235,s[52:55],s81 offen offset:1024 lds
ds_read_b32 v153,v239 offset:1280
v_mfma_f32_16x16x32_bf16 a[200:203],v[84:87],v[148:151],a[200:203]
s_mul_i32 s86,s86,30
s_add_u32 s86,s86,s85
v_mfma_f32_16x16x32_bf16 a[204:207],v[88:91],v[144:147],a[204:207]
v_lshl_add_u32 v239,v241,3,s86
ds_read_b64 v[154:155],v239 offset:896
v_mfma_f32_16x16x32_bf16 a[204:207],v[92:95],v[148:151],a[204:207]
ds_read_b64 v[156:157],v239 offset:1024
ds_read_b64 v[158:159],v239 offset:1152
v_mfma_f32_16x16x32_bf16 a[208:211],v[96:99],v[144:147],a[208:211]
buffer_load_dwordx4 v236,s[52:55],s81 offen offset:2048 lds
ds_read_b64 v[246:247],v239 offset:1280
v_mfma_f32_16x16x32_bf16 a[208:211],v[100:103],v[148:151],a[208:211]
ds_read_b64 v[248:249],v239 offset:1408
ds_read_b64 v[250:251],v239 offset:1536
v_mfma_f32_16x16x32_bf16 a[212:215],v[104:107],v[144:147],a[212:215]
s_mov_b64 s[36:37],s[22:23]
v_mfma_f32_16x16x32_bf16 a[212:215],v[108:111],v[148:151],a[212:215]
s_add_u32 s36,s36,65536
s_addc_u32 s37,s37,0
v_mfma_f32_16x16x32_bf16 a[216:219],v[112:115],v[144:147],a[216:219]
buffer_load_dwordx4 v237,s[52:55],s81 offen offset:3072 lds
s_mov_b32 s38,0x14000
v_mfma_f32_16x16x32_bf16 a[216:219],v[116:119],v[148:151],a[216:219]
s_mov_b32 s39,0x20000
v_mfma_f32_16x16x32_bf16 a[220:223],v[120:123],v[144:147],a[220:223]
s_cmp_eq_u32 s65,0
s_cselect_b32 s38,0,s38
v_mfma_f32_16x16x32_bf16 a[220:223],v[124:127],v[148:151],a[220:223]
s_mul_i32 s85,s75,5120
s_lshl_b32 s86,s76,9
v_mfma_f32_16x16x32_bf16 a[224:227],v[64:67],v[128:131],a[224:227]
s_add_u32 m0,s84,36864
s_nop 0
buffer_load_dwordx4 v234,s[52:55],s82 offen offset:0 lds
v_mfma_f32_16x16x32_bf16 a[224:227],v[68:71],v[132:135],a[224:227]
ds_read_b128 v[144:147],v228 offset:2048
s_add_u32 s85,s85,s86
v_mfma_f32_16x16x32_bf16 a[228:231],v[72:75],v[128:131],a[228:231]
ds_read_b128 v[148:151],v229 offset:2048
s_and_b32 s86,s56,1
v_mfma_f32_16x16x32_bf16 a[228:231],v[76:79],v[132:135],a[228:231]
s_lshl_b32 s86,s86,8
s_add_u32 s85,s85,s86
v_mfma_f32_16x16x32_bf16 a[232:235],v[80:83],v[128:131],a[232:235]
buffer_load_dwordx4 v235,s[52:55],s82 offen offset:1024 lds
s_lshr_b32 s86,s56,1
v_mfma_f32_16x16x32_bf16 a[232:235],v[84:87],v[132:135],a[232:235]
s_lshl_b32 s86,s86,7
s_add_u32 s85,s85,s86
v_mfma_f32_16x16x32_bf16 a[236:239],v[88:91],v[128:131],a[236:239]
v_lshlrev_b32 v252,3,v241
s_waitcnt lgkmcnt(6)
v_fma_f32 v156,v152,v154,v156
v_mfma_f32_16x16x32_bf16 a[236:239],v[92:95],v[132:135],a[236:239]
v_fma_f32 v157,v152,v155,v157
v_fma_f32 v156,-v153,v155,v156
v_mfma_f32_16x16x32_bf16 a[240:243],v[96:99],v[128:131],a[240:243]
buffer_load_dwordx4 v236,s[52:55],s82 offen offset:2048 lds
v_fma_f32 v157,v153,v154,v157
v_mfma_f32_16x16x32_bf16 a[240:243],v[100:103],v[132:135],a[240:243]
ds_read_b64 v[154:155],v239 offset:1664
s_waitcnt lgkmcnt(6)
v_fma_f32 v158,v152,v156,v158
v_mfma_f32_16x16x32_bf16 a[244:247],v[104:107],v[128:131],a[244:247]
v_fma_f32 v159,v152,v157,v159
v_fma_f32 v158,-v153,v157,v158
v_mfma_f32_16x16x32_bf16 a[244:247],v[108:111],v[132:135],a[244:247]
v_fma_f32 v159,v153,v156,v159
ds_read_b64 v[156:157],v239 offset:1792
v_mfma_f32_16x16x32_bf16 a[248:251],v[112:115],v[128:131],a[248:251]
buffer_load_dwordx4 v237,s[52:55],s82 offen offset:3072 lds
s_waitcnt lgkmcnt(6)
v_fma_f32 v246,v152,v158,v246
v_mfma_f32_16x16x32_bf16 a[248:251],v[116:119],v[132:135],a[248:251]
v_fma_f32 v247,v152,v159,v247
v_fma_f32 v246,-v153,v159,v246
v_mfma_f32_16x16x32_bf16 a[252:255],v[120:123],v[128:131],a[252:255]
v_fma_f32 v247,v153,v158,v247
s_waitcnt lgkmcnt(5)
v_fma_f32 v248,v152,v246,v248
v_mfma_f32_16x16x32_bf16 a[252:255],v[124:127],v[132:135],a[252:255]
s_add_u32 s81,s81,128
s_add_u32 s82,s82,128
v_mfma_f32_16x16x32_bf16 a[0:3],v[0:3],v[136:139],a[0:3]
v_fma_f32 v249,v152,v247,v249
v_fma_f32 v248,-v153,v247,v248
v_mfma_f32_16x16x32_bf16 a[0:3],v[4:7],v[140:143],a[0:3]
ds_read_b128 v[128:131],v228 offset:4096
v_fma_f32 v249,v153,v246,v249
v_mfma_f32_16x16x32_bf16 a[4:7],v[8:11],v[136:139],a[4:7]
ds_read_b128 v[132:135],v229 offset:4096
ds_read_b64_tr_b16 v[64:65],v224 offset:32768
v_mfma_f32_16x16x32_bf16 a[4:7],v[12:15],v[140:143],a[4:7]
s_add_u32 m0,s83,0
s_nop 0
buffer_load_dwordx4 v230,s[48:51],s79 offen offset:0 lds
v_mfma_f32_16x16x32_bf16 a[8:11],v[16:19],v[136:139],a[8:11]
ds_read_b64_tr_b16 v[66:67],v224 offset:33280
s_waitcnt lgkmcnt(8)
v_fma_f32 v250,v152,v248,v250
v_mfma_f32_16x16x32_bf16 a[8:11],v[20:23],v[140:143],a[8:11]
v_fma_f32 v251,v152,v249,v251
v_fma_f32 v250,-v153,v249,v250
v_mfma_f32_16x16x32_bf16 a[12:15],v[24:27],v[136:139],a[12:15]
v_fma_f32 v251,v153,v248,v251
s_waitcnt lgkmcnt(5)
v_fma_f32 v154,v152,v250,v154
v_mfma_f32_16x16x32_bf16 a[12:15],v[28:31],v[140:143],a[12:15]
ds_read_b64_tr_b16 v[68:69],v224 offset:36864
buffer_load_dwordx4 v231,s[48:51],s79 offen offset:1024 lds
v_mfma_f32_16x16x32_bf16 a[16:19],v[32:35],v[136:139],a[16:19]
v_fma_f32 v155,v152,v251,v155
v_fma_f32 v154,-v153,v251,v154
v_mfma_f32_16x16x32_bf16 a[16:19],v[36:39],v[140:143],a[16:19]
ds_read_b64_tr_b16 v[70:71],v224 offset:37376
v_fma_f32 v155,v153,v250,v155
v_mfma_f32_16x16x32_bf16 a[20:23],v[40:43],v[136:139],a[20:23]
s_waitcnt lgkmcnt(6)
v_fma_f32 v156,v152,v154,v156
v_fma_f32 v157,v152,v155,v157
v_mfma_f32_16x16x32_bf16 a[20:23],v[44:47],v[140:143],a[20:23]
buffer_load_dwordx4 v232,s[48:51],s79 offen offset:2048 lds
v_fma_f32 v156,-v153,v155,v156
v_mfma_f32_16x16x32_bf16 a[24:27],v[48:51],v[136:139],a[24:27]
ds_read_b64_tr_b16 v[72:73],v225 offset:32768
v_fma_f32 v157,v153,v154,v157
v_mfma_f32_16x16x32_bf16 a[24:27],v[52:55],v[140:143],a[24:27]
buffer_store_dwordx2 v[156:157],v252,s[36:39],s85 offen sc1
ds_read_b32 v152,v238 offset:1024
v_mfma_f32_16x16x32_bf16 a[28:31],v[56:59],v[136:139],a[28:31]
ds_read_b32 v153,v238 offset:1280
ds_read_b64 v[154:155],v240 offset:0
v_mfma_f32_16x16x32_bf16 a[28:31],v[60:63],v[140:143],a[28:31]
ds_read_b64_tr_b16 v[74:75],v225 offset:33280
buffer_load_dwordx4 v233,s[48:51],s79 offen offset:3072 lds
v_mfma_f32_16x16x32_bf16 a[32:35],v[0:3],v[144:147],a[32:35]
ds_read_b64 v[156:157],v240 offset:128
ds_read_b64 v[158:159],v240 offset:256
v_mfma_f32_16x16x32_bf16 a[32:35],v[4:7],v[148:151],a[32:35]
ds_read_b128 v[136:139],v228 offset:6144
ds_read_b64_tr_b16 v[76:77],v225 offset:36864
v_mfma_f32_16x16x32_bf16 a[36:39],v[8:11],v[144:147],a[36:39]
ds_read_b128 v[140:143],v229 offset:6144
ds_read_b64 v[246:247],v240 offset:384
v_mfma_f32_16x16x32_bf16 a[36:39],v[12:15],v[148:151],a[36:39]
s_add_u32 m0,s83,4096
s_nop 0
buffer_load_dwordx4 v230,s[48:51],s80 offen offset:0 lds
v_mfma_f32_16x16x32_bf16 a[40:43],v[16:19],v[144:147],a[40:43]
ds_read_b64_tr_b16 v[78:79],v225 offset:37376
v_mfma_f32_16x16x32_bf16 a[40:43],v[20:23],v[148:151],a[40:43]
v_mfma_f32_16x16x32_bf16 a[44:47],v[24:27],v[144:147],a[44:47]
v_mfma_f32_16x16x32_bf16 a[44:47],v[28:31],v[148:151],a[44:47]
ds_read_b64_tr_b16 v[80:81],v226 offset:32768
buffer_load_dwordx4 v231,s[48:51],s80 offen offset:1024 lds
v_mfma_f32_16x16x32_bf16 a[48:51],v[32:35],v[144:147],a[48:51]
s_waitcnt lgkmcnt(7)
v_fma_f32 v156,v152,v154,v156
v_fma_f32 v157,v152,v155,v157
v_mfma_f32_16x16x32_bf16 a[48:51],v[36:39],v[148:151],a[48:51]
ds_read_b64_tr_b16 v[82:83],v226 offset:33280
v_fma_f32 v156,-v153,v155,v156
v_mfma_f32_16x16x32_bf16 a[52:55],v[40:43],v[144:147],a[52:55]
v_fma_f32 v157,v153,v154,v157
ds_read_b64 v[154:155],v240 offset:512
v_mfma_f32_16x16x32_bf16 a[52:55],v[44:47],v[148:151],a[52:55]
buffer_load_dwordx4 v232,s[48:51],s80 offen offset:2048 lds
s_waitcnt lgkmcnt(8)
v_fma_f32 v158,v152,v156,v158
v_mfma_f32_16x16x32_bf16 a[56:59],v[48:51],v[144:147],a[56:59]
ds_read_b64_tr_b16 v[84:85],v226 offset:36864
v_fma_f32 v159,v152,v157,v159
v_mfma_f32_16x16x32_bf16 a[56:59],v[52:55],v[148:151],a[56:59]
v_fma_f32 v158,-v153,v157,v158
v_fma_f32 v159,v153,v156,v159
v_mfma_f32_16x16x32_bf16 a[60:63],v[56:59],v[144:147],a[60:63]
ds_read_b64 v[156:157],v240 offset:640
s_waitcnt lgkmcnt(6)
v_fma_f32 v246,v152,v158,v246
v_mfma_f32_16x16x32_bf16 a[60:63],v[60:63],v[148:151],a[60:63]
ds_read_b64_tr_b16 v[86:87],v226 offset:37376
buffer_load_dwordx4 v233,s[48:51],s80 offen offset:3072 lds
v_mfma_f32_16x16x32_bf16 a[64:67],v[0:3],v[128:131],a[64:67]
v_fma_f32 v247,v152,v159,v247
v_fma_f32 v246,-v153,v159,v246
v_mfma_f32_16x16x32_bf16 a[64:67],v[4:7],v[132:135],a[64:67]
ds_read_b128 v[144:147],v228 offset:8192
ds_read_b64_tr_b16 v[88:89],v227 offset:32768
v_mfma_f32_16x16x32_bf16 a[68:71],v[8:11],v[128:131],a[68:71]
ds_read_b128 v[148:151],v229 offset:8192
v_fma_f32 v247,v153,v158,v247
v_mfma_f32_16x16x32_bf16 a[68:71],v[12:15],v[132:135],a[68:71]
s_add_u32 s79,s79,524288
s_add_u32 s80,s80,524288
v_mfma_f32_16x16x32_bf16 a[72:75],v[16:19],v[128:131],a[72:75]
ds_read_b64_tr_b16 v[90:91],v227 offset:33280
ds_read_b64 v[158:159],v240 offset:768
v_mfma_f32_16x16x32_bf16 a[72:75],v[20:23],v[132:135],a[72:75]
s_waitcnt lgkmcnt(8)
v_fma_f32 v154,v152,v246,v154
v_fma_f32 v155,v152,v247,v155
v_mfma_f32_16x16x32_bf16 a[76:79],v[24:27],v[128:131],a[76:79]
v_fma_f32 v154,-v153,v247,v154
v_fma_f32 v155,v153,v246,v155
v_mfma_f32_16x16x32_bf16 a[76:79],v[28:31],v[132:135],a[76:79]
ds_read_b64_tr_b16 v[92:93],v227 offset:36864
s_waitcnt lgkmcnt(7)
v_fma_f32 v156,v152,v154,v156
v_mfma_f32_16x16x32_bf16 a[80:83],v[32:35],v[128:131],a[80:83]
v_fma_f32 v157,v152,v155,v157
v_fma_f32 v156,-v153,v155,v156
v_mfma_f32_16x16x32_bf16 a[80:83],v[36:39],v[132:135],a[80:83]
ds_read_b64_tr_b16 v[94:95],v227 offset:37376
v_fma_f32 v157,v153,v154,v157
v_mfma_f32_16x16x32_bf16 a[84:87],v[40:43],v[128:131],a[84:87]
s_waitcnt lgkmcnt(2)
v_fma_f32 v158,v152,v156,v158
v_fma_f32 v159,v152,v157,v159
v_mfma_f32_16x16x32_bf16 a[84:87],v[44:47],v[132:135],a[84:87]
v_fma_f32 v158,-v153,v157,v158
v_fma_f32 v159,v153,v156,v159
v_mfma_f32_16x16x32_bf16 a[88:91],v[48:51],v[128:131],a[88:91]
ds_read_b64_tr_b16 v[96:97],v224 offset:40960
v_mov_b32 v250,v158
v_mfma_f32_16x16x32_bf16 a[88:91],v[52:55],v[132:135],a[88:91]
v_mov_b32 v251,v159
ds_read_b32 v152,v238 offset:1088
v_mfma_f32_16x16x32_bf16 a[92:95],v[56:59],v[128:131],a[92:95]
ds_read_b32 v153,v238 offset:1344
ds_read_b64 v[154:155],v240 offset:1920
v_mfma_f32_16x16x32_bf16 a[92:95],v[60:63],v[132:135],a[92:95]
ds_read_b64_tr_b16 v[98:99],v224 offset:41472
ds_read_b64 v[156:157],v240 offset:2048
v_mfma_f32_16x16x32_bf16 a[96:99],v[0:3],v[136:139],a[96:99]
ds_read_b64 v[158:159],v240 offset:2176
ds_read_b64 v[246:247],v240 offset:2304
v_mfma_f32_16x16x32_bf16 a[96:99],v[4:7],v[140:143],a[96:99]
ds_read_b128 v[128:131],v228 offset:10240
ds_read_b64_tr_b16 v[100:101],v224 offset:45056
v_mfma_f32_16x16x32_bf16 a[100:103],v[8:11],v[136:139],a[100:103]
ds_read_b128 v[132:135],v229 offset:10240
v_mfma_f32_16x16x32_bf16 a[100:103],v[12:15],v[140:143],a[100:103]
v_mfma_f32_16x16x32_bf16 a[104:107],v[16:19],v[136:139],a[104:107]
ds_read_b64_tr_b16 v[102:103],v224 offset:45568
v_mfma_f32_16x16x32_bf16 a[104:107],v[20:23],v[140:143],a[104:107]
v_mfma_f32_16x16x32_bf16 a[108:111],v[24:27],v[136:139],a[108:111]
)"
R"(s_waitcnt lgkmcnt(6)
v_fma_f32 v156,v152,v154,v156
v_fma_f32 v157,v152,v155,v157
v_mfma_f32_16x16x32_bf16 a[108:111],v[28:31],v[140:143],a[108:111]
ds_read_b64_tr_b16 v[104:105],v225 offset:40960
v_fma_f32 v156,-v153,v155,v156
v_mfma_f32_16x16x32_bf16 a[112:115],v[32:35],v[136:139],a[112:115]
v_fma_f32 v157,v153,v154,v157
ds_read_b64 v[154:155],v240 offset:2432
v_mfma_f32_16x16x32_bf16 a[112:115],v[36:39],v[140:143],a[112:115]
ds_read_b64_tr_b16 v[106:107],v225 offset:41472
s_waitcnt lgkmcnt(8)
v_fma_f32 v158,v152,v156,v158
v_mfma_f32_16x16x32_bf16 a[116:119],v[40:43],v[136:139],a[116:119]
v_fma_f32 v159,v152,v157,v159
v_fma_f32 v158,-v153,v157,v158
v_mfma_f32_16x16x32_bf16 a[116:119],v[44:47],v[140:143],a[116:119]
v_fma_f32 v159,v153,v156,v159
ds_read_b64 v[156:157],v240 offset:2560
v_mfma_f32_16x16x32_bf16 a[120:123],v[48:51],v[136:139],a[120:123]
ds_read_b64_tr_b16 v[108:109],v225 offset:45056
s_waitcnt lgkmcnt(9)
v_fma_f32 v246,v152,v158,v246
v_mfma_f32_16x16x32_bf16 a[120:123],v[52:55],v[140:143],a[120:123]
v_fma_f32 v247,v152,v159,v247
v_fma_f32 v246,-v153,v159,v246
v_mfma_f32_16x16x32_bf16 a[124:127],v[56:59],v[136:139],a[124:127]
v_fma_f32 v247,v153,v158,v247
ds_read_b64 v[158:159],v240 offset:2688
v_mfma_f32_16x16x32_bf16 a[124:127],v[60:63],v[140:143],a[124:127]
ds_read_b64_tr_b16 v[110:111],v225 offset:45568
s_waitcnt lgkmcnt(5)
v_fma_f32 v154,v152,v246,v154
v_mfma_f32_16x16x32_bf16 a[128:131],v[0:3],v[144:147],a[128:131]
v_fma_f32 v155,v152,v247,v155
v_fma_f32 v154,-v153,v247,v154
v_mfma_f32_16x16x32_bf16 a[128:131],v[4:7],v[148:151],a[128:131]
ds_read_b128 v[136:139],v228 offset:12288
ds_read_b64_tr_b16 v[112:113],v226 offset:40960
v_mfma_f32_16x16x32_bf16 a[132:135],v[8:11],v[144:147],a[132:135]
ds_read_b128 v[140:143],v229 offset:12288
v_fma_f32 v155,v153,v246,v155
v_mfma_f32_16x16x32_bf16 a[132:135],v[12:15],v[148:151],a[132:135]
s_waitcnt lgkmcnt(6)
v_fma_f32 v156,v152,v154,v156
v_fma_f32 v157,v152,v155,v157
v_mfma_f32_16x16x32_bf16 a[136:139],v[16:19],v[144:147],a[136:139]
ds_read_b64_tr_b16 v[114:115],v226 offset:41472
v_fma_f32 v156,-v153,v155,v156
v_mfma_f32_16x16x32_bf16 a[136:139],v[20:23],v[148:151],a[136:139]
v_fma_f32 v157,v153,v154,v157
s_waitcnt lgkmcnt(5)
v_fma_f32 v158,v152,v156,v158
v_mfma_f32_16x16x32_bf16 a[140:143],v[24:27],v[144:147],a[140:143]
v_fma_f32 v159,v152,v157,v159
v_fma_f32 v158,-v153,v157,v158
v_mfma_f32_16x16x32_bf16 a[140:143],v[28:31],v[148:151],a[140:143]
ds_read_b64_tr_b16 v[116:117],v226 offset:45056
v_fma_f32 v159,v153,v156,v159
v_mfma_f32_16x16x32_bf16 a[144:147],v[32:35],v[144:147],a[144:147]
v_mov_b32 v252,v158
v_mov_b32 v253,v159
v_mfma_f32_16x16x32_bf16 a[144:147],v[36:39],v[148:151],a[144:147]
ds_read_b64_tr_b16 v[118:119],v226 offset:45568
ds_read_b32 v152,v238 offset:512
v_mfma_f32_16x16x32_bf16 a[148:151],v[40:43],v[144:147],a[148:151]
ds_read_b32 v153,v238 offset:768
v_mfma_f32_16x16x32_bf16 a[148:151],v[44:47],v[148:151],a[148:151]
v_mfma_f32_16x16x32_bf16 a[152:155],v[48:51],v[144:147],a[152:155]
ds_read_b64_tr_b16 v[120:121],v227 offset:40960
v_mfma_f32_16x16x32_bf16 a[152:155],v[52:55],v[148:151],a[152:155]
v_mfma_f32_16x16x32_bf16 a[156:159],v[56:59],v[144:147],a[156:159]
v_mfma_f32_16x16x32_bf16 a[156:159],v[60:63],v[148:151],a[156:159]
ds_read_b64_tr_b16 v[122:123],v227 offset:41472
v_mfma_f32_16x16x32_bf16 a[160:163],v[0:3],v[128:131],a[160:163]
ds_read_b128 v[144:147],v228 offset:14336
ds_read_b128 v[148:151],v229 offset:14336
v_mfma_f32_16x16x32_bf16 a[160:163],v[4:7],v[132:135],a[160:163]
ds_read_b64_tr_b16 v[124:125],v227 offset:45056
s_waitcnt lgkmcnt(5)
v_mul_f32 v154,v153,v160
v_mfma_f32_16x16x32_bf16 a[164:167],v[8:11],v[128:131],a[164:167]
v_mul_f32 v160,v152,v160
v_fma_f32 v160,-v153,v176,v160
v_mfma_f32_16x16x32_bf16 a[164:167],v[12:15],v[132:135],a[164:167]
v_fma_f32 v176,v152,v176,v154
v_mul_f32 v155,v153,v161
v_mfma_f32_16x16x32_bf16 a[168:171],v[16:19],v[128:131],a[168:171]
ds_read_b64_tr_b16 v[126:127],v227 offset:45568
v_mul_f32 v161,v152,v161
v_mfma_f32_16x16x32_bf16 a[168:171],v[20:23],v[132:135],a[168:171]
v_fma_f32 v161,-v153,v177,v161
v_fma_f32 v177,v152,v177,v155
v_mfma_f32_16x16x32_bf16 a[172:175],v[24:27],v[128:131],a[172:175]
v_mul_f32 v154,v153,v162
v_mul_f32 v162,v152,v162
v_mfma_f32_16x16x32_bf16 a[172:175],v[28:31],v[132:135],a[172:175]
v_fma_f32 v162,-v153,v178,v162
v_fma_f32 v178,v152,v178,v154
v_mfma_f32_16x16x32_bf16 a[176:179],v[32:35],v[128:131],a[176:179]
v_mul_f32 v155,v153,v163
v_mul_f32 v163,v152,v163
v_mfma_f32_16x16x32_bf16 a[176:179],v[36:39],v[132:135],a[176:179]
v_fma_f32 v163,-v153,v179,v163
v_fma_f32 v179,v152,v179,v155
v_mfma_f32_16x16x32_bf16 a[180:183],v[40:43],v[128:131],a[180:183]
v_mul_f32 v154,v153,v164
v_mul_f32 v164,v152,v164
v_mfma_f32_16x16x32_bf16 a[180:183],v[44:47],v[132:135],a[180:183]
v_fma_f32 v164,-v153,v180,v164
v_fma_f32 v180,v152,v180,v154
v_mfma_f32_16x16x32_bf16 a[184:187],v[48:51],v[128:131],a[184:187]
v_mul_f32 v155,v153,v165
v_mul_f32 v165,v152,v165
v_mfma_f32_16x16x32_bf16 a[184:187],v[52:55],v[132:135],a[184:187]
v_fma_f32 v165,-v153,v181,v165
v_fma_f32 v181,v152,v181,v155
v_mfma_f32_16x16x32_bf16 a[188:191],v[56:59],v[128:131],a[188:191]
v_mul_f32 v154,v153,v166
v_mul_f32 v166,v152,v166
v_mfma_f32_16x16x32_bf16 a[188:191],v[60:63],v[132:135],a[188:191]
s_waitcnt vmcnt(5)
s_waitcnt vmcnt(0) lgkmcnt(0)
s_barrier
s_xor_b32 s58,s65,1
s_or_b32 s58,s58,s56
s_cbranch_scc1 L_PUBE0_%=
v_mov_b32 v242,1
v_mov_b32 v243,s94
s_mov_b64 exec,1
global_atomic_add v243,v242,s[22:23]
s_mov_b64 exec,-1
L_PUBE0_%=:
v_mfma_f32_16x16x32_bf16 a[192:195],v[0:3],v[136:139],a[192:195]
s_add_u32 m0,s84,0
s_nop 0
buffer_load_dwordx4 v234,s[52:55],s81 offen offset:0 lds
v_mfma_f32_16x16x32_bf16 a[192:195],v[4:7],v[140:143],a[192:195]
ds_read_b128 v[128:131],v228 offset:32768
v_fma_f32 v166,-v153,v182,v166
v_mfma_f32_16x16x32_bf16 a[196:199],v[8:11],v[136:139],a[196:199]
ds_read_b128 v[132:135],v229 offset:32768
v_fma_f32 v182,v152,v182,v154
v_mfma_f32_16x16x32_bf16 a[196:199],v[12:15],v[140:143],a[196:199]
v_mul_f32 v155,v153,v167
v_mul_f32 v167,v152,v167
v_mfma_f32_16x16x32_bf16 a[200:203],v[16:19],v[136:139],a[200:203]
buffer_load_dwordx4 v235,s[52:55],s81 offen offset:1024 lds
v_fma_f32 v167,-v153,v183,v167
v_mfma_f32_16x16x32_bf16 a[200:203],v[20:23],v[140:143],a[200:203]
v_fma_f32 v183,v152,v183,v155
v_mul_f32 v154,v153,v168
v_mfma_f32_16x16x32_bf16 a[204:207],v[24:27],v[136:139],a[204:207]
v_mul_f32 v168,v152,v168
v_fma_f32 v168,-v153,v184,v168
v_mfma_f32_16x16x32_bf16 a[204:207],v[28:31],v[140:143],a[204:207]
v_fma_f32 v184,v152,v184,v154
v_mul_f32 v155,v153,v169
v_mfma_f32_16x16x32_bf16 a[208:211],v[32:35],v[136:139],a[208:211]
buffer_load_dwordx4 v236,s[52:55],s81 offen offset:2048 lds
v_mul_f32 v169,v152,v169
v_mfma_f32_16x16x32_bf16 a[208:211],v[36:39],v[140:143],a[208:211]
v_fma_f32 v169,-v153,v185,v169
v_fma_f32 v185,v152,v185,v155
v_mfma_f32_16x16x32_bf16 a[212:215],v[40:43],v[136:139],a[212:215]
v_mul_f32 v154,v153,v170
v_mul_f32 v170,v152,v170
v_mfma_f32_16x16x32_bf16 a[212:215],v[44:47],v[140:143],a[212:215]
v_fma_f32 v170,-v153,v186,v170
v_fma_f32 v186,v152,v186,v154
v_mfma_f32_16x16x32_bf16 a[216:219],v[48:51],v[136:139],a[216:219]
buffer_load_dwordx4 v237,s[52:55],s81 offen offset:3072 lds
v_mul_f32 v155,v153,v171
v_mfma_f32_16x16x32_bf16 a[216:219],v[52:55],v[140:143],a[216:219]
v_mul_f32 v171,v152,v171
v_fma_f32 v171,-v153,v187,v171
v_mfma_f32_16x16x32_bf16 a[220:223],v[56:59],v[136:139],a[220:223]
v_fma_f32 v187,v152,v187,v155
v_mul_f32 v154,v153,v172
v_mfma_f32_16x16x32_bf16 a[220:223],v[60:63],v[140:143],a[220:223]
v_mul_f32 v172,v152,v172
v_fma_f32 v172,-v153,v188,v172
v_mfma_f32_16x16x32_bf16 a[224:227],v[0:3],v[144:147],a[224:227]
s_add_u32 m0,s84,4096
s_nop 0
buffer_load_dwordx4 v234,s[52:55],s82 offen offset:0 lds
v_mfma_f32_16x16x32_bf16 a[224:227],v[4:7],v[148:151],a[224:227]
ds_read_b128 v[136:139],v228 offset:34816
v_fma_f32 v188,v152,v188,v154
v_mfma_f32_16x16x32_bf16 a[228:231],v[8:11],v[144:147],a[228:231]
ds_read_b128 v[140:143],v229 offset:34816
v_mul_f32 v155,v153,v173
v_mfma_f32_16x16x32_bf16 a[228:231],v[12:15],v[148:151],a[228:231]
v_mul_f32 v173,v152,v173
v_fma_f32 v173,-v153,v189,v173
v_mfma_f32_16x16x32_bf16 a[232:235],v[16:19],v[144:147],a[232:235]
buffer_load_dwordx4 v235,s[52:55],s82 offen offset:1024 lds
v_fma_f32 v189,v152,v189,v155
v_mfma_f32_16x16x32_bf16 a[232:235],v[20:23],v[148:151],a[232:235]
v_mul_f32 v154,v153,v174
v_mul_f32 v174,v152,v174
v_mfma_f32_16x16x32_bf16 a[236:239],v[24:27],v[144:147],a[236:239]
v_fma_f32 v174,-v153,v190,v174
v_fma_f32 v190,v152,v190,v154
v_mfma_f32_16x16x32_bf16 a[236:239],v[28:31],v[148:151],a[236:239]
v_mul_f32 v155,v153,v175
v_mul_f32 v175,v152,v175
v_mfma_f32_16x16x32_bf16 a[240:243],v[32:35],v[144:147],a[240:243]
buffer_load_dwordx4 v236,s[52:55],s82 offen offset:2048 lds
v_fma_f32 v175,-v153,v191,v175
v_mfma_f32_16x16x32_bf16 a[240:243],v[36:39],v[148:151],a[240:243]
v_fma_f32 v191,v152,v191,v155
ds_read_b32 v152,v238 offset:576
v_mfma_f32_16x16x32_bf16 a[244:247],v[40:43],v[144:147],a[244:247]
ds_read_b32 v153,v238 offset:832
v_mfma_f32_16x16x32_bf16 a[244:247],v[44:47],v[148:151],a[244:247]
v_mfma_f32_16x16x32_bf16 a[248:251],v[48:51],v[144:147],a[248:251]
buffer_load_dwordx4 v237,s[52:55],s82 offen offset:3072 lds
v_mfma_f32_16x16x32_bf16 a[248:251],v[52:55],v[148:151],a[248:251]
v_mfma_f32_16x16x32_bf16 a[252:255],v[56:59],v[144:147],a[252:255]
v_mfma_f32_16x16x32_bf16 a[252:255],v[60:63],v[148:151],a[252:255]
s_add_u32 s81,s81,128
s_add_u32 s82,s82,128
s_waitcnt lgkmcnt(5)
v_mfma_f32_16x16x32_bf16 a[0:3],v[64:67],v[128:131],a[0:3]
s_waitcnt lgkmcnt(4)
v_mfma_f32_16x16x32_bf16 a[0:3],v[68:71],v[132:135],a[0:3]
ds_read_b128 v[144:147],v228 offset:36864
s_waitcnt lgkmcnt(1)
v_mul_f32 v154,v153,v192
v_mfma_f32_16x16x32_bf16 a[4:7],v[72:75],v[128:131],a[4:7]
ds_read_b128 v[148:151],v229 offset:36864
ds_read_b64_tr_b16 v[0:1],v224 offset:0
v_mfma_f32_16x16x32_bf16 a[4:7],v[76:79],v[132:135],a[4:7]
s_add_u32 m0,s83,32768
s_nop 0
buffer_load_dwordx4 v230,s[48:51],s79 offen offset:0 lds
v_mfma_f32_16x16x32_bf16 a[8:11],v[80:83],v[128:131],a[8:11]
ds_read_b64_tr_b16 v[2:3],v224 offset:512
v_mul_f32 v192,v152,v192
v_mfma_f32_16x16x32_bf16 a[8:11],v[84:87],v[132:135],a[8:11]
v_fma_f32 v192,-v153,v208,v192
v_fma_f32 v208,v152,v208,v154
v_mfma_f32_16x16x32_bf16 a[12:15],v[88:91],v[128:131],a[12:15]
v_mul_f32 v155,v153,v193
v_mul_f32 v193,v152,v193
v_mfma_f32_16x16x32_bf16 a[12:15],v[92:95],v[132:135],a[12:15]
ds_read_b64_tr_b16 v[4:5],v224 offset:4096
buffer_load_dwordx4 v231,s[48:51],s79 offen offset:1024 lds
v_mfma_f32_16x16x32_bf16 a[16:19],v[96:99],v[128:131],a[16:19]
v_fma_f32 v193,-v153,v209,v193
v_fma_f32 v209,v152,v209,v155
v_mfma_f32_16x16x32_bf16 a[16:19],v[100:103],v[132:135],a[16:19]
ds_read_b64_tr_b16 v[6:7],v224 offset:4608
v_mul_f32 v154,v153,v194
v_mfma_f32_16x16x32_bf16 a[20:23],v[104:107],v[128:131],a[20:23]
v_mul_f32 v194,v152,v194
v_fma_f32 v194,-v153,v210,v194
v_mfma_f32_16x16x32_bf16 a[20:23],v[108:111],v[132:135],a[20:23]
buffer_load_dwordx4 v232,s[48:51],s79 offen offset:2048 lds
v_fma_f32 v210,v152,v210,v154
v_mfma_f32_16x16x32_bf16 a[24:27],v[112:115],v[128:131],a[24:27]
ds_read_b64_tr_b16 v[8:9],v225 offset:0
v_mul_f32 v155,v153,v195
v_mfma_f32_16x16x32_bf16 a[24:27],v[116:119],v[132:135],a[24:27]
v_mul_f32 v195,v152,v195
v_fma_f32 v195,-v153,v211,v195
v_mfma_f32_16x16x32_bf16 a[28:31],v[120:123],v[128:131],a[28:31]
v_fma_f32 v211,v152,v211,v155
v_mul_f32 v154,v153,v196
v_mfma_f32_16x16x32_bf16 a[28:31],v[124:127],v[132:135],a[28:31]
ds_read_b64_tr_b16 v[10:11],v225 offset:512
buffer_load_dwordx4 v233,s[48:51],s79 offen offset:3072 lds
v_mfma_f32_16x16x32_bf16 a[32:35],v[64:67],v[136:139],a[32:35]
v_mul_f32 v196,v152,v196
v_fma_f32 v196,-v153,v212,v196
v_mfma_f32_16x16x32_bf16 a[32:35],v[68:71],v[140:143],a[32:35]
ds_read_b128 v[128:131],v228 offset:38912
ds_read_b64_tr_b16 v[12:13],v225 offset:4096
v_mfma_f32_16x16x32_bf16 a[36:39],v[72:75],v[136:139],a[36:39]
ds_read_b128 v[132:135],v229 offset:38912
v_fma_f32 v212,v152,v212,v154
v_mfma_f32_16x16x32_bf16 a[36:39],v[76:79],v[140:143],a[36:39]
s_add_u32 m0,s83,36864
s_nop 0
buffer_load_dwordx4 v230,s[48:51],s80 offen offset:0 lds
v_mfma_f32_16x16x32_bf16 a[40:43],v[80:83],v[136:139],a[40:43]
ds_read_b64_tr_b16 v[14:15],v225 offset:4608
v_mul_f32 v155,v153,v197
v_mfma_f32_16x16x32_bf16 a[40:43],v[84:87],v[140:143],a[40:43]
v_mul_f32 v197,v152,v197
v_fma_f32 v197,-v153,v213,v197
v_mfma_f32_16x16x32_bf16 a[44:47],v[88:91],v[136:139],a[44:47]
v_fma_f32 v213,v152,v213,v155
v_mul_f32 v154,v153,v198
v_mfma_f32_16x16x32_bf16 a[44:47],v[92:95],v[140:143],a[44:47]
ds_read_b64_tr_b16 v[16:17],v226 offset:0
buffer_load_dwordx4 v231,s[48:51],s80 offen offset:1024 lds
v_mfma_f32_16x16x32_bf16 a[48:51],v[96:99],v[136:139],a[48:51]
v_mul_f32 v198,v152,v198
v_fma_f32 v198,-v153,v214,v198
v_mfma_f32_16x16x32_bf16 a[48:51],v[100:103],v[140:143],a[48:51]
ds_read_b64_tr_b16 v[18:19],v226 offset:512
v_fma_f32 v214,v152,v214,v154
v_mfma_f32_16x16x32_bf16 a[52:55],v[104:107],v[136:139],a[52:55]
v_mul_f32 v155,v153,v199
v_mul_f32 v199,v152,v199
v_mfma_f32_16x16x32_bf16 a[52:55],v[108:111],v[140:143],a[52:55]
buffer_load_dwordx4 v232,s[48:51],s80 offen offset:2048 lds
v_fma_f32 v199,-v153,v215,v199
v_mfma_f32_16x16x32_bf16 a[56:59],v[112:115],v[136:139],a[56:59]
ds_read_b64_tr_b16 v[20:21],v226 offset:4096
v_fma_f32 v215,v152,v215,v155
v_mfma_f32_16x16x32_bf16 a[56:59],v[116:119],v[140:143],a[56:59]
v_mul_f32 v154,v153,v200
v_mul_f32 v200,v152,v200
v_mfma_f32_16x16x32_bf16 a[60:63],v[120:123],v[136:139],a[60:63]
v_fma_f32 v200,-v153,v216,v200
v_fma_f32 v216,v152,v216,v154
v_mfma_f32_16x16x32_bf16 a[60:63],v[124:127],v[140:143],a[60:63]
ds_read_b64_tr_b16 v[22:23],v226 offset:4608
buffer_load_dwordx4 v233,s[48:51],s80 offen offset:3072 lds
s_waitcnt lgkmcnt(15)
v_mfma_f32_16x16x32_bf16 a[64:67],v[64:67],v[144:147],a[64:67]
v_mul_f32 v155,v153,v201
v_mul_f32 v201,v152,v201
s_waitcnt lgkmcnt(14)
v_mfma_f32_16x16x32_bf16 a[64:67],v[68:71],v[148:151],a[64:67]
ds_read_b128 v[136:139],v228 offset:40960
ds_read_b64_tr_b16 v[24:25],v227 offset:0
v_mfma_f32_16x16x32_bf16 a[68:71],v[72:75],v[144:147],a[68:71]
ds_read_b128 v[140:143],v229 offset:40960
v_fma_f32 v201,-v153,v217,v201
v_mfma_f32_16x16x32_bf16 a[68:71],v[76:79],v[148:151],a[68:71]
s_add_u32 s79,s79,524288
s_add_u32 s80,s80,524288
v_mfma_f32_16x16x32_bf16 a[72:75],v[80:83],v[144:147],a[72:75]
ds_read_b64_tr_b16 v[26:27],v227 offset:512
v_fma_f32 v217,v152,v217,v155
v_mfma_f32_16x16x32_bf16 a[72:75],v[84:87],v[148:151],a[72:75]
v_mul_f32 v154,v153,v202
v_mul_f32 v202,v152,v202
v_mfma_f32_16x16x32_bf16 a[76:79],v[88:91],v[144:147],a[76:79]
v_fma_f32 v202,-v153,v218,v202
v_fma_f32 v218,v152,v218,v154
v_mfma_f32_16x16x32_bf16 a[76:79],v[92:95],v[148:151],a[76:79]
ds_read_b64_tr_b16 v[28:29],v227 offset:4096
v_mul_f32 v155,v153,v203
v_mfma_f32_16x16x32_bf16 a[80:83],v[96:99],v[144:147],a[80:83]
v_mul_f32 v203,v152,v203
v_fma_f32 v203,-v153,v219,v203
v_mfma_f32_16x16x32_bf16 a[80:83],v[100:103],v[148:151],a[80:83]
ds_read_b64_tr_b16 v[30:31],v227 offset:4608
v_fma_f32 v219,v152,v219,v155
v_mfma_f32_16x16x32_bf16 a[84:87],v[104:107],v[144:147],a[84:87]
v_mul_f32 v154,v153,v204
v_mul_f32 v204,v152,v204
v_mfma_f32_16x16x32_bf16 a[84:87],v[108:111],v[148:151],a[84:87]
v_fma_f32 v204,-v153,v220,v204
v_fma_f32 v220,v152,v220,v154
v_mfma_f32_16x16x32_bf16 a[88:91],v[112:115],v[144:147],a[88:91]
ds_read_b64_tr_b16 v[32:33],v224 offset:8192
v_mul_f32 v155,v153,v205
v_mfma_f32_16x16x32_bf16 a[88:91],v[116:119],v[148:151],a[88:91]
v_mul_f32 v205,v152,v205
v_fma_f32 v205,-v153,v221,v205
v_mfma_f32_16x16x32_bf16 a[92:95],v[120:123],v[144:147],a[92:95]
v_fma_f32 v221,v152,v221,v155
v_mul_f32 v154,v153,v206
v_mfma_f32_16x16x32_bf16 a[92:95],v[124:127],v[148:151],a[92:95]
ds_read_b64_tr_b16 v[34:35],v224 offset:8704
v_mul_f32 v206,v152,v206
s_waitcnt lgkmcnt(15)
v_mfma_f32_16x16x32_bf16 a[96:99],v[64:67],v[128:131],a[96:99]
v_fma_f32 v206,-v153,v222,v206
v_fma_f32 v222,v152,v222,v154
s_waitcnt lgkmcnt(13)
v_mfma_f32_16x16x32_bf16 a[96:99],v[68:71],v[132:135],a[96:99]
ds_read_b128 v[144:147],v228 offset:43008
ds_read_b64_tr_b16 v[36:37],v224 offset:12288
v_mfma_f32_16x16x32_bf16 a[100:103],v[72:75],v[128:131],a[100:103]
ds_read_b128 v[148:151],v229 offset:43008
v_mul_f32 v155,v153,v207
v_mfma_f32_16x16x32_bf16 a[100:103],v[76:79],v[132:135],a[100:103]
v_mul_f32 v207,v152,v207
v_fma_f32 v207,-v153,v223,v207
v_mfma_f32_16x16x32_bf16 a[104:107],v[80:83],v[128:131],a[104:107]
ds_read_b64_tr_b16 v[38:39],v224 offset:12800
v_fma_f32 v223,v152,v223,v155
v_mfma_f32_16x16x32_bf16 a[104:107],v[84:87],v[132:135],a[104:107]
v_lshrrev_b32 v246,5,v254
v_mul_u32_u24 v246,5120,v246
v_mfma_f32_16x16x32_bf16 a[108:111],v[88:91],v[128:131],a[108:111]
v_and_b32 v247,31,v254
v_lshl_add_u32 v246,v247,4,v246
v_mfma_f32_16x16x32_bf16 a[108:111],v[92:95],v[132:135],a[108:111]
ds_read_b64_tr_b16 v[40:41],v225 offset:8192
v_add_u32 v247,10240,v246
v_mfma_f32_16x16x32_bf16 a[112:115],v[96:99],v[128:131],a[112:115]
s_mul_i32 s85,s56,20480
)"
R"(s_lshl_b32 s86,s76,9
v_mfma_f32_16x16x32_bf16 a[112:115],v[100:103],v[132:135],a[112:115]
ds_read_b64_tr_b16 v[42:43],v225 offset:8704
s_add_u32 s85,s85,s86
v_mfma_f32_16x16x32_bf16 a[116:119],v[104:107],v[128:131],a[116:119]
s_lshl_b32 s86,s56,11
s_add_u32 s86,s86,s57
v_mfma_f32_16x16x32_bf16 a[116:119],v[108:111],v[132:135],a[116:119]
s_add_u32 s86,s86,138752
v_add_u32 v248,s86,v255
v_mfma_f32_16x16x32_bf16 a[120:123],v[112:115],v[128:131],a[120:123]
ds_read_b64_tr_b16 v[44:45],v225 offset:12288
v_mfma_f32_16x16x32_bf16 a[120:123],v[116:119],v[132:135],a[120:123]
v_mfma_f32_16x16x32_bf16 a[124:127],v[120:123],v[128:131],a[124:127]
v_mfma_f32_16x16x32_bf16 a[124:127],v[124:127],v[132:135],a[124:127]
ds_read_b64_tr_b16 v[46:47],v225 offset:12800
s_waitcnt lgkmcnt(15)
v_mfma_f32_16x16x32_bf16 a[128:131],v[64:67],v[136:139],a[128:131]
s_waitcnt lgkmcnt(13)
v_mfma_f32_16x16x32_bf16 a[128:131],v[68:71],v[140:143],a[128:131]
ds_read_b128 v[128:131],v228 offset:45056
ds_read_b64_tr_b16 v[48:49],v226 offset:8192
v_mfma_f32_16x16x32_bf16 a[132:135],v[72:75],v[136:139],a[132:135]
ds_read_b128 v[132:135],v229 offset:45056
v_mfma_f32_16x16x32_bf16 a[132:135],v[76:79],v[140:143],a[132:135]
v_mfma_f32_16x16x32_bf16 a[136:139],v[80:83],v[136:139],a[136:139]
ds_read_b64_tr_b16 v[50:51],v226 offset:8704
v_mfma_f32_16x16x32_bf16 a[136:139],v[84:87],v[140:143],a[136:139]
v_mfma_f32_16x16x32_bf16 a[140:143],v[88:91],v[136:139],a[140:143]
v_mfma_f32_16x16x32_bf16 a[140:143],v[92:95],v[140:143],a[140:143]
ds_read_b64_tr_b16 v[52:53],v226 offset:12288
v_mfma_f32_16x16x32_bf16 a[144:147],v[96:99],v[136:139],a[144:147]
v_mfma_f32_16x16x32_bf16 a[144:147],v[100:103],v[140:143],a[144:147]
ds_read_b64_tr_b16 v[54:55],v226 offset:12800
v_mfma_f32_16x16x32_bf16 a[148:151],v[104:107],v[136:139],a[148:151]
v_mfma_f32_16x16x32_bf16 a[148:151],v[108:111],v[140:143],a[148:151]
v_mfma_f32_16x16x32_bf16 a[152:155],v[112:115],v[136:139],a[152:155]
ds_read_b64_tr_b16 v[56:57],v227 offset:8192
v_mfma_f32_16x16x32_bf16 a[152:155],v[116:119],v[140:143],a[152:155]
v_mfma_f32_16x16x32_bf16 a[156:159],v[120:123],v[136:139],a[156:159]
v_mfma_f32_16x16x32_bf16 a[156:159],v[124:127],v[140:143],a[156:159]
ds_read_b64_tr_b16 v[58:59],v227 offset:8704
s_waitcnt lgkmcnt(15)
v_mfma_f32_16x16x32_bf16 a[160:163],v[64:67],v[144:147],a[160:163]
ds_read_b128 v[136:139],v228 offset:47104
ds_read_b128 v[140:143],v229 offset:47104
s_waitcnt lgkmcnt(15)
v_mfma_f32_16x16x32_bf16 a[160:163],v[68:71],v[148:151],a[160:163]
ds_read_b64_tr_b16 v[60:61],v227 offset:12288
v_mfma_f32_16x16x32_bf16 a[164:167],v[72:75],v[144:147],a[164:167]
v_mfma_f32_16x16x32_bf16 a[164:167],v[76:79],v[148:151],a[164:167]
v_mfma_f32_16x16x32_bf16 a[168:171],v[80:83],v[144:147],a[168:171]
ds_read_b64_tr_b16 v[62:63],v227 offset:12800
v_mfma_f32_16x16x32_bf16 a[168:171],v[84:87],v[148:151],a[168:171]
v_mfma_f32_16x16x32_bf16 a[172:175],v[88:91],v[144:147],a[172:175]
v_mfma_f32_16x16x32_bf16 a[172:175],v[92:95],v[148:151],a[172:175]
v_mfma_f32_16x16x32_bf16 a[176:179],v[96:99],v[144:147],a[176:179]
v_mfma_f32_16x16x32_bf16 a[176:179],v[100:103],v[148:151],a[176:179]
v_mfma_f32_16x16x32_bf16 a[180:183],v[104:107],v[144:147],a[180:183]
v_mfma_f32_16x16x32_bf16 a[180:183],v[108:111],v[148:151],a[180:183]
v_mfma_f32_16x16x32_bf16 a[184:187],v[112:115],v[144:147],a[184:187]
v_mfma_f32_16x16x32_bf16 a[184:187],v[116:119],v[148:151],a[184:187]
v_mfma_f32_16x16x32_bf16 a[188:191],v[120:123],v[144:147],a[188:191]
v_mfma_f32_16x16x32_bf16 a[188:191],v[124:127],v[148:151],a[188:191]
s_xor_b32 s58,s65,1
s_or_b32 s58,s58,s56
s_cbranch_scc1 L_PXE0_%=
v_mov_b32 v243,s94
s_mov_b32 s95,0
L_PLE0_%=:
global_load_dword v242,v243,s[22:23] sc1
s_waitcnt vmcnt(0)
L_PCE0_%=:
v_readfirstlane_b32 s58,v242
s_nop 3
s_cmp_ge_u32 s58,16
s_cbranch_scc1 L_PDE0_%=
s_sleep 2
s_add_u32 s95,s95,1
s_cmp_lt_u32 s95,0x20000
s_cbranch_scc1 L_PLE0_%=
L_PDE0_%=:
L_PXE0_%=:
s_waitcnt vmcnt(0) lgkmcnt(0)
s_barrier
v_mfma_f32_16x16x32_bf16 a[192:195],v[64:67],v[128:131],a[192:195]
s_add_u32 m0,s84,32768
s_nop 0
buffer_load_dwordx4 v234,s[52:55],s81 offen offset:0 lds
v_mfma_f32_16x16x32_bf16 a[192:195],v[68:71],v[132:135],a[192:195]
ds_read_b128 v[144:147],v228 offset:0
buffer_load_dwordx4 v[152:155],v246,s[36:39],s85 offen sc1
v_mfma_f32_16x16x32_bf16 a[196:199],v[72:75],v[128:131],a[196:199]
ds_read_b128 v[148:151],v229 offset:0
buffer_load_dwordx4 v[156:159],v247,s[36:39],s85 offen sc1
v_mfma_f32_16x16x32_bf16 a[196:199],v[76:79],v[132:135],a[196:199]
s_lshl_b32 s85,s75,9
s_and_b32 s86,s56,1
v_mfma_f32_16x16x32_bf16 a[200:203],v[80:83],v[128:131],a[200:203]
buffer_load_dwordx4 v235,s[52:55],s81 offen offset:1024 lds
s_lshl_b32 s86,s86,8
v_mfma_f32_16x16x32_bf16 a[200:203],v[84:87],v[132:135],a[200:203]
s_add_u32 s85,s85,s86
s_add_u32 s85,s85,s57
v_mfma_f32_16x16x32_bf16 a[204:207],v[88:91],v[128:131],a[204:207]
s_add_u32 s85,s85,131072
v_lshl_add_u32 v239,v241,3,s85
v_mfma_f32_16x16x32_bf16 a[204:207],v[92:95],v[132:135],a[204:207]
v_lshrrev_b32 v240,4,v254
v_lshlrev_b32 v240,9,v240
v_mfma_f32_16x16x32_bf16 a[208:211],v[96:99],v[128:131],a[208:211]
buffer_load_dwordx4 v236,s[52:55],s81 offen offset:2048 lds
v_lshl_add_u32 v240,v241,3,v240
v_mfma_f32_16x16x32_bf16 a[208:211],v[100:103],v[132:135],a[208:211]
s_lshr_b32 s85,s56,1
s_lshl_b32 s85,s85,11
v_mfma_f32_16x16x32_bf16 a[212:215],v[104:107],v[128:131],a[212:215]
s_add_u32 s85,s85,s86
s_add_u32 s85,s85,s57
v_mfma_f32_16x16x32_bf16 a[212:215],v[108:111],v[132:135],a[212:215]
s_add_u32 s85,s85,157696
v_add_u32 v240,s85,v240
v_mfma_f32_16x16x32_bf16 a[216:219],v[112:115],v[128:131],a[216:219]
buffer_load_dwordx4 v237,s[52:55],s81 offen offset:3072 lds
v_mfma_f32_16x16x32_bf16 a[216:219],v[116:119],v[132:135],a[216:219]
v_mfma_f32_16x16x32_bf16 a[220:223],v[120:123],v[128:131],a[220:223]
v_mfma_f32_16x16x32_bf16 a[220:223],v[124:127],v[132:135],a[220:223]
v_mfma_f32_16x16x32_bf16 a[224:227],v[64:67],v[136:139],a[224:227]
s_add_u32 m0,s84,36864
s_nop 0
buffer_load_dwordx4 v234,s[52:55],s82 offen offset:0 lds
v_mfma_f32_16x16x32_bf16 a[224:227],v[68:71],v[140:143],a[224:227]
ds_read_b128 v[128:131],v228 offset:2048
v_mfma_f32_16x16x32_bf16 a[228:231],v[72:75],v[136:139],a[228:231]
ds_read_b128 v[132:135],v229 offset:2048
v_mfma_f32_16x16x32_bf16 a[228:231],v[76:79],v[140:143],a[228:231]
v_mfma_f32_16x16x32_bf16 a[232:235],v[80:83],v[136:139],a[232:235]
buffer_load_dwordx4 v235,s[52:55],s82 offen offset:1024 lds
v_mfma_f32_16x16x32_bf16 a[232:235],v[84:87],v[140:143],a[232:235]
v_mfma_f32_16x16x32_bf16 a[236:239],v[88:91],v[136:139],a[236:239]
v_mfma_f32_16x16x32_bf16 a[236:239],v[92:95],v[140:143],a[236:239]
v_mfma_f32_16x16x32_bf16 a[240:243],v[96:99],v[136:139],a[240:243]
buffer_load_dwordx4 v236,s[52:55],s82 offen offset:2048 lds
v_mfma_f32_16x16x32_bf16 a[240:243],v[100:103],v[140:143],a[240:243]
v_mfma_f32_16x16x32_bf16 a[244:247],v[104:107],v[136:139],a[244:247]
v_mfma_f32_16x16x32_bf16 a[244:247],v[108:111],v[140:143],a[244:247]
v_mfma_f32_16x16x32_bf16 a[248:251],v[112:115],v[136:139],a[248:251]
buffer_load_dwordx4 v237,s[52:55],s82 offen offset:3072 lds
v_mfma_f32_16x16x32_bf16 a[248:251],v[116:119],v[140:143],a[248:251]
v_mfma_f32_16x16x32_bf16 a[252:255],v[120:123],v[136:139],a[252:255]
v_mfma_f32_16x16x32_bf16 a[252:255],v[124:127],v[140:143],a[252:255]
s_add_u32 s81,s81,128
s_add_u32 s82,s82,128
s_waitcnt lgkmcnt(3)
v_mfma_f32_16x16x32_bf16 a[0:3],v[0:3],v[144:147],a[0:3]
s_waitcnt lgkmcnt(2)
v_mfma_f32_16x16x32_bf16 a[0:3],v[4:7],v[148:151],a[0:3]
ds_read_b128 v[136:139],v228 offset:4096
v_mfma_f32_16x16x32_bf16 a[4:7],v[8:11],v[144:147],a[4:7]
ds_read_b128 v[140:143],v229 offset:4096
ds_read_b64_tr_b16 v[64:65],v224 offset:32768
v_mfma_f32_16x16x32_bf16 a[4:7],v[12:15],v[148:151],a[4:7]
s_add_u32 m0,s83,0
s_nop 0
buffer_load_dwordx4 v230,s[48:51],s79 offen offset:0 lds
v_mfma_f32_16x16x32_bf16 a[8:11],v[16:19],v[144:147],a[8:11]
ds_read_b64_tr_b16 v[66:67],v224 offset:33280
v_mfma_f32_16x16x32_bf16 a[8:11],v[20:23],v[148:151],a[8:11]
v_mfma_f32_16x16x32_bf16 a[12:15],v[24:27],v[144:147],a[12:15]
v_mfma_f32_16x16x32_bf16 a[12:15],v[28:31],v[148:151],a[12:15]
ds_read_b64_tr_b16 v[68:69],v224 offset:36864
buffer_load_dwordx4 v231,s[48:51],s79 offen offset:1024 lds
v_mfma_f32_16x16x32_bf16 a[16:19],v[32:35],v[144:147],a[16:19]
v_mfma_f32_16x16x32_bf16 a[16:19],v[36:39],v[148:151],a[16:19]
ds_read_b64_tr_b16 v[70:71],v224 offset:37376
v_mfma_f32_16x16x32_bf16 a[20:23],v[40:43],v[144:147],a[20:23]
v_mfma_f32_16x16x32_bf16 a[20:23],v[44:47],v[148:151],a[20:23]
buffer_load_dwordx4 v232,s[48:51],s79 offen offset:2048 lds
v_mfma_f32_16x16x32_bf16 a[24:27],v[48:51],v[144:147],a[24:27]
ds_read_b64_tr_b16 v[72:73],v225 offset:32768
v_mfma_f32_16x16x32_bf16 a[24:27],v[52:55],v[148:151],a[24:27]
v_mfma_f32_16x16x32_bf16 a[28:31],v[56:59],v[144:147],a[28:31]
v_mfma_f32_16x16x32_bf16 a[28:31],v[60:63],v[148:151],a[28:31]
ds_read_b64_tr_b16 v[74:75],v225 offset:33280
buffer_load_dwordx4 v233,s[48:51],s79 offen offset:3072 lds
s_waitcnt lgkmcnt(9)
v_mfma_f32_16x16x32_bf16 a[32:35],v[0:3],v[128:131],a[32:35]
s_waitcnt lgkmcnt(8)
v_mfma_f32_16x16x32_bf16 a[32:35],v[4:7],v[132:135],a[32:35]
ds_read_b128 v[144:147],v228 offset:6144
ds_read_b64_tr_b16 v[76:77],v225 offset:36864
v_mfma_f32_16x16x32_bf16 a[36:39],v[8:11],v[128:131],a[36:39]
ds_read_b128 v[148:151],v229 offset:6144
v_mfma_f32_16x16x32_bf16 a[36:39],v[12:15],v[132:135],a[36:39]
s_add_u32 m0,s83,4096
s_nop 0
buffer_load_dwordx4 v230,s[48:51],s80 offen offset:0 lds
v_mfma_f32_16x16x32_bf16 a[40:43],v[16:19],v[128:131],a[40:43]
ds_read_b64_tr_b16 v[78:79],v225 offset:37376
v_mfma_f32_16x16x32_bf16 a[40:43],v[20:23],v[132:135],a[40:43]
v_mfma_f32_16x16x32_bf16 a[44:47],v[24:27],v[128:131],a[44:47]
v_mfma_f32_16x16x32_bf16 a[44:47],v[28:31],v[132:135],a[44:47]
ds_read_b64_tr_b16 v[80:81],v226 offset:32768
buffer_load_dwordx4 v231,s[48:51],s80 offen offset:1024 lds
v_mfma_f32_16x16x32_bf16 a[48:51],v[32:35],v[128:131],a[48:51]
v_mfma_f32_16x16x32_bf16 a[48:51],v[36:39],v[132:135],a[48:51]
ds_read_b64_tr_b16 v[82:83],v226 offset:33280
v_mfma_f32_16x16x32_bf16 a[52:55],v[40:43],v[128:131],a[52:55]
v_mfma_f32_16x16x32_bf16 a[52:55],v[44:47],v[132:135],a[52:55]
buffer_load_dwordx4 v232,s[48:51],s80 offen offset:2048 lds
v_mfma_f32_16x16x32_bf16 a[56:59],v[48:51],v[128:131],a[56:59]
ds_read_b64_tr_b16 v[84:85],v226 offset:36864
v_mfma_f32_16x16x32_bf16 a[56:59],v[52:55],v[132:135],a[56:59]
v_mfma_f32_16x16x32_bf16 a[60:63],v[56:59],v[128:131],a[60:63]
v_mfma_f32_16x16x32_bf16 a[60:63],v[60:63],v[132:135],a[60:63]
ds_read_b64_tr_b16 v[86:87],v226 offset:37376
buffer_load_dwordx4 v233,s[48:51],s80 offen offset:3072 lds
s_waitcnt lgkmcnt(15)
v_mfma_f32_16x16x32_bf16 a[64:67],v[0:3],v[136:139],a[64:67]
s_waitcnt lgkmcnt(14)
v_mfma_f32_16x16x32_bf16 a[64:67],v[4:7],v[140:143],a[64:67]
ds_read_b128 v[128:131],v228 offset:8192
ds_read_b64_tr_b16 v[88:89],v227 offset:32768
v_mfma_f32_16x16x32_bf16 a[68:71],v[8:11],v[136:139],a[68:71]
ds_read_b128 v[132:135],v229 offset:8192
v_mfma_f32_16x16x32_bf16 a[68:71],v[12:15],v[140:143],a[68:71]
s_add_u32 s79,s79,524288
s_add_u32 s80,s80,524288
v_mfma_f32_16x16x32_bf16 a[72:75],v[16:19],v[136:139],a[72:75]
ds_read_b64_tr_b16 v[90:91],v227 offset:33280
v_mfma_f32_16x16x32_bf16 a[72:75],v[20:23],v[140:143],a[72:75]
v_mfma_f32_16x16x32_bf16 a[76:79],v[24:27],v[136:139],a[76:79]
v_mfma_f32_16x16x32_bf16 a[76:79],v[28:31],v[140:143],a[76:79]
ds_read_b64_tr_b16 v[92:93],v227 offset:36864
v_mfma_f32_16x16x32_bf16 a[80:83],v[32:35],v[136:139],a[80:83]
v_mfma_f32_16x16x32_bf16 a[80:83],v[36:39],v[140:143],a[80:83]
ds_read_b64_tr_b16 v[94:95],v227 offset:37376
v_mfma_f32_16x16x32_bf16 a[84:87],v[40:43],v[136:139],a[84:87]
v_mfma_f32_16x16x32_bf16 a[84:87],v[44:47],v[140:143],a[84:87]
v_mfma_f32_16x16x32_bf16 a[88:91],v[48:51],v[136:139],a[88:91]
ds_read_b64_tr_b16 v[96:97],v224 offset:40960
v_mfma_f32_16x16x32_bf16 a[88:91],v[52:55],v[140:143],a[88:91]
v_mfma_f32_16x16x32_bf16 a[92:95],v[56:59],v[136:139],a[92:95]
v_mfma_f32_16x16x32_bf16 a[92:95],v[60:63],v[140:143],a[92:95]
ds_read_b64_tr_b16 v[98:99],v224 offset:41472
s_waitcnt lgkmcnt(15)
v_mfma_f32_16x16x32_bf16 a[96:99],v[0:3],v[144:147],a[96:99]
s_waitcnt lgkmcnt(13)
v_mfma_f32_16x16x32_bf16 a[96:99],v[4:7],v[148:151],a[96:99]
ds_read_b128 v[136:139],v228 offset:10240
ds_read_b64_tr_b16 v[100:101],v224 offset:45056
v_mfma_f32_16x16x32_bf16 a[100:103],v[8:11],v[144:147],a[100:103]
ds_read_b128 v[140:143],v229 offset:10240
v_mfma_f32_16x16x32_bf16 a[100:103],v[12:15],v[148:151],a[100:103]
v_mfma_f32_16x16x32_bf16 a[104:107],v[16:19],v[144:147],a[104:107]
ds_read_b64_tr_b16 v[102:103],v224 offset:45568
v_mfma_f32_16x16x32_bf16 a[104:107],v[20:23],v[148:151],a[104:107]
v_mfma_f32_16x16x32_bf16 a[108:111],v[24:27],v[144:147],a[108:111]
v_mfma_f32_16x16x32_bf16 a[108:111],v[28:31],v[148:151],a[108:111]
ds_read_b64_tr_b16 v[104:105],v225 offset:40960
v_mfma_f32_16x16x32_bf16 a[112:115],v[32:35],v[144:147],a[112:115]
v_mfma_f32_16x16x32_bf16 a[112:115],v[36:39],v[148:151],a[112:115]
ds_read_b64_tr_b16 v[106:107],v225 offset:41472
v_mfma_f32_16x16x32_bf16 a[116:119],v[40:43],v[144:147],a[116:119]
v_mfma_f32_16x16x32_bf16 a[116:119],v[44:47],v[148:151],a[116:119]
v_mfma_f32_16x16x32_bf16 a[120:123],v[48:51],v[144:147],a[120:123]
ds_read_b64_tr_b16 v[108:109],v225 offset:45056
v_mfma_f32_16x16x32_bf16 a[120:123],v[52:55],v[148:151],a[120:123]
v_mfma_f32_16x16x32_bf16 a[124:127],v[56:59],v[144:147],a[124:127]
v_mfma_f32_16x16x32_bf16 a[124:127],v[60:63],v[148:151],a[124:127]
ds_read_b64_tr_b16 v[110:111],v225 offset:45568
s_waitcnt lgkmcnt(15)
v_mfma_f32_16x16x32_bf16 a[128:131],v[0:3],v[128:131],a[128:131]
s_waitcnt lgkmcnt(13)
v_mfma_f32_16x16x32_bf16 a[128:131],v[4:7],v[132:135],a[128:131]
ds_read_b128 v[144:147],v228 offset:12288
ds_read_b64_tr_b16 v[112:113],v226 offset:40960
v_mfma_f32_16x16x32_bf16 a[132:135],v[8:11],v[128:131],a[132:135]
ds_read_b128 v[148:151],v229 offset:12288
v_mfma_f32_16x16x32_bf16 a[132:135],v[12:15],v[132:135],a[132:135]
v_mfma_f32_16x16x32_bf16 a[136:139],v[16:19],v[128:131],a[136:139]
ds_read_b64_tr_b16 v[114:115],v226 offset:41472
v_mfma_f32_16x16x32_bf16 a[136:139],v[20:23],v[132:135],a[136:139]
v_mfma_f32_16x16x32_bf16 a[140:143],v[24:27],v[128:131],a[140:143]
s_waitcnt vmcnt(15)
ds_write_b128 v248,v[152:155]
v_mfma_f32_16x16x32_bf16 a[140:143],v[28:31],v[132:135],a[140:143]
ds_read_b64_tr_b16 v[116:117],v226 offset:45056
ds_write_b128 v248,v[156:159] offset:1024
v_mfma_f32_16x16x32_bf16 a[144:147],v[32:35],v[128:131],a[144:147]
v_mfma_f32_16x16x32_bf16 a[144:147],v[36:39],v[132:135],a[144:147]
ds_read_b64_tr_b16 v[118:119],v226 offset:45568
v_mfma_f32_16x16x32_bf16 a[148:151],v[40:43],v[128:131],a[148:151]
v_mfma_f32_16x16x32_bf16 a[148:151],v[44:47],v[132:135],a[148:151]
v_mfma_f32_16x16x32_bf16 a[152:155],v[48:51],v[128:131],a[152:155]
ds_read_b64_tr_b16 v[120:121],v227 offset:40960
v_mfma_f32_16x16x32_bf16 a[152:155],v[52:55],v[132:135],a[152:155]
v_mfma_f32_16x16x32_bf16 a[156:159],v[56:59],v[128:131],a[156:159]
v_mfma_f32_16x16x32_bf16 a[156:159],v[60:63],v[132:135],a[156:159]
ds_read_b64_tr_b16 v[122:123],v227 offset:41472
s_waitcnt lgkmcnt(15)
v_mfma_f32_16x16x32_bf16 a[160:163],v[0:3],v[136:139],a[160:163]
ds_read_b128 v[128:131],v228 offset:14336
ds_read_b128 v[132:135],v229 offset:14336
v_mfma_f32_16x16x32_bf16 a[160:163],v[4:7],v[140:143],a[160:163]
ds_read_b64_tr_b16 v[124:125],v227 offset:45056
v_mfma_f32_16x16x32_bf16 a[164:167],v[8:11],v[136:139],a[164:167]
v_mfma_f32_16x16x32_bf16 a[164:167],v[12:15],v[140:143],a[164:167]
v_mfma_f32_16x16x32_bf16 a[168:171],v[16:19],v[136:139],a[168:171]
ds_read_b64_tr_b16 v[126:127],v227 offset:45568
v_mfma_f32_16x16x32_bf16 a[168:171],v[20:23],v[140:143],a[168:171]
v_mfma_f32_16x16x32_bf16 a[172:175],v[24:27],v[136:139],a[172:175]
v_mfma_f32_16x16x32_bf16 a[172:175],v[28:31],v[140:143],a[172:175]
v_mfma_f32_16x16x32_bf16 a[176:179],v[32:35],v[136:139],a[176:179]
v_mfma_f32_16x16x32_bf16 a[176:179],v[36:39],v[140:143],a[176:179]
v_mfma_f32_16x16x32_bf16 a[180:183],v[40:43],v[136:139],a[180:183]
v_mfma_f32_16x16x32_bf16 a[180:183],v[44:47],v[140:143],a[180:183]
v_mfma_f32_16x16x32_bf16 a[184:187],v[48:51],v[136:139],a[184:187]
v_mfma_f32_16x16x32_bf16 a[184:187],v[52:55],v[140:143],a[184:187]
v_mfma_f32_16x16x32_bf16 a[188:191],v[56:59],v[136:139],a[188:191]
v_mfma_f32_16x16x32_bf16 a[188:191],v[60:63],v[140:143],a[188:191]
s_waitcnt vmcnt(0) lgkmcnt(0)
s_barrier
v_mfma_f32_16x16x32_bf16 a[192:195],v[0:3],v[144:147],a[192:195]
s_add_u32 m0,s84,0
s_nop 0
buffer_load_dwordx4 v234,s[52:55],s81 offen offset:0 lds
v_mfma_f32_16x16x32_bf16 a[192:195],v[4:7],v[148:151],a[192:195]
ds_read_b128 v[136:139],v228 offset:32768
ds_read_b32 v152,v238 offset:1536
v_mfma_f32_16x16x32_bf16 a[196:199],v[8:11],v[144:147],a[196:199]
ds_read_b128 v[140:143],v229 offset:32768
ds_read_b32 v153,v238 offset:1792
v_mfma_f32_16x16x32_bf16 a[196:199],v[12:15],v[148:151],a[196:199]
ds_read_b64 v[154:155],v239 offset:0
ds_read_b64 v[156:157],v239 offset:512
v_mfma_f32_16x16x32_bf16 a[200:203],v[16:19],v[144:147],a[200:203]
buffer_load_dwordx4 v235,s[52:55],s81 offen offset:1024 lds
ds_read_b64 v[158:159],v239 offset:1024
v_mfma_f32_16x16x32_bf16 a[200:203],v[20:23],v[148:151],a[200:203]
v_mfma_f32_16x16x32_bf16 a[204:207],v[24:27],v[144:147],a[204:207]
v_mfma_f32_16x16x32_bf16 a[204:207],v[28:31],v[148:151],a[204:207]
v_mfma_f32_16x16x32_bf16 a[208:211],v[32:35],v[144:147],a[208:211]
buffer_load_dwordx4 v236,s[52:55],s81 offen offset:2048 lds
v_mfma_f32_16x16x32_bf16 a[208:211],v[36:39],v[148:151],a[208:211]
v_mfma_f32_16x16x32_bf16 a[212:215],v[40:43],v[144:147],a[212:215]
s_waitcnt lgkmcnt(1)
v_fma_f32 v156,v152,v154,v156
v_fma_f32 v157,v152,v155,v157
v_mfma_f32_16x16x32_bf16 a[212:215],v[44:47],v[148:151],a[212:215]
v_fma_f32 v156,-v153,v155,v156
v_fma_f32 v157,v153,v154,v157
v_mfma_f32_16x16x32_bf16 a[216:219],v[48:51],v[144:147],a[216:219]
buffer_load_dwordx4 v237,s[52:55],s81 offen offset:3072 lds
ds_read_b64 v[154:155],v239 offset:1536
v_mfma_f32_16x16x32_bf16 a[216:219],v[52:55],v[148:151],a[216:219]
v_mfma_f32_16x16x32_bf16 a[220:223],v[56:59],v[144:147],a[220:223]
s_waitcnt lgkmcnt(1)
v_fma_f32 v158,v152,v156,v158
v_fma_f32 v159,v152,v157,v159
v_mfma_f32_16x16x32_bf16 a[220:223],v[60:63],v[148:151],a[220:223]
v_fma_f32 v158,-v153,v157,v158
v_fma_f32 v159,v153,v156,v159
v_mfma_f32_16x16x32_bf16 a[224:227],v[0:3],v[128:131],a[224:227]
s_add_u32 m0,s84,4096
s_nop 0
buffer_load_dwordx4 v234,s[52:55],s82 offen offset:0 lds
v_mfma_f32_16x16x32_bf16 a[224:227],v[4:7],v[132:135],a[224:227]
ds_read_b128 v[144:147],v228 offset:34816
ds_read_b64 v[156:157],v239 offset:2048
v_mfma_f32_16x16x32_bf16 a[228:231],v[8:11],v[128:131],a[228:231]
ds_read_b128 v[148:151],v229 offset:34816
v_mfma_f32_16x16x32_bf16 a[228:231],v[12:15],v[132:135],a[228:231]
s_waitcnt lgkmcnt(3)
v_fma_f32 v154,v152,v158,v154
v_fma_f32 v155,v152,v159,v155
v_mfma_f32_16x16x32_bf16 a[232:235],v[16:19],v[128:131],a[232:235]
buffer_load_dwordx4 v235,s[52:55],s82 offen offset:1024 lds
v_fma_f32 v154,-v153,v159,v154
v_mfma_f32_16x16x32_bf16 a[232:235],v[20:23],v[132:135],a[232:235]
v_fma_f32 v155,v153,v158,v155
ds_read_b64 v[158:159],v239 offset:2560
v_mfma_f32_16x16x32_bf16 a[236:239],v[24:27],v[128:131],a[236:239]
v_mfma_f32_16x16x32_bf16 a[236:239],v[28:31],v[132:135],a[236:239]
s_waitcnt lgkmcnt(2)
v_fma_f32 v156,v152,v154,v156
v_fma_f32 v157,v152,v155,v157
v_mfma_f32_16x16x32_bf16 a[240:243],v[32:35],v[128:131],a[240:243]
buffer_load_dwordx4 v236,s[52:55],s82 offen offset:2048 lds
v_fma_f32 v156,-v153,v155,v156
v_mfma_f32_16x16x32_bf16 a[240:243],v[36:39],v[132:135],a[240:243]
v_fma_f32 v157,v153,v154,v157
ds_read_b64 v[154:155],v239 offset:3072
v_mfma_f32_16x16x32_bf16 a[244:247],v[40:43],v[128:131],a[244:247]
v_mfma_f32_16x16x32_bf16 a[244:247],v[44:47],v[132:135],a[244:247]
s_waitcnt lgkmcnt(1)
v_fma_f32 v158,v152,v156,v158
v_fma_f32 v159,v152,v157,v159
v_mfma_f32_16x16x32_bf16 a[248:251],v[48:51],v[128:131],a[248:251]
buffer_load_dwordx4 v237,s[52:55],s82 offen offset:3072 lds
)"
R"(v_fma_f32 v158,-v153,v157,v158
v_mfma_f32_16x16x32_bf16 a[248:251],v[52:55],v[132:135],a[248:251]
v_fma_f32 v159,v153,v156,v159
ds_read_b64 v[156:157],v239 offset:3584
v_mfma_f32_16x16x32_bf16 a[252:255],v[56:59],v[128:131],a[252:255]
v_mfma_f32_16x16x32_bf16 a[252:255],v[60:63],v[132:135],a[252:255]
s_add_u32 s81,s81,128
s_add_u32 s82,s82,128
v_mfma_f32_16x16x32_bf16 a[0:3],v[64:67],v[136:139],a[0:3]
s_waitcnt lgkmcnt(1)
v_fma_f32 v154,v152,v158,v154
v_fma_f32 v155,v152,v159,v155
v_mfma_f32_16x16x32_bf16 a[0:3],v[68:71],v[140:143],a[0:3]
ds_read_b128 v[128:131],v228 offset:36864
v_fma_f32 v154,-v153,v159,v154
v_mfma_f32_16x16x32_bf16 a[4:7],v[72:75],v[136:139],a[4:7]
ds_read_b128 v[132:135],v229 offset:36864
ds_read_b64_tr_b16 v[0:1],v224 offset:0
v_mfma_f32_16x16x32_bf16 a[4:7],v[76:79],v[140:143],a[4:7]
s_add_u32 m0,s83,32768
s_nop 0
buffer_load_dwordx4 v230,s[48:51],s79 offen offset:0 lds
v_mfma_f32_16x16x32_bf16 a[8:11],v[80:83],v[136:139],a[8:11]
ds_read_b64_tr_b16 v[2:3],v224 offset:512
v_fma_f32 v155,v153,v158,v155
v_mfma_f32_16x16x32_bf16 a[8:11],v[84:87],v[140:143],a[8:11]
ds_read_b64 v[158:159],v239 offset:4096
v_mfma_f32_16x16x32_bf16 a[12:15],v[88:91],v[136:139],a[12:15]
v_mfma_f32_16x16x32_bf16 a[12:15],v[92:95],v[140:143],a[12:15]
ds_read_b64_tr_b16 v[4:5],v224 offset:4096
buffer_load_dwordx4 v231,s[48:51],s79 offen offset:1024 lds
v_mfma_f32_16x16x32_bf16 a[16:19],v[96:99],v[136:139],a[16:19]
s_waitcnt lgkmcnt(6)
v_fma_f32 v156,v152,v154,v156
v_fma_f32 v157,v152,v155,v157
v_mfma_f32_16x16x32_bf16 a[16:19],v[100:103],v[140:143],a[16:19]
ds_read_b64_tr_b16 v[6:7],v224 offset:4608
v_fma_f32 v156,-v153,v155,v156
v_mfma_f32_16x16x32_bf16 a[20:23],v[104:107],v[136:139],a[20:23]
v_fma_f32 v157,v153,v154,v157
ds_read_b64 v[154:155],v239 offset:4608
v_mfma_f32_16x16x32_bf16 a[20:23],v[108:111],v[140:143],a[20:23]
buffer_load_dwordx4 v232,s[48:51],s79 offen offset:2048 lds
v_mfma_f32_16x16x32_bf16 a[24:27],v[112:115],v[136:139],a[24:27]
ds_read_b64_tr_b16 v[8:9],v225 offset:0
s_waitcnt lgkmcnt(4)
v_fma_f32 v158,v152,v156,v158
v_mfma_f32_16x16x32_bf16 a[24:27],v[116:119],v[140:143],a[24:27]
v_fma_f32 v159,v152,v157,v159
v_fma_f32 v158,-v153,v157,v158
v_mfma_f32_16x16x32_bf16 a[28:31],v[120:123],v[136:139],a[28:31]
v_fma_f32 v159,v153,v156,v159
ds_read_b64 v[156:157],v239 offset:5120
v_mfma_f32_16x16x32_bf16 a[28:31],v[124:127],v[140:143],a[28:31]
ds_read_b64_tr_b16 v[10:11],v225 offset:512
buffer_load_dwordx4 v233,s[48:51],s79 offen offset:3072 lds
v_mfma_f32_16x16x32_bf16 a[32:35],v[64:67],v[144:147],a[32:35]
s_waitcnt lgkmcnt(3)
v_fma_f32 v154,v152,v158,v154
v_fma_f32 v155,v152,v159,v155
v_mfma_f32_16x16x32_bf16 a[32:35],v[68:71],v[148:151],a[32:35]
ds_read_b128 v[136:139],v228 offset:38912
ds_read_b64_tr_b16 v[12:13],v225 offset:4096
v_mfma_f32_16x16x32_bf16 a[36:39],v[72:75],v[144:147],a[36:39]
ds_read_b128 v[140:143],v229 offset:38912
v_fma_f32 v154,-v153,v159,v154
v_mfma_f32_16x16x32_bf16 a[36:39],v[76:79],v[148:151],a[36:39]
s_add_u32 m0,s83,36864
s_nop 0
buffer_load_dwordx4 v230,s[48:51],s80 offen offset:0 lds
v_mfma_f32_16x16x32_bf16 a[40:43],v[80:83],v[144:147],a[40:43]
ds_read_b64_tr_b16 v[14:15],v225 offset:4608
v_fma_f32 v155,v153,v158,v155
v_mfma_f32_16x16x32_bf16 a[40:43],v[84:87],v[148:151],a[40:43]
ds_read_b64 v[158:159],v239 offset:5632
v_mfma_f32_16x16x32_bf16 a[44:47],v[88:91],v[144:147],a[44:47]
v_mfma_f32_16x16x32_bf16 a[44:47],v[92:95],v[148:151],a[44:47]
ds_read_b64_tr_b16 v[16:17],v226 offset:0
buffer_load_dwordx4 v231,s[48:51],s80 offen offset:1024 lds
v_mfma_f32_16x16x32_bf16 a[48:51],v[96:99],v[144:147],a[48:51]
s_waitcnt lgkmcnt(7)
v_fma_f32 v156,v152,v154,v156
v_fma_f32 v157,v152,v155,v157
v_mfma_f32_16x16x32_bf16 a[48:51],v[100:103],v[148:151],a[48:51]
ds_read_b64_tr_b16 v[18:19],v226 offset:512
v_fma_f32 v156,-v153,v155,v156
v_mfma_f32_16x16x32_bf16 a[52:55],v[104:107],v[144:147],a[52:55]
v_fma_f32 v157,v153,v154,v157
ds_read_b64 v[154:155],v239 offset:6144
v_mfma_f32_16x16x32_bf16 a[52:55],v[108:111],v[148:151],a[52:55]
buffer_load_dwordx4 v232,s[48:51],s80 offen offset:2048 lds
v_mfma_f32_16x16x32_bf16 a[56:59],v[112:115],v[144:147],a[56:59]
ds_read_b64_tr_b16 v[20:21],v226 offset:4096
s_waitcnt lgkmcnt(4)
v_fma_f32 v158,v152,v156,v158
v_mfma_f32_16x16x32_bf16 a[56:59],v[116:119],v[148:151],a[56:59]
v_fma_f32 v159,v152,v157,v159
v_fma_f32 v158,-v153,v157,v158
v_mfma_f32_16x16x32_bf16 a[60:63],v[120:123],v[144:147],a[60:63]
v_fma_f32 v159,v153,v156,v159
ds_read_b64 v[156:157],v239 offset:6656
v_mfma_f32_16x16x32_bf16 a[60:63],v[124:127],v[148:151],a[60:63]
ds_read_b64_tr_b16 v[22:23],v226 offset:4608
buffer_load_dwordx4 v233,s[48:51],s80 offen offset:3072 lds
v_mfma_f32_16x16x32_bf16 a[64:67],v[64:67],v[128:131],a[64:67]
s_waitcnt lgkmcnt(3)
v_fma_f32 v154,v152,v158,v154
v_fma_f32 v155,v152,v159,v155
v_mfma_f32_16x16x32_bf16 a[64:67],v[68:71],v[132:135],a[64:67]
ds_read_b128 v[144:147],v228 offset:40960
ds_read_b64_tr_b16 v[24:25],v227 offset:0
v_mfma_f32_16x16x32_bf16 a[68:71],v[72:75],v[128:131],a[68:71]
ds_read_b128 v[148:151],v229 offset:40960
v_fma_f32 v154,-v153,v159,v154
v_mfma_f32_16x16x32_bf16 a[68:71],v[76:79],v[132:135],a[68:71]
s_add_u32 s79,s79,524288
s_add_u32 s80,s80,524288
v_mfma_f32_16x16x32_bf16 a[72:75],v[80:83],v[128:131],a[72:75]
ds_read_b64_tr_b16 v[26:27],v227 offset:512
v_fma_f32 v155,v153,v158,v155
v_mfma_f32_16x16x32_bf16 a[72:75],v[84:87],v[132:135],a[72:75]
ds_read_b64 v[158:159],v239 offset:7168
v_mfma_f32_16x16x32_bf16 a[76:79],v[88:91],v[128:131],a[76:79]
v_mfma_f32_16x16x32_bf16 a[76:79],v[92:95],v[132:135],a[76:79]
ds_read_b64_tr_b16 v[28:29],v227 offset:4096
s_waitcnt lgkmcnt(7)
v_fma_f32 v156,v152,v154,v156
v_mfma_f32_16x16x32_bf16 a[80:83],v[96:99],v[128:131],a[80:83]
v_fma_f32 v157,v152,v155,v157
v_fma_f32 v156,-v153,v155,v156
v_mfma_f32_16x16x32_bf16 a[80:83],v[100:103],v[132:135],a[80:83]
ds_read_b64_tr_b16 v[30:31],v227 offset:4608
v_fma_f32 v157,v153,v154,v157
v_mfma_f32_16x16x32_bf16 a[84:87],v[104:107],v[128:131],a[84:87]
ds_read_b64 v[154:155],v240 offset:0
ds_read_b32 v246,v238 offset:0
v_mfma_f32_16x16x32_bf16 a[84:87],v[108:111],v[132:135],a[84:87]
ds_read_b32 v247,v238 offset:256
s_waitcnt lgkmcnt(5)
v_fma_f32 v158,v152,v156,v158
v_mfma_f32_16x16x32_bf16 a[88:91],v[112:115],v[128:131],a[88:91]
ds_read_b64_tr_b16 v[32:33],v224 offset:8192
v_fma_f32 v159,v152,v157,v159
v_mfma_f32_16x16x32_bf16 a[88:91],v[116:119],v[132:135],a[88:91]
v_fma_f32 v158,-v153,v157,v158
v_fma_f32 v159,v153,v156,v159
v_mfma_f32_16x16x32_bf16 a[92:95],v[120:123],v[128:131],a[92:95]
v_mfma_f32_16x16x32_bf16 a[92:95],v[124:127],v[132:135],a[92:95]
ds_read_b64_tr_b16 v[34:35],v224 offset:8704
v_mfma_f32_16x16x32_bf16 a[96:99],v[64:67],v[136:139],a[96:99]
s_waitcnt lgkmcnt(4)
v_fma_f32 v250,v154,v158,v250
v_fma_f32 v251,v154,v159,v251
v_mfma_f32_16x16x32_bf16 a[96:99],v[68:71],v[140:143],a[96:99]
ds_read_b128 v[128:131],v228 offset:43008
ds_read_b64_tr_b16 v[36:37],v224 offset:12288
v_mfma_f32_16x16x32_bf16 a[100:103],v[72:75],v[136:139],a[100:103]
ds_read_b128 v[132:135],v229 offset:43008
v_fma_f32 v250,-v155,v159,v250
v_mfma_f32_16x16x32_bf16 a[100:103],v[76:79],v[140:143],a[100:103]
v_fma_f32 v251,v155,v158,v251
ds_read_b32 v152,v238 offset:1600
v_mfma_f32_16x16x32_bf16 a[104:107],v[80:83],v[136:139],a[104:107]
ds_read_b64_tr_b16 v[38:39],v224 offset:12800
ds_read_b32 v153,v238 offset:1856
v_mfma_f32_16x16x32_bf16 a[104:107],v[84:87],v[140:143],a[104:107]
ds_read_b64 v[154:155],v239 offset:128
ds_read_b64 v[156:157],v239 offset:640
v_mfma_f32_16x16x32_bf16 a[108:111],v[88:91],v[136:139],a[108:111]
ds_read_b64 v[158:159],v239 offset:1152
v_mfma_f32_16x16x32_bf16 a[108:111],v[92:95],v[140:143],a[108:111]
ds_read_b64_tr_b16 v[40:41],v225 offset:8192
v_mfma_f32_16x16x32_bf16 a[112:115],v[96:99],v[136:139],a[112:115]
v_mfma_f32_16x16x32_bf16 a[112:115],v[100:103],v[140:143],a[112:115]
ds_read_b64_tr_b16 v[42:43],v225 offset:8704
v_mfma_f32_16x16x32_bf16 a[116:119],v[104:107],v[136:139],a[116:119]
v_mfma_f32_16x16x32_bf16 a[116:119],v[108:111],v[140:143],a[116:119]
v_mfma_f32_16x16x32_bf16 a[120:123],v[112:115],v[136:139],a[120:123]
ds_read_b64_tr_b16 v[44:45],v225 offset:12288
s_waitcnt lgkmcnt(4)
v_fma_f32 v156,v152,v154,v156
v_mfma_f32_16x16x32_bf16 a[120:123],v[116:119],v[140:143],a[120:123]
v_fma_f32 v157,v152,v155,v157
v_fma_f32 v156,-v153,v155,v156
v_mfma_f32_16x16x32_bf16 a[124:127],v[120:123],v[136:139],a[124:127]
v_fma_f32 v157,v153,v154,v157
ds_read_b64 v[154:155],v239 offset:1664
v_mfma_f32_16x16x32_bf16 a[124:127],v[124:127],v[140:143],a[124:127]
ds_read_b64_tr_b16 v[46:47],v225 offset:12800
v_mfma_f32_16x16x32_bf16 a[128:131],v[64:67],v[144:147],a[128:131]
s_waitcnt lgkmcnt(5)
v_fma_f32 v158,v152,v156,v158
v_fma_f32 v159,v152,v157,v159
v_mfma_f32_16x16x32_bf16 a[128:131],v[68:71],v[148:151],a[128:131]
ds_read_b128 v[136:139],v228 offset:45056
ds_read_b64_tr_b16 v[48:49],v226 offset:8192
v_mfma_f32_16x16x32_bf16 a[132:135],v[72:75],v[144:147],a[132:135]
ds_read_b128 v[140:143],v229 offset:45056
v_fma_f32 v158,-v153,v157,v158
v_mfma_f32_16x16x32_bf16 a[132:135],v[76:79],v[148:151],a[132:135]
v_fma_f32 v159,v153,v156,v159
ds_read_b64 v[156:157],v239 offset:2176
v_mfma_f32_16x16x32_bf16 a[136:139],v[80:83],v[144:147],a[136:139]
ds_read_b64_tr_b16 v[50:51],v226 offset:8704
v_mfma_f32_16x16x32_bf16 a[136:139],v[84:87],v[148:151],a[136:139]
s_waitcnt lgkmcnt(6)
v_fma_f32 v154,v152,v158,v154
v_fma_f32 v155,v152,v159,v155
v_mfma_f32_16x16x32_bf16 a[140:143],v[88:91],v[144:147],a[140:143]
v_fma_f32 v154,-v153,v159,v154
v_fma_f32 v155,v153,v158,v155
v_mfma_f32_16x16x32_bf16 a[140:143],v[92:95],v[148:151],a[140:143]
ds_read_b64_tr_b16 v[52:53],v226 offset:12288
ds_read_b64 v[158:159],v239 offset:2688
v_mfma_f32_16x16x32_bf16 a[144:147],v[96:99],v[144:147],a[144:147]
v_mfma_f32_16x16x32_bf16 a[144:147],v[100:103],v[148:151],a[144:147]
ds_read_b64_tr_b16 v[54:55],v226 offset:12800
s_waitcnt lgkmcnt(4)
v_fma_f32 v156,v152,v154,v156
v_mfma_f32_16x16x32_bf16 a[148:151],v[104:107],v[144:147],a[148:151]
v_fma_f32 v157,v152,v155,v157
v_fma_f32 v156,-v153,v155,v156
v_mfma_f32_16x16x32_bf16 a[148:151],v[108:111],v[148:151],a[148:151]
v_fma_f32 v157,v153,v154,v157
ds_read_b64 v[154:155],v239 offset:3200
v_mfma_f32_16x16x32_bf16 a[152:155],v[112:115],v[144:147],a[152:155]
ds_read_b64_tr_b16 v[56:57],v227 offset:8192
v_mfma_f32_16x16x32_bf16 a[152:155],v[116:119],v[148:151],a[152:155]
s_waitcnt lgkmcnt(3)
v_fma_f32 v158,v152,v156,v158
v_fma_f32 v159,v152,v157,v159
v_mfma_f32_16x16x32_bf16 a[156:159],v[120:123],v[144:147],a[156:159]
v_fma_f32 v158,-v153,v157,v158
v_fma_f32 v159,v153,v156,v159
v_mfma_f32_16x16x32_bf16 a[156:159],v[124:127],v[148:151],a[156:159]
ds_read_b64_tr_b16 v[58:59],v227 offset:8704
ds_read_b64 v[156:157],v239 offset:3712
v_mfma_f32_16x16x32_bf16 a[160:163],v[64:67],v[128:131],a[160:163]
ds_read_b128 v[144:147],v228 offset:47104
ds_read_b128 v[148:151],v229 offset:47104
v_mfma_f32_16x16x32_bf16 a[160:163],v[68:71],v[132:135],a[160:163]
ds_read_b64_tr_b16 v[60:61],v227 offset:12288
s_waitcnt lgkmcnt(6)
v_fma_f32 v154,v152,v158,v154
v_mfma_f32_16x16x32_bf16 a[164:167],v[72:75],v[128:131],a[164:167]
v_fma_f32 v155,v152,v159,v155
v_fma_f32 v154,-v153,v159,v154
v_mfma_f32_16x16x32_bf16 a[164:167],v[76:79],v[132:135],a[164:167]
v_fma_f32 v155,v153,v158,v155
ds_read_b64 v[158:159],v239 offset:4224
v_mfma_f32_16x16x32_bf16 a[168:171],v[80:83],v[128:131],a[168:171]
ds_read_b64_tr_b16 v[62:63],v227 offset:12800
v_mfma_f32_16x16x32_bf16 a[168:171],v[84:87],v[132:135],a[168:171]
s_waitcnt lgkmcnt(5)
v_fma_f32 v156,v152,v154,v156
v_fma_f32 v157,v152,v155,v157
v_mfma_f32_16x16x32_bf16 a[172:175],v[88:91],v[128:131],a[172:175]
v_fma_f32 v156,-v153,v155,v156
v_fma_f32 v157,v153,v154,v157
v_mfma_f32_16x16x32_bf16 a[172:175],v[92:95],v[132:135],a[172:175]
ds_read_b64 v[154:155],v239 offset:4736
v_mfma_f32_16x16x32_bf16 a[176:179],v[96:99],v[128:131],a[176:179]
v_mfma_f32_16x16x32_bf16 a[176:179],v[100:103],v[132:135],a[176:179]
s_waitcnt lgkmcnt(2)
v_fma_f32 v158,v152,v156,v158
v_fma_f32 v159,v152,v157,v159
v_mfma_f32_16x16x32_bf16 a[180:183],v[104:107],v[128:131],a[180:183]
v_fma_f32 v158,-v153,v157,v158
v_fma_f32 v159,v153,v156,v159
v_mfma_f32_16x16x32_bf16 a[180:183],v[108:111],v[132:135],a[180:183]
ds_read_b64 v[156:157],v239 offset:5248
v_mfma_f32_16x16x32_bf16 a[184:187],v[112:115],v[128:131],a[184:187]
v_mfma_f32_16x16x32_bf16 a[184:187],v[116:119],v[132:135],a[184:187]
s_waitcnt lgkmcnt(1)
v_fma_f32 v154,v152,v158,v154
v_fma_f32 v155,v152,v159,v155
v_mfma_f32_16x16x32_bf16 a[188:191],v[120:123],v[128:131],a[188:191]
v_fma_f32 v154,-v153,v159,v154
v_fma_f32 v155,v153,v158,v155
v_mfma_f32_16x16x32_bf16 a[188:191],v[124:127],v[132:135],a[188:191]
s_waitcnt vmcnt(0) lgkmcnt(0)
s_barrier
v_mfma_f32_16x16x32_bf16 a[192:195],v[64:67],v[136:139],a[192:195]
s_add_u32 m0,s84,32768
s_nop 0
buffer_load_dwordx4 v234,s[52:55],s81 offen offset:0 lds
v_mfma_f32_16x16x32_bf16 a[192:195],v[68:71],v[140:143],a[192:195]
ds_read_b128 v[128:131],v228 offset:0
ds_read_b64 v[158:159],v239 offset:5760
v_mfma_f32_16x16x32_bf16 a[196:199],v[72:75],v[136:139],a[196:199]
ds_read_b128 v[132:135],v229 offset:0
v_mfma_f32_16x16x32_bf16 a[196:199],v[76:79],v[140:143],a[196:199]
v_fma_f32 v156,v152,v154,v156
v_fma_f32 v157,v152,v155,v157
v_mfma_f32_16x16x32_bf16 a[200:203],v[80:83],v[136:139],a[200:203]
buffer_load_dwordx4 v235,s[52:55],s81 offen offset:1024 lds
v_fma_f32 v156,-v153,v155,v156
v_mfma_f32_16x16x32_bf16 a[200:203],v[84:87],v[140:143],a[200:203]
v_fma_f32 v157,v153,v154,v157
ds_read_b64 v[154:155],v239 offset:6272
v_mfma_f32_16x16x32_bf16 a[204:207],v[88:91],v[136:139],a[204:207]
v_mfma_f32_16x16x32_bf16 a[204:207],v[92:95],v[140:143],a[204:207]
s_waitcnt lgkmcnt(2)
v_fma_f32 v158,v152,v156,v158
v_fma_f32 v159,v152,v157,v159
v_mfma_f32_16x16x32_bf16 a[208:211],v[96:99],v[136:139],a[208:211]
buffer_load_dwordx4 v236,s[52:55],s81 offen offset:2048 lds
v_fma_f32 v158,-v153,v157,v158
v_mfma_f32_16x16x32_bf16 a[208:211],v[100:103],v[140:143],a[208:211]
v_fma_f32 v159,v153,v156,v159
ds_read_b64 v[156:157],v239 offset:6784
v_mfma_f32_16x16x32_bf16 a[212:215],v[104:107],v[136:139],a[212:215]
v_mfma_f32_16x16x32_bf16 a[212:215],v[108:111],v[140:143],a[212:215]
s_waitcnt lgkmcnt(1)
v_fma_f32 v154,v152,v158,v154
v_fma_f32 v155,v152,v159,v155
v_mfma_f32_16x16x32_bf16 a[216:219],v[112:115],v[136:139],a[216:219]
buffer_load_dwordx4 v237,s[52:55],s81 offen offset:3072 lds
v_fma_f32 v154,-v153,v159,v154
v_mfma_f32_16x16x32_bf16 a[216:219],v[116:119],v[140:143],a[216:219]
v_fma_f32 v155,v153,v158,v155
ds_read_b64 v[158:159],v239 offset:7296
v_mfma_f32_16x16x32_bf16 a[220:223],v[120:123],v[136:139],a[220:223]
v_mfma_f32_16x16x32_bf16 a[220:223],v[124:127],v[140:143],a[220:223]
s_waitcnt lgkmcnt(1)
v_fma_f32 v156,v152,v154,v156
v_fma_f32 v157,v152,v155,v157
v_mfma_f32_16x16x32_bf16 a[224:227],v[64:67],v[144:147],a[224:227]
s_add_u32 m0,s84,36864
s_nop 0
buffer_load_dwordx4 v234,s[52:55],s82 offen offset:0 lds
v_mfma_f32_16x16x32_bf16 a[224:227],v[68:71],v[148:151],a[224:227]
ds_read_b128 v[136:139],v228 offset:2048
v_fma_f32 v156,-v153,v155,v156
v_mfma_f32_16x16x32_bf16 a[228:231],v[72:75],v[144:147],a[228:231]
ds_read_b128 v[140:143],v229 offset:2048
v_fma_f32 v157,v153,v154,v157
v_mfma_f32_16x16x32_bf16 a[228:231],v[76:79],v[148:151],a[228:231]
ds_read_b64 v[154:155],v240 offset:128
ds_read_b32 v248,v238 offset:64
v_mfma_f32_16x16x32_bf16 a[232:235],v[80:83],v[144:147],a[232:235]
buffer_load_dwordx4 v235,s[52:55],s82 offen offset:1024 lds
ds_read_b32 v249,v238 offset:320
v_mfma_f32_16x16x32_bf16 a[232:235],v[84:87],v[148:151],a[232:235]
s_waitcnt lgkmcnt(5)
v_fma_f32 v158,v152,v156,v158
v_fma_f32 v159,v152,v157,v159
v_mfma_f32_16x16x32_bf16 a[236:239],v[88:91],v[144:147],a[236:239]
v_fma_f32 v158,-v153,v157,v158
v_fma_f32 v159,v153,v156,v159
v_mfma_f32_16x16x32_bf16 a[236:239],v[92:95],v[148:151],a[236:239]
v_mfma_f32_16x16x32_bf16 a[240:243],v[96:99],v[144:147],a[240:243]
buffer_load_dwordx4 v236,s[52:55],s82 offen offset:2048 lds
v_mfma_f32_16x16x32_bf16 a[240:243],v[100:103],v[148:151],a[240:243]
s_waitcnt lgkmcnt(2)
v_fma_f32 v252,v154,v158,v252
v_fma_f32 v253,v154,v159,v253
v_mfma_f32_16x16x32_bf16 a[244:247],v[104:107],v[144:147],a[244:247]
v_fma_f32 v252,-v155,v159,v252
v_fma_f32 v253,v155,v158,v253
v_mfma_f32_16x16x32_bf16 a[244:247],v[108:111],v[148:151],a[244:247]
ds_read_b32 v152,v238 offset:512
ds_read_b32 v153,v238 offset:768
v_mfma_f32_16x16x32_bf16 a[248:251],v[112:115],v[144:147],a[248:251]
buffer_load_dwordx4 v237,s[52:55],s82 offen offset:3072 lds
ds_read_b32 v154,v238 offset:576
v_mfma_f32_16x16x32_bf16 a[248:251],v[116:119],v[148:151],a[248:251]
ds_read_b32 v155,v238 offset:832
v_mfma_f32_16x16x32_bf16 a[252:255],v[120:123],v[144:147],a[252:255]
v_mfma_f32_16x16x32_bf16 a[252:255],v[124:127],v[148:151],a[252:255]
s_add_u32 s81,s81,128
s_add_u32 s82,s82,128
v_mfma_f32_16x16x32_bf16 a[0:3],v[0:3],v[128:131],a[0:3]
v_mfma_f32_16x16x32_bf16 a[0:3],v[4:7],v[132:135],a[0:3]
ds_read_b128 v[144:147],v228 offset:4096
v_mfma_f32_16x16x32_bf16 a[4:7],v[8:11],v[128:131],a[4:7]
ds_read_b128 v[148:151],v229 offset:4096
ds_read_b64_tr_b16 v[64:65],v224 offset:32768
v_mfma_f32_16x16x32_bf16 a[4:7],v[12:15],v[132:135],a[4:7]
s_mov_b64 s[48:49],s[24:25]
s_mov_b32 s50,0x280000
v_mfma_f32_16x16x32_bf16 a[8:11],v[16:19],v[128:131],a[8:11]
ds_read_b64_tr_b16 v[66:67],v224 offset:33280
s_mov_b32 s51,0x20000
v_mfma_f32_16x16x32_bf16 a[8:11],v[20:23],v[132:135],a[8:11]
s_mov_b64 s[52:53],s[26:27]
s_mov_b32 s54,0x500000
v_mfma_f32_16x16x32_bf16 a[12:15],v[24:27],v[128:131],a[12:15]
s_mov_b32 s55,0x20000
s_sub_u32 s60,15,s78
v_mfma_f32_16x16x32_bf16 a[12:15],v[28:31],v[132:135],a[12:15]
ds_read_b64_tr_b16 v[68:69],v224 offset:36864
s_lshl_b32 s60,s60,7
v_mfma_f32_16x16x32_bf16 a[16:19],v[32:35],v[128:131],a[16:19]
s_lshl_b32 s61,s56,4
s_add_u32 s60,s60,s61
v_mfma_f32_16x16x32_bf16 a[16:19],v[36:39],v[132:135],a[16:19]
ds_read_b64_tr_b16 v[70:71],v224 offset:37376
s_mul_i32 s66,s60,1280
v_mfma_f32_16x16x32_bf16 a[20:23],v[40:43],v[128:131],a[20:23]
)"
R"(s_lshl_b32 s60,s78,7
s_add_u32 s60,s60,s61
v_mfma_f32_16x16x32_bf16 a[20:23],v[44:47],v[132:135],a[20:23]
s_mul_i32 s67,s60,1280
s_lshl_b32 s60,s77,8
v_mfma_f32_16x16x32_bf16 a[24:27],v[48:51],v[128:131],a[24:27]
ds_read_b64_tr_b16 v[72:73],v225 offset:32768
s_lshr_b32 s61,s56,1
v_mfma_f32_16x16x32_bf16 a[24:27],v[52:55],v[132:135],a[24:27]
s_lshl_b32 s61,s61,7
s_add_u32 s60,s60,s61
v_mfma_f32_16x16x32_bf16 a[28:31],v[56:59],v[128:131],a[28:31]
s_and_b32 s61,s56,1
s_lshl_b32 s61,s61,18
v_mfma_f32_16x16x32_bf16 a[28:31],v[60:63],v[132:135],a[28:31]
ds_read_b64_tr_b16 v[74:75],v225 offset:33280
s_add_u32 s68,s60,s61
v_mfma_f32_16x16x32_bf16 a[32:35],v[0:3],v[136:139],a[32:35]
s_lshl_b32 s60,s56,13
s_add_u32 s70,s57,s60
v_mfma_f32_16x16x32_bf16 a[32:35],v[4:7],v[140:143],a[32:35]
ds_read_b128 v[128:131],v228 offset:6144
ds_read_b64_tr_b16 v[76:77],v225 offset:36864
v_mfma_f32_16x16x32_bf16 a[36:39],v[8:11],v[136:139],a[36:39]
ds_read_b128 v[132:135],v229 offset:6144
s_lshl_b32 s60,s56,12
v_mfma_f32_16x16x32_bf16 a[36:39],v[12:15],v[140:143],a[36:39]
s_add_u32 s71,s57,s60
v_lshrrev_b32 v243,3,v254
v_mfma_f32_16x16x32_bf16 a[40:43],v[16:19],v[136:139],a[40:43]
ds_read_b64_tr_b16 v[78:79],v225 offset:37376
v_lshlrev_b32 v243,8,v243
v_mfma_f32_16x16x32_bf16 a[40:43],v[20:23],v[140:143],a[40:43]
v_add_u32 v244,v243,v234
v_mov_b32 v234,v230
v_mfma_f32_16x16x32_bf16 a[44:47],v[24:27],v[136:139],a[44:47]
v_add_u32 v230,0,v244
v_add_u32 v244,v243,v235
v_mfma_f32_16x16x32_bf16 a[44:47],v[28:31],v[140:143],a[44:47]
ds_read_b64_tr_b16 v[80:81],v226 offset:32768
v_mov_b32 v235,v231
v_mfma_f32_16x16x32_bf16 a[48:51],v[32:35],v[136:139],a[48:51]
v_add_u32 v231,2048,v244
v_add_u32 v244,v243,v236
v_mfma_f32_16x16x32_bf16 a[48:51],v[36:39],v[140:143],a[48:51]
ds_read_b64_tr_b16 v[82:83],v226 offset:33280
v_mov_b32 v236,v232
v_mfma_f32_16x16x32_bf16 a[52:55],v[40:43],v[136:139],a[52:55]
v_add_u32 v232,65536,v244
v_add_u32 v244,v243,v237
v_mfma_f32_16x16x32_bf16 a[52:55],v[44:47],v[140:143],a[52:55]
v_mov_b32 v237,v233
v_add_u32 v233,67584,v244
v_mfma_f32_16x16x32_bf16 a[56:59],v[48:51],v[136:139],a[56:59]
ds_read_b64_tr_b16 v[84:85],v226 offset:36864
s_waitcnt lgkmcnt(15)
v_mul_f32 v156,v153,v250
v_mfma_f32_16x16x32_bf16 a[56:59],v[52:55],v[140:143],a[56:59]
v_mul_f32 v250,v152,v250
v_fma_f32 v250,-v153,v251,v250
v_mfma_f32_16x16x32_bf16 a[60:63],v[56:59],v[136:139],a[60:63]
v_fma_f32 v251,v152,v251,v156
v_mul_f32 v156,v155,v252
v_mfma_f32_16x16x32_bf16 a[60:63],v[60:63],v[140:143],a[60:63]
ds_read_b64_tr_b16 v[86:87],v226 offset:37376
v_mul_f32 v252,v154,v252
s_waitcnt lgkmcnt(15)
v_mfma_f32_16x16x32_bf16 a[64:67],v[0:3],v[144:147],a[64:67]
v_fma_f32 v252,-v155,v253,v252
v_fma_f32 v253,v154,v253,v156
s_waitcnt lgkmcnt(14)
v_mfma_f32_16x16x32_bf16 a[64:67],v[4:7],v[148:151],a[64:67]
ds_read_b128 v[136:139],v228 offset:8192
ds_read_b64_tr_b16 v[88:89],v227 offset:32768
v_mfma_f32_16x16x32_bf16 a[68:71],v[8:11],v[144:147],a[68:71]
ds_read_b128 v[140:143],v229 offset:8192
v_lshrrev_b32 v158,4,v254
v_mfma_f32_16x16x32_bf16 a[68:71],v[12:15],v[148:151],a[68:71]
v_lshlrev_b32 v158,11,v158
v_lshl_add_u32 v242,v241,4,v158
v_mfma_f32_16x16x32_bf16 a[72:75],v[16:19],v[144:147],a[72:75]
ds_read_b64_tr_b16 v[90:91],v227 offset:33280
v_xor_b32 v239,64,v242
v_mfma_f32_16x16x32_bf16 a[72:75],v[20:23],v[148:151],a[72:75]
s_mov_b64 s[36:37],s[22:23]
v_mfma_f32_16x16x32_bf16 a[76:79],v[24:27],v[144:147],a[76:79]
s_add_u32 s36,s36,52428800
s_addc_u32 s37,s37,0
v_mfma_f32_16x16x32_bf16 a[76:79],v[28:31],v[148:151],a[76:79]
ds_read_b64_tr_b16 v[92:93],v227 offset:36864
s_mov_b32 s38,0x500000
v_mfma_f32_16x16x32_bf16 a[80:83],v[32:35],v[144:147],a[80:83]
s_mov_b32 s39,0x20000
v_mfma_f32_16x16x32_bf16 a[80:83],v[36:39],v[148:151],a[80:83]
ds_read_b64_tr_b16 v[94:95],v227 offset:37376
v_mfma_f32_16x16x32_bf16 a[84:87],v[40:43],v[144:147],a[84:87]
s_cmp_eq_u32 s65,0
s_cselect_b32 s38,0,s38
v_mfma_f32_16x16x32_bf16 a[84:87],v[44:47],v[148:151],a[84:87]
s_lshl_b32 s85,s76,5
s_lshl_b32 s86,s75,1
v_mfma_f32_16x16x32_bf16 a[88:91],v[48:51],v[144:147],a[88:91]
ds_read_b64_tr_b16 v[96:97],v224 offset:40960
s_add_u32 s85,s85,s86
v_mfma_f32_16x16x32_bf16 a[88:91],v[52:55],v[148:151],a[88:91]
s_lshr_b32 s86,s56,1
s_add_u32 s85,s85,s86
v_mfma_f32_16x16x32_bf16 a[92:95],v[56:59],v[144:147],a[92:95]
s_lshl_b32 s85,s85,13
s_and_b32 s86,s56,1
v_mfma_f32_16x16x32_bf16 a[92:95],v[60:63],v[148:151],a[92:95]
ds_read_b64_tr_b16 v[98:99],v224 offset:41472
s_lshl_b32 s86,s86,9
s_waitcnt lgkmcnt(15)
v_mfma_f32_16x16x32_bf16 a[96:99],v[0:3],v[128:131],a[96:99]
s_add_u32 s85,s85,s86
s_add_u32 s86,s85,2621440
s_waitcnt lgkmcnt(13)
v_mfma_f32_16x16x32_bf16 a[96:99],v[4:7],v[132:135],a[96:99]
ds_read_b128 v[144:147],v228 offset:10240
ds_read_b64_tr_b16 v[100:101],v224 offset:45056
v_mfma_f32_16x16x32_bf16 a[100:103],v[8:11],v[128:131],a[100:103]
ds_read_b128 v[148:151],v229 offset:10240
v_fma_f32 v160,v246,v250,v160
v_mfma_f32_16x16x32_bf16 a[100:103],v[12:15],v[132:135],a[100:103]
v_fma_f32 v176,v246,v251,v176
v_fma_f32 v160,-v247,v251,v160
v_mfma_f32_16x16x32_bf16 a[104:107],v[16:19],v[128:131],a[104:107]
ds_read_b64_tr_b16 v[102:103],v224 offset:45568
v_fma_f32 v176,v247,v250,v176
v_mfma_f32_16x16x32_bf16 a[104:107],v[20:23],v[132:135],a[104:107]
v_fma_f32 v192,v248,v252,v192
v_fma_f32 v208,v248,v253,v208
v_mfma_f32_16x16x32_bf16 a[108:111],v[24:27],v[128:131],a[108:111]
v_fma_f32 v192,-v249,v253,v192
v_fma_f32 v208,v249,v252,v208
v_mfma_f32_16x16x32_bf16 a[108:111],v[28:31],v[132:135],a[108:111]
ds_read_b64_tr_b16 v[104:105],v225 offset:40960
v_fma_f32 v161,v246,v160,v161
v_mfma_f32_16x16x32_bf16 a[112:115],v[32:35],v[128:131],a[112:115]
v_fma_f32 v177,v246,v176,v177
v_fma_f32 v161,-v247,v176,v161
v_mfma_f32_16x16x32_bf16 a[112:115],v[36:39],v[132:135],a[112:115]
ds_read_b64_tr_b16 v[106:107],v225 offset:41472
v_fma_f32 v177,v247,v160,v177
v_mfma_f32_16x16x32_bf16 a[116:119],v[40:43],v[128:131],a[116:119]
v_fma_f32 v193,v248,v192,v193
v_fma_f32 v209,v248,v208,v209
v_mfma_f32_16x16x32_bf16 a[116:119],v[44:47],v[132:135],a[116:119]
v_fma_f32 v193,-v249,v208,v193
v_fma_f32 v209,v249,v192,v209
v_mfma_f32_16x16x32_bf16 a[120:123],v[48:51],v[128:131],a[120:123]
ds_read_b64_tr_b16 v[108:109],v225 offset:45056
v_fma_f32 v162,v246,v161,v162
v_mfma_f32_16x16x32_bf16 a[120:123],v[52:55],v[132:135],a[120:123]
v_fma_f32 v178,v246,v177,v178
v_fma_f32 v162,-v247,v177,v162
v_mfma_f32_16x16x32_bf16 a[124:127],v[56:59],v[128:131],a[124:127]
v_fma_f32 v178,v247,v161,v178
v_fma_f32 v194,v248,v193,v194
v_mfma_f32_16x16x32_bf16 a[124:127],v[60:63],v[132:135],a[124:127]
ds_read_b64_tr_b16 v[110:111],v225 offset:45568
v_fma_f32 v210,v248,v209,v210
s_waitcnt lgkmcnt(15)
v_mfma_f32_16x16x32_bf16 a[128:131],v[0:3],v[136:139],a[128:131]
v_fma_f32 v194,-v249,v209,v194
v_fma_f32 v210,v249,v193,v210
s_waitcnt lgkmcnt(13)
v_mfma_f32_16x16x32_bf16 a[128:131],v[4:7],v[140:143],a[128:131]
ds_read_b128 v[128:131],v228 offset:12288
ds_read_b64_tr_b16 v[112:113],v226 offset:40960
v_mfma_f32_16x16x32_bf16 a[132:135],v[8:11],v[136:139],a[132:135]
ds_read_b128 v[132:135],v229 offset:12288
v_fma_f32 v163,v246,v162,v163
v_mfma_f32_16x16x32_bf16 a[132:135],v[12:15],v[140:143],a[132:135]
v_fma_f32 v179,v246,v178,v179
v_fma_f32 v163,-v247,v178,v163
v_mfma_f32_16x16x32_bf16 a[136:139],v[16:19],v[136:139],a[136:139]
ds_read_b64_tr_b16 v[114:115],v226 offset:41472
v_fma_f32 v179,v247,v162,v179
v_mfma_f32_16x16x32_bf16 a[136:139],v[20:23],v[140:143],a[136:139]
v_fma_f32 v195,v248,v194,v195
v_fma_f32 v211,v248,v210,v211
v_mfma_f32_16x16x32_bf16 a[140:143],v[24:27],v[136:139],a[140:143]
v_fma_f32 v195,-v249,v210,v195
v_fma_f32 v211,v249,v194,v211
v_mfma_f32_16x16x32_bf16 a[140:143],v[28:31],v[140:143],a[140:143]
ds_read_b64_tr_b16 v[116:117],v226 offset:45056
v_fma_f32 v164,v246,v163,v164
v_mfma_f32_16x16x32_bf16 a[144:147],v[32:35],v[136:139],a[144:147]
v_fma_f32 v180,v246,v179,v180
v_fma_f32 v164,-v247,v179,v164
v_mfma_f32_16x16x32_bf16 a[144:147],v[36:39],v[140:143],a[144:147]
ds_read_b64_tr_b16 v[118:119],v226 offset:45568
v_fma_f32 v180,v247,v163,v180
v_mfma_f32_16x16x32_bf16 a[148:151],v[40:43],v[136:139],a[148:151]
v_fma_f32 v196,v248,v195,v196
v_fma_f32 v212,v248,v211,v212
v_mfma_f32_16x16x32_bf16 a[148:151],v[44:47],v[140:143],a[148:151]
v_fma_f32 v196,-v249,v211,v196
v_fma_f32 v212,v249,v195,v212
v_mfma_f32_16x16x32_bf16 a[152:155],v[48:51],v[136:139],a[152:155]
ds_read_b64_tr_b16 v[120:121],v227 offset:40960
v_fma_f32 v165,v246,v164,v165
v_mfma_f32_16x16x32_bf16 a[152:155],v[52:55],v[140:143],a[152:155]
v_fma_f32 v181,v246,v180,v181
v_fma_f32 v165,-v247,v180,v165
v_mfma_f32_16x16x32_bf16 a[156:159],v[56:59],v[136:139],a[156:159]
v_fma_f32 v181,v247,v164,v181
v_fma_f32 v197,v248,v196,v197
v_mfma_f32_16x16x32_bf16 a[156:159],v[60:63],v[140:143],a[156:159]
ds_read_b64_tr_b16 v[122:123],v227 offset:41472
v_fma_f32 v213,v248,v212,v213
s_waitcnt lgkmcnt(15)
v_mfma_f32_16x16x32_bf16 a[160:163],v[0:3],v[144:147],a[160:163]
ds_read_b128 v[136:139],v228 offset:14336
ds_read_b128 v[140:143],v229 offset:14336
s_waitcnt lgkmcnt(15)
v_mfma_f32_16x16x32_bf16 a[160:163],v[4:7],v[148:151],a[160:163]
ds_read_b64_tr_b16 v[124:125],v227 offset:45056
v_fma_f32 v197,-v249,v212,v197
v_mfma_f32_16x16x32_bf16 a[164:167],v[8:11],v[144:147],a[164:167]
v_fma_f32 v213,v249,v196,v213
v_fma_f32 v166,v246,v165,v166
v_mfma_f32_16x16x32_bf16 a[164:167],v[12:15],v[148:151],a[164:167]
v_fma_f32 v182,v246,v181,v182
v_fma_f32 v166,-v247,v181,v166
v_mfma_f32_16x16x32_bf16 a[168:171],v[16:19],v[144:147],a[168:171]
ds_read_b64_tr_b16 v[126:127],v227 offset:45568
v_fma_f32 v182,v247,v165,v182
v_mfma_f32_16x16x32_bf16 a[168:171],v[20:23],v[148:151],a[168:171]
v_fma_f32 v198,v248,v197,v198
v_fma_f32 v214,v248,v213,v214
v_mfma_f32_16x16x32_bf16 a[172:175],v[24:27],v[144:147],a[172:175]
v_fma_f32 v198,-v249,v213,v198
v_fma_f32 v214,v249,v197,v214
v_mfma_f32_16x16x32_bf16 a[172:175],v[28:31],v[148:151],a[172:175]
v_fma_f32 v167,v246,v166,v167
v_fma_f32 v183,v246,v182,v183
v_mfma_f32_16x16x32_bf16 a[176:179],v[32:35],v[144:147],a[176:179]
v_fma_f32 v167,-v247,v182,v167
v_fma_f32 v183,v247,v166,v183
v_mfma_f32_16x16x32_bf16 a[176:179],v[36:39],v[148:151],a[176:179]
v_fma_f32 v199,v248,v198,v199
v_fma_f32 v215,v248,v214,v215
v_mfma_f32_16x16x32_bf16 a[180:183],v[40:43],v[144:147],a[180:183]
v_fma_f32 v199,-v249,v214,v199
v_fma_f32 v215,v249,v198,v215
v_mfma_f32_16x16x32_bf16 a[180:183],v[44:47],v[148:151],a[180:183]
v_fma_f32 v168,v246,v167,v168
v_fma_f32 v184,v246,v183,v184
v_mfma_f32_16x16x32_bf16 a[184:187],v[48:51],v[144:147],a[184:187]
v_fma_f32 v168,-v247,v183,v168
v_fma_f32 v184,v247,v167,v184
v_mfma_f32_16x16x32_bf16 a[184:187],v[52:55],v[148:151],a[184:187]
v_fma_f32 v200,v248,v199,v200
v_fma_f32 v216,v248,v215,v216
v_mfma_f32_16x16x32_bf16 a[188:191],v[56:59],v[144:147],a[188:191]
v_fma_f32 v200,-v249,v215,v200
v_fma_f32 v216,v249,v199,v216
v_mfma_f32_16x16x32_bf16 a[188:191],v[60:63],v[148:151],a[188:191]
s_waitcnt vmcnt(0) lgkmcnt(0)
s_barrier
v_mfma_f32_16x16x32_bf16 a[192:195],v[0:3],v[128:131],a[192:195]
s_add_u32 m0,s70,32768
s_nop 0
buffer_load_dwordx4 v230,s[48:51],s66 offen offset:0 lds
v_mfma_f32_16x16x32_bf16 a[192:195],v[4:7],v[132:135],a[192:195]
ds_read_b128 v[144:147],v228 offset:32768
v_fma_f32 v169,v246,v168,v169
v_mfma_f32_16x16x32_bf16 a[196:199],v[8:11],v[128:131],a[196:199]
ds_read_b128 v[148:151],v229 offset:32768
v_fma_f32 v185,v246,v184,v185
v_mfma_f32_16x16x32_bf16 a[196:199],v[12:15],v[132:135],a[196:199]
v_fma_f32 v169,-v247,v184,v169
v_fma_f32 v185,v247,v168,v185
v_mfma_f32_16x16x32_bf16 a[200:203],v[16:19],v[128:131],a[200:203]
buffer_load_dwordx4 v231,s[48:51],s66 offen offset:1024 lds
v_fma_f32 v201,v248,v200,v201
v_mfma_f32_16x16x32_bf16 a[200:203],v[20:23],v[132:135],a[200:203]
v_fma_f32 v217,v248,v216,v217
v_fma_f32 v201,-v249,v216,v201
v_mfma_f32_16x16x32_bf16 a[204:207],v[24:27],v[128:131],a[204:207]
v_fma_f32 v217,v249,v200,v217
v_fma_f32 v170,v246,v169,v170
v_mfma_f32_16x16x32_bf16 a[204:207],v[28:31],v[132:135],a[204:207]
v_fma_f32 v186,v246,v185,v186
v_fma_f32 v170,-v247,v185,v170
v_mfma_f32_16x16x32_bf16 a[208:211],v[32:35],v[128:131],a[208:211]
buffer_load_dwordx4 v232,s[48:51],s66 offen offset:2048 lds
v_fma_f32 v186,v247,v169,v186
v_mfma_f32_16x16x32_bf16 a[208:211],v[36:39],v[132:135],a[208:211]
v_fma_f32 v202,v248,v201,v202
v_fma_f32 v218,v248,v217,v218
v_mfma_f32_16x16x32_bf16 a[212:215],v[40:43],v[128:131],a[212:215]
v_fma_f32 v202,-v249,v217,v202
v_fma_f32 v218,v249,v201,v218
v_mfma_f32_16x16x32_bf16 a[212:215],v[44:47],v[132:135],a[212:215]
v_fma_f32 v171,v246,v170,v171
v_fma_f32 v187,v246,v186,v187
v_mfma_f32_16x16x32_bf16 a[216:219],v[48:51],v[128:131],a[216:219]
buffer_load_dwordx4 v233,s[48:51],s66 offen offset:3072 lds
v_fma_f32 v171,-v247,v186,v171
v_mfma_f32_16x16x32_bf16 a[216:219],v[52:55],v[132:135],a[216:219]
v_fma_f32 v187,v247,v170,v187
v_fma_f32 v203,v248,v202,v203
v_mfma_f32_16x16x32_bf16 a[220:223],v[56:59],v[128:131],a[220:223]
v_fma_f32 v219,v248,v218,v219
v_fma_f32 v203,-v249,v218,v203
v_mfma_f32_16x16x32_bf16 a[220:223],v[60:63],v[132:135],a[220:223]
v_fma_f32 v219,v249,v202,v219
v_fma_f32 v172,v246,v171,v172
v_mfma_f32_16x16x32_bf16 a[224:227],v[0:3],v[136:139],a[224:227]
s_add_u32 m0,s70,36864
s_nop 0
buffer_load_dwordx4 v230,s[48:51],s67 offen offset:0 lds
v_mfma_f32_16x16x32_bf16 a[224:227],v[4:7],v[140:143],a[224:227]
ds_read_b128 v[128:131],v228 offset:34816
v_fma_f32 v188,v246,v187,v188
v_mfma_f32_16x16x32_bf16 a[228:231],v[8:11],v[136:139],a[228:231]
ds_read_b128 v[132:135],v229 offset:34816
v_fma_f32 v172,-v247,v187,v172
v_mfma_f32_16x16x32_bf16 a[228:231],v[12:15],v[140:143],a[228:231]
v_fma_f32 v188,v247,v171,v188
v_fma_f32 v204,v248,v203,v204
v_mfma_f32_16x16x32_bf16 a[232:235],v[16:19],v[136:139],a[232:235]
buffer_load_dwordx4 v231,s[48:51],s67 offen offset:1024 lds
v_fma_f32 v220,v248,v219,v220
v_mfma_f32_16x16x32_bf16 a[232:235],v[20:23],v[140:143],a[232:235]
v_fma_f32 v204,-v249,v219,v204
v_fma_f32 v220,v249,v203,v220
v_mfma_f32_16x16x32_bf16 a[236:239],v[24:27],v[136:139],a[236:239]
v_fma_f32 v173,v246,v172,v173
v_fma_f32 v189,v246,v188,v189
v_mfma_f32_16x16x32_bf16 a[236:239],v[28:31],v[140:143],a[236:239]
v_fma_f32 v173,-v247,v188,v173
v_fma_f32 v189,v247,v172,v189
v_mfma_f32_16x16x32_bf16 a[240:243],v[32:35],v[136:139],a[240:243]
buffer_load_dwordx4 v232,s[48:51],s67 offen offset:2048 lds
v_fma_f32 v205,v248,v204,v205
v_mfma_f32_16x16x32_bf16 a[240:243],v[36:39],v[140:143],a[240:243]
v_fma_f32 v221,v248,v220,v221
v_fma_f32 v205,-v249,v220,v205
v_mfma_f32_16x16x32_bf16 a[244:247],v[40:43],v[136:139],a[244:247]
v_fma_f32 v221,v249,v204,v221
v_fma_f32 v174,v246,v173,v174
v_mfma_f32_16x16x32_bf16 a[244:247],v[44:47],v[140:143],a[244:247]
v_fma_f32 v190,v246,v189,v190
v_fma_f32 v174,-v247,v189,v174
v_mfma_f32_16x16x32_bf16 a[248:251],v[48:51],v[136:139],a[248:251]
buffer_load_dwordx4 v233,s[48:51],s67 offen offset:3072 lds
v_fma_f32 v190,v247,v173,v190
v_mfma_f32_16x16x32_bf16 a[248:251],v[52:55],v[140:143],a[248:251]
v_fma_f32 v206,v248,v205,v206
v_fma_f32 v222,v248,v221,v222
v_mfma_f32_16x16x32_bf16 a[252:255],v[56:59],v[136:139],a[252:255]
v_fma_f32 v206,-v249,v221,v206
v_fma_f32 v222,v249,v205,v222
v_mfma_f32_16x16x32_bf16 a[252:255],v[60:63],v[140:143],a[252:255]
v_fma_f32 v175,v246,v174,v175
v_fma_f32 v191,v246,v190,v191
s_waitcnt lgkmcnt(3)
v_mfma_f32_16x16x32_bf16 a[0:3],v[64:67],v[144:147],a[0:3]
s_add_u32 s66,s66,128
s_add_u32 s67,s67,128
s_waitcnt lgkmcnt(2)
v_mfma_f32_16x16x32_bf16 a[0:3],v[68:71],v[148:151],a[0:3]
ds_read_b128 v[136:139],v228 offset:36864
v_fma_f32 v175,-v247,v190,v175
v_mfma_f32_16x16x32_bf16 a[4:7],v[72:75],v[144:147],a[4:7]
ds_read_b128 v[140:143],v229 offset:36864
v_fma_f32 v191,v247,v174,v191
v_mfma_f32_16x16x32_bf16 a[4:7],v[76:79],v[148:151],a[4:7]
s_add_u32 m0,s71,131072
s_nop 0
buffer_load_dwordx4 v234,s[52:55],s68 offen offset:0 lds
v_mfma_f32_16x16x32_bf16 a[8:11],v[80:83],v[144:147],a[8:11]
v_fma_f32 v207,v248,v206,v207
v_fma_f32 v223,v248,v222,v223
v_mfma_f32_16x16x32_bf16 a[8:11],v[84:87],v[148:151],a[8:11]
v_fma_f32 v207,-v249,v222,v207
v_fma_f32 v223,v249,v206,v223
v_mfma_f32_16x16x32_bf16 a[12:15],v[88:91],v[144:147],a[12:15]
v_cvt_pk_bf16_f32 v160,v160,v161
v_cvt_pk_bf16_f32 v161,v162,v163
v_mfma_f32_16x16x32_bf16 a[12:15],v[92:95],v[148:151],a[12:15]
buffer_load_dwordx4 v235,s[52:55],s68 offen offset:1024 lds
v_cvt_pk_bf16_f32 v162,v164,v165
v_mfma_f32_16x16x32_bf16 a[16:19],v[96:99],v[144:147],a[16:19]
v_cvt_pk_bf16_f32 v163,v166,v167
v_cvt_pk_bf16_f32 v164,v168,v169
v_mfma_f32_16x16x32_bf16 a[16:19],v[100:103],v[148:151],a[16:19]
v_cvt_pk_bf16_f32 v165,v170,v171
v_cvt_pk_bf16_f32 v166,v172,v173
v_mfma_f32_16x16x32_bf16 a[20:23],v[104:107],v[144:147],a[20:23]
v_cvt_pk_bf16_f32 v167,v174,v175
buffer_store_dwordx4 v[160:163],v242,s[36:39],s85 offen offset:0 sc1
v_mfma_f32_16x16x32_bf16 a[20:23],v[108:111],v[148:151],a[20:23]
buffer_load_dwordx4 v236,s[52:55],s68 offen offset:2048 lds
v_mfma_f32_16x16x32_bf16 a[24:27],v[112:115],v[144:147],a[24:27]
)"
R"(v_mfma_f32_16x16x32_bf16 a[24:27],v[116:119],v[148:151],a[24:27]
buffer_store_dwordx4 v[164:167],v239,s[36:39],s85 offen offset:1024 sc1
v_mfma_f32_16x16x32_bf16 a[28:31],v[120:123],v[144:147],a[28:31]
v_mfma_f32_16x16x32_bf16 a[28:31],v[124:127],v[148:151],a[28:31]
buffer_load_dwordx4 v237,s[52:55],s68 offen offset:3072 lds
s_waitcnt lgkmcnt(3)
v_mfma_f32_16x16x32_bf16 a[32:35],v[64:67],v[128:131],a[32:35]
v_cvt_pk_bf16_f32 v176,-v176,-v177
v_cvt_pk_bf16_f32 v177,-v178,-v179
s_waitcnt lgkmcnt(2)
v_mfma_f32_16x16x32_bf16 a[32:35],v[68:71],v[132:135],a[32:35]
ds_read_b128 v[144:147],v228 offset:38912
v_cvt_pk_bf16_f32 v178,-v180,-v181
v_mfma_f32_16x16x32_bf16 a[36:39],v[72:75],v[128:131],a[36:39]
ds_read_b128 v[148:151],v229 offset:38912
v_cvt_pk_bf16_f32 v179,-v182,-v183
v_mfma_f32_16x16x32_bf16 a[36:39],v[76:79],v[132:135],a[36:39]
s_add_u32 s68,s68,524288
v_cvt_pk_bf16_f32 v180,-v184,-v185
v_mfma_f32_16x16x32_bf16 a[40:43],v[80:83],v[128:131],a[40:43]
s_add_u32 m0,s70,65536
s_nop 0
buffer_load_dwordx4 v230,s[48:51],s66 offen offset:0 lds
v_mfma_f32_16x16x32_bf16 a[40:43],v[84:87],v[132:135],a[40:43]
v_cvt_pk_bf16_f32 v181,-v186,-v187
v_cvt_pk_bf16_f32 v182,-v188,-v189
v_mfma_f32_16x16x32_bf16 a[44:47],v[88:91],v[128:131],a[44:47]
v_cvt_pk_bf16_f32 v183,-v190,-v191
buffer_store_dwordx4 v[176:179],v242,s[36:39],s86 offen offset:0 sc1
v_mfma_f32_16x16x32_bf16 a[44:47],v[92:95],v[132:135],a[44:47]
v_mfma_f32_16x16x32_bf16 a[48:51],v[96:99],v[128:131],a[48:51]
buffer_load_dwordx4 v231,s[48:51],s66 offen offset:1024 lds
v_mfma_f32_16x16x32_bf16 a[48:51],v[100:103],v[132:135],a[48:51]
buffer_store_dwordx4 v[180:183],v239,s[36:39],s86 offen offset:1024 sc1
v_mfma_f32_16x16x32_bf16 a[52:55],v[104:107],v[128:131],a[52:55]
v_mfma_f32_16x16x32_bf16 a[52:55],v[108:111],v[132:135],a[52:55]
v_mfma_f32_16x16x32_bf16 a[56:59],v[112:115],v[128:131],a[56:59]
buffer_load_dwordx4 v232,s[48:51],s66 offen offset:2048 lds
v_cvt_pk_bf16_f32 v192,v192,v193
v_mfma_f32_16x16x32_bf16 a[56:59],v[116:119],v[132:135],a[56:59]
v_cvt_pk_bf16_f32 v193,v194,v195
v_cvt_pk_bf16_f32 v194,v196,v197
v_mfma_f32_16x16x32_bf16 a[60:63],v[120:123],v[128:131],a[60:63]
v_cvt_pk_bf16_f32 v195,v198,v199
v_cvt_pk_bf16_f32 v196,v200,v201
v_mfma_f32_16x16x32_bf16 a[60:63],v[124:127],v[132:135],a[60:63]
v_cvt_pk_bf16_f32 v197,v202,v203
v_cvt_pk_bf16_f32 v198,v204,v205
s_waitcnt lgkmcnt(3)
v_mfma_f32_16x16x32_bf16 a[64:67],v[64:67],v[136:139],a[64:67]
buffer_load_dwordx4 v233,s[48:51],s66 offen offset:3072 lds
v_cvt_pk_bf16_f32 v199,v206,v207
s_waitcnt lgkmcnt(2)
v_mfma_f32_16x16x32_bf16 a[64:67],v[68:71],v[140:143],a[64:67]
ds_read_b128 v[128:131],v228 offset:40960
buffer_store_dwordx4 v[192:195],v242,s[36:39],s85 offen offset:256 sc1
v_mfma_f32_16x16x32_bf16 a[68:71],v[72:75],v[136:139],a[68:71]
ds_read_b128 v[132:135],v229 offset:40960
v_mfma_f32_16x16x32_bf16 a[68:71],v[76:79],v[140:143],a[68:71]
v_mfma_f32_16x16x32_bf16 a[72:75],v[80:83],v[136:139],a[72:75]
s_add_u32 m0,s70,69632
s_nop 0
buffer_load_dwordx4 v230,s[48:51],s67 offen offset:0 lds
v_mfma_f32_16x16x32_bf16 a[72:75],v[84:87],v[140:143],a[72:75]
buffer_store_dwordx4 v[196:199],v239,s[36:39],s85 offen offset:1280 sc1
v_mfma_f32_16x16x32_bf16 a[76:79],v[88:91],v[136:139],a[76:79]
v_mfma_f32_16x16x32_bf16 a[76:79],v[92:95],v[140:143],a[76:79]
v_mfma_f32_16x16x32_bf16 a[80:83],v[96:99],v[136:139],a[80:83]
buffer_load_dwordx4 v231,s[48:51],s67 offen offset:1024 lds
v_cvt_pk_bf16_f32 v208,-v208,-v209
v_mfma_f32_16x16x32_bf16 a[80:83],v[100:103],v[140:143],a[80:83]
v_cvt_pk_bf16_f32 v209,-v210,-v211
v_cvt_pk_bf16_f32 v210,-v212,-v213
v_mfma_f32_16x16x32_bf16 a[84:87],v[104:107],v[136:139],a[84:87]
v_cvt_pk_bf16_f32 v211,-v214,-v215
v_cvt_pk_bf16_f32 v212,-v216,-v217
v_mfma_f32_16x16x32_bf16 a[84:87],v[108:111],v[140:143],a[84:87]
v_cvt_pk_bf16_f32 v213,-v218,-v219
v_cvt_pk_bf16_f32 v214,-v220,-v221
v_mfma_f32_16x16x32_bf16 a[88:91],v[112:115],v[136:139],a[88:91]
buffer_load_dwordx4 v232,s[48:51],s67 offen offset:2048 lds
v_cvt_pk_bf16_f32 v215,-v222,-v223
v_mfma_f32_16x16x32_bf16 a[88:91],v[116:119],v[140:143],a[88:91]
buffer_store_dwordx4 v[208:211],v242,s[36:39],s86 offen offset:256 sc1
v_mfma_f32_16x16x32_bf16 a[92:95],v[120:123],v[136:139],a[92:95]
v_mfma_f32_16x16x32_bf16 a[92:95],v[124:127],v[140:143],a[92:95]
s_waitcnt lgkmcnt(3)
v_mfma_f32_16x16x32_bf16 a[96:99],v[64:67],v[144:147],a[96:99]
buffer_load_dwordx4 v233,s[48:51],s67 offen offset:3072 lds
buffer_store_dwordx4 v[212:215],v239,s[36:39],s86 offen offset:1280 sc1
s_waitcnt lgkmcnt(2)
v_mfma_f32_16x16x32_bf16 a[96:99],v[68:71],v[148:151],a[96:99]
ds_read_b128 v[136:139],v228 offset:43008
v_mfma_f32_16x16x32_bf16 a[100:103],v[72:75],v[144:147],a[100:103]
ds_read_b128 v[140:143],v229 offset:43008
v_mfma_f32_16x16x32_bf16 a[100:103],v[76:79],v[148:151],a[100:103]
v_mfma_f32_16x16x32_bf16 a[104:107],v[80:83],v[144:147],a[104:107]
s_add_u32 s66,s66,128
s_add_u32 s67,s67,128
v_mfma_f32_16x16x32_bf16 a[104:107],v[84:87],v[148:151],a[104:107]
s_add_u32 m0,s71,147456
s_nop 0
buffer_load_dwordx4 v234,s[52:55],s68 offen offset:0 lds
v_mfma_f32_16x16x32_bf16 a[108:111],v[88:91],v[144:147],a[108:111]
v_mfma_f32_16x16x32_bf16 a[108:111],v[92:95],v[148:151],a[108:111]
v_mfma_f32_16x16x32_bf16 a[112:115],v[96:99],v[144:147],a[112:115]
v_mfma_f32_16x16x32_bf16 a[112:115],v[100:103],v[148:151],a[112:115]
buffer_load_dwordx4 v235,s[52:55],s68 offen offset:1024 lds
v_mfma_f32_16x16x32_bf16 a[116:119],v[104:107],v[144:147],a[116:119]
v_mfma_f32_16x16x32_bf16 a[116:119],v[108:111],v[148:151],a[116:119]
v_mfma_f32_16x16x32_bf16 a[120:123],v[112:115],v[144:147],a[120:123]
v_mfma_f32_16x16x32_bf16 a[120:123],v[116:119],v[148:151],a[120:123]
buffer_load_dwordx4 v236,s[52:55],s68 offen offset:2048 lds
v_mfma_f32_16x16x32_bf16 a[124:127],v[120:123],v[144:147],a[124:127]
v_mfma_f32_16x16x32_bf16 a[124:127],v[124:127],v[148:151],a[124:127]
s_waitcnt lgkmcnt(3)
v_mfma_f32_16x16x32_bf16 a[128:131],v[64:67],v[128:131],a[128:131]
s_waitcnt lgkmcnt(2)
v_mfma_f32_16x16x32_bf16 a[128:131],v[68:71],v[132:135],a[128:131]
ds_read_b128 v[144:147],v228 offset:45056
buffer_load_dwordx4 v237,s[52:55],s68 offen offset:3072 lds
v_mfma_f32_16x16x32_bf16 a[132:135],v[72:75],v[128:131],a[132:135]
ds_read_b128 v[148:151],v229 offset:45056
v_accvgpr_read_b32 v0,a0
v_mfma_f32_16x16x32_bf16 a[132:135],v[76:79],v[132:135],a[132:135]
v_accvgpr_read_b32 v1,a1
v_accvgpr_read_b32 v2,a2
v_mfma_f32_16x16x32_bf16 a[136:139],v[80:83],v[128:131],a[136:139]
v_accvgpr_read_b32 v3,a3
v_accvgpr_read_b32 v4,a4
v_mfma_f32_16x16x32_bf16 a[136:139],v[84:87],v[132:135],a[136:139]
s_add_u32 s68,s68,524288
v_accvgpr_read_b32 v5,a5
v_mfma_f32_16x16x32_bf16 a[140:143],v[88:91],v[128:131],a[140:143]
v_accvgpr_read_b32 v6,a6
v_accvgpr_read_b32 v7,a7
v_mfma_f32_16x16x32_bf16 a[140:143],v[92:95],v[132:135],a[140:143]
v_cvt_pk_bf16_f32 v0,v0,v1
v_cvt_pk_bf16_f32 v1,v2,v3
v_mfma_f32_16x16x32_bf16 a[144:147],v[96:99],v[128:131],a[144:147]
v_cvt_pk_bf16_f32 v2,v4,v5
v_cvt_pk_bf16_f32 v3,v6,v7
v_mfma_f32_16x16x32_bf16 a[144:147],v[100:103],v[132:135],a[144:147]
s_add_u32 s87,s72,0
buffer_store_dwordx4 v[0:3],v255,s[40:43],s87 offen offset:0 sc1
v_mfma_f32_16x16x32_bf16 a[148:151],v[104:107],v[128:131],a[148:151]
v_accvgpr_read_b32 v8,a16
v_accvgpr_read_b32 v9,a17
v_mfma_f32_16x16x32_bf16 a[148:151],v[108:111],v[132:135],a[148:151]
v_accvgpr_read_b32 v10,a18
v_accvgpr_read_b32 v11,a19
v_mfma_f32_16x16x32_bf16 a[152:155],v[112:115],v[128:131],a[152:155]
v_accvgpr_read_b32 v12,a20
v_accvgpr_read_b32 v13,a21
v_mfma_f32_16x16x32_bf16 a[152:155],v[116:119],v[132:135],a[152:155]
v_accvgpr_read_b32 v14,a22
v_accvgpr_read_b32 v15,a23
v_mfma_f32_16x16x32_bf16 a[156:159],v[120:123],v[128:131],a[156:159]
v_cvt_pk_bf16_f32 v8,v8,v9
v_cvt_pk_bf16_f32 v9,v10,v11
v_mfma_f32_16x16x32_bf16 a[156:159],v[124:127],v[132:135],a[156:159]
v_cvt_pk_bf16_f32 v10,v12,v13
v_cvt_pk_bf16_f32 v11,v14,v15
s_waitcnt lgkmcnt(3)
v_mfma_f32_16x16x32_bf16 a[160:163],v[64:67],v[136:139],a[160:163]
ds_read_b128 v[128:131],v228 offset:47104
ds_read_b128 v[132:135],v229 offset:47104
s_waitcnt lgkmcnt(4)
v_mfma_f32_16x16x32_bf16 a[160:163],v[68:71],v[140:143],a[160:163]
s_add_u32 s87,s72,0
buffer_store_dwordx4 v[8:11],v255,s[40:43],s87 offen offset:2048 sc1
v_mfma_f32_16x16x32_bf16 a[164:167],v[72:75],v[136:139],a[164:167]
v_accvgpr_read_b32 v16,a32
v_accvgpr_read_b32 v17,a33
v_mfma_f32_16x16x32_bf16 a[164:167],v[76:79],v[140:143],a[164:167]
v_accvgpr_read_b32 v18,a34
v_accvgpr_read_b32 v19,a35
v_mfma_f32_16x16x32_bf16 a[168:171],v[80:83],v[136:139],a[168:171]
v_accvgpr_read_b32 v20,a36
v_accvgpr_read_b32 v21,a37
v_mfma_f32_16x16x32_bf16 a[168:171],v[84:87],v[140:143],a[168:171]
v_accvgpr_read_b32 v22,a38
v_accvgpr_read_b32 v23,a39
v_mfma_f32_16x16x32_bf16 a[172:175],v[88:91],v[136:139],a[172:175]
v_cvt_pk_bf16_f32 v16,v16,v17
v_cvt_pk_bf16_f32 v17,v18,v19
v_mfma_f32_16x16x32_bf16 a[172:175],v[92:95],v[140:143],a[172:175]
v_cvt_pk_bf16_f32 v18,v20,v21
v_cvt_pk_bf16_f32 v19,v22,v23
v_mfma_f32_16x16x32_bf16 a[176:179],v[96:99],v[136:139],a[176:179]
s_add_u32 s87,s72,4096
buffer_store_dwordx4 v[16:19],v255,s[40:43],s87 offen offset:0 sc1
v_mfma_f32_16x16x32_bf16 a[176:179],v[100:103],v[140:143],a[176:179]
v_accvgpr_read_b32 v24,a48
v_accvgpr_read_b32 v25,a49
v_mfma_f32_16x16x32_bf16 a[180:183],v[104:107],v[136:139],a[180:183]
v_accvgpr_read_b32 v26,a50
v_accvgpr_read_b32 v27,a51
v_mfma_f32_16x16x32_bf16 a[180:183],v[108:111],v[140:143],a[180:183]
v_accvgpr_read_b32 v28,a52
v_accvgpr_read_b32 v29,a53
v_mfma_f32_16x16x32_bf16 a[184:187],v[112:115],v[136:139],a[184:187]
v_accvgpr_read_b32 v30,a54
v_accvgpr_read_b32 v31,a55
v_mfma_f32_16x16x32_bf16 a[184:187],v[116:119],v[140:143],a[184:187]
v_cvt_pk_bf16_f32 v24,v24,v25
v_cvt_pk_bf16_f32 v25,v26,v27
v_mfma_f32_16x16x32_bf16 a[188:191],v[120:123],v[136:139],a[188:191]
v_cvt_pk_bf16_f32 v26,v28,v29
v_cvt_pk_bf16_f32 v27,v30,v31
v_mfma_f32_16x16x32_bf16 a[188:191],v[124:127],v[140:143],a[188:191]
s_add_u32 s87,s72,4096
buffer_store_dwordx4 v[24:27],v255,s[40:43],s87 offen offset:2048 sc1
s_waitcnt lgkmcnt(3)
v_mfma_f32_16x16x32_bf16 a[192:195],v[64:67],v[144:147],a[192:195]
v_accvgpr_read_b32 v0,a64
v_accvgpr_read_b32 v1,a65
s_waitcnt lgkmcnt(2)
v_mfma_f32_16x16x32_bf16 a[192:195],v[68:71],v[148:151],a[192:195]
v_accvgpr_read_b32 v2,a66
v_accvgpr_read_b32 v3,a67
v_mfma_f32_16x16x32_bf16 a[196:199],v[72:75],v[144:147],a[196:199]
v_accvgpr_read_b32 v4,a68
v_accvgpr_read_b32 v5,a69
v_mfma_f32_16x16x32_bf16 a[196:199],v[76:79],v[148:151],a[196:199]
v_accvgpr_read_b32 v6,a70
v_accvgpr_read_b32 v7,a71
v_mfma_f32_16x16x32_bf16 a[200:203],v[80:83],v[144:147],a[200:203]
v_cvt_pk_bf16_f32 v0,v0,v1
v_cvt_pk_bf16_f32 v1,v2,v3
v_mfma_f32_16x16x32_bf16 a[200:203],v[84:87],v[148:151],a[200:203]
v_cvt_pk_bf16_f32 v2,v4,v5
v_cvt_pk_bf16_f32 v3,v6,v7
v_mfma_f32_16x16x32_bf16 a[204:207],v[88:91],v[144:147],a[204:207]
s_add_u32 s87,s72,8192
buffer_store_dwordx4 v[0:3],v255,s[40:43],s87 offen offset:0 sc1
v_mfma_f32_16x16x32_bf16 a[204:207],v[92:95],v[148:151],a[204:207]
v_accvgpr_read_b32 v8,a80
v_accvgpr_read_b32 v9,a81
v_mfma_f32_16x16x32_bf16 a[208:211],v[96:99],v[144:147],a[208:211]
s_and_b32 s60,s56,1
s_lshl_b32 s60,s60,14
v_mfma_f32_16x16x32_bf16 a[208:211],v[100:103],v[148:151],a[208:211]
s_sub_u32 s60,98304,s60
v_add_u32 v224,s60,v224
v_mfma_f32_16x16x32_bf16 a[212:215],v[104:107],v[144:147],a[212:215]
v_add_u32 v225,s60,v225
v_add_u32 v226,s60,v226
v_mfma_f32_16x16x32_bf16 a[212:215],v[108:111],v[148:151],a[212:215]
v_add_u32 v227,s60,v227
s_lshr_b32 s60,s56,1
v_mfma_f32_16x16x32_bf16 a[216:219],v[112:115],v[144:147],a[216:219]
s_lshl_b32 s60,s60,14
s_lshl_b32 s61,s56,13
v_mfma_f32_16x16x32_bf16 a[216:219],v[116:119],v[148:151],a[216:219]
s_sub_u32 s60,s60,s61
v_subrev_u32 v240,s60,v228
v_mfma_f32_16x16x32_bf16 a[220:223],v[120:123],v[144:147],a[220:223]
v_subrev_u32 v238,0x10000,v240
v_subrev_u32 v241,s60,v229
v_mfma_f32_16x16x32_bf16 a[220:223],v[124:127],v[148:151],a[220:223]
v_subrev_u32 v239,0x10000,v241
v_accvgpr_read_b32 v10,a82
s_waitcnt lgkmcnt(1)
v_mfma_f32_16x16x32_bf16 a[224:227],v[64:67],v[128:131],a[224:227]
v_accvgpr_read_b32 v11,a83
v_accvgpr_read_b32 v12,a84
s_waitcnt lgkmcnt(0)
v_mfma_f32_16x16x32_bf16 a[224:227],v[68:71],v[132:135],a[224:227]
v_accvgpr_read_b32 v13,a85
v_accvgpr_read_b32 v14,a86
v_mfma_f32_16x16x32_bf16 a[228:231],v[72:75],v[128:131],a[228:231]
v_accvgpr_read_b32 v15,a87
v_cvt_pk_bf16_f32 v8,v8,v9
v_mfma_f32_16x16x32_bf16 a[228:231],v[76:79],v[132:135],a[228:231]
v_cvt_pk_bf16_f32 v9,v10,v11
v_cvt_pk_bf16_f32 v10,v12,v13
v_mfma_f32_16x16x32_bf16 a[232:235],v[80:83],v[128:131],a[232:235]
v_cvt_pk_bf16_f32 v11,v14,v15
s_add_u32 s87,s72,8192
v_mfma_f32_16x16x32_bf16 a[232:235],v[84:87],v[132:135],a[232:235]
buffer_store_dwordx4 v[8:11],v255,s[40:43],s87 offen offset:2048 sc1
v_mfma_f32_16x16x32_bf16 a[236:239],v[88:91],v[128:131],a[236:239]
v_accvgpr_read_b32 v16,a96
v_accvgpr_read_b32 v17,a97
v_mfma_f32_16x16x32_bf16 a[236:239],v[92:95],v[132:135],a[236:239]
v_accvgpr_read_b32 v18,a98
v_accvgpr_read_b32 v19,a99
v_mfma_f32_16x16x32_bf16 a[240:243],v[96:99],v[128:131],a[240:243]
v_accvgpr_read_b32 v20,a100
v_accvgpr_read_b32 v21,a101
v_mfma_f32_16x16x32_bf16 a[240:243],v[100:103],v[132:135],a[240:243]
v_accvgpr_read_b32 v22,a102
v_accvgpr_read_b32 v23,a103
v_mfma_f32_16x16x32_bf16 a[244:247],v[104:107],v[128:131],a[244:247]
v_cvt_pk_bf16_f32 v16,v16,v17
v_cvt_pk_bf16_f32 v17,v18,v19
v_mfma_f32_16x16x32_bf16 a[244:247],v[108:111],v[132:135],a[244:247]
v_cvt_pk_bf16_f32 v18,v20,v21
v_cvt_pk_bf16_f32 v19,v22,v23
v_mfma_f32_16x16x32_bf16 a[248:251],v[112:115],v[128:131],a[248:251]
s_add_u32 s87,s72,12288
buffer_store_dwordx4 v[16:19],v255,s[40:43],s87 offen offset:0 sc1
v_mfma_f32_16x16x32_bf16 a[248:251],v[116:119],v[132:135],a[248:251]
v_accvgpr_read_b32 v24,a112
v_accvgpr_read_b32 v25,a113
v_mfma_f32_16x16x32_bf16 a[252:255],v[120:123],v[128:131],a[252:255]
v_accvgpr_read_b32 v26,a114
v_accvgpr_read_b32 v27,a115
v_mfma_f32_16x16x32_bf16 a[252:255],v[124:127],v[132:135],a[252:255]
v_accvgpr_read_b32 v28,a116
v_accvgpr_read_b32 v29,a117
v_accvgpr_read_b32 v30,a118
v_accvgpr_read_b32 v31,a119
v_cvt_pk_bf16_f32 v24,v24,v25
v_cvt_pk_bf16_f32 v25,v26,v27
v_cvt_pk_bf16_f32 v26,v28,v29
v_cvt_pk_bf16_f32 v27,v30,v31
s_add_u32 s87,s72,12288
buffer_store_dwordx4 v[24:27],v255,s[40:43],s87 offen offset:2048 sc1
s_waitcnt lgkmcnt(0)
s_barrier
s_branch L_KVE_%=
L_KVV_%=:
s_waitcnt vmcnt(16) lgkmcnt(0)
s_barrier
ds_read_b64_tr_b16 v[0:1],v224 offset:0
ds_read_b64_tr_b16 v[2:3],v224 offset:512
ds_read_b64_tr_b16 v[4:5],v224 offset:4096
ds_read_b64_tr_b16 v[6:7],v224 offset:4608
ds_read_b64_tr_b16 v[8:9],v225 offset:0
ds_read_b64_tr_b16 v[10:11],v225 offset:512
ds_read_b64_tr_b16 v[12:13],v225 offset:4096
ds_read_b64_tr_b16 v[14:15],v225 offset:4608
ds_read_b64_tr_b16 v[16:17],v226 offset:0
ds_read_b64_tr_b16 v[18:19],v226 offset:512
ds_read_b64_tr_b16 v[20:21],v226 offset:4096
ds_read_b64_tr_b16 v[22:23],v226 offset:4608
ds_read_b64_tr_b16 v[24:25],v227 offset:0
ds_read_b64_tr_b16 v[26:27],v227 offset:512
ds_read_b64_tr_b16 v[28:29],v227 offset:4096
ds_read_b64_tr_b16 v[30:31],v227 offset:4608
ds_read_b64_tr_b16 v[32:33],v224 offset:8192
ds_read_b64_tr_b16 v[34:35],v224 offset:8704
ds_read_b64_tr_b16 v[36:37],v224 offset:12288
ds_read_b64_tr_b16 v[38:39],v224 offset:12800
ds_read_b64_tr_b16 v[40:41],v225 offset:8192
ds_read_b64_tr_b16 v[42:43],v225 offset:8704
ds_read_b64_tr_b16 v[44:45],v225 offset:12288
ds_read_b64_tr_b16 v[46:47],v225 offset:12800
ds_read_b64_tr_b16 v[48:49],v226 offset:8192
ds_read_b64_tr_b16 v[50:51],v226 offset:8704
ds_read_b64_tr_b16 v[52:53],v226 offset:12288
ds_read_b64_tr_b16 v[54:55],v226 offset:12800
ds_read_b64_tr_b16 v[56:57],v227 offset:8192
ds_read_b64_tr_b16 v[58:59],v227 offset:8704
ds_read_b64_tr_b16 v[60:61],v227 offset:12288
ds_read_b64_tr_b16 v[62:63],v227 offset:12800
ds_read_b128 v[128:131],v228 offset:0
ds_read_b128 v[132:135],v229 offset:0
ds_read_b128 v[136:139],v228 offset:2048
ds_read_b128 v[140:143],v229 offset:2048
s_waitcnt lgkmcnt(3)
v_mfma_f32_16x16x32_bf16 a[0:3],v[128:131],v[0:3],0
s_waitcnt lgkmcnt(2)
v_mfma_f32_16x16x32_bf16 a[0:3],v[132:135],v[4:7],a[0:3]
ds_read_b128 v[144:147],v228 offset:4096
s_and_b32 s85,s56,1
v_mfma_f32_16x16x32_bf16 a[16:19],v[128:131],v[8:11],0
ds_read_b128 v[148:151],v229 offset:4096
s_mov_b64 s[40:41],s[22:23]
v_mfma_f32_16x16x32_bf16 a[16:19],v[132:135],v[12:15],a[16:19]
s_add_u32 s40,s40,1048576
s_addc_u32 s41,s41,0
v_mfma_f32_16x16x32_bf16 a[32:35],v[128:131],v[16:19],0
s_mov_b32 s42,0x2000000
s_mov_b32 s43,0x20000
v_mfma_f32_16x16x32_bf16 a[32:35],v[132:135],v[20:23],a[32:35]
s_lshl_b32 s86,s85,7
s_add_u32 s86,s86,s57
v_mfma_f32_16x16x32_bf16 a[48:51],v[128:131],v[24:27],0
s_add_u32 s86,s86,155648
v_and_b32 v241,15,v254
v_mfma_f32_16x16x32_bf16 a[48:51],v[132:135],v[28:31],a[48:51]
v_lshl_add_u32 v238,v241,2,s86
s_mul_i32 s85,s85,3840
v_mfma_f32_16x16x32_bf16 a[64:67],v[128:131],v[32:35],0
s_add_u32 s85,s85,s57
s_add_u32 s85,s85,147456
v_mfma_f32_16x16x32_bf16 a[64:67],v[132:135],v[36:39],a[64:67]
s_lshr_b32 s86,s56,1
s_lshl_b32 s86,s86,9
v_mfma_f32_16x16x32_bf16 a[80:83],v[128:131],v[40:43],0
s_add_u32 s86,s86,s85
v_lshl_add_u32 v240,v254,3,s86
v_mfma_f32_16x16x32_bf16 a[80:83],v[132:135],v[44:47],a[80:83]
ds_read_b32 v152,v238 offset:0
ds_read_b32 v153,v238 offset:256
v_mfma_f32_16x16x32_bf16 a[96:99],v[128:131],v[48:51],0
v_mfma_f32_16x16x32_bf16 a[96:99],v[132:135],v[52:55],a[96:99]
)"
R"(v_mfma_f32_16x16x32_bf16 a[112:115],v[128:131],v[56:59],0
v_mfma_f32_16x16x32_bf16 a[112:115],v[132:135],v[60:63],a[112:115]
s_waitcnt lgkmcnt(5)
v_mfma_f32_16x16x32_bf16 a[4:7],v[136:139],v[0:3],0
s_waitcnt lgkmcnt(4)
v_mfma_f32_16x16x32_bf16 a[4:7],v[140:143],v[4:7],a[4:7]
ds_read_b128 v[128:131],v228 offset:6144
s_waitcnt lgkmcnt(2)
v_fma_f32 v246,v152,v160,v161
v_mfma_f32_16x16x32_bf16 a[20:23],v[136:139],v[8:11],0
ds_read_b128 v[132:135],v229 offset:6144
v_fma_f32 v247,v152,v176,v177
v_mfma_f32_16x16x32_bf16 a[20:23],v[140:143],v[12:15],a[20:23]
s_waitcnt lgkmcnt(2)
v_fma_f32 v246,-v153,v176,v246
v_fma_f32 v247,v153,v160,v247
v_mfma_f32_16x16x32_bf16 a[36:39],v[136:139],v[16:19],0
v_fma_f32 v248,v152,v246,v162
v_fma_f32 v249,v152,v247,v178
v_mfma_f32_16x16x32_bf16 a[36:39],v[140:143],v[20:23],a[36:39]
v_fma_f32 v248,-v153,v247,v248
v_fma_f32 v249,v153,v246,v249
v_mfma_f32_16x16x32_bf16 a[52:55],v[136:139],v[24:27],0
v_fma_f32 v246,v152,v248,v163
v_fma_f32 v247,v152,v249,v179
v_mfma_f32_16x16x32_bf16 a[52:55],v[140:143],v[28:31],a[52:55]
v_fma_f32 v246,-v153,v249,v246
v_fma_f32 v247,v153,v248,v247
v_mfma_f32_16x16x32_bf16 a[68:71],v[136:139],v[32:35],0
v_fma_f32 v248,v152,v246,v164
v_fma_f32 v249,v152,v247,v180
v_mfma_f32_16x16x32_bf16 a[68:71],v[140:143],v[36:39],a[68:71]
v_fma_f32 v248,-v153,v247,v248
v_fma_f32 v249,v153,v246,v249
v_mfma_f32_16x16x32_bf16 a[84:87],v[136:139],v[40:43],0
v_fma_f32 v246,v152,v248,v165
v_fma_f32 v247,v152,v249,v181
v_mfma_f32_16x16x32_bf16 a[84:87],v[140:143],v[44:47],a[84:87]
v_fma_f32 v246,-v153,v249,v246
v_fma_f32 v247,v153,v248,v247
v_mfma_f32_16x16x32_bf16 a[100:103],v[136:139],v[48:51],0
v_fma_f32 v248,v152,v246,v166
v_fma_f32 v249,v152,v247,v182
v_mfma_f32_16x16x32_bf16 a[100:103],v[140:143],v[52:55],a[100:103]
v_fma_f32 v248,-v153,v247,v248
v_fma_f32 v249,v153,v246,v249
v_mfma_f32_16x16x32_bf16 a[116:119],v[136:139],v[56:59],0
v_fma_f32 v246,v152,v248,v167
v_fma_f32 v247,v152,v249,v183
v_mfma_f32_16x16x32_bf16 a[116:119],v[140:143],v[60:63],a[116:119]
v_fma_f32 v246,-v153,v249,v246
v_fma_f32 v247,v153,v248,v247
v_mfma_f32_16x16x32_bf16 a[8:11],v[144:147],v[0:3],0
v_fma_f32 v248,v152,v246,v168
v_fma_f32 v249,v152,v247,v184
v_mfma_f32_16x16x32_bf16 a[8:11],v[148:151],v[4:7],a[8:11]
ds_read_b128 v[136:139],v228 offset:8192
v_fma_f32 v248,-v153,v247,v248
v_mfma_f32_16x16x32_bf16 a[24:27],v[144:147],v[8:11],0
ds_read_b128 v[140:143],v229 offset:8192
v_fma_f32 v249,v153,v246,v249
v_mfma_f32_16x16x32_bf16 a[24:27],v[148:151],v[12:15],a[24:27]
s_waitcnt vmcnt(8) lgkmcnt(0)
s_barrier
v_mfma_f32_16x16x32_bf16 a[40:43],v[144:147],v[16:19],0
ds_read_b64_tr_b16 v[64:65],v224 offset:32768
v_fma_f32 v246,v152,v248,v169
v_mfma_f32_16x16x32_bf16 a[40:43],v[148:151],v[20:23],a[40:43]
ds_read_b64_tr_b16 v[66:67],v224 offset:33280
v_fma_f32 v247,v152,v249,v185
v_mfma_f32_16x16x32_bf16 a[56:59],v[144:147],v[24:27],0
s_add_u32 m0,s83,0
s_nop 0
buffer_load_dwordx4 v230,s[48:51],s79 offen offset:0 lds
v_mfma_f32_16x16x32_bf16 a[56:59],v[148:151],v[28:31],a[56:59]
ds_read_b64_tr_b16 v[68:69],v224 offset:36864
v_fma_f32 v246,-v153,v249,v246
v_mfma_f32_16x16x32_bf16 a[72:75],v[144:147],v[32:35],0
ds_read_b64_tr_b16 v[70:71],v224 offset:37376
v_fma_f32 v247,v153,v248,v247
v_mfma_f32_16x16x32_bf16 a[72:75],v[148:151],v[36:39],a[72:75]
v_fma_f32 v248,v152,v246,v170
v_fma_f32 v249,v152,v247,v186
v_mfma_f32_16x16x32_bf16 a[88:91],v[144:147],v[40:43],0
ds_read_b64_tr_b16 v[72:73],v225 offset:32768
buffer_load_dwordx4 v231,s[48:51],s79 offen offset:1024 lds
v_mfma_f32_16x16x32_bf16 a[88:91],v[148:151],v[44:47],a[88:91]
ds_read_b64_tr_b16 v[74:75],v225 offset:33280
v_fma_f32 v248,-v153,v247,v248
v_mfma_f32_16x16x32_bf16 a[104:107],v[144:147],v[48:51],0
v_fma_f32 v249,v153,v246,v249
v_fma_f32 v246,v152,v248,v171
v_mfma_f32_16x16x32_bf16 a[104:107],v[148:151],v[52:55],a[104:107]
ds_read_b64_tr_b16 v[76:77],v225 offset:36864
v_fma_f32 v247,v152,v249,v187
v_mfma_f32_16x16x32_bf16 a[120:123],v[144:147],v[56:59],0
buffer_load_dwordx4 v232,s[48:51],s79 offen offset:2048 lds
v_fma_f32 v246,-v153,v249,v246
v_mfma_f32_16x16x32_bf16 a[120:123],v[148:151],v[60:63],a[120:123]
ds_read_b64_tr_b16 v[78:79],v225 offset:37376
v_fma_f32 v247,v153,v248,v247
v_mfma_f32_16x16x32_bf16 a[12:15],v[128:131],v[0:3],0
ds_read_b64_tr_b16 v[80:81],v226 offset:32768
v_fma_f32 v248,v152,v246,v172
v_mfma_f32_16x16x32_bf16 a[12:15],v[132:135],v[4:7],a[12:15]
ds_read_b128 v[144:147],v228 offset:10240
v_fma_f32 v249,v152,v247,v188
v_mfma_f32_16x16x32_bf16 a[28:31],v[128:131],v[8:11],0
ds_read_b128 v[148:151],v229 offset:10240
ds_read_b64_tr_b16 v[82:83],v226 offset:33280
v_mfma_f32_16x16x32_bf16 a[28:31],v[132:135],v[12:15],a[28:31]
ds_read_b64_tr_b16 v[84:85],v226 offset:36864
buffer_load_dwordx4 v233,s[48:51],s79 offen offset:3072 lds
v_mfma_f32_16x16x32_bf16 a[44:47],v[128:131],v[16:19],0
v_fma_f32 v248,-v153,v247,v248
v_fma_f32 v249,v153,v246,v249
v_mfma_f32_16x16x32_bf16 a[44:47],v[132:135],v[20:23],a[44:47]
ds_read_b64_tr_b16 v[86:87],v226 offset:37376
v_fma_f32 v246,v152,v248,v173
v_mfma_f32_16x16x32_bf16 a[60:63],v[128:131],v[24:27],0
v_fma_f32 v247,v152,v249,v189
v_fma_f32 v246,-v153,v249,v246
v_mfma_f32_16x16x32_bf16 a[60:63],v[132:135],v[28:31],a[60:63]
ds_read_b64_tr_b16 v[88:89],v227 offset:32768
v_fma_f32 v247,v153,v248,v247
v_mfma_f32_16x16x32_bf16 a[76:79],v[128:131],v[32:35],0
ds_read_b64_tr_b16 v[90:91],v227 offset:33280
v_fma_f32 v248,v152,v246,v174
v_mfma_f32_16x16x32_bf16 a[76:79],v[132:135],v[36:39],a[76:79]
s_add_u32 m0,s83,4096
s_nop 0
buffer_load_dwordx4 v230,s[48:51],s80 offen offset:0 lds
v_mfma_f32_16x16x32_bf16 a[92:95],v[128:131],v[40:43],0
ds_read_b64_tr_b16 v[92:93],v227 offset:36864
v_fma_f32 v249,v152,v247,v190
v_mfma_f32_16x16x32_bf16 a[92:95],v[132:135],v[44:47],a[92:95]
ds_read_b64_tr_b16 v[94:95],v227 offset:37376
v_fma_f32 v248,-v153,v247,v248
v_mfma_f32_16x16x32_bf16 a[108:111],v[128:131],v[48:51],0
v_fma_f32 v249,v153,v246,v249
v_fma_f32 v246,v152,v248,v175
v_mfma_f32_16x16x32_bf16 a[108:111],v[132:135],v[52:55],a[108:111]
ds_read_b64_tr_b16 v[96:97],v224 offset:40960
buffer_load_dwordx4 v231,s[48:51],s80 offen offset:1024 lds
v_mfma_f32_16x16x32_bf16 a[124:127],v[128:131],v[56:59],0
v_fma_f32 v247,v152,v249,v191
v_fma_f32 v246,-v153,v249,v246
v_mfma_f32_16x16x32_bf16 a[124:127],v[132:135],v[60:63],a[124:127]
ds_read_b64_tr_b16 v[98:99],v224 offset:41472
v_fma_f32 v247,v153,v248,v247
v_mfma_f32_16x16x32_bf16 a[128:131],v[136:139],v[0:3],0
ds_read_b64_tr_b16 v[100:101],v224 offset:45056
ds_write_b64 v240,v[246:247] offset:896
v_mfma_f32_16x16x32_bf16 a[128:131],v[140:143],v[4:7],a[128:131]
ds_read_b128 v[128:131],v228 offset:12288
buffer_load_dwordx4 v232,s[48:51],s80 offen offset:2048 lds
v_mfma_f32_16x16x32_bf16 a[144:147],v[136:139],v[8:11],0
ds_read_b128 v[132:135],v229 offset:12288
ds_read_b64_tr_b16 v[102:103],v224 offset:45568
v_mfma_f32_16x16x32_bf16 a[144:147],v[140:143],v[12:15],a[144:147]
ds_read_b64_tr_b16 v[104:105],v225 offset:40960
ds_read_b32 v152,v238 offset:64
v_mfma_f32_16x16x32_bf16 a[160:163],v[136:139],v[16:19],0
ds_read_b32 v153,v238 offset:320
v_mfma_f32_16x16x32_bf16 a[160:163],v[140:143],v[20:23],a[160:163]
ds_read_b64_tr_b16 v[106:107],v225 offset:41472
buffer_load_dwordx4 v233,s[48:51],s80 offen offset:3072 lds
v_mfma_f32_16x16x32_bf16 a[176:179],v[136:139],v[24:27],0
v_mfma_f32_16x16x32_bf16 a[176:179],v[140:143],v[28:31],a[176:179]
ds_read_b64_tr_b16 v[108:109],v225 offset:45056
v_mfma_f32_16x16x32_bf16 a[192:195],v[136:139],v[32:35],0
ds_read_b64_tr_b16 v[110:111],v225 offset:45568
v_mfma_f32_16x16x32_bf16 a[192:195],v[140:143],v[36:39],a[192:195]
s_add_u32 s79,s79,524288
s_add_u32 s80,s80,524288
v_mfma_f32_16x16x32_bf16 a[208:211],v[136:139],v[40:43],0
ds_read_b64_tr_b16 v[112:113],v226 offset:40960
s_waitcnt lgkmcnt(5)
v_fma_f32 v246,v152,v192,v193
v_mfma_f32_16x16x32_bf16 a[208:211],v[140:143],v[44:47],a[208:211]
ds_read_b64_tr_b16 v[114:115],v226 offset:41472
v_fma_f32 v247,v152,v208,v209
v_mfma_f32_16x16x32_bf16 a[224:227],v[136:139],v[48:51],0
s_waitcnt lgkmcnt(5)
v_fma_f32 v246,-v153,v208,v246
v_fma_f32 v247,v153,v192,v247
v_mfma_f32_16x16x32_bf16 a[224:227],v[140:143],v[52:55],a[224:227]
ds_read_b64_tr_b16 v[116:117],v226 offset:45056
v_fma_f32 v248,v152,v246,v194
v_mfma_f32_16x16x32_bf16 a[240:243],v[136:139],v[56:59],0
v_fma_f32 v249,v152,v247,v210
v_fma_f32 v248,-v153,v247,v248
v_mfma_f32_16x16x32_bf16 a[240:243],v[140:143],v[60:63],a[240:243]
ds_read_b64_tr_b16 v[118:119],v226 offset:45568
v_fma_f32 v249,v153,v246,v249
v_mfma_f32_16x16x32_bf16 a[132:135],v[144:147],v[0:3],0
ds_read_b128 v[136:139],v228 offset:14336
ds_read_b128 v[140:143],v229 offset:14336
v_mfma_f32_16x16x32_bf16 a[132:135],v[148:151],v[4:7],a[132:135]
ds_read_b64_tr_b16 v[120:121],v227 offset:40960
v_fma_f32 v246,v152,v248,v195
v_mfma_f32_16x16x32_bf16 a[148:151],v[144:147],v[8:11],0
ds_read_b64_tr_b16 v[122:123],v227 offset:41472
v_fma_f32 v247,v152,v249,v211
v_mfma_f32_16x16x32_bf16 a[148:151],v[148:151],v[12:15],a[148:151]
ds_read_b64_tr_b16 v[124:125],v227 offset:45056
v_fma_f32 v246,-v153,v249,v246
v_mfma_f32_16x16x32_bf16 a[164:167],v[144:147],v[16:19],0
v_fma_f32 v247,v153,v248,v247
v_fma_f32 v248,v152,v246,v196
v_mfma_f32_16x16x32_bf16 a[164:167],v[148:151],v[20:23],a[164:167]
ds_read_b64_tr_b16 v[126:127],v227 offset:45568
v_fma_f32 v249,v152,v247,v212
v_mfma_f32_16x16x32_bf16 a[180:183],v[144:147],v[24:27],0
v_fma_f32 v248,-v153,v247,v248
v_fma_f32 v249,v153,v246,v249
v_mfma_f32_16x16x32_bf16 a[180:183],v[148:151],v[28:31],a[180:183]
v_fma_f32 v246,v152,v248,v197
v_fma_f32 v247,v152,v249,v213
v_mfma_f32_16x16x32_bf16 a[196:199],v[144:147],v[32:35],0
v_fma_f32 v246,-v153,v249,v246
v_fma_f32 v247,v153,v248,v247
v_mfma_f32_16x16x32_bf16 a[196:199],v[148:151],v[36:39],a[196:199]
v_fma_f32 v248,v152,v246,v198
v_fma_f32 v249,v152,v247,v214
v_mfma_f32_16x16x32_bf16 a[212:215],v[144:147],v[40:43],0
v_fma_f32 v248,-v153,v247,v248
v_fma_f32 v249,v153,v246,v249
v_mfma_f32_16x16x32_bf16 a[212:215],v[148:151],v[44:47],a[212:215]
v_fma_f32 v246,v152,v248,v199
v_fma_f32 v247,v152,v249,v215
v_mfma_f32_16x16x32_bf16 a[228:231],v[144:147],v[48:51],0
v_fma_f32 v246,-v153,v249,v246
v_fma_f32 v247,v153,v248,v247
v_mfma_f32_16x16x32_bf16 a[228:231],v[148:151],v[52:55],a[228:231]
v_fma_f32 v248,v152,v246,v200
v_fma_f32 v249,v152,v247,v216
v_mfma_f32_16x16x32_bf16 a[244:247],v[144:147],v[56:59],0
v_fma_f32 v248,-v153,v247,v248
v_fma_f32 v249,v153,v246,v249
v_mfma_f32_16x16x32_bf16 a[244:247],v[148:151],v[60:63],a[244:247]
s_waitcnt vmcnt(0) lgkmcnt(0)
s_barrier
v_mfma_f32_16x16x32_bf16 a[136:139],v[128:131],v[0:3],0
s_add_u32 m0,s84,0
s_nop 0
buffer_load_dwordx4 v234,s[52:55],s81 offen offset:0 lds
v_mfma_f32_16x16x32_bf16 a[136:139],v[132:135],v[4:7],a[136:139]
ds_read_b128 v[144:147],v228 offset:32768
v_fma_f32 v246,v152,v248,v201
v_mfma_f32_16x16x32_bf16 a[152:155],v[128:131],v[8:11],0
ds_read_b128 v[148:151],v229 offset:32768
v_fma_f32 v247,v152,v249,v217
v_mfma_f32_16x16x32_bf16 a[152:155],v[132:135],v[12:15],a[152:155]
v_fma_f32 v246,-v153,v249,v246
v_fma_f32 v247,v153,v248,v247
v_mfma_f32_16x16x32_bf16 a[168:171],v[128:131],v[16:19],0
buffer_load_dwordx4 v235,s[52:55],s81 offen offset:1024 lds
v_fma_f32 v248,v152,v246,v202
v_mfma_f32_16x16x32_bf16 a[168:171],v[132:135],v[20:23],a[168:171]
v_fma_f32 v249,v152,v247,v218
v_fma_f32 v248,-v153,v247,v248
v_mfma_f32_16x16x32_bf16 a[184:187],v[128:131],v[24:27],0
v_fma_f32 v249,v153,v246,v249
v_fma_f32 v246,v152,v248,v203
v_mfma_f32_16x16x32_bf16 a[184:187],v[132:135],v[28:31],a[184:187]
v_fma_f32 v247,v152,v249,v219
v_fma_f32 v246,-v153,v249,v246
v_mfma_f32_16x16x32_bf16 a[200:203],v[128:131],v[32:35],0
buffer_load_dwordx4 v236,s[52:55],s81 offen offset:2048 lds
v_fma_f32 v247,v153,v248,v247
v_mfma_f32_16x16x32_bf16 a[200:203],v[132:135],v[36:39],a[200:203]
v_fma_f32 v248,v152,v246,v204
v_fma_f32 v249,v152,v247,v220
v_mfma_f32_16x16x32_bf16 a[216:219],v[128:131],v[40:43],0
v_fma_f32 v248,-v153,v247,v248
v_fma_f32 v249,v153,v246,v249
v_mfma_f32_16x16x32_bf16 a[216:219],v[132:135],v[44:47],a[216:219]
v_fma_f32 v246,v152,v248,v205
v_fma_f32 v247,v152,v249,v221
v_mfma_f32_16x16x32_bf16 a[232:235],v[128:131],v[48:51],0
buffer_load_dwordx4 v237,s[52:55],s81 offen offset:3072 lds
v_fma_f32 v246,-v153,v249,v246
v_mfma_f32_16x16x32_bf16 a[232:235],v[132:135],v[52:55],a[232:235]
v_fma_f32 v247,v153,v248,v247
v_fma_f32 v248,v152,v246,v206
v_mfma_f32_16x16x32_bf16 a[248:251],v[128:131],v[56:59],0
v_fma_f32 v249,v152,v247,v222
v_fma_f32 v248,-v153,v247,v248
v_mfma_f32_16x16x32_bf16 a[248:251],v[132:135],v[60:63],a[248:251]
v_fma_f32 v249,v153,v246,v249
v_fma_f32 v246,v152,v248,v207
v_mfma_f32_16x16x32_bf16 a[140:143],v[136:139],v[0:3],0
s_add_u32 m0,s84,4096
s_nop 0
buffer_load_dwordx4 v234,s[52:55],s82 offen offset:0 lds
v_mfma_f32_16x16x32_bf16 a[140:143],v[140:143],v[4:7],a[140:143]
ds_read_b128 v[128:131],v228 offset:34816
v_fma_f32 v247,v152,v249,v223
v_mfma_f32_16x16x32_bf16 a[156:159],v[136:139],v[8:11],0
ds_read_b128 v[132:135],v229 offset:34816
v_fma_f32 v246,-v153,v249,v246
v_mfma_f32_16x16x32_bf16 a[156:159],v[140:143],v[12:15],a[156:159]
v_fma_f32 v247,v153,v248,v247
ds_write_b64 v240,v[246:247] offset:2816
v_mfma_f32_16x16x32_bf16 a[172:175],v[136:139],v[16:19],0
buffer_load_dwordx4 v235,s[52:55],s82 offen offset:1024 lds
v_mfma_f32_16x16x32_bf16 a[172:175],v[140:143],v[20:23],a[172:175]
v_mfma_f32_16x16x32_bf16 a[188:191],v[136:139],v[24:27],0
v_mfma_f32_16x16x32_bf16 a[188:191],v[140:143],v[28:31],a[188:191]
v_mfma_f32_16x16x32_bf16 a[204:207],v[136:139],v[32:35],0
buffer_load_dwordx4 v236,s[52:55],s82 offen offset:2048 lds
v_mfma_f32_16x16x32_bf16 a[204:207],v[140:143],v[36:39],a[204:207]
v_mfma_f32_16x16x32_bf16 a[220:223],v[136:139],v[40:43],0
v_mfma_f32_16x16x32_bf16 a[220:223],v[140:143],v[44:47],a[220:223]
v_mfma_f32_16x16x32_bf16 a[236:239],v[136:139],v[48:51],0
buffer_load_dwordx4 v237,s[52:55],s82 offen offset:3072 lds
v_mfma_f32_16x16x32_bf16 a[236:239],v[140:143],v[52:55],a[236:239]
v_mfma_f32_16x16x32_bf16 a[252:255],v[136:139],v[56:59],0
v_mfma_f32_16x16x32_bf16 a[252:255],v[140:143],v[60:63],a[252:255]
s_add_u32 s81,s81,128
s_add_u32 s82,s82,128
s_waitcnt lgkmcnt(4)
v_mfma_f32_16x16x32_bf16 a[0:3],v[144:147],v[64:67],a[0:3]
s_waitcnt lgkmcnt(3)
v_mfma_f32_16x16x32_bf16 a[0:3],v[148:151],v[68:71],a[0:3]
ds_read_b128 v[136:139],v228 offset:36864
v_mfma_f32_16x16x32_bf16 a[16:19],v[144:147],v[72:75],a[16:19]
ds_read_b128 v[140:143],v229 offset:36864
ds_read_b64_tr_b16 v[0:1],v224 offset:0
v_mfma_f32_16x16x32_bf16 a[16:19],v[148:151],v[76:79],a[16:19]
s_add_u32 m0,s83,32768
s_nop 0
buffer_load_dwordx4 v230,s[48:51],s79 offen offset:0 lds
v_mfma_f32_16x16x32_bf16 a[32:35],v[144:147],v[80:83],a[32:35]
ds_read_b64_tr_b16 v[2:3],v224 offset:512
v_mfma_f32_16x16x32_bf16 a[32:35],v[148:151],v[84:87],a[32:35]
v_mfma_f32_16x16x32_bf16 a[48:51],v[144:147],v[88:91],a[48:51]
v_mfma_f32_16x16x32_bf16 a[48:51],v[148:151],v[92:95],a[48:51]
ds_read_b64_tr_b16 v[4:5],v224 offset:4096
buffer_load_dwordx4 v231,s[48:51],s79 offen offset:1024 lds
v_mfma_f32_16x16x32_bf16 a[64:67],v[144:147],v[96:99],a[64:67]
v_mfma_f32_16x16x32_bf16 a[64:67],v[148:151],v[100:103],a[64:67]
ds_read_b64_tr_b16 v[6:7],v224 offset:4608
v_mfma_f32_16x16x32_bf16 a[80:83],v[144:147],v[104:107],a[80:83]
v_mfma_f32_16x16x32_bf16 a[80:83],v[148:151],v[108:111],a[80:83]
buffer_load_dwordx4 v232,s[48:51],s79 offen offset:2048 lds
v_mfma_f32_16x16x32_bf16 a[96:99],v[144:147],v[112:115],a[96:99]
ds_read_b64_tr_b16 v[8:9],v225 offset:0
v_mfma_f32_16x16x32_bf16 a[96:99],v[148:151],v[116:119],a[96:99]
v_mfma_f32_16x16x32_bf16 a[112:115],v[144:147],v[120:123],a[112:115]
v_mfma_f32_16x16x32_bf16 a[112:115],v[148:151],v[124:127],a[112:115]
ds_read_b64_tr_b16 v[10:11],v225 offset:512
buffer_load_dwordx4 v233,s[48:51],s79 offen offset:3072 lds
s_waitcnt lgkmcnt(10)
v_mfma_f32_16x16x32_bf16 a[4:7],v[128:131],v[64:67],a[4:7]
s_waitcnt lgkmcnt(9)
v_mfma_f32_16x16x32_bf16 a[4:7],v[132:135],v[68:71],a[4:7]
ds_read_b128 v[144:147],v228 offset:38912
ds_read_b64_tr_b16 v[12:13],v225 offset:4096
v_mfma_f32_16x16x32_bf16 a[20:23],v[128:131],v[72:75],a[20:23]
ds_read_b128 v[148:151],v229 offset:38912
v_mfma_f32_16x16x32_bf16 a[20:23],v[132:135],v[76:79],a[20:23]
s_add_u32 m0,s83,36864
s_nop 0
buffer_load_dwordx4 v230,s[48:51],s80 offen offset:0 lds
v_mfma_f32_16x16x32_bf16 a[36:39],v[128:131],v[80:83],a[36:39]
ds_read_b64_tr_b16 v[14:15],v225 offset:4608
v_mfma_f32_16x16x32_bf16 a[36:39],v[132:135],v[84:87],a[36:39]
v_mfma_f32_16x16x32_bf16 a[52:55],v[128:131],v[88:91],a[52:55]
v_mfma_f32_16x16x32_bf16 a[52:55],v[132:135],v[92:95],a[52:55]
ds_read_b64_tr_b16 v[16:17],v226 offset:0
buffer_load_dwordx4 v231,s[48:51],s80 offen offset:1024 lds
v_mfma_f32_16x16x32_bf16 a[68:71],v[128:131],v[96:99],a[68:71]
v_mfma_f32_16x16x32_bf16 a[68:71],v[132:135],v[100:103],a[68:71]
ds_read_b64_tr_b16 v[18:19],v226 offset:512
v_mfma_f32_16x16x32_bf16 a[84:87],v[128:131],v[104:107],a[84:87]
v_mfma_f32_16x16x32_bf16 a[84:87],v[132:135],v[108:111],a[84:87]
buffer_load_dwordx4 v232,s[48:51],s80 offen offset:2048 lds
v_mfma_f32_16x16x32_bf16 a[100:103],v[128:131],v[112:115],a[100:103]
ds_read_b64_tr_b16 v[20:21],v226 offset:4096
v_mfma_f32_16x16x32_bf16 a[100:103],v[132:135],v[116:119],a[100:103]
v_mfma_f32_16x16x32_bf16 a[116:119],v[128:131],v[120:123],a[116:119]
v_mfma_f32_16x16x32_bf16 a[116:119],v[132:135],v[124:127],a[116:119]
ds_read_b64_tr_b16 v[22:23],v226 offset:4608
buffer_load_dwordx4 v233,s[48:51],s80 offen offset:3072 lds
s_waitcnt lgkmcnt(15)
v_mfma_f32_16x16x32_bf16 a[8:11],v[136:139],v[64:67],a[8:11]
s_waitcnt lgkmcnt(14)
v_mfma_f32_16x16x32_bf16 a[8:11],v[140:143],v[68:71],a[8:11]
ds_read_b128 v[128:131],v228 offset:40960
ds_read_b64_tr_b16 v[24:25],v227 offset:0
v_mfma_f32_16x16x32_bf16 a[24:27],v[136:139],v[72:75],a[24:27]
ds_read_b128 v[132:135],v229 offset:40960
v_mfma_f32_16x16x32_bf16 a[24:27],v[140:143],v[76:79],a[24:27]
s_add_u32 s79,s79,524288
s_add_u32 s80,s80,524288
v_mfma_f32_16x16x32_bf16 a[40:43],v[136:139],v[80:83],a[40:43]
ds_read_b64_tr_b16 v[26:27],v227 offset:512
v_mfma_f32_16x16x32_bf16 a[40:43],v[140:143],v[84:87],a[40:43]
)"
R"(v_mfma_f32_16x16x32_bf16 a[56:59],v[136:139],v[88:91],a[56:59]
v_mfma_f32_16x16x32_bf16 a[56:59],v[140:143],v[92:95],a[56:59]
ds_read_b64_tr_b16 v[28:29],v227 offset:4096
v_mfma_f32_16x16x32_bf16 a[72:75],v[136:139],v[96:99],a[72:75]
v_mfma_f32_16x16x32_bf16 a[72:75],v[140:143],v[100:103],a[72:75]
ds_read_b64_tr_b16 v[30:31],v227 offset:4608
v_mfma_f32_16x16x32_bf16 a[88:91],v[136:139],v[104:107],a[88:91]
v_mfma_f32_16x16x32_bf16 a[88:91],v[140:143],v[108:111],a[88:91]
v_mfma_f32_16x16x32_bf16 a[104:107],v[136:139],v[112:115],a[104:107]
ds_read_b64_tr_b16 v[32:33],v224 offset:8192
v_mfma_f32_16x16x32_bf16 a[104:107],v[140:143],v[116:119],a[104:107]
v_mfma_f32_16x16x32_bf16 a[120:123],v[136:139],v[120:123],a[120:123]
v_mfma_f32_16x16x32_bf16 a[120:123],v[140:143],v[124:127],a[120:123]
ds_read_b64_tr_b16 v[34:35],v224 offset:8704
s_waitcnt lgkmcnt(15)
v_mfma_f32_16x16x32_bf16 a[12:15],v[144:147],v[64:67],a[12:15]
s_waitcnt lgkmcnt(13)
v_mfma_f32_16x16x32_bf16 a[12:15],v[148:151],v[68:71],a[12:15]
ds_read_b128 v[136:139],v228 offset:43008
ds_read_b64_tr_b16 v[36:37],v224 offset:12288
v_mfma_f32_16x16x32_bf16 a[28:31],v[144:147],v[72:75],a[28:31]
ds_read_b128 v[140:143],v229 offset:43008
s_mov_b64 s[44:45],s[26:27]
v_mfma_f32_16x16x32_bf16 a[28:31],v[148:151],v[76:79],a[28:31]
s_mov_b32 s46,0x500000
s_mov_b32 s47,0x20000
v_mfma_f32_16x16x32_bf16 a[44:47],v[144:147],v[80:83],a[44:47]
ds_read_b64_tr_b16 v[38:39],v224 offset:12800
s_mov_b64 s[88:89],s[24:25]
v_mfma_f32_16x16x32_bf16 a[44:47],v[148:151],v[84:87],a[44:47]
s_mov_b32 s90,0x280000
s_mov_b32 s91,0x20000
v_mfma_f32_16x16x32_bf16 a[60:63],v[144:147],v[88:91],a[60:63]
s_cmp_eq_u32 s65,0
s_cselect_b32 s46,0,s46
s_cselect_b32 s90,0,s90
v_mfma_f32_16x16x32_bf16 a[60:63],v[148:151],v[92:95],a[60:63]
ds_read_b64_tr_b16 v[40:41],v225 offset:8192
s_lshl_b32 s60,s73,2
v_mfma_f32_16x16x32_bf16 a[76:79],v[144:147],v[96:99],a[76:79]
s_add_u32 s61,s60,s56
s_lshl_b32 s58,s61,16
v_mfma_f32_16x16x32_bf16 a[76:79],v[148:151],v[100:103],a[76:79]
ds_read_b64_tr_b16 v[42:43],v225 offset:8704
s_lshl_b32 s60,s63,10
v_mfma_f32_16x16x32_bf16 a[92:95],v[144:147],v[104:107],a[92:95]
s_add_u32 s58,s58,s60
v_lshrrev_b32 v243,3,v254
v_mfma_f32_16x16x32_bf16 a[92:95],v[148:151],v[108:111],a[92:95]
v_lshlrev_b32 v243,13,v243
v_and_b32 v242,7,v254
v_mfma_f32_16x16x32_bf16 a[108:111],v[144:147],v[112:115],a[108:111]
ds_read_b64_tr_b16 v[44:45],v225 offset:12288
v_lshl_add_u32 v243,v242,7,v243
v_mfma_f32_16x16x32_bf16 a[108:111],v[148:151],v[116:119],a[108:111]
s_lshl_b32 s61,s61,12
s_mul_i32 s60,s63,327680
v_mfma_f32_16x16x32_bf16 a[124:127],v[144:147],v[120:123],a[124:127]
s_add_u32 s61,s61,s60
v_min_u32 v242,31,v254
v_mfma_f32_16x16x32_bf16 a[124:127],v[148:151],v[124:127],a[124:127]
ds_read_b64_tr_b16 v[46:47],v225 offset:12800
v_lshlrev_b32 v242,7,v242
s_waitcnt lgkmcnt(15)
v_mfma_f32_16x16x32_bf16 a[128:131],v[128:131],v[64:67],a[128:131]
buffer_load_dword v244,v242,s[88:91],s61 offen
buffer_load_dword v244,v243,s[44:47],s58 offen
s_waitcnt lgkmcnt(13)
v_mfma_f32_16x16x32_bf16 a[128:131],v[132:135],v[68:71],a[128:131]
ds_read_b128 v[144:147],v228 offset:45056
ds_read_b64_tr_b16 v[48:49],v226 offset:8192
v_mfma_f32_16x16x32_bf16 a[144:147],v[128:131],v[72:75],a[144:147]
ds_read_b128 v[148:151],v229 offset:45056
v_mfma_f32_16x16x32_bf16 a[144:147],v[132:135],v[76:79],a[144:147]
v_mfma_f32_16x16x32_bf16 a[160:163],v[128:131],v[80:83],a[160:163]
ds_read_b64_tr_b16 v[50:51],v226 offset:8704
v_mfma_f32_16x16x32_bf16 a[160:163],v[132:135],v[84:87],a[160:163]
v_mfma_f32_16x16x32_bf16 a[176:179],v[128:131],v[88:91],a[176:179]
v_mfma_f32_16x16x32_bf16 a[176:179],v[132:135],v[92:95],a[176:179]
ds_read_b64_tr_b16 v[52:53],v226 offset:12288
v_mfma_f32_16x16x32_bf16 a[192:195],v[128:131],v[96:99],a[192:195]
v_mfma_f32_16x16x32_bf16 a[192:195],v[132:135],v[100:103],a[192:195]
ds_read_b64_tr_b16 v[54:55],v226 offset:12800
v_mfma_f32_16x16x32_bf16 a[208:211],v[128:131],v[104:107],a[208:211]
v_mfma_f32_16x16x32_bf16 a[208:211],v[132:135],v[108:111],a[208:211]
v_mfma_f32_16x16x32_bf16 a[224:227],v[128:131],v[112:115],a[224:227]
ds_read_b64_tr_b16 v[56:57],v227 offset:8192
v_mfma_f32_16x16x32_bf16 a[224:227],v[132:135],v[116:119],a[224:227]
v_mfma_f32_16x16x32_bf16 a[240:243],v[128:131],v[120:123],a[240:243]
v_mfma_f32_16x16x32_bf16 a[240:243],v[132:135],v[124:127],a[240:243]
ds_read_b64_tr_b16 v[58:59],v227 offset:8704
s_waitcnt lgkmcnt(15)
v_mfma_f32_16x16x32_bf16 a[132:135],v[136:139],v[64:67],a[132:135]
ds_read_b128 v[128:131],v228 offset:47104
ds_read_b128 v[132:135],v229 offset:47104
s_waitcnt lgkmcnt(15)
v_mfma_f32_16x16x32_bf16 a[132:135],v[140:143],v[68:71],a[132:135]
ds_read_b64_tr_b16 v[60:61],v227 offset:12288
v_mfma_f32_16x16x32_bf16 a[148:151],v[136:139],v[72:75],a[148:151]
v_mfma_f32_16x16x32_bf16 a[148:151],v[140:143],v[76:79],a[148:151]
v_mfma_f32_16x16x32_bf16 a[164:167],v[136:139],v[80:83],a[164:167]
ds_read_b64_tr_b16 v[62:63],v227 offset:12800
v_mfma_f32_16x16x32_bf16 a[164:167],v[140:143],v[84:87],a[164:167]
v_mfma_f32_16x16x32_bf16 a[180:183],v[136:139],v[88:91],a[180:183]
v_mfma_f32_16x16x32_bf16 a[180:183],v[140:143],v[92:95],a[180:183]
v_mfma_f32_16x16x32_bf16 a[196:199],v[136:139],v[96:99],a[196:199]
v_mfma_f32_16x16x32_bf16 a[196:199],v[140:143],v[100:103],a[196:199]
v_mfma_f32_16x16x32_bf16 a[212:215],v[136:139],v[104:107],a[212:215]
v_mfma_f32_16x16x32_bf16 a[212:215],v[140:143],v[108:111],a[212:215]
v_mfma_f32_16x16x32_bf16 a[228:231],v[136:139],v[112:115],a[228:231]
v_mfma_f32_16x16x32_bf16 a[228:231],v[140:143],v[116:119],a[228:231]
v_mfma_f32_16x16x32_bf16 a[244:247],v[136:139],v[120:123],a[244:247]
v_mfma_f32_16x16x32_bf16 a[244:247],v[140:143],v[124:127],a[244:247]
s_waitcnt vmcnt(2) lgkmcnt(0)
s_barrier
v_mfma_f32_16x16x32_bf16 a[136:139],v[144:147],v[64:67],a[136:139]
s_add_u32 m0,s84,32768
s_nop 0
buffer_load_dwordx4 v234,s[52:55],s81 offen offset:0 lds
v_mfma_f32_16x16x32_bf16 a[136:139],v[148:151],v[68:71],a[136:139]
ds_read_b128 v[136:139],v228 offset:0
s_lshr_b32 s86,s56,1
v_mfma_f32_16x16x32_bf16 a[152:155],v[144:147],v[72:75],a[152:155]
ds_read_b128 v[140:143],v229 offset:0
s_lshl_b32 s86,s86,6
v_mfma_f32_16x16x32_bf16 a[152:155],v[148:151],v[76:79],a[152:155]
v_add_u32 v239,s86,v238
ds_read_b32 v152,v239 offset:1024
v_mfma_f32_16x16x32_bf16 a[168:171],v[144:147],v[80:83],a[168:171]
buffer_load_dwordx4 v235,s[52:55],s81 offen offset:1024 lds
ds_read_b32 v153,v239 offset:1280
v_mfma_f32_16x16x32_bf16 a[168:171],v[148:151],v[84:87],a[168:171]
s_mul_i32 s86,s86,30
s_add_u32 s86,s86,s85
v_mfma_f32_16x16x32_bf16 a[184:187],v[144:147],v[88:91],a[184:187]
v_lshl_add_u32 v239,v241,3,s86
ds_read_b64 v[154:155],v239 offset:896
v_mfma_f32_16x16x32_bf16 a[184:187],v[148:151],v[92:95],a[184:187]
ds_read_b64 v[156:157],v239 offset:1024
ds_read_b64 v[158:159],v239 offset:1152
v_mfma_f32_16x16x32_bf16 a[200:203],v[144:147],v[96:99],a[200:203]
buffer_load_dwordx4 v236,s[52:55],s81 offen offset:2048 lds
ds_read_b64 v[246:247],v239 offset:1280
v_mfma_f32_16x16x32_bf16 a[200:203],v[148:151],v[100:103],a[200:203]
ds_read_b64 v[248:249],v239 offset:1408
ds_read_b64 v[250:251],v239 offset:1536
v_mfma_f32_16x16x32_bf16 a[216:219],v[144:147],v[104:107],a[216:219]
s_mov_b64 s[36:37],s[22:23]
v_mfma_f32_16x16x32_bf16 a[216:219],v[148:151],v[108:111],a[216:219]
s_add_u32 s36,s36,65536
s_addc_u32 s37,s37,0
v_mfma_f32_16x16x32_bf16 a[232:235],v[144:147],v[112:115],a[232:235]
buffer_load_dwordx4 v237,s[52:55],s81 offen offset:3072 lds
s_mov_b32 s38,0x14000
v_mfma_f32_16x16x32_bf16 a[232:235],v[148:151],v[116:119],a[232:235]
s_mov_b32 s39,0x20000
v_mfma_f32_16x16x32_bf16 a[248:251],v[144:147],v[120:123],a[248:251]
s_cmp_eq_u32 s65,0
s_cselect_b32 s38,0,s38
v_mfma_f32_16x16x32_bf16 a[248:251],v[148:151],v[124:127],a[248:251]
s_mul_i32 s85,s75,5120
s_lshl_b32 s86,s76,9
v_mfma_f32_16x16x32_bf16 a[140:143],v[128:131],v[64:67],a[140:143]
s_add_u32 m0,s84,36864
s_nop 0
buffer_load_dwordx4 v234,s[52:55],s82 offen offset:0 lds
v_mfma_f32_16x16x32_bf16 a[140:143],v[132:135],v[68:71],a[140:143]
ds_read_b128 v[144:147],v228 offset:2048
s_add_u32 s85,s85,s86
v_mfma_f32_16x16x32_bf16 a[156:159],v[128:131],v[72:75],a[156:159]
ds_read_b128 v[148:151],v229 offset:2048
s_and_b32 s86,s56,1
v_mfma_f32_16x16x32_bf16 a[156:159],v[132:135],v[76:79],a[156:159]
s_lshl_b32 s86,s86,8
s_add_u32 s85,s85,s86
v_mfma_f32_16x16x32_bf16 a[172:175],v[128:131],v[80:83],a[172:175]
buffer_load_dwordx4 v235,s[52:55],s82 offen offset:1024 lds
s_lshr_b32 s86,s56,1
v_mfma_f32_16x16x32_bf16 a[172:175],v[132:135],v[84:87],a[172:175]
s_lshl_b32 s86,s86,7
s_add_u32 s85,s85,s86
v_mfma_f32_16x16x32_bf16 a[188:191],v[128:131],v[88:91],a[188:191]
v_lshlrev_b32 v252,3,v241
s_waitcnt lgkmcnt(6)
v_fma_f32 v156,v152,v154,v156
v_mfma_f32_16x16x32_bf16 a[188:191],v[132:135],v[92:95],a[188:191]
v_fma_f32 v157,v152,v155,v157
v_fma_f32 v156,-v153,v155,v156
v_mfma_f32_16x16x32_bf16 a[204:207],v[128:131],v[96:99],a[204:207]
buffer_load_dwordx4 v236,s[52:55],s82 offen offset:2048 lds
v_fma_f32 v157,v153,v154,v157
v_mfma_f32_16x16x32_bf16 a[204:207],v[132:135],v[100:103],a[204:207]
ds_read_b64 v[154:155],v239 offset:1664
s_waitcnt lgkmcnt(6)
v_fma_f32 v158,v152,v156,v158
v_mfma_f32_16x16x32_bf16 a[220:223],v[128:131],v[104:107],a[220:223]
v_fma_f32 v159,v152,v157,v159
v_fma_f32 v158,-v153,v157,v158
v_mfma_f32_16x16x32_bf16 a[220:223],v[132:135],v[108:111],a[220:223]
v_fma_f32 v159,v153,v156,v159
ds_read_b64 v[156:157],v239 offset:1792
v_mfma_f32_16x16x32_bf16 a[236:239],v[128:131],v[112:115],a[236:239]
buffer_load_dwordx4 v237,s[52:55],s82 offen offset:3072 lds
s_waitcnt lgkmcnt(6)
v_fma_f32 v246,v152,v158,v246
v_mfma_f32_16x16x32_bf16 a[236:239],v[132:135],v[116:119],a[236:239]
v_fma_f32 v247,v152,v159,v247
v_fma_f32 v246,-v153,v159,v246
v_mfma_f32_16x16x32_bf16 a[252:255],v[128:131],v[120:123],a[252:255]
v_fma_f32 v247,v153,v158,v247
s_waitcnt lgkmcnt(5)
v_fma_f32 v248,v152,v246,v248
v_mfma_f32_16x16x32_bf16 a[252:255],v[132:135],v[124:127],a[252:255]
s_add_u32 s81,s81,128
s_add_u32 s82,s82,128
v_mfma_f32_16x16x32_bf16 a[0:3],v[136:139],v[0:3],a[0:3]
v_fma_f32 v249,v152,v247,v249
v_fma_f32 v248,-v153,v247,v248
v_mfma_f32_16x16x32_bf16 a[0:3],v[140:143],v[4:7],a[0:3]
ds_read_b128 v[128:131],v228 offset:4096
v_fma_f32 v249,v153,v246,v249
v_mfma_f32_16x16x32_bf16 a[16:19],v[136:139],v[8:11],a[16:19]
ds_read_b128 v[132:135],v229 offset:4096
ds_read_b64_tr_b16 v[64:65],v224 offset:32768
v_mfma_f32_16x16x32_bf16 a[16:19],v[140:143],v[12:15],a[16:19]
s_add_u32 m0,s83,0
s_nop 0
buffer_load_dwordx4 v230,s[48:51],s79 offen offset:0 lds
v_mfma_f32_16x16x32_bf16 a[32:35],v[136:139],v[16:19],a[32:35]
ds_read_b64_tr_b16 v[66:67],v224 offset:33280
s_waitcnt lgkmcnt(8)
v_fma_f32 v250,v152,v248,v250
v_mfma_f32_16x16x32_bf16 a[32:35],v[140:143],v[20:23],a[32:35]
v_fma_f32 v251,v152,v249,v251
v_fma_f32 v250,-v153,v249,v250
v_mfma_f32_16x16x32_bf16 a[48:51],v[136:139],v[24:27],a[48:51]
v_fma_f32 v251,v153,v248,v251
s_waitcnt lgkmcnt(5)
v_fma_f32 v154,v152,v250,v154
v_mfma_f32_16x16x32_bf16 a[48:51],v[140:143],v[28:31],a[48:51]
ds_read_b64_tr_b16 v[68:69],v224 offset:36864
buffer_load_dwordx4 v231,s[48:51],s79 offen offset:1024 lds
v_mfma_f32_16x16x32_bf16 a[64:67],v[136:139],v[32:35],a[64:67]
v_fma_f32 v155,v152,v251,v155
v_fma_f32 v154,-v153,v251,v154
v_mfma_f32_16x16x32_bf16 a[64:67],v[140:143],v[36:39],a[64:67]
ds_read_b64_tr_b16 v[70:71],v224 offset:37376
v_fma_f32 v155,v153,v250,v155
v_mfma_f32_16x16x32_bf16 a[80:83],v[136:139],v[40:43],a[80:83]
s_waitcnt lgkmcnt(6)
v_fma_f32 v156,v152,v154,v156
v_fma_f32 v157,v152,v155,v157
v_mfma_f32_16x16x32_bf16 a[80:83],v[140:143],v[44:47],a[80:83]
buffer_load_dwordx4 v232,s[48:51],s79 offen offset:2048 lds
v_fma_f32 v156,-v153,v155,v156
v_mfma_f32_16x16x32_bf16 a[96:99],v[136:139],v[48:51],a[96:99]
ds_read_b64_tr_b16 v[72:73],v225 offset:32768
v_fma_f32 v157,v153,v154,v157
v_mfma_f32_16x16x32_bf16 a[96:99],v[140:143],v[52:55],a[96:99]
buffer_store_dwordx2 v[156:157],v252,s[36:39],s85 offen sc1
ds_read_b32 v152,v238 offset:1024
v_mfma_f32_16x16x32_bf16 a[112:115],v[136:139],v[56:59],a[112:115]
ds_read_b32 v153,v238 offset:1280
ds_read_b64 v[154:155],v240 offset:0
v_mfma_f32_16x16x32_bf16 a[112:115],v[140:143],v[60:63],a[112:115]
ds_read_b64_tr_b16 v[74:75],v225 offset:33280
buffer_load_dwordx4 v233,s[48:51],s79 offen offset:3072 lds
v_mfma_f32_16x16x32_bf16 a[4:7],v[144:147],v[0:3],a[4:7]
ds_read_b64 v[156:157],v240 offset:128
ds_read_b64 v[158:159],v240 offset:256
v_mfma_f32_16x16x32_bf16 a[4:7],v[148:151],v[4:7],a[4:7]
ds_read_b128 v[136:139],v228 offset:6144
ds_read_b64_tr_b16 v[76:77],v225 offset:36864
v_mfma_f32_16x16x32_bf16 a[20:23],v[144:147],v[8:11],a[20:23]
ds_read_b128 v[140:143],v229 offset:6144
ds_read_b64 v[246:247],v240 offset:384
v_mfma_f32_16x16x32_bf16 a[20:23],v[148:151],v[12:15],a[20:23]
s_add_u32 m0,s83,4096
s_nop 0
buffer_load_dwordx4 v230,s[48:51],s80 offen offset:0 lds
v_mfma_f32_16x16x32_bf16 a[36:39],v[144:147],v[16:19],a[36:39]
ds_read_b64_tr_b16 v[78:79],v225 offset:37376
v_mfma_f32_16x16x32_bf16 a[36:39],v[148:151],v[20:23],a[36:39]
v_mfma_f32_16x16x32_bf16 a[52:55],v[144:147],v[24:27],a[52:55]
v_mfma_f32_16x16x32_bf16 a[52:55],v[148:151],v[28:31],a[52:55]
ds_read_b64_tr_b16 v[80:81],v226 offset:32768
buffer_load_dwordx4 v231,s[48:51],s80 offen offset:1024 lds
v_mfma_f32_16x16x32_bf16 a[68:71],v[144:147],v[32:35],a[68:71]
s_waitcnt lgkmcnt(7)
v_fma_f32 v156,v152,v154,v156
v_fma_f32 v157,v152,v155,v157
v_mfma_f32_16x16x32_bf16 a[68:71],v[148:151],v[36:39],a[68:71]
ds_read_b64_tr_b16 v[82:83],v226 offset:33280
v_fma_f32 v156,-v153,v155,v156
v_mfma_f32_16x16x32_bf16 a[84:87],v[144:147],v[40:43],a[84:87]
v_fma_f32 v157,v153,v154,v157
ds_read_b64 v[154:155],v240 offset:512
v_mfma_f32_16x16x32_bf16 a[84:87],v[148:151],v[44:47],a[84:87]
buffer_load_dwordx4 v232,s[48:51],s80 offen offset:2048 lds
s_waitcnt lgkmcnt(8)
v_fma_f32 v158,v152,v156,v158
v_mfma_f32_16x16x32_bf16 a[100:103],v[144:147],v[48:51],a[100:103]
ds_read_b64_tr_b16 v[84:85],v226 offset:36864
v_fma_f32 v159,v152,v157,v159
v_mfma_f32_16x16x32_bf16 a[100:103],v[148:151],v[52:55],a[100:103]
v_fma_f32 v158,-v153,v157,v158
v_fma_f32 v159,v153,v156,v159
v_mfma_f32_16x16x32_bf16 a[116:119],v[144:147],v[56:59],a[116:119]
ds_read_b64 v[156:157],v240 offset:640
s_waitcnt lgkmcnt(6)
v_fma_f32 v246,v152,v158,v246
v_mfma_f32_16x16x32_bf16 a[116:119],v[148:151],v[60:63],a[116:119]
ds_read_b64_tr_b16 v[86:87],v226 offset:37376
buffer_load_dwordx4 v233,s[48:51],s80 offen offset:3072 lds
v_mfma_f32_16x16x32_bf16 a[8:11],v[128:131],v[0:3],a[8:11]
v_fma_f32 v247,v152,v159,v247
v_fma_f32 v246,-v153,v159,v246
v_mfma_f32_16x16x32_bf16 a[8:11],v[132:135],v[4:7],a[8:11]
ds_read_b128 v[144:147],v228 offset:8192
ds_read_b64_tr_b16 v[88:89],v227 offset:32768
v_mfma_f32_16x16x32_bf16 a[24:27],v[128:131],v[8:11],a[24:27]
ds_read_b128 v[148:151],v229 offset:8192
v_fma_f32 v247,v153,v158,v247
v_mfma_f32_16x16x32_bf16 a[24:27],v[132:135],v[12:15],a[24:27]
s_add_u32 s79,s79,524288
s_add_u32 s80,s80,524288
v_mfma_f32_16x16x32_bf16 a[40:43],v[128:131],v[16:19],a[40:43]
ds_read_b64_tr_b16 v[90:91],v227 offset:33280
ds_read_b64 v[158:159],v240 offset:768
v_mfma_f32_16x16x32_bf16 a[40:43],v[132:135],v[20:23],a[40:43]
s_waitcnt lgkmcnt(8)
v_fma_f32 v154,v152,v246,v154
v_fma_f32 v155,v152,v247,v155
v_mfma_f32_16x16x32_bf16 a[56:59],v[128:131],v[24:27],a[56:59]
v_fma_f32 v154,-v153,v247,v154
v_fma_f32 v155,v153,v246,v155
v_mfma_f32_16x16x32_bf16 a[56:59],v[132:135],v[28:31],a[56:59]
ds_read_b64_tr_b16 v[92:93],v227 offset:36864
s_waitcnt lgkmcnt(7)
v_fma_f32 v156,v152,v154,v156
v_mfma_f32_16x16x32_bf16 a[72:75],v[128:131],v[32:35],a[72:75]
v_fma_f32 v157,v152,v155,v157
v_fma_f32 v156,-v153,v155,v156
v_mfma_f32_16x16x32_bf16 a[72:75],v[132:135],v[36:39],a[72:75]
ds_read_b64_tr_b16 v[94:95],v227 offset:37376
v_fma_f32 v157,v153,v154,v157
v_mfma_f32_16x16x32_bf16 a[88:91],v[128:131],v[40:43],a[88:91]
s_waitcnt lgkmcnt(2)
v_fma_f32 v158,v152,v156,v158
v_fma_f32 v159,v152,v157,v159
v_mfma_f32_16x16x32_bf16 a[88:91],v[132:135],v[44:47],a[88:91]
v_fma_f32 v158,-v153,v157,v158
v_fma_f32 v159,v153,v156,v159
v_mfma_f32_16x16x32_bf16 a[104:107],v[128:131],v[48:51],a[104:107]
ds_read_b64_tr_b16 v[96:97],v224 offset:40960
v_mov_b32 v250,v158
v_mfma_f32_16x16x32_bf16 a[104:107],v[132:135],v[52:55],a[104:107]
v_mov_b32 v251,v159
ds_read_b32 v152,v238 offset:1088
v_mfma_f32_16x16x32_bf16 a[120:123],v[128:131],v[56:59],a[120:123]
ds_read_b32 v153,v238 offset:1344
ds_read_b64 v[154:155],v240 offset:1920
v_mfma_f32_16x16x32_bf16 a[120:123],v[132:135],v[60:63],a[120:123]
ds_read_b64_tr_b16 v[98:99],v224 offset:41472
ds_read_b64 v[156:157],v240 offset:2048
v_mfma_f32_16x16x32_bf16 a[12:15],v[136:139],v[0:3],a[12:15]
ds_read_b64 v[158:159],v240 offset:2176
ds_read_b64 v[246:247],v240 offset:2304
v_mfma_f32_16x16x32_bf16 a[12:15],v[140:143],v[4:7],a[12:15]
ds_read_b128 v[128:131],v228 offset:10240
ds_read_b64_tr_b16 v[100:101],v224 offset:45056
v_mfma_f32_16x16x32_bf16 a[28:31],v[136:139],v[8:11],a[28:31]
ds_read_b128 v[132:135],v229 offset:10240
v_mfma_f32_16x16x32_bf16 a[28:31],v[140:143],v[12:15],a[28:31]
v_mfma_f32_16x16x32_bf16 a[44:47],v[136:139],v[16:19],a[44:47]
ds_read_b64_tr_b16 v[102:103],v224 offset:45568
v_mfma_f32_16x16x32_bf16 a[44:47],v[140:143],v[20:23],a[44:47]
v_mfma_f32_16x16x32_bf16 a[60:63],v[136:139],v[24:27],a[60:63]
s_waitcnt lgkmcnt(6)
v_fma_f32 v156,v152,v154,v156
v_fma_f32 v157,v152,v155,v157
v_mfma_f32_16x16x32_bf16 a[60:63],v[140:143],v[28:31],a[60:63]
ds_read_b64_tr_b16 v[104:105],v225 offset:40960
v_fma_f32 v156,-v153,v155,v156
v_mfma_f32_16x16x32_bf16 a[76:79],v[136:139],v[32:35],a[76:79]
v_fma_f32 v157,v153,v154,v157
ds_read_b64 v[154:155],v240 offset:2432
v_mfma_f32_16x16x32_bf16 a[76:79],v[140:143],v[36:39],a[76:79]
ds_read_b64_tr_b16 v[106:107],v225 offset:41472
s_waitcnt lgkmcnt(8)
v_fma_f32 v158,v152,v156,v158
v_mfma_f32_16x16x32_bf16 a[92:95],v[136:139],v[40:43],a[92:95]
v_fma_f32 v159,v152,v157,v159
v_fma_f32 v158,-v153,v157,v158
v_mfma_f32_16x16x32_bf16 a[92:95],v[140:143],v[44:47],a[92:95]
v_fma_f32 v159,v153,v156,v159
ds_read_b64 v[156:157],v240 offset:2560
v_mfma_f32_16x16x32_bf16 a[108:111],v[136:139],v[48:51],a[108:111]
ds_read_b64_tr_b16 v[108:109],v225 offset:45056
s_waitcnt lgkmcnt(9)
)"
R"(v_fma_f32 v246,v152,v158,v246
v_mfma_f32_16x16x32_bf16 a[108:111],v[140:143],v[52:55],a[108:111]
v_fma_f32 v247,v152,v159,v247
v_fma_f32 v246,-v153,v159,v246
v_mfma_f32_16x16x32_bf16 a[124:127],v[136:139],v[56:59],a[124:127]
v_fma_f32 v247,v153,v158,v247
ds_read_b64 v[158:159],v240 offset:2688
v_mfma_f32_16x16x32_bf16 a[124:127],v[140:143],v[60:63],a[124:127]
ds_read_b64_tr_b16 v[110:111],v225 offset:45568
s_waitcnt lgkmcnt(5)
v_fma_f32 v154,v152,v246,v154
v_mfma_f32_16x16x32_bf16 a[128:131],v[144:147],v[0:3],a[128:131]
v_fma_f32 v155,v152,v247,v155
v_fma_f32 v154,-v153,v247,v154
v_mfma_f32_16x16x32_bf16 a[128:131],v[148:151],v[4:7],a[128:131]
ds_read_b128 v[136:139],v228 offset:12288
ds_read_b64_tr_b16 v[112:113],v226 offset:40960
v_mfma_f32_16x16x32_bf16 a[144:147],v[144:147],v[8:11],a[144:147]
ds_read_b128 v[140:143],v229 offset:12288
v_fma_f32 v155,v153,v246,v155
v_mfma_f32_16x16x32_bf16 a[144:147],v[148:151],v[12:15],a[144:147]
s_waitcnt lgkmcnt(6)
v_fma_f32 v156,v152,v154,v156
v_fma_f32 v157,v152,v155,v157
v_mfma_f32_16x16x32_bf16 a[160:163],v[144:147],v[16:19],a[160:163]
ds_read_b64_tr_b16 v[114:115],v226 offset:41472
v_fma_f32 v156,-v153,v155,v156
v_mfma_f32_16x16x32_bf16 a[160:163],v[148:151],v[20:23],a[160:163]
v_fma_f32 v157,v153,v154,v157
s_waitcnt lgkmcnt(5)
v_fma_f32 v158,v152,v156,v158
v_mfma_f32_16x16x32_bf16 a[176:179],v[144:147],v[24:27],a[176:179]
v_fma_f32 v159,v152,v157,v159
v_fma_f32 v158,-v153,v157,v158
v_mfma_f32_16x16x32_bf16 a[176:179],v[148:151],v[28:31],a[176:179]
ds_read_b64_tr_b16 v[116:117],v226 offset:45056
v_fma_f32 v159,v153,v156,v159
v_mfma_f32_16x16x32_bf16 a[192:195],v[144:147],v[32:35],a[192:195]
v_mov_b32 v252,v158
v_mov_b32 v253,v159
v_mfma_f32_16x16x32_bf16 a[192:195],v[148:151],v[36:39],a[192:195]
ds_read_b64_tr_b16 v[118:119],v226 offset:45568
ds_read_b32 v152,v238 offset:512
v_mfma_f32_16x16x32_bf16 a[208:211],v[144:147],v[40:43],a[208:211]
ds_read_b32 v153,v238 offset:768
v_mfma_f32_16x16x32_bf16 a[208:211],v[148:151],v[44:47],a[208:211]
v_mfma_f32_16x16x32_bf16 a[224:227],v[144:147],v[48:51],a[224:227]
ds_read_b64_tr_b16 v[120:121],v227 offset:40960
v_mfma_f32_16x16x32_bf16 a[224:227],v[148:151],v[52:55],a[224:227]
v_mfma_f32_16x16x32_bf16 a[240:243],v[144:147],v[56:59],a[240:243]
v_mfma_f32_16x16x32_bf16 a[240:243],v[148:151],v[60:63],a[240:243]
ds_read_b64_tr_b16 v[122:123],v227 offset:41472
v_mfma_f32_16x16x32_bf16 a[132:135],v[128:131],v[0:3],a[132:135]
ds_read_b128 v[144:147],v228 offset:14336
ds_read_b128 v[148:151],v229 offset:14336
v_mfma_f32_16x16x32_bf16 a[132:135],v[132:135],v[4:7],a[132:135]
ds_read_b64_tr_b16 v[124:125],v227 offset:45056
s_waitcnt lgkmcnt(5)
v_mul_f32 v154,v153,v160
v_mfma_f32_16x16x32_bf16 a[148:151],v[128:131],v[8:11],a[148:151]
v_mul_f32 v160,v152,v160
v_fma_f32 v160,-v153,v176,v160
v_mfma_f32_16x16x32_bf16 a[148:151],v[132:135],v[12:15],a[148:151]
v_fma_f32 v176,v152,v176,v154
v_mul_f32 v155,v153,v161
v_mfma_f32_16x16x32_bf16 a[164:167],v[128:131],v[16:19],a[164:167]
ds_read_b64_tr_b16 v[126:127],v227 offset:45568
v_mul_f32 v161,v152,v161
v_mfma_f32_16x16x32_bf16 a[164:167],v[132:135],v[20:23],a[164:167]
v_fma_f32 v161,-v153,v177,v161
v_fma_f32 v177,v152,v177,v155
v_mfma_f32_16x16x32_bf16 a[180:183],v[128:131],v[24:27],a[180:183]
v_mul_f32 v154,v153,v162
v_mul_f32 v162,v152,v162
v_mfma_f32_16x16x32_bf16 a[180:183],v[132:135],v[28:31],a[180:183]
v_fma_f32 v162,-v153,v178,v162
v_fma_f32 v178,v152,v178,v154
v_mfma_f32_16x16x32_bf16 a[196:199],v[128:131],v[32:35],a[196:199]
v_mul_f32 v155,v153,v163
v_mul_f32 v163,v152,v163
v_mfma_f32_16x16x32_bf16 a[196:199],v[132:135],v[36:39],a[196:199]
v_fma_f32 v163,-v153,v179,v163
v_fma_f32 v179,v152,v179,v155
v_mfma_f32_16x16x32_bf16 a[212:215],v[128:131],v[40:43],a[212:215]
v_mul_f32 v154,v153,v164
v_mul_f32 v164,v152,v164
v_mfma_f32_16x16x32_bf16 a[212:215],v[132:135],v[44:47],a[212:215]
v_fma_f32 v164,-v153,v180,v164
v_fma_f32 v180,v152,v180,v154
v_mfma_f32_16x16x32_bf16 a[228:231],v[128:131],v[48:51],a[228:231]
v_mul_f32 v155,v153,v165
v_mul_f32 v165,v152,v165
v_mfma_f32_16x16x32_bf16 a[228:231],v[132:135],v[52:55],a[228:231]
v_fma_f32 v165,-v153,v181,v165
v_fma_f32 v181,v152,v181,v155
v_mfma_f32_16x16x32_bf16 a[244:247],v[128:131],v[56:59],a[244:247]
v_mul_f32 v154,v153,v166
v_mul_f32 v166,v152,v166
v_mfma_f32_16x16x32_bf16 a[244:247],v[132:135],v[60:63],a[244:247]
s_waitcnt vmcnt(5)
s_waitcnt vmcnt(0) lgkmcnt(0)
s_barrier
s_xor_b32 s58,s65,1
s_or_b32 s58,s58,s56
s_cbranch_scc1 L_PUBE1_%=
v_mov_b32 v242,1
v_mov_b32 v243,s94
s_mov_b64 exec,1
global_atomic_add v243,v242,s[22:23]
s_mov_b64 exec,-1
L_PUBE1_%=:
v_mfma_f32_16x16x32_bf16 a[136:139],v[136:139],v[0:3],a[136:139]
s_add_u32 m0,s84,0
s_nop 0
buffer_load_dwordx4 v234,s[52:55],s81 offen offset:0 lds
v_mfma_f32_16x16x32_bf16 a[136:139],v[140:143],v[4:7],a[136:139]
ds_read_b128 v[128:131],v228 offset:32768
v_fma_f32 v166,-v153,v182,v166
v_mfma_f32_16x16x32_bf16 a[152:155],v[136:139],v[8:11],a[152:155]
ds_read_b128 v[132:135],v229 offset:32768
v_fma_f32 v182,v152,v182,v154
v_mfma_f32_16x16x32_bf16 a[152:155],v[140:143],v[12:15],a[152:155]
v_mul_f32 v155,v153,v167
v_mul_f32 v167,v152,v167
v_mfma_f32_16x16x32_bf16 a[168:171],v[136:139],v[16:19],a[168:171]
buffer_load_dwordx4 v235,s[52:55],s81 offen offset:1024 lds
v_fma_f32 v167,-v153,v183,v167
v_mfma_f32_16x16x32_bf16 a[168:171],v[140:143],v[20:23],a[168:171]
v_fma_f32 v183,v152,v183,v155
v_mul_f32 v154,v153,v168
v_mfma_f32_16x16x32_bf16 a[184:187],v[136:139],v[24:27],a[184:187]
v_mul_f32 v168,v152,v168
v_fma_f32 v168,-v153,v184,v168
v_mfma_f32_16x16x32_bf16 a[184:187],v[140:143],v[28:31],a[184:187]
v_fma_f32 v184,v152,v184,v154
v_mul_f32 v155,v153,v169
v_mfma_f32_16x16x32_bf16 a[200:203],v[136:139],v[32:35],a[200:203]
buffer_load_dwordx4 v236,s[52:55],s81 offen offset:2048 lds
v_mul_f32 v169,v152,v169
v_mfma_f32_16x16x32_bf16 a[200:203],v[140:143],v[36:39],a[200:203]
v_fma_f32 v169,-v153,v185,v169
v_fma_f32 v185,v152,v185,v155
v_mfma_f32_16x16x32_bf16 a[216:219],v[136:139],v[40:43],a[216:219]
v_mul_f32 v154,v153,v170
v_mul_f32 v170,v152,v170
v_mfma_f32_16x16x32_bf16 a[216:219],v[140:143],v[44:47],a[216:219]
v_fma_f32 v170,-v153,v186,v170
v_fma_f32 v186,v152,v186,v154
v_mfma_f32_16x16x32_bf16 a[232:235],v[136:139],v[48:51],a[232:235]
buffer_load_dwordx4 v237,s[52:55],s81 offen offset:3072 lds
v_mul_f32 v155,v153,v171
v_mfma_f32_16x16x32_bf16 a[232:235],v[140:143],v[52:55],a[232:235]
v_mul_f32 v171,v152,v171
v_fma_f32 v171,-v153,v187,v171
v_mfma_f32_16x16x32_bf16 a[248:251],v[136:139],v[56:59],a[248:251]
v_fma_f32 v187,v152,v187,v155
v_mul_f32 v154,v153,v172
v_mfma_f32_16x16x32_bf16 a[248:251],v[140:143],v[60:63],a[248:251]
v_mul_f32 v172,v152,v172
v_fma_f32 v172,-v153,v188,v172
v_mfma_f32_16x16x32_bf16 a[140:143],v[144:147],v[0:3],a[140:143]
s_add_u32 m0,s84,4096
s_nop 0
buffer_load_dwordx4 v234,s[52:55],s82 offen offset:0 lds
v_mfma_f32_16x16x32_bf16 a[140:143],v[148:151],v[4:7],a[140:143]
ds_read_b128 v[136:139],v228 offset:34816
v_fma_f32 v188,v152,v188,v154
v_mfma_f32_16x16x32_bf16 a[156:159],v[144:147],v[8:11],a[156:159]
ds_read_b128 v[140:143],v229 offset:34816
v_mul_f32 v155,v153,v173
v_mfma_f32_16x16x32_bf16 a[156:159],v[148:151],v[12:15],a[156:159]
v_mul_f32 v173,v152,v173
v_fma_f32 v173,-v153,v189,v173
v_mfma_f32_16x16x32_bf16 a[172:175],v[144:147],v[16:19],a[172:175]
buffer_load_dwordx4 v235,s[52:55],s82 offen offset:1024 lds
v_fma_f32 v189,v152,v189,v155
v_mfma_f32_16x16x32_bf16 a[172:175],v[148:151],v[20:23],a[172:175]
v_mul_f32 v154,v153,v174
v_mul_f32 v174,v152,v174
v_mfma_f32_16x16x32_bf16 a[188:191],v[144:147],v[24:27],a[188:191]
v_fma_f32 v174,-v153,v190,v174
v_fma_f32 v190,v152,v190,v154
v_mfma_f32_16x16x32_bf16 a[188:191],v[148:151],v[28:31],a[188:191]
v_mul_f32 v155,v153,v175
v_mul_f32 v175,v152,v175
v_mfma_f32_16x16x32_bf16 a[204:207],v[144:147],v[32:35],a[204:207]
buffer_load_dwordx4 v236,s[52:55],s82 offen offset:2048 lds
v_fma_f32 v175,-v153,v191,v175
v_mfma_f32_16x16x32_bf16 a[204:207],v[148:151],v[36:39],a[204:207]
v_fma_f32 v191,v152,v191,v155
ds_read_b32 v152,v238 offset:576
v_mfma_f32_16x16x32_bf16 a[220:223],v[144:147],v[40:43],a[220:223]
ds_read_b32 v153,v238 offset:832
v_mfma_f32_16x16x32_bf16 a[220:223],v[148:151],v[44:47],a[220:223]
v_mfma_f32_16x16x32_bf16 a[236:239],v[144:147],v[48:51],a[236:239]
buffer_load_dwordx4 v237,s[52:55],s82 offen offset:3072 lds
v_mfma_f32_16x16x32_bf16 a[236:239],v[148:151],v[52:55],a[236:239]
v_mfma_f32_16x16x32_bf16 a[252:255],v[144:147],v[56:59],a[252:255]
v_mfma_f32_16x16x32_bf16 a[252:255],v[148:151],v[60:63],a[252:255]
s_add_u32 s81,s81,128
s_add_u32 s82,s82,128
s_waitcnt lgkmcnt(5)
v_mfma_f32_16x16x32_bf16 a[0:3],v[128:131],v[64:67],a[0:3]
s_waitcnt lgkmcnt(4)
v_mfma_f32_16x16x32_bf16 a[0:3],v[132:135],v[68:71],a[0:3]
ds_read_b128 v[144:147],v228 offset:36864
s_waitcnt lgkmcnt(1)
v_mul_f32 v154,v153,v192
v_mfma_f32_16x16x32_bf16 a[16:19],v[128:131],v[72:75],a[16:19]
ds_read_b128 v[148:151],v229 offset:36864
ds_read_b64_tr_b16 v[0:1],v224 offset:0
v_mfma_f32_16x16x32_bf16 a[16:19],v[132:135],v[76:79],a[16:19]
s_add_u32 m0,s83,32768
s_nop 0
buffer_load_dwordx4 v230,s[48:51],s79 offen offset:0 lds
v_mfma_f32_16x16x32_bf16 a[32:35],v[128:131],v[80:83],a[32:35]
ds_read_b64_tr_b16 v[2:3],v224 offset:512
v_mul_f32 v192,v152,v192
v_mfma_f32_16x16x32_bf16 a[32:35],v[132:135],v[84:87],a[32:35]
v_fma_f32 v192,-v153,v208,v192
v_fma_f32 v208,v152,v208,v154
v_mfma_f32_16x16x32_bf16 a[48:51],v[128:131],v[88:91],a[48:51]
v_mul_f32 v155,v153,v193
v_mul_f32 v193,v152,v193
v_mfma_f32_16x16x32_bf16 a[48:51],v[132:135],v[92:95],a[48:51]
ds_read_b64_tr_b16 v[4:5],v224 offset:4096
buffer_load_dwordx4 v231,s[48:51],s79 offen offset:1024 lds
v_mfma_f32_16x16x32_bf16 a[64:67],v[128:131],v[96:99],a[64:67]
v_fma_f32 v193,-v153,v209,v193
v_fma_f32 v209,v152,v209,v155
v_mfma_f32_16x16x32_bf16 a[64:67],v[132:135],v[100:103],a[64:67]
ds_read_b64_tr_b16 v[6:7],v224 offset:4608
v_mul_f32 v154,v153,v194
v_mfma_f32_16x16x32_bf16 a[80:83],v[128:131],v[104:107],a[80:83]
v_mul_f32 v194,v152,v194
v_fma_f32 v194,-v153,v210,v194
v_mfma_f32_16x16x32_bf16 a[80:83],v[132:135],v[108:111],a[80:83]
buffer_load_dwordx4 v232,s[48:51],s79 offen offset:2048 lds
v_fma_f32 v210,v152,v210,v154
v_mfma_f32_16x16x32_bf16 a[96:99],v[128:131],v[112:115],a[96:99]
ds_read_b64_tr_b16 v[8:9],v225 offset:0
v_mul_f32 v155,v153,v195
v_mfma_f32_16x16x32_bf16 a[96:99],v[132:135],v[116:119],a[96:99]
v_mul_f32 v195,v152,v195
v_fma_f32 v195,-v153,v211,v195
v_mfma_f32_16x16x32_bf16 a[112:115],v[128:131],v[120:123],a[112:115]
v_fma_f32 v211,v152,v211,v155
v_mul_f32 v154,v153,v196
v_mfma_f32_16x16x32_bf16 a[112:115],v[132:135],v[124:127],a[112:115]
ds_read_b64_tr_b16 v[10:11],v225 offset:512
buffer_load_dwordx4 v233,s[48:51],s79 offen offset:3072 lds
v_mfma_f32_16x16x32_bf16 a[4:7],v[136:139],v[64:67],a[4:7]
v_mul_f32 v196,v152,v196
v_fma_f32 v196,-v153,v212,v196
v_mfma_f32_16x16x32_bf16 a[4:7],v[140:143],v[68:71],a[4:7]
ds_read_b128 v[128:131],v228 offset:38912
ds_read_b64_tr_b16 v[12:13],v225 offset:4096
v_mfma_f32_16x16x32_bf16 a[20:23],v[136:139],v[72:75],a[20:23]
ds_read_b128 v[132:135],v229 offset:38912
v_fma_f32 v212,v152,v212,v154
v_mfma_f32_16x16x32_bf16 a[20:23],v[140:143],v[76:79],a[20:23]
s_add_u32 m0,s83,36864
s_nop 0
buffer_load_dwordx4 v230,s[48:51],s80 offen offset:0 lds
v_mfma_f32_16x16x32_bf16 a[36:39],v[136:139],v[80:83],a[36:39]
ds_read_b64_tr_b16 v[14:15],v225 offset:4608
v_mul_f32 v155,v153,v197
v_mfma_f32_16x16x32_bf16 a[36:39],v[140:143],v[84:87],a[36:39]
v_mul_f32 v197,v152,v197
v_fma_f32 v197,-v153,v213,v197
v_mfma_f32_16x16x32_bf16 a[52:55],v[136:139],v[88:91],a[52:55]
v_fma_f32 v213,v152,v213,v155
v_mul_f32 v154,v153,v198
v_mfma_f32_16x16x32_bf16 a[52:55],v[140:143],v[92:95],a[52:55]
ds_read_b64_tr_b16 v[16:17],v226 offset:0
buffer_load_dwordx4 v231,s[48:51],s80 offen offset:1024 lds
v_mfma_f32_16x16x32_bf16 a[68:71],v[136:139],v[96:99],a[68:71]
v_mul_f32 v198,v152,v198
v_fma_f32 v198,-v153,v214,v198
v_mfma_f32_16x16x32_bf16 a[68:71],v[140:143],v[100:103],a[68:71]
ds_read_b64_tr_b16 v[18:19],v226 offset:512
v_fma_f32 v214,v152,v214,v154
v_mfma_f32_16x16x32_bf16 a[84:87],v[136:139],v[104:107],a[84:87]
v_mul_f32 v155,v153,v199
v_mul_f32 v199,v152,v199
v_mfma_f32_16x16x32_bf16 a[84:87],v[140:143],v[108:111],a[84:87]
buffer_load_dwordx4 v232,s[48:51],s80 offen offset:2048 lds
v_fma_f32 v199,-v153,v215,v199
v_mfma_f32_16x16x32_bf16 a[100:103],v[136:139],v[112:115],a[100:103]
ds_read_b64_tr_b16 v[20:21],v226 offset:4096
v_fma_f32 v215,v152,v215,v155
v_mfma_f32_16x16x32_bf16 a[100:103],v[140:143],v[116:119],a[100:103]
v_mul_f32 v154,v153,v200
v_mul_f32 v200,v152,v200
v_mfma_f32_16x16x32_bf16 a[116:119],v[136:139],v[120:123],a[116:119]
v_fma_f32 v200,-v153,v216,v200
v_fma_f32 v216,v152,v216,v154
v_mfma_f32_16x16x32_bf16 a[116:119],v[140:143],v[124:127],a[116:119]
ds_read_b64_tr_b16 v[22:23],v226 offset:4608
buffer_load_dwordx4 v233,s[48:51],s80 offen offset:3072 lds
s_waitcnt lgkmcnt(15)
v_mfma_f32_16x16x32_bf16 a[8:11],v[144:147],v[64:67],a[8:11]
v_mul_f32 v155,v153,v201
v_mul_f32 v201,v152,v201
s_waitcnt lgkmcnt(14)
v_mfma_f32_16x16x32_bf16 a[8:11],v[148:151],v[68:71],a[8:11]
ds_read_b128 v[136:139],v228 offset:40960
ds_read_b64_tr_b16 v[24:25],v227 offset:0
v_mfma_f32_16x16x32_bf16 a[24:27],v[144:147],v[72:75],a[24:27]
ds_read_b128 v[140:143],v229 offset:40960
v_fma_f32 v201,-v153,v217,v201
v_mfma_f32_16x16x32_bf16 a[24:27],v[148:151],v[76:79],a[24:27]
s_add_u32 s79,s79,524288
s_add_u32 s80,s80,524288
v_mfma_f32_16x16x32_bf16 a[40:43],v[144:147],v[80:83],a[40:43]
ds_read_b64_tr_b16 v[26:27],v227 offset:512
v_fma_f32 v217,v152,v217,v155
v_mfma_f32_16x16x32_bf16 a[40:43],v[148:151],v[84:87],a[40:43]
v_mul_f32 v154,v153,v202
v_mul_f32 v202,v152,v202
v_mfma_f32_16x16x32_bf16 a[56:59],v[144:147],v[88:91],a[56:59]
v_fma_f32 v202,-v153,v218,v202
v_fma_f32 v218,v152,v218,v154
v_mfma_f32_16x16x32_bf16 a[56:59],v[148:151],v[92:95],a[56:59]
ds_read_b64_tr_b16 v[28:29],v227 offset:4096
v_mul_f32 v155,v153,v203
v_mfma_f32_16x16x32_bf16 a[72:75],v[144:147],v[96:99],a[72:75]
v_mul_f32 v203,v152,v203
v_fma_f32 v203,-v153,v219,v203
v_mfma_f32_16x16x32_bf16 a[72:75],v[148:151],v[100:103],a[72:75]
ds_read_b64_tr_b16 v[30:31],v227 offset:4608
v_fma_f32 v219,v152,v219,v155
v_mfma_f32_16x16x32_bf16 a[88:91],v[144:147],v[104:107],a[88:91]
v_mul_f32 v154,v153,v204
v_mul_f32 v204,v152,v204
v_mfma_f32_16x16x32_bf16 a[88:91],v[148:151],v[108:111],a[88:91]
v_fma_f32 v204,-v153,v220,v204
v_fma_f32 v220,v152,v220,v154
v_mfma_f32_16x16x32_bf16 a[104:107],v[144:147],v[112:115],a[104:107]
ds_read_b64_tr_b16 v[32:33],v224 offset:8192
v_mul_f32 v155,v153,v205
v_mfma_f32_16x16x32_bf16 a[104:107],v[148:151],v[116:119],a[104:107]
v_mul_f32 v205,v152,v205
v_fma_f32 v205,-v153,v221,v205
v_mfma_f32_16x16x32_bf16 a[120:123],v[144:147],v[120:123],a[120:123]
v_fma_f32 v221,v152,v221,v155
v_mul_f32 v154,v153,v206
v_mfma_f32_16x16x32_bf16 a[120:123],v[148:151],v[124:127],a[120:123]
ds_read_b64_tr_b16 v[34:35],v224 offset:8704
v_mul_f32 v206,v152,v206
s_waitcnt lgkmcnt(15)
v_mfma_f32_16x16x32_bf16 a[12:15],v[128:131],v[64:67],a[12:15]
v_fma_f32 v206,-v153,v222,v206
v_fma_f32 v222,v152,v222,v154
s_waitcnt lgkmcnt(13)
v_mfma_f32_16x16x32_bf16 a[12:15],v[132:135],v[68:71],a[12:15]
ds_read_b128 v[144:147],v228 offset:43008
ds_read_b64_tr_b16 v[36:37],v224 offset:12288
v_mfma_f32_16x16x32_bf16 a[28:31],v[128:131],v[72:75],a[28:31]
ds_read_b128 v[148:151],v229 offset:43008
v_mul_f32 v155,v153,v207
v_mfma_f32_16x16x32_bf16 a[28:31],v[132:135],v[76:79],a[28:31]
v_mul_f32 v207,v152,v207
v_fma_f32 v207,-v153,v223,v207
v_mfma_f32_16x16x32_bf16 a[44:47],v[128:131],v[80:83],a[44:47]
ds_read_b64_tr_b16 v[38:39],v224 offset:12800
v_fma_f32 v223,v152,v223,v155
v_mfma_f32_16x16x32_bf16 a[44:47],v[132:135],v[84:87],a[44:47]
v_lshrrev_b32 v246,5,v254
v_mul_u32_u24 v246,5120,v246
v_mfma_f32_16x16x32_bf16 a[60:63],v[128:131],v[88:91],a[60:63]
v_and_b32 v247,31,v254
v_lshl_add_u32 v246,v247,4,v246
v_mfma_f32_16x16x32_bf16 a[60:63],v[132:135],v[92:95],a[60:63]
ds_read_b64_tr_b16 v[40:41],v225 offset:8192
v_add_u32 v247,10240,v246
v_mfma_f32_16x16x32_bf16 a[76:79],v[128:131],v[96:99],a[76:79]
s_mul_i32 s85,s56,20480
s_lshl_b32 s86,s76,9
v_mfma_f32_16x16x32_bf16 a[76:79],v[132:135],v[100:103],a[76:79]
ds_read_b64_tr_b16 v[42:43],v225 offset:8704
s_add_u32 s85,s85,s86
v_mfma_f32_16x16x32_bf16 a[92:95],v[128:131],v[104:107],a[92:95]
s_lshl_b32 s86,s56,11
s_add_u32 s86,s86,s57
v_mfma_f32_16x16x32_bf16 a[92:95],v[132:135],v[108:111],a[92:95]
s_add_u32 s86,s86,138752
v_add_u32 v248,s86,v255
v_mfma_f32_16x16x32_bf16 a[108:111],v[128:131],v[112:115],a[108:111]
ds_read_b64_tr_b16 v[44:45],v225 offset:12288
v_mfma_f32_16x16x32_bf16 a[108:111],v[132:135],v[116:119],a[108:111]
v_mfma_f32_16x16x32_bf16 a[124:127],v[128:131],v[120:123],a[124:127]
v_mfma_f32_16x16x32_bf16 a[124:127],v[132:135],v[124:127],a[124:127]
ds_read_b64_tr_b16 v[46:47],v225 offset:12800
s_waitcnt lgkmcnt(15)
v_mfma_f32_16x16x32_bf16 a[128:131],v[136:139],v[64:67],a[128:131]
s_waitcnt lgkmcnt(13)
v_mfma_f32_16x16x32_bf16 a[128:131],v[140:143],v[68:71],a[128:131]
ds_read_b128 v[128:131],v228 offset:45056
ds_read_b64_tr_b16 v[48:49],v226 offset:8192
)"
R"(v_mfma_f32_16x16x32_bf16 a[144:147],v[136:139],v[72:75],a[144:147]
ds_read_b128 v[132:135],v229 offset:45056
v_mfma_f32_16x16x32_bf16 a[144:147],v[140:143],v[76:79],a[144:147]
v_mfma_f32_16x16x32_bf16 a[160:163],v[136:139],v[80:83],a[160:163]
ds_read_b64_tr_b16 v[50:51],v226 offset:8704
v_mfma_f32_16x16x32_bf16 a[160:163],v[140:143],v[84:87],a[160:163]
v_mfma_f32_16x16x32_bf16 a[176:179],v[136:139],v[88:91],a[176:179]
v_mfma_f32_16x16x32_bf16 a[176:179],v[140:143],v[92:95],a[176:179]
ds_read_b64_tr_b16 v[52:53],v226 offset:12288
v_mfma_f32_16x16x32_bf16 a[192:195],v[136:139],v[96:99],a[192:195]
v_mfma_f32_16x16x32_bf16 a[192:195],v[140:143],v[100:103],a[192:195]
ds_read_b64_tr_b16 v[54:55],v226 offset:12800
v_mfma_f32_16x16x32_bf16 a[208:211],v[136:139],v[104:107],a[208:211]
v_mfma_f32_16x16x32_bf16 a[208:211],v[140:143],v[108:111],a[208:211]
v_mfma_f32_16x16x32_bf16 a[224:227],v[136:139],v[112:115],a[224:227]
ds_read_b64_tr_b16 v[56:57],v227 offset:8192
v_mfma_f32_16x16x32_bf16 a[224:227],v[140:143],v[116:119],a[224:227]
v_mfma_f32_16x16x32_bf16 a[240:243],v[136:139],v[120:123],a[240:243]
v_mfma_f32_16x16x32_bf16 a[240:243],v[140:143],v[124:127],a[240:243]
ds_read_b64_tr_b16 v[58:59],v227 offset:8704
s_waitcnt lgkmcnt(15)
v_mfma_f32_16x16x32_bf16 a[132:135],v[144:147],v[64:67],a[132:135]
ds_read_b128 v[136:139],v228 offset:47104
ds_read_b128 v[140:143],v229 offset:47104
s_waitcnt lgkmcnt(15)
v_mfma_f32_16x16x32_bf16 a[132:135],v[148:151],v[68:71],a[132:135]
ds_read_b64_tr_b16 v[60:61],v227 offset:12288
v_mfma_f32_16x16x32_bf16 a[148:151],v[144:147],v[72:75],a[148:151]
v_mfma_f32_16x16x32_bf16 a[148:151],v[148:151],v[76:79],a[148:151]
v_mfma_f32_16x16x32_bf16 a[164:167],v[144:147],v[80:83],a[164:167]
ds_read_b64_tr_b16 v[62:63],v227 offset:12800
v_mfma_f32_16x16x32_bf16 a[164:167],v[148:151],v[84:87],a[164:167]
v_mfma_f32_16x16x32_bf16 a[180:183],v[144:147],v[88:91],a[180:183]
v_mfma_f32_16x16x32_bf16 a[180:183],v[148:151],v[92:95],a[180:183]
v_mfma_f32_16x16x32_bf16 a[196:199],v[144:147],v[96:99],a[196:199]
v_mfma_f32_16x16x32_bf16 a[196:199],v[148:151],v[100:103],a[196:199]
v_mfma_f32_16x16x32_bf16 a[212:215],v[144:147],v[104:107],a[212:215]
v_mfma_f32_16x16x32_bf16 a[212:215],v[148:151],v[108:111],a[212:215]
v_mfma_f32_16x16x32_bf16 a[228:231],v[144:147],v[112:115],a[228:231]
v_mfma_f32_16x16x32_bf16 a[228:231],v[148:151],v[116:119],a[228:231]
v_mfma_f32_16x16x32_bf16 a[244:247],v[144:147],v[120:123],a[244:247]
v_mfma_f32_16x16x32_bf16 a[244:247],v[148:151],v[124:127],a[244:247]
s_xor_b32 s58,s65,1
s_or_b32 s58,s58,s56
s_cbranch_scc1 L_PXE1_%=
v_mov_b32 v243,s94
s_mov_b32 s95,0
L_PLE1_%=:
global_load_dword v242,v243,s[22:23] sc1
s_waitcnt vmcnt(0)
L_PCE1_%=:
v_readfirstlane_b32 s58,v242
s_nop 3
s_cmp_ge_u32 s58,16
s_cbranch_scc1 L_PDE1_%=
s_sleep 2
s_add_u32 s95,s95,1
s_cmp_lt_u32 s95,0x20000
s_cbranch_scc1 L_PLE1_%=
L_PDE1_%=:
L_PXE1_%=:
s_waitcnt vmcnt(0) lgkmcnt(0)
s_barrier
v_mfma_f32_16x16x32_bf16 a[136:139],v[128:131],v[64:67],a[136:139]
s_add_u32 m0,s84,32768
s_nop 0
buffer_load_dwordx4 v234,s[52:55],s81 offen offset:0 lds
v_mfma_f32_16x16x32_bf16 a[136:139],v[132:135],v[68:71],a[136:139]
ds_read_b128 v[144:147],v228 offset:0
buffer_load_dwordx4 v[152:155],v246,s[36:39],s85 offen sc1
v_mfma_f32_16x16x32_bf16 a[152:155],v[128:131],v[72:75],a[152:155]
ds_read_b128 v[148:151],v229 offset:0
buffer_load_dwordx4 v[156:159],v247,s[36:39],s85 offen sc1
v_mfma_f32_16x16x32_bf16 a[152:155],v[132:135],v[76:79],a[152:155]
s_lshl_b32 s85,s75,9
s_and_b32 s86,s56,1
v_mfma_f32_16x16x32_bf16 a[168:171],v[128:131],v[80:83],a[168:171]
buffer_load_dwordx4 v235,s[52:55],s81 offen offset:1024 lds
s_lshl_b32 s86,s86,8
v_mfma_f32_16x16x32_bf16 a[168:171],v[132:135],v[84:87],a[168:171]
s_add_u32 s85,s85,s86
s_add_u32 s85,s85,s57
v_mfma_f32_16x16x32_bf16 a[184:187],v[128:131],v[88:91],a[184:187]
s_add_u32 s85,s85,131072
v_lshl_add_u32 v239,v241,3,s85
v_mfma_f32_16x16x32_bf16 a[184:187],v[132:135],v[92:95],a[184:187]
v_lshrrev_b32 v240,4,v254
v_lshlrev_b32 v240,9,v240
v_mfma_f32_16x16x32_bf16 a[200:203],v[128:131],v[96:99],a[200:203]
buffer_load_dwordx4 v236,s[52:55],s81 offen offset:2048 lds
v_lshl_add_u32 v240,v241,3,v240
v_mfma_f32_16x16x32_bf16 a[200:203],v[132:135],v[100:103],a[200:203]
s_lshr_b32 s85,s56,1
s_lshl_b32 s85,s85,11
v_mfma_f32_16x16x32_bf16 a[216:219],v[128:131],v[104:107],a[216:219]
s_add_u32 s85,s85,s86
s_add_u32 s85,s85,s57
v_mfma_f32_16x16x32_bf16 a[216:219],v[132:135],v[108:111],a[216:219]
s_add_u32 s85,s85,157696
v_add_u32 v240,s85,v240
v_mfma_f32_16x16x32_bf16 a[232:235],v[128:131],v[112:115],a[232:235]
buffer_load_dwordx4 v237,s[52:55],s81 offen offset:3072 lds
v_mfma_f32_16x16x32_bf16 a[232:235],v[132:135],v[116:119],a[232:235]
v_mfma_f32_16x16x32_bf16 a[248:251],v[128:131],v[120:123],a[248:251]
v_mfma_f32_16x16x32_bf16 a[248:251],v[132:135],v[124:127],a[248:251]
v_mfma_f32_16x16x32_bf16 a[140:143],v[136:139],v[64:67],a[140:143]
s_add_u32 m0,s84,36864
s_nop 0
buffer_load_dwordx4 v234,s[52:55],s82 offen offset:0 lds
v_mfma_f32_16x16x32_bf16 a[140:143],v[140:143],v[68:71],a[140:143]
ds_read_b128 v[128:131],v228 offset:2048
v_mfma_f32_16x16x32_bf16 a[156:159],v[136:139],v[72:75],a[156:159]
ds_read_b128 v[132:135],v229 offset:2048
v_mfma_f32_16x16x32_bf16 a[156:159],v[140:143],v[76:79],a[156:159]
v_mfma_f32_16x16x32_bf16 a[172:175],v[136:139],v[80:83],a[172:175]
buffer_load_dwordx4 v235,s[52:55],s82 offen offset:1024 lds
v_mfma_f32_16x16x32_bf16 a[172:175],v[140:143],v[84:87],a[172:175]
v_mfma_f32_16x16x32_bf16 a[188:191],v[136:139],v[88:91],a[188:191]
v_mfma_f32_16x16x32_bf16 a[188:191],v[140:143],v[92:95],a[188:191]
v_mfma_f32_16x16x32_bf16 a[204:207],v[136:139],v[96:99],a[204:207]
buffer_load_dwordx4 v236,s[52:55],s82 offen offset:2048 lds
v_mfma_f32_16x16x32_bf16 a[204:207],v[140:143],v[100:103],a[204:207]
v_mfma_f32_16x16x32_bf16 a[220:223],v[136:139],v[104:107],a[220:223]
v_mfma_f32_16x16x32_bf16 a[220:223],v[140:143],v[108:111],a[220:223]
v_mfma_f32_16x16x32_bf16 a[236:239],v[136:139],v[112:115],a[236:239]
buffer_load_dwordx4 v237,s[52:55],s82 offen offset:3072 lds
v_mfma_f32_16x16x32_bf16 a[236:239],v[140:143],v[116:119],a[236:239]
v_mfma_f32_16x16x32_bf16 a[252:255],v[136:139],v[120:123],a[252:255]
v_mfma_f32_16x16x32_bf16 a[252:255],v[140:143],v[124:127],a[252:255]
s_add_u32 s81,s81,128
s_add_u32 s82,s82,128
s_waitcnt lgkmcnt(3)
v_mfma_f32_16x16x32_bf16 a[0:3],v[144:147],v[0:3],a[0:3]
s_waitcnt lgkmcnt(2)
v_mfma_f32_16x16x32_bf16 a[0:3],v[148:151],v[4:7],a[0:3]
ds_read_b128 v[136:139],v228 offset:4096
v_mfma_f32_16x16x32_bf16 a[16:19],v[144:147],v[8:11],a[16:19]
ds_read_b128 v[140:143],v229 offset:4096
ds_read_b64_tr_b16 v[64:65],v224 offset:32768
v_mfma_f32_16x16x32_bf16 a[16:19],v[148:151],v[12:15],a[16:19]
s_add_u32 m0,s83,0
s_nop 0
buffer_load_dwordx4 v230,s[48:51],s79 offen offset:0 lds
v_mfma_f32_16x16x32_bf16 a[32:35],v[144:147],v[16:19],a[32:35]
ds_read_b64_tr_b16 v[66:67],v224 offset:33280
v_mfma_f32_16x16x32_bf16 a[32:35],v[148:151],v[20:23],a[32:35]
v_mfma_f32_16x16x32_bf16 a[48:51],v[144:147],v[24:27],a[48:51]
v_mfma_f32_16x16x32_bf16 a[48:51],v[148:151],v[28:31],a[48:51]
ds_read_b64_tr_b16 v[68:69],v224 offset:36864
buffer_load_dwordx4 v231,s[48:51],s79 offen offset:1024 lds
v_mfma_f32_16x16x32_bf16 a[64:67],v[144:147],v[32:35],a[64:67]
v_mfma_f32_16x16x32_bf16 a[64:67],v[148:151],v[36:39],a[64:67]
ds_read_b64_tr_b16 v[70:71],v224 offset:37376
v_mfma_f32_16x16x32_bf16 a[80:83],v[144:147],v[40:43],a[80:83]
v_mfma_f32_16x16x32_bf16 a[80:83],v[148:151],v[44:47],a[80:83]
buffer_load_dwordx4 v232,s[48:51],s79 offen offset:2048 lds
v_mfma_f32_16x16x32_bf16 a[96:99],v[144:147],v[48:51],a[96:99]
ds_read_b64_tr_b16 v[72:73],v225 offset:32768
v_mfma_f32_16x16x32_bf16 a[96:99],v[148:151],v[52:55],a[96:99]
v_mfma_f32_16x16x32_bf16 a[112:115],v[144:147],v[56:59],a[112:115]
v_mfma_f32_16x16x32_bf16 a[112:115],v[148:151],v[60:63],a[112:115]
ds_read_b64_tr_b16 v[74:75],v225 offset:33280
buffer_load_dwordx4 v233,s[48:51],s79 offen offset:3072 lds
s_waitcnt lgkmcnt(9)
v_mfma_f32_16x16x32_bf16 a[4:7],v[128:131],v[0:3],a[4:7]
s_waitcnt lgkmcnt(8)
v_mfma_f32_16x16x32_bf16 a[4:7],v[132:135],v[4:7],a[4:7]
ds_read_b128 v[144:147],v228 offset:6144
ds_read_b64_tr_b16 v[76:77],v225 offset:36864
v_mfma_f32_16x16x32_bf16 a[20:23],v[128:131],v[8:11],a[20:23]
ds_read_b128 v[148:151],v229 offset:6144
v_mfma_f32_16x16x32_bf16 a[20:23],v[132:135],v[12:15],a[20:23]
s_add_u32 m0,s83,4096
s_nop 0
buffer_load_dwordx4 v230,s[48:51],s80 offen offset:0 lds
v_mfma_f32_16x16x32_bf16 a[36:39],v[128:131],v[16:19],a[36:39]
ds_read_b64_tr_b16 v[78:79],v225 offset:37376
v_mfma_f32_16x16x32_bf16 a[36:39],v[132:135],v[20:23],a[36:39]
v_mfma_f32_16x16x32_bf16 a[52:55],v[128:131],v[24:27],a[52:55]
v_mfma_f32_16x16x32_bf16 a[52:55],v[132:135],v[28:31],a[52:55]
ds_read_b64_tr_b16 v[80:81],v226 offset:32768
buffer_load_dwordx4 v231,s[48:51],s80 offen offset:1024 lds
v_mfma_f32_16x16x32_bf16 a[68:71],v[128:131],v[32:35],a[68:71]
v_mfma_f32_16x16x32_bf16 a[68:71],v[132:135],v[36:39],a[68:71]
ds_read_b64_tr_b16 v[82:83],v226 offset:33280
v_mfma_f32_16x16x32_bf16 a[84:87],v[128:131],v[40:43],a[84:87]
v_mfma_f32_16x16x32_bf16 a[84:87],v[132:135],v[44:47],a[84:87]
buffer_load_dwordx4 v232,s[48:51],s80 offen offset:2048 lds
v_mfma_f32_16x16x32_bf16 a[100:103],v[128:131],v[48:51],a[100:103]
ds_read_b64_tr_b16 v[84:85],v226 offset:36864
v_mfma_f32_16x16x32_bf16 a[100:103],v[132:135],v[52:55],a[100:103]
v_mfma_f32_16x16x32_bf16 a[116:119],v[128:131],v[56:59],a[116:119]
v_mfma_f32_16x16x32_bf16 a[116:119],v[132:135],v[60:63],a[116:119]
ds_read_b64_tr_b16 v[86:87],v226 offset:37376
buffer_load_dwordx4 v233,s[48:51],s80 offen offset:3072 lds
s_waitcnt lgkmcnt(15)
v_mfma_f32_16x16x32_bf16 a[8:11],v[136:139],v[0:3],a[8:11]
s_waitcnt lgkmcnt(14)
v_mfma_f32_16x16x32_bf16 a[8:11],v[140:143],v[4:7],a[8:11]
ds_read_b128 v[128:131],v228 offset:8192
ds_read_b64_tr_b16 v[88:89],v227 offset:32768
v_mfma_f32_16x16x32_bf16 a[24:27],v[136:139],v[8:11],a[24:27]
ds_read_b128 v[132:135],v229 offset:8192
v_mfma_f32_16x16x32_bf16 a[24:27],v[140:143],v[12:15],a[24:27]
s_add_u32 s79,s79,524288
s_add_u32 s80,s80,524288
v_mfma_f32_16x16x32_bf16 a[40:43],v[136:139],v[16:19],a[40:43]
ds_read_b64_tr_b16 v[90:91],v227 offset:33280
v_mfma_f32_16x16x32_bf16 a[40:43],v[140:143],v[20:23],a[40:43]
v_mfma_f32_16x16x32_bf16 a[56:59],v[136:139],v[24:27],a[56:59]
v_mfma_f32_16x16x32_bf16 a[56:59],v[140:143],v[28:31],a[56:59]
ds_read_b64_tr_b16 v[92:93],v227 offset:36864
v_mfma_f32_16x16x32_bf16 a[72:75],v[136:139],v[32:35],a[72:75]
v_mfma_f32_16x16x32_bf16 a[72:75],v[140:143],v[36:39],a[72:75]
ds_read_b64_tr_b16 v[94:95],v227 offset:37376
v_mfma_f32_16x16x32_bf16 a[88:91],v[136:139],v[40:43],a[88:91]
v_mfma_f32_16x16x32_bf16 a[88:91],v[140:143],v[44:47],a[88:91]
v_mfma_f32_16x16x32_bf16 a[104:107],v[136:139],v[48:51],a[104:107]
ds_read_b64_tr_b16 v[96:97],v224 offset:40960
v_mfma_f32_16x16x32_bf16 a[104:107],v[140:143],v[52:55],a[104:107]
v_mfma_f32_16x16x32_bf16 a[120:123],v[136:139],v[56:59],a[120:123]
v_mfma_f32_16x16x32_bf16 a[120:123],v[140:143],v[60:63],a[120:123]
ds_read_b64_tr_b16 v[98:99],v224 offset:41472
s_waitcnt lgkmcnt(15)
v_mfma_f32_16x16x32_bf16 a[12:15],v[144:147],v[0:3],a[12:15]
s_waitcnt lgkmcnt(13)
v_mfma_f32_16x16x32_bf16 a[12:15],v[148:151],v[4:7],a[12:15]
ds_read_b128 v[136:139],v228 offset:10240
ds_read_b64_tr_b16 v[100:101],v224 offset:45056
v_mfma_f32_16x16x32_bf16 a[28:31],v[144:147],v[8:11],a[28:31]
ds_read_b128 v[140:143],v229 offset:10240
v_mfma_f32_16x16x32_bf16 a[28:31],v[148:151],v[12:15],a[28:31]
v_mfma_f32_16x16x32_bf16 a[44:47],v[144:147],v[16:19],a[44:47]
ds_read_b64_tr_b16 v[102:103],v224 offset:45568
v_mfma_f32_16x16x32_bf16 a[44:47],v[148:151],v[20:23],a[44:47]
v_mfma_f32_16x16x32_bf16 a[60:63],v[144:147],v[24:27],a[60:63]
v_mfma_f32_16x16x32_bf16 a[60:63],v[148:151],v[28:31],a[60:63]
ds_read_b64_tr_b16 v[104:105],v225 offset:40960
v_mfma_f32_16x16x32_bf16 a[76:79],v[144:147],v[32:35],a[76:79]
v_mfma_f32_16x16x32_bf16 a[76:79],v[148:151],v[36:39],a[76:79]
ds_read_b64_tr_b16 v[106:107],v225 offset:41472
v_mfma_f32_16x16x32_bf16 a[92:95],v[144:147],v[40:43],a[92:95]
v_mfma_f32_16x16x32_bf16 a[92:95],v[148:151],v[44:47],a[92:95]
v_mfma_f32_16x16x32_bf16 a[108:111],v[144:147],v[48:51],a[108:111]
ds_read_b64_tr_b16 v[108:109],v225 offset:45056
v_mfma_f32_16x16x32_bf16 a[108:111],v[148:151],v[52:55],a[108:111]
v_mfma_f32_16x16x32_bf16 a[124:127],v[144:147],v[56:59],a[124:127]
v_mfma_f32_16x16x32_bf16 a[124:127],v[148:151],v[60:63],a[124:127]
ds_read_b64_tr_b16 v[110:111],v225 offset:45568
s_waitcnt lgkmcnt(15)
v_mfma_f32_16x16x32_bf16 a[128:131],v[128:131],v[0:3],a[128:131]
s_waitcnt lgkmcnt(13)
v_mfma_f32_16x16x32_bf16 a[128:131],v[132:135],v[4:7],a[128:131]
ds_read_b128 v[144:147],v228 offset:12288
ds_read_b64_tr_b16 v[112:113],v226 offset:40960
v_mfma_f32_16x16x32_bf16 a[144:147],v[128:131],v[8:11],a[144:147]
ds_read_b128 v[148:151],v229 offset:12288
v_mfma_f32_16x16x32_bf16 a[144:147],v[132:135],v[12:15],a[144:147]
v_mfma_f32_16x16x32_bf16 a[160:163],v[128:131],v[16:19],a[160:163]
ds_read_b64_tr_b16 v[114:115],v226 offset:41472
v_mfma_f32_16x16x32_bf16 a[160:163],v[132:135],v[20:23],a[160:163]
v_mfma_f32_16x16x32_bf16 a[176:179],v[128:131],v[24:27],a[176:179]
s_waitcnt vmcnt(15)
ds_write_b128 v248,v[152:155]
v_mfma_f32_16x16x32_bf16 a[176:179],v[132:135],v[28:31],a[176:179]
ds_read_b64_tr_b16 v[116:117],v226 offset:45056
ds_write_b128 v248,v[156:159] offset:1024
v_mfma_f32_16x16x32_bf16 a[192:195],v[128:131],v[32:35],a[192:195]
v_mfma_f32_16x16x32_bf16 a[192:195],v[132:135],v[36:39],a[192:195]
ds_read_b64_tr_b16 v[118:119],v226 offset:45568
v_mfma_f32_16x16x32_bf16 a[208:211],v[128:131],v[40:43],a[208:211]
v_mfma_f32_16x16x32_bf16 a[208:211],v[132:135],v[44:47],a[208:211]
v_mfma_f32_16x16x32_bf16 a[224:227],v[128:131],v[48:51],a[224:227]
ds_read_b64_tr_b16 v[120:121],v227 offset:40960
v_mfma_f32_16x16x32_bf16 a[224:227],v[132:135],v[52:55],a[224:227]
v_mfma_f32_16x16x32_bf16 a[240:243],v[128:131],v[56:59],a[240:243]
v_mfma_f32_16x16x32_bf16 a[240:243],v[132:135],v[60:63],a[240:243]
ds_read_b64_tr_b16 v[122:123],v227 offset:41472
s_waitcnt lgkmcnt(15)
v_mfma_f32_16x16x32_bf16 a[132:135],v[136:139],v[0:3],a[132:135]
ds_read_b128 v[128:131],v228 offset:14336
ds_read_b128 v[132:135],v229 offset:14336
v_mfma_f32_16x16x32_bf16 a[132:135],v[140:143],v[4:7],a[132:135]
ds_read_b64_tr_b16 v[124:125],v227 offset:45056
v_mfma_f32_16x16x32_bf16 a[148:151],v[136:139],v[8:11],a[148:151]
v_mfma_f32_16x16x32_bf16 a[148:151],v[140:143],v[12:15],a[148:151]
v_mfma_f32_16x16x32_bf16 a[164:167],v[136:139],v[16:19],a[164:167]
ds_read_b64_tr_b16 v[126:127],v227 offset:45568
v_mfma_f32_16x16x32_bf16 a[164:167],v[140:143],v[20:23],a[164:167]
v_mfma_f32_16x16x32_bf16 a[180:183],v[136:139],v[24:27],a[180:183]
v_mfma_f32_16x16x32_bf16 a[180:183],v[140:143],v[28:31],a[180:183]
v_mfma_f32_16x16x32_bf16 a[196:199],v[136:139],v[32:35],a[196:199]
v_mfma_f32_16x16x32_bf16 a[196:199],v[140:143],v[36:39],a[196:199]
v_mfma_f32_16x16x32_bf16 a[212:215],v[136:139],v[40:43],a[212:215]
v_mfma_f32_16x16x32_bf16 a[212:215],v[140:143],v[44:47],a[212:215]
v_mfma_f32_16x16x32_bf16 a[228:231],v[136:139],v[48:51],a[228:231]
v_mfma_f32_16x16x32_bf16 a[228:231],v[140:143],v[52:55],a[228:231]
v_mfma_f32_16x16x32_bf16 a[244:247],v[136:139],v[56:59],a[244:247]
v_mfma_f32_16x16x32_bf16 a[244:247],v[140:143],v[60:63],a[244:247]
s_waitcnt vmcnt(0) lgkmcnt(0)
s_barrier
v_mfma_f32_16x16x32_bf16 a[136:139],v[144:147],v[0:3],a[136:139]
s_add_u32 m0,s84,0
s_nop 0
buffer_load_dwordx4 v234,s[52:55],s81 offen offset:0 lds
v_mfma_f32_16x16x32_bf16 a[136:139],v[148:151],v[4:7],a[136:139]
ds_read_b128 v[136:139],v228 offset:32768
ds_read_b32 v152,v238 offset:1536
v_mfma_f32_16x16x32_bf16 a[152:155],v[144:147],v[8:11],a[152:155]
ds_read_b128 v[140:143],v229 offset:32768
ds_read_b32 v153,v238 offset:1792
v_mfma_f32_16x16x32_bf16 a[152:155],v[148:151],v[12:15],a[152:155]
ds_read_b64 v[154:155],v239 offset:0
ds_read_b64 v[156:157],v239 offset:512
v_mfma_f32_16x16x32_bf16 a[168:171],v[144:147],v[16:19],a[168:171]
buffer_load_dwordx4 v235,s[52:55],s81 offen offset:1024 lds
ds_read_b64 v[158:159],v239 offset:1024
v_mfma_f32_16x16x32_bf16 a[168:171],v[148:151],v[20:23],a[168:171]
v_mfma_f32_16x16x32_bf16 a[184:187],v[144:147],v[24:27],a[184:187]
v_mfma_f32_16x16x32_bf16 a[184:187],v[148:151],v[28:31],a[184:187]
v_mfma_f32_16x16x32_bf16 a[200:203],v[144:147],v[32:35],a[200:203]
buffer_load_dwordx4 v236,s[52:55],s81 offen offset:2048 lds
v_mfma_f32_16x16x32_bf16 a[200:203],v[148:151],v[36:39],a[200:203]
v_mfma_f32_16x16x32_bf16 a[216:219],v[144:147],v[40:43],a[216:219]
s_waitcnt lgkmcnt(1)
v_fma_f32 v156,v152,v154,v156
v_fma_f32 v157,v152,v155,v157
v_mfma_f32_16x16x32_bf16 a[216:219],v[148:151],v[44:47],a[216:219]
v_fma_f32 v156,-v153,v155,v156
v_fma_f32 v157,v153,v154,v157
v_mfma_f32_16x16x32_bf16 a[232:235],v[144:147],v[48:51],a[232:235]
buffer_load_dwordx4 v237,s[52:55],s81 offen offset:3072 lds
ds_read_b64 v[154:155],v239 offset:1536
v_mfma_f32_16x16x32_bf16 a[232:235],v[148:151],v[52:55],a[232:235]
v_mfma_f32_16x16x32_bf16 a[248:251],v[144:147],v[56:59],a[248:251]
s_waitcnt lgkmcnt(1)
v_fma_f32 v158,v152,v156,v158
v_fma_f32 v159,v152,v157,v159
v_mfma_f32_16x16x32_bf16 a[248:251],v[148:151],v[60:63],a[248:251]
v_fma_f32 v158,-v153,v157,v158
v_fma_f32 v159,v153,v156,v159
v_mfma_f32_16x16x32_bf16 a[140:143],v[128:131],v[0:3],a[140:143]
s_add_u32 m0,s84,4096
s_nop 0
buffer_load_dwordx4 v234,s[52:55],s82 offen offset:0 lds
v_mfma_f32_16x16x32_bf16 a[140:143],v[132:135],v[4:7],a[140:143]
ds_read_b128 v[144:147],v228 offset:34816
ds_read_b64 v[156:157],v239 offset:2048
v_mfma_f32_16x16x32_bf16 a[156:159],v[128:131],v[8:11],a[156:159]
ds_read_b128 v[148:151],v229 offset:34816
v_mfma_f32_16x16x32_bf16 a[156:159],v[132:135],v[12:15],a[156:159]
s_waitcnt lgkmcnt(3)
v_fma_f32 v154,v152,v158,v154
v_fma_f32 v155,v152,v159,v155
v_mfma_f32_16x16x32_bf16 a[172:175],v[128:131],v[16:19],a[172:175]
buffer_load_dwordx4 v235,s[52:55],s82 offen offset:1024 lds
v_fma_f32 v154,-v153,v159,v154
v_mfma_f32_16x16x32_bf16 a[172:175],v[132:135],v[20:23],a[172:175]
v_fma_f32 v155,v153,v158,v155
ds_read_b64 v[158:159],v239 offset:2560
v_mfma_f32_16x16x32_bf16 a[188:191],v[128:131],v[24:27],a[188:191]
v_mfma_f32_16x16x32_bf16 a[188:191],v[132:135],v[28:31],a[188:191]
s_waitcnt lgkmcnt(2)
v_fma_f32 v156,v152,v154,v156
v_fma_f32 v157,v152,v155,v157
v_mfma_f32_16x16x32_bf16 a[204:207],v[128:131],v[32:35],a[204:207]
buffer_load_dwordx4 v236,s[52:55],s82 offen offset:2048 lds
v_fma_f32 v156,-v153,v155,v156
v_mfma_f32_16x16x32_bf16 a[204:207],v[132:135],v[36:39],a[204:207]
v_fma_f32 v157,v153,v154,v157
ds_read_b64 v[154:155],v239 offset:3072
v_mfma_f32_16x16x32_bf16 a[220:223],v[128:131],v[40:43],a[220:223]
v_mfma_f32_16x16x32_bf16 a[220:223],v[132:135],v[44:47],a[220:223]
s_waitcnt lgkmcnt(1)
v_fma_f32 v158,v152,v156,v158
v_fma_f32 v159,v152,v157,v159
v_mfma_f32_16x16x32_bf16 a[236:239],v[128:131],v[48:51],a[236:239]
buffer_load_dwordx4 v237,s[52:55],s82 offen offset:3072 lds
v_fma_f32 v158,-v153,v157,v158
v_mfma_f32_16x16x32_bf16 a[236:239],v[132:135],v[52:55],a[236:239]
v_fma_f32 v159,v153,v156,v159
ds_read_b64 v[156:157],v239 offset:3584
v_mfma_f32_16x16x32_bf16 a[252:255],v[128:131],v[56:59],a[252:255]
v_mfma_f32_16x16x32_bf16 a[252:255],v[132:135],v[60:63],a[252:255]
s_add_u32 s81,s81,128
s_add_u32 s82,s82,128
v_mfma_f32_16x16x32_bf16 a[0:3],v[136:139],v[64:67],a[0:3]
s_waitcnt lgkmcnt(1)
v_fma_f32 v154,v152,v158,v154
v_fma_f32 v155,v152,v159,v155
v_mfma_f32_16x16x32_bf16 a[0:3],v[140:143],v[68:71],a[0:3]
ds_read_b128 v[128:131],v228 offset:36864
v_fma_f32 v154,-v153,v159,v154
v_mfma_f32_16x16x32_bf16 a[16:19],v[136:139],v[72:75],a[16:19]
ds_read_b128 v[132:135],v229 offset:36864
ds_read_b64_tr_b16 v[0:1],v224 offset:0
v_mfma_f32_16x16x32_bf16 a[16:19],v[140:143],v[76:79],a[16:19]
s_add_u32 m0,s83,32768
s_nop 0
buffer_load_dwordx4 v230,s[48:51],s79 offen offset:0 lds
)"
R"(v_mfma_f32_16x16x32_bf16 a[32:35],v[136:139],v[80:83],a[32:35]
ds_read_b64_tr_b16 v[2:3],v224 offset:512
v_fma_f32 v155,v153,v158,v155
v_mfma_f32_16x16x32_bf16 a[32:35],v[140:143],v[84:87],a[32:35]
ds_read_b64 v[158:159],v239 offset:4096
v_mfma_f32_16x16x32_bf16 a[48:51],v[136:139],v[88:91],a[48:51]
v_mfma_f32_16x16x32_bf16 a[48:51],v[140:143],v[92:95],a[48:51]
ds_read_b64_tr_b16 v[4:5],v224 offset:4096
buffer_load_dwordx4 v231,s[48:51],s79 offen offset:1024 lds
v_mfma_f32_16x16x32_bf16 a[64:67],v[136:139],v[96:99],a[64:67]
s_waitcnt lgkmcnt(6)
v_fma_f32 v156,v152,v154,v156
v_fma_f32 v157,v152,v155,v157
v_mfma_f32_16x16x32_bf16 a[64:67],v[140:143],v[100:103],a[64:67]
ds_read_b64_tr_b16 v[6:7],v224 offset:4608
v_fma_f32 v156,-v153,v155,v156
v_mfma_f32_16x16x32_bf16 a[80:83],v[136:139],v[104:107],a[80:83]
v_fma_f32 v157,v153,v154,v157
ds_read_b64 v[154:155],v239 offset:4608
v_mfma_f32_16x16x32_bf16 a[80:83],v[140:143],v[108:111],a[80:83]
buffer_load_dwordx4 v232,s[48:51],s79 offen offset:2048 lds
v_mfma_f32_16x16x32_bf16 a[96:99],v[136:139],v[112:115],a[96:99]
ds_read_b64_tr_b16 v[8:9],v225 offset:0
s_waitcnt lgkmcnt(4)
v_fma_f32 v158,v152,v156,v158
v_mfma_f32_16x16x32_bf16 a[96:99],v[140:143],v[116:119],a[96:99]
v_fma_f32 v159,v152,v157,v159
v_fma_f32 v158,-v153,v157,v158
v_mfma_f32_16x16x32_bf16 a[112:115],v[136:139],v[120:123],a[112:115]
v_fma_f32 v159,v153,v156,v159
ds_read_b64 v[156:157],v239 offset:5120
v_mfma_f32_16x16x32_bf16 a[112:115],v[140:143],v[124:127],a[112:115]
ds_read_b64_tr_b16 v[10:11],v225 offset:512
buffer_load_dwordx4 v233,s[48:51],s79 offen offset:3072 lds
v_mfma_f32_16x16x32_bf16 a[4:7],v[144:147],v[64:67],a[4:7]
s_waitcnt lgkmcnt(3)
v_fma_f32 v154,v152,v158,v154
v_fma_f32 v155,v152,v159,v155
v_mfma_f32_16x16x32_bf16 a[4:7],v[148:151],v[68:71],a[4:7]
ds_read_b128 v[136:139],v228 offset:38912
ds_read_b64_tr_b16 v[12:13],v225 offset:4096
v_mfma_f32_16x16x32_bf16 a[20:23],v[144:147],v[72:75],a[20:23]
ds_read_b128 v[140:143],v229 offset:38912
v_fma_f32 v154,-v153,v159,v154
v_mfma_f32_16x16x32_bf16 a[20:23],v[148:151],v[76:79],a[20:23]
s_add_u32 m0,s83,36864
s_nop 0
buffer_load_dwordx4 v230,s[48:51],s80 offen offset:0 lds
v_mfma_f32_16x16x32_bf16 a[36:39],v[144:147],v[80:83],a[36:39]
ds_read_b64_tr_b16 v[14:15],v225 offset:4608
v_fma_f32 v155,v153,v158,v155
v_mfma_f32_16x16x32_bf16 a[36:39],v[148:151],v[84:87],a[36:39]
ds_read_b64 v[158:159],v239 offset:5632
v_mfma_f32_16x16x32_bf16 a[52:55],v[144:147],v[88:91],a[52:55]
v_mfma_f32_16x16x32_bf16 a[52:55],v[148:151],v[92:95],a[52:55]
ds_read_b64_tr_b16 v[16:17],v226 offset:0
buffer_load_dwordx4 v231,s[48:51],s80 offen offset:1024 lds
v_mfma_f32_16x16x32_bf16 a[68:71],v[144:147],v[96:99],a[68:71]
s_waitcnt lgkmcnt(7)
v_fma_f32 v156,v152,v154,v156
v_fma_f32 v157,v152,v155,v157
v_mfma_f32_16x16x32_bf16 a[68:71],v[148:151],v[100:103],a[68:71]
ds_read_b64_tr_b16 v[18:19],v226 offset:512
v_fma_f32 v156,-v153,v155,v156
v_mfma_f32_16x16x32_bf16 a[84:87],v[144:147],v[104:107],a[84:87]
v_fma_f32 v157,v153,v154,v157
ds_read_b64 v[154:155],v239 offset:6144
v_mfma_f32_16x16x32_bf16 a[84:87],v[148:151],v[108:111],a[84:87]
buffer_load_dwordx4 v232,s[48:51],s80 offen offset:2048 lds
v_mfma_f32_16x16x32_bf16 a[100:103],v[144:147],v[112:115],a[100:103]
ds_read_b64_tr_b16 v[20:21],v226 offset:4096
s_waitcnt lgkmcnt(4)
v_fma_f32 v158,v152,v156,v158
v_mfma_f32_16x16x32_bf16 a[100:103],v[148:151],v[116:119],a[100:103]
v_fma_f32 v159,v152,v157,v159
v_fma_f32 v158,-v153,v157,v158
v_mfma_f32_16x16x32_bf16 a[116:119],v[144:147],v[120:123],a[116:119]
v_fma_f32 v159,v153,v156,v159
ds_read_b64 v[156:157],v239 offset:6656
v_mfma_f32_16x16x32_bf16 a[116:119],v[148:151],v[124:127],a[116:119]
ds_read_b64_tr_b16 v[22:23],v226 offset:4608
buffer_load_dwordx4 v233,s[48:51],s80 offen offset:3072 lds
v_mfma_f32_16x16x32_bf16 a[8:11],v[128:131],v[64:67],a[8:11]
s_waitcnt lgkmcnt(3)
v_fma_f32 v154,v152,v158,v154
v_fma_f32 v155,v152,v159,v155
v_mfma_f32_16x16x32_bf16 a[8:11],v[132:135],v[68:71],a[8:11]
ds_read_b128 v[144:147],v228 offset:40960
ds_read_b64_tr_b16 v[24:25],v227 offset:0
v_mfma_f32_16x16x32_bf16 a[24:27],v[128:131],v[72:75],a[24:27]
ds_read_b128 v[148:151],v229 offset:40960
v_fma_f32 v154,-v153,v159,v154
v_mfma_f32_16x16x32_bf16 a[24:27],v[132:135],v[76:79],a[24:27]
s_add_u32 s79,s79,524288
s_add_u32 s80,s80,524288
v_mfma_f32_16x16x32_bf16 a[40:43],v[128:131],v[80:83],a[40:43]
ds_read_b64_tr_b16 v[26:27],v227 offset:512
v_fma_f32 v155,v153,v158,v155
v_mfma_f32_16x16x32_bf16 a[40:43],v[132:135],v[84:87],a[40:43]
ds_read_b64 v[158:159],v239 offset:7168
v_mfma_f32_16x16x32_bf16 a[56:59],v[128:131],v[88:91],a[56:59]
v_mfma_f32_16x16x32_bf16 a[56:59],v[132:135],v[92:95],a[56:59]
ds_read_b64_tr_b16 v[28:29],v227 offset:4096
s_waitcnt lgkmcnt(7)
v_fma_f32 v156,v152,v154,v156
v_mfma_f32_16x16x32_bf16 a[72:75],v[128:131],v[96:99],a[72:75]
v_fma_f32 v157,v152,v155,v157
v_fma_f32 v156,-v153,v155,v156
v_mfma_f32_16x16x32_bf16 a[72:75],v[132:135],v[100:103],a[72:75]
ds_read_b64_tr_b16 v[30:31],v227 offset:4608
v_fma_f32 v157,v153,v154,v157
v_mfma_f32_16x16x32_bf16 a[88:91],v[128:131],v[104:107],a[88:91]
ds_read_b64 v[154:155],v240 offset:0
ds_read_b32 v246,v238 offset:0
v_mfma_f32_16x16x32_bf16 a[88:91],v[132:135],v[108:111],a[88:91]
ds_read_b32 v247,v238 offset:256
s_waitcnt lgkmcnt(5)
v_fma_f32 v158,v152,v156,v158
v_mfma_f32_16x16x32_bf16 a[104:107],v[128:131],v[112:115],a[104:107]
ds_read_b64_tr_b16 v[32:33],v224 offset:8192
v_fma_f32 v159,v152,v157,v159
v_mfma_f32_16x16x32_bf16 a[104:107],v[132:135],v[116:119],a[104:107]
v_fma_f32 v158,-v153,v157,v158
v_fma_f32 v159,v153,v156,v159
v_mfma_f32_16x16x32_bf16 a[120:123],v[128:131],v[120:123],a[120:123]
v_mfma_f32_16x16x32_bf16 a[120:123],v[132:135],v[124:127],a[120:123]
ds_read_b64_tr_b16 v[34:35],v224 offset:8704
v_mfma_f32_16x16x32_bf16 a[12:15],v[136:139],v[64:67],a[12:15]
s_waitcnt lgkmcnt(4)
v_fma_f32 v250,v154,v158,v250
v_fma_f32 v251,v154,v159,v251
v_mfma_f32_16x16x32_bf16 a[12:15],v[140:143],v[68:71],a[12:15]
ds_read_b128 v[128:131],v228 offset:43008
ds_read_b64_tr_b16 v[36:37],v224 offset:12288
v_mfma_f32_16x16x32_bf16 a[28:31],v[136:139],v[72:75],a[28:31]
ds_read_b128 v[132:135],v229 offset:43008
v_fma_f32 v250,-v155,v159,v250
v_mfma_f32_16x16x32_bf16 a[28:31],v[140:143],v[76:79],a[28:31]
v_fma_f32 v251,v155,v158,v251
ds_read_b32 v152,v238 offset:1600
v_mfma_f32_16x16x32_bf16 a[44:47],v[136:139],v[80:83],a[44:47]
ds_read_b64_tr_b16 v[38:39],v224 offset:12800
ds_read_b32 v153,v238 offset:1856
v_mfma_f32_16x16x32_bf16 a[44:47],v[140:143],v[84:87],a[44:47]
ds_read_b64 v[154:155],v239 offset:128
ds_read_b64 v[156:157],v239 offset:640
v_mfma_f32_16x16x32_bf16 a[60:63],v[136:139],v[88:91],a[60:63]
ds_read_b64 v[158:159],v239 offset:1152
v_mfma_f32_16x16x32_bf16 a[60:63],v[140:143],v[92:95],a[60:63]
ds_read_b64_tr_b16 v[40:41],v225 offset:8192
v_mfma_f32_16x16x32_bf16 a[76:79],v[136:139],v[96:99],a[76:79]
v_mfma_f32_16x16x32_bf16 a[76:79],v[140:143],v[100:103],a[76:79]
ds_read_b64_tr_b16 v[42:43],v225 offset:8704
v_mfma_f32_16x16x32_bf16 a[92:95],v[136:139],v[104:107],a[92:95]
v_mfma_f32_16x16x32_bf16 a[92:95],v[140:143],v[108:111],a[92:95]
v_mfma_f32_16x16x32_bf16 a[108:111],v[136:139],v[112:115],a[108:111]
ds_read_b64_tr_b16 v[44:45],v225 offset:12288
s_waitcnt lgkmcnt(4)
v_fma_f32 v156,v152,v154,v156
v_mfma_f32_16x16x32_bf16 a[108:111],v[140:143],v[116:119],a[108:111]
v_fma_f32 v157,v152,v155,v157
v_fma_f32 v156,-v153,v155,v156
v_mfma_f32_16x16x32_bf16 a[124:127],v[136:139],v[120:123],a[124:127]
v_fma_f32 v157,v153,v154,v157
ds_read_b64 v[154:155],v239 offset:1664
v_mfma_f32_16x16x32_bf16 a[124:127],v[140:143],v[124:127],a[124:127]
ds_read_b64_tr_b16 v[46:47],v225 offset:12800
v_mfma_f32_16x16x32_bf16 a[128:131],v[144:147],v[64:67],a[128:131]
s_waitcnt lgkmcnt(5)
v_fma_f32 v158,v152,v156,v158
v_fma_f32 v159,v152,v157,v159
v_mfma_f32_16x16x32_bf16 a[128:131],v[148:151],v[68:71],a[128:131]
ds_read_b128 v[136:139],v228 offset:45056
ds_read_b64_tr_b16 v[48:49],v226 offset:8192
v_mfma_f32_16x16x32_bf16 a[144:147],v[144:147],v[72:75],a[144:147]
ds_read_b128 v[140:143],v229 offset:45056
v_fma_f32 v158,-v153,v157,v158
v_mfma_f32_16x16x32_bf16 a[144:147],v[148:151],v[76:79],a[144:147]
v_fma_f32 v159,v153,v156,v159
ds_read_b64 v[156:157],v239 offset:2176
v_mfma_f32_16x16x32_bf16 a[160:163],v[144:147],v[80:83],a[160:163]
ds_read_b64_tr_b16 v[50:51],v226 offset:8704
v_mfma_f32_16x16x32_bf16 a[160:163],v[148:151],v[84:87],a[160:163]
s_waitcnt lgkmcnt(6)
v_fma_f32 v154,v152,v158,v154
v_fma_f32 v155,v152,v159,v155
v_mfma_f32_16x16x32_bf16 a[176:179],v[144:147],v[88:91],a[176:179]
v_fma_f32 v154,-v153,v159,v154
v_fma_f32 v155,v153,v158,v155
v_mfma_f32_16x16x32_bf16 a[176:179],v[148:151],v[92:95],a[176:179]
ds_read_b64_tr_b16 v[52:53],v226 offset:12288
ds_read_b64 v[158:159],v239 offset:2688
v_mfma_f32_16x16x32_bf16 a[192:195],v[144:147],v[96:99],a[192:195]
v_mfma_f32_16x16x32_bf16 a[192:195],v[148:151],v[100:103],a[192:195]
ds_read_b64_tr_b16 v[54:55],v226 offset:12800
s_waitcnt lgkmcnt(4)
v_fma_f32 v156,v152,v154,v156
v_mfma_f32_16x16x32_bf16 a[208:211],v[144:147],v[104:107],a[208:211]
v_fma_f32 v157,v152,v155,v157
v_fma_f32 v156,-v153,v155,v156
v_mfma_f32_16x16x32_bf16 a[208:211],v[148:151],v[108:111],a[208:211]
v_fma_f32 v157,v153,v154,v157
ds_read_b64 v[154:155],v239 offset:3200
v_mfma_f32_16x16x32_bf16 a[224:227],v[144:147],v[112:115],a[224:227]
ds_read_b64_tr_b16 v[56:57],v227 offset:8192
v_mfma_f32_16x16x32_bf16 a[224:227],v[148:151],v[116:119],a[224:227]
s_waitcnt lgkmcnt(3)
v_fma_f32 v158,v152,v156,v158
v_fma_f32 v159,v152,v157,v159
v_mfma_f32_16x16x32_bf16 a[240:243],v[144:147],v[120:123],a[240:243]
v_fma_f32 v158,-v153,v157,v158
v_fma_f32 v159,v153,v156,v159
v_mfma_f32_16x16x32_bf16 a[240:243],v[148:151],v[124:127],a[240:243]
ds_read_b64_tr_b16 v[58:59],v227 offset:8704
ds_read_b64 v[156:157],v239 offset:3712
v_mfma_f32_16x16x32_bf16 a[132:135],v[128:131],v[64:67],a[132:135]
ds_read_b128 v[144:147],v228 offset:47104
ds_read_b128 v[148:151],v229 offset:47104
v_mfma_f32_16x16x32_bf16 a[132:135],v[132:135],v[68:71],a[132:135]
ds_read_b64_tr_b16 v[60:61],v227 offset:12288
s_waitcnt lgkmcnt(6)
v_fma_f32 v154,v152,v158,v154
v_mfma_f32_16x16x32_bf16 a[148:151],v[128:131],v[72:75],a[148:151]
v_fma_f32 v155,v152,v159,v155
v_fma_f32 v154,-v153,v159,v154
v_mfma_f32_16x16x32_bf16 a[148:151],v[132:135],v[76:79],a[148:151]
v_fma_f32 v155,v153,v158,v155
ds_read_b64 v[158:159],v239 offset:4224
v_mfma_f32_16x16x32_bf16 a[164:167],v[128:131],v[80:83],a[164:167]
ds_read_b64_tr_b16 v[62:63],v227 offset:12800
v_mfma_f32_16x16x32_bf16 a[164:167],v[132:135],v[84:87],a[164:167]
s_waitcnt lgkmcnt(5)
v_fma_f32 v156,v152,v154,v156
v_fma_f32 v157,v152,v155,v157
v_mfma_f32_16x16x32_bf16 a[180:183],v[128:131],v[88:91],a[180:183]
v_fma_f32 v156,-v153,v155,v156
v_fma_f32 v157,v153,v154,v157
v_mfma_f32_16x16x32_bf16 a[180:183],v[132:135],v[92:95],a[180:183]
ds_read_b64 v[154:155],v239 offset:4736
v_mfma_f32_16x16x32_bf16 a[196:199],v[128:131],v[96:99],a[196:199]
v_mfma_f32_16x16x32_bf16 a[196:199],v[132:135],v[100:103],a[196:199]
s_waitcnt lgkmcnt(2)
v_fma_f32 v158,v152,v156,v158
v_fma_f32 v159,v152,v157,v159
v_mfma_f32_16x16x32_bf16 a[212:215],v[128:131],v[104:107],a[212:215]
v_fma_f32 v158,-v153,v157,v158
v_fma_f32 v159,v153,v156,v159
v_mfma_f32_16x16x32_bf16 a[212:215],v[132:135],v[108:111],a[212:215]
ds_read_b64 v[156:157],v239 offset:5248
v_mfma_f32_16x16x32_bf16 a[228:231],v[128:131],v[112:115],a[228:231]
v_mfma_f32_16x16x32_bf16 a[228:231],v[132:135],v[116:119],a[228:231]
s_waitcnt lgkmcnt(1)
v_fma_f32 v154,v152,v158,v154
v_fma_f32 v155,v152,v159,v155
v_mfma_f32_16x16x32_bf16 a[244:247],v[128:131],v[120:123],a[244:247]
v_fma_f32 v154,-v153,v159,v154
v_fma_f32 v155,v153,v158,v155
v_mfma_f32_16x16x32_bf16 a[244:247],v[132:135],v[124:127],a[244:247]
s_waitcnt vmcnt(0) lgkmcnt(0)
s_barrier
v_mfma_f32_16x16x32_bf16 a[136:139],v[136:139],v[64:67],a[136:139]
s_add_u32 m0,s84,32768
s_nop 0
buffer_load_dwordx4 v234,s[52:55],s81 offen offset:0 lds
v_mfma_f32_16x16x32_bf16 a[136:139],v[140:143],v[68:71],a[136:139]
ds_read_b128 v[128:131],v228 offset:0
ds_read_b64 v[158:159],v239 offset:5760
v_mfma_f32_16x16x32_bf16 a[152:155],v[136:139],v[72:75],a[152:155]
ds_read_b128 v[132:135],v229 offset:0
v_mfma_f32_16x16x32_bf16 a[152:155],v[140:143],v[76:79],a[152:155]
v_fma_f32 v156,v152,v154,v156
v_fma_f32 v157,v152,v155,v157
v_mfma_f32_16x16x32_bf16 a[168:171],v[136:139],v[80:83],a[168:171]
buffer_load_dwordx4 v235,s[52:55],s81 offen offset:1024 lds
v_fma_f32 v156,-v153,v155,v156
v_mfma_f32_16x16x32_bf16 a[168:171],v[140:143],v[84:87],a[168:171]
v_fma_f32 v157,v153,v154,v157
ds_read_b64 v[154:155],v239 offset:6272
v_mfma_f32_16x16x32_bf16 a[184:187],v[136:139],v[88:91],a[184:187]
v_mfma_f32_16x16x32_bf16 a[184:187],v[140:143],v[92:95],a[184:187]
s_waitcnt lgkmcnt(2)
v_fma_f32 v158,v152,v156,v158
v_fma_f32 v159,v152,v157,v159
v_mfma_f32_16x16x32_bf16 a[200:203],v[136:139],v[96:99],a[200:203]
buffer_load_dwordx4 v236,s[52:55],s81 offen offset:2048 lds
v_fma_f32 v158,-v153,v157,v158
v_mfma_f32_16x16x32_bf16 a[200:203],v[140:143],v[100:103],a[200:203]
v_fma_f32 v159,v153,v156,v159
ds_read_b64 v[156:157],v239 offset:6784
v_mfma_f32_16x16x32_bf16 a[216:219],v[136:139],v[104:107],a[216:219]
v_mfma_f32_16x16x32_bf16 a[216:219],v[140:143],v[108:111],a[216:219]
s_waitcnt lgkmcnt(1)
v_fma_f32 v154,v152,v158,v154
v_fma_f32 v155,v152,v159,v155
v_mfma_f32_16x16x32_bf16 a[232:235],v[136:139],v[112:115],a[232:235]
buffer_load_dwordx4 v237,s[52:55],s81 offen offset:3072 lds
v_fma_f32 v154,-v153,v159,v154
v_mfma_f32_16x16x32_bf16 a[232:235],v[140:143],v[116:119],a[232:235]
v_fma_f32 v155,v153,v158,v155
ds_read_b64 v[158:159],v239 offset:7296
v_mfma_f32_16x16x32_bf16 a[248:251],v[136:139],v[120:123],a[248:251]
v_mfma_f32_16x16x32_bf16 a[248:251],v[140:143],v[124:127],a[248:251]
s_waitcnt lgkmcnt(1)
v_fma_f32 v156,v152,v154,v156
v_fma_f32 v157,v152,v155,v157
v_mfma_f32_16x16x32_bf16 a[140:143],v[144:147],v[64:67],a[140:143]
s_add_u32 m0,s84,36864
s_nop 0
buffer_load_dwordx4 v234,s[52:55],s82 offen offset:0 lds
v_mfma_f32_16x16x32_bf16 a[140:143],v[148:151],v[68:71],a[140:143]
ds_read_b128 v[136:139],v228 offset:2048
v_fma_f32 v156,-v153,v155,v156
v_mfma_f32_16x16x32_bf16 a[156:159],v[144:147],v[72:75],a[156:159]
ds_read_b128 v[140:143],v229 offset:2048
v_fma_f32 v157,v153,v154,v157
v_mfma_f32_16x16x32_bf16 a[156:159],v[148:151],v[76:79],a[156:159]
ds_read_b64 v[154:155],v240 offset:128
ds_read_b32 v248,v238 offset:64
v_mfma_f32_16x16x32_bf16 a[172:175],v[144:147],v[80:83],a[172:175]
buffer_load_dwordx4 v235,s[52:55],s82 offen offset:1024 lds
ds_read_b32 v249,v238 offset:320
v_mfma_f32_16x16x32_bf16 a[172:175],v[148:151],v[84:87],a[172:175]
s_waitcnt lgkmcnt(5)
v_fma_f32 v158,v152,v156,v158
v_fma_f32 v159,v152,v157,v159
v_mfma_f32_16x16x32_bf16 a[188:191],v[144:147],v[88:91],a[188:191]
v_fma_f32 v158,-v153,v157,v158
v_fma_f32 v159,v153,v156,v159
v_mfma_f32_16x16x32_bf16 a[188:191],v[148:151],v[92:95],a[188:191]
v_mfma_f32_16x16x32_bf16 a[204:207],v[144:147],v[96:99],a[204:207]
buffer_load_dwordx4 v236,s[52:55],s82 offen offset:2048 lds
v_mfma_f32_16x16x32_bf16 a[204:207],v[148:151],v[100:103],a[204:207]
s_waitcnt lgkmcnt(2)
v_fma_f32 v252,v154,v158,v252
v_fma_f32 v253,v154,v159,v253
v_mfma_f32_16x16x32_bf16 a[220:223],v[144:147],v[104:107],a[220:223]
v_fma_f32 v252,-v155,v159,v252
v_fma_f32 v253,v155,v158,v253
v_mfma_f32_16x16x32_bf16 a[220:223],v[148:151],v[108:111],a[220:223]
ds_read_b32 v152,v238 offset:512
ds_read_b32 v153,v238 offset:768
v_mfma_f32_16x16x32_bf16 a[236:239],v[144:147],v[112:115],a[236:239]
buffer_load_dwordx4 v237,s[52:55],s82 offen offset:3072 lds
ds_read_b32 v154,v238 offset:576
v_mfma_f32_16x16x32_bf16 a[236:239],v[148:151],v[116:119],a[236:239]
ds_read_b32 v155,v238 offset:832
v_mfma_f32_16x16x32_bf16 a[252:255],v[144:147],v[120:123],a[252:255]
v_mfma_f32_16x16x32_bf16 a[252:255],v[148:151],v[124:127],a[252:255]
s_add_u32 s81,s81,128
s_add_u32 s82,s82,128
v_mfma_f32_16x16x32_bf16 a[0:3],v[128:131],v[0:3],a[0:3]
v_mfma_f32_16x16x32_bf16 a[0:3],v[132:135],v[4:7],a[0:3]
ds_read_b128 v[144:147],v228 offset:4096
v_mfma_f32_16x16x32_bf16 a[16:19],v[128:131],v[8:11],a[16:19]
ds_read_b128 v[148:151],v229 offset:4096
ds_read_b64_tr_b16 v[64:65],v224 offset:32768
v_mfma_f32_16x16x32_bf16 a[16:19],v[132:135],v[12:15],a[16:19]
s_mov_b64 s[48:49],s[24:25]
s_mov_b32 s50,0x280000
v_mfma_f32_16x16x32_bf16 a[32:35],v[128:131],v[16:19],a[32:35]
ds_read_b64_tr_b16 v[66:67],v224 offset:33280
s_mov_b32 s51,0x20000
v_mfma_f32_16x16x32_bf16 a[32:35],v[132:135],v[20:23],a[32:35]
s_mov_b64 s[52:53],s[26:27]
s_mov_b32 s54,0x500000
v_mfma_f32_16x16x32_bf16 a[48:51],v[128:131],v[24:27],a[48:51]
s_mov_b32 s55,0x20000
s_sub_u32 s60,15,s78
v_mfma_f32_16x16x32_bf16 a[48:51],v[132:135],v[28:31],a[48:51]
ds_read_b64_tr_b16 v[68:69],v224 offset:36864
s_lshl_b32 s60,s60,7
v_mfma_f32_16x16x32_bf16 a[64:67],v[128:131],v[32:35],a[64:67]
s_lshl_b32 s61,s56,4
s_add_u32 s60,s60,s61
v_mfma_f32_16x16x32_bf16 a[64:67],v[132:135],v[36:39],a[64:67]
ds_read_b64_tr_b16 v[70:71],v224 offset:37376
s_mul_i32 s66,s60,1280
v_mfma_f32_16x16x32_bf16 a[80:83],v[128:131],v[40:43],a[80:83]
s_lshl_b32 s60,s78,7
s_add_u32 s60,s60,s61
v_mfma_f32_16x16x32_bf16 a[80:83],v[132:135],v[44:47],a[80:83]
s_mul_i32 s67,s60,1280
s_lshl_b32 s60,s77,8
v_mfma_f32_16x16x32_bf16 a[96:99],v[128:131],v[48:51],a[96:99]
ds_read_b64_tr_b16 v[72:73],v225 offset:32768
s_lshr_b32 s61,s56,1
v_mfma_f32_16x16x32_bf16 a[96:99],v[132:135],v[52:55],a[96:99]
s_lshl_b32 s61,s61,7
s_add_u32 s60,s60,s61
v_mfma_f32_16x16x32_bf16 a[112:115],v[128:131],v[56:59],a[112:115]
s_and_b32 s61,s56,1
s_lshl_b32 s61,s61,18
v_mfma_f32_16x16x32_bf16 a[112:115],v[132:135],v[60:63],a[112:115]
ds_read_b64_tr_b16 v[74:75],v225 offset:33280
s_add_u32 s68,s60,s61
v_mfma_f32_16x16x32_bf16 a[4:7],v[136:139],v[0:3],a[4:7]
s_lshl_b32 s60,s56,13
s_add_u32 s70,s57,s60
v_mfma_f32_16x16x32_bf16 a[4:7],v[140:143],v[4:7],a[4:7]
ds_read_b128 v[128:131],v228 offset:6144
)"
R"(ds_read_b64_tr_b16 v[76:77],v225 offset:36864
v_mfma_f32_16x16x32_bf16 a[20:23],v[136:139],v[8:11],a[20:23]
ds_read_b128 v[132:135],v229 offset:6144
s_lshl_b32 s60,s56,12
v_mfma_f32_16x16x32_bf16 a[20:23],v[140:143],v[12:15],a[20:23]
s_add_u32 s71,s57,s60
v_lshrrev_b32 v243,3,v254
v_mfma_f32_16x16x32_bf16 a[36:39],v[136:139],v[16:19],a[36:39]
ds_read_b64_tr_b16 v[78:79],v225 offset:37376
v_lshlrev_b32 v243,8,v243
v_mfma_f32_16x16x32_bf16 a[36:39],v[140:143],v[20:23],a[36:39]
v_add_u32 v244,v243,v234
v_mov_b32 v234,v230
v_mfma_f32_16x16x32_bf16 a[52:55],v[136:139],v[24:27],a[52:55]
v_add_u32 v230,0,v244
v_add_u32 v244,v243,v235
v_mfma_f32_16x16x32_bf16 a[52:55],v[140:143],v[28:31],a[52:55]
ds_read_b64_tr_b16 v[80:81],v226 offset:32768
v_mov_b32 v235,v231
v_mfma_f32_16x16x32_bf16 a[68:71],v[136:139],v[32:35],a[68:71]
v_add_u32 v231,2048,v244
v_add_u32 v244,v243,v236
v_mfma_f32_16x16x32_bf16 a[68:71],v[140:143],v[36:39],a[68:71]
ds_read_b64_tr_b16 v[82:83],v226 offset:33280
v_mov_b32 v236,v232
v_mfma_f32_16x16x32_bf16 a[84:87],v[136:139],v[40:43],a[84:87]
v_add_u32 v232,65536,v244
v_add_u32 v244,v243,v237
v_mfma_f32_16x16x32_bf16 a[84:87],v[140:143],v[44:47],a[84:87]
v_mov_b32 v237,v233
v_add_u32 v233,67584,v244
v_mfma_f32_16x16x32_bf16 a[100:103],v[136:139],v[48:51],a[100:103]
ds_read_b64_tr_b16 v[84:85],v226 offset:36864
s_waitcnt lgkmcnt(15)
v_mul_f32 v156,v153,v250
v_mfma_f32_16x16x32_bf16 a[100:103],v[140:143],v[52:55],a[100:103]
v_mul_f32 v250,v152,v250
v_fma_f32 v250,-v153,v251,v250
v_mfma_f32_16x16x32_bf16 a[116:119],v[136:139],v[56:59],a[116:119]
v_fma_f32 v251,v152,v251,v156
v_mul_f32 v156,v155,v252
v_mfma_f32_16x16x32_bf16 a[116:119],v[140:143],v[60:63],a[116:119]
ds_read_b64_tr_b16 v[86:87],v226 offset:37376
v_mul_f32 v252,v154,v252
s_waitcnt lgkmcnt(15)
v_mfma_f32_16x16x32_bf16 a[8:11],v[144:147],v[0:3],a[8:11]
v_fma_f32 v252,-v155,v253,v252
v_fma_f32 v253,v154,v253,v156
s_waitcnt lgkmcnt(14)
v_mfma_f32_16x16x32_bf16 a[8:11],v[148:151],v[4:7],a[8:11]
ds_read_b128 v[136:139],v228 offset:8192
ds_read_b64_tr_b16 v[88:89],v227 offset:32768
v_mfma_f32_16x16x32_bf16 a[24:27],v[144:147],v[8:11],a[24:27]
ds_read_b128 v[140:143],v229 offset:8192
v_lshrrev_b32 v158,4,v254
v_mfma_f32_16x16x32_bf16 a[24:27],v[148:151],v[12:15],a[24:27]
v_lshlrev_b32 v158,11,v158
v_lshl_add_u32 v242,v241,4,v158
v_mfma_f32_16x16x32_bf16 a[40:43],v[144:147],v[16:19],a[40:43]
ds_read_b64_tr_b16 v[90:91],v227 offset:33280
v_xor_b32 v239,64,v242
v_mfma_f32_16x16x32_bf16 a[40:43],v[148:151],v[20:23],a[40:43]
s_mov_b64 s[36:37],s[22:23]
v_mfma_f32_16x16x32_bf16 a[56:59],v[144:147],v[24:27],a[56:59]
s_add_u32 s36,s36,52428800
s_addc_u32 s37,s37,0
v_mfma_f32_16x16x32_bf16 a[56:59],v[148:151],v[28:31],a[56:59]
ds_read_b64_tr_b16 v[92:93],v227 offset:36864
s_mov_b32 s38,0x500000
v_mfma_f32_16x16x32_bf16 a[72:75],v[144:147],v[32:35],a[72:75]
s_mov_b32 s39,0x20000
v_mfma_f32_16x16x32_bf16 a[72:75],v[148:151],v[36:39],a[72:75]
ds_read_b64_tr_b16 v[94:95],v227 offset:37376
v_mfma_f32_16x16x32_bf16 a[88:91],v[144:147],v[40:43],a[88:91]
s_cmp_eq_u32 s65,0
s_cselect_b32 s38,0,s38
v_mfma_f32_16x16x32_bf16 a[88:91],v[148:151],v[44:47],a[88:91]
s_lshl_b32 s85,s76,5
s_lshl_b32 s86,s75,1
v_mfma_f32_16x16x32_bf16 a[104:107],v[144:147],v[48:51],a[104:107]
ds_read_b64_tr_b16 v[96:97],v224 offset:40960
s_add_u32 s85,s85,s86
v_mfma_f32_16x16x32_bf16 a[104:107],v[148:151],v[52:55],a[104:107]
s_lshr_b32 s86,s56,1
s_add_u32 s85,s85,s86
v_mfma_f32_16x16x32_bf16 a[120:123],v[144:147],v[56:59],a[120:123]
s_lshl_b32 s85,s85,13
s_and_b32 s86,s56,1
v_mfma_f32_16x16x32_bf16 a[120:123],v[148:151],v[60:63],a[120:123]
ds_read_b64_tr_b16 v[98:99],v224 offset:41472
s_lshl_b32 s86,s86,9
s_waitcnt lgkmcnt(15)
v_mfma_f32_16x16x32_bf16 a[12:15],v[128:131],v[0:3],a[12:15]
s_add_u32 s85,s85,s86
s_add_u32 s86,s85,2621440
s_waitcnt lgkmcnt(13)
v_mfma_f32_16x16x32_bf16 a[12:15],v[132:135],v[4:7],a[12:15]
ds_read_b128 v[144:147],v228 offset:10240
ds_read_b64_tr_b16 v[100:101],v224 offset:45056
v_mfma_f32_16x16x32_bf16 a[28:31],v[128:131],v[8:11],a[28:31]
ds_read_b128 v[148:151],v229 offset:10240
v_fma_f32 v160,v246,v250,v160
v_mfma_f32_16x16x32_bf16 a[28:31],v[132:135],v[12:15],a[28:31]
v_fma_f32 v176,v246,v251,v176
v_fma_f32 v160,-v247,v251,v160
v_mfma_f32_16x16x32_bf16 a[44:47],v[128:131],v[16:19],a[44:47]
ds_read_b64_tr_b16 v[102:103],v224 offset:45568
v_fma_f32 v176,v247,v250,v176
v_mfma_f32_16x16x32_bf16 a[44:47],v[132:135],v[20:23],a[44:47]
v_fma_f32 v192,v248,v252,v192
v_fma_f32 v208,v248,v253,v208
v_mfma_f32_16x16x32_bf16 a[60:63],v[128:131],v[24:27],a[60:63]
v_fma_f32 v192,-v249,v253,v192
v_fma_f32 v208,v249,v252,v208
v_mfma_f32_16x16x32_bf16 a[60:63],v[132:135],v[28:31],a[60:63]
ds_read_b64_tr_b16 v[104:105],v225 offset:40960
v_fma_f32 v161,v246,v160,v161
v_mfma_f32_16x16x32_bf16 a[76:79],v[128:131],v[32:35],a[76:79]
v_fma_f32 v177,v246,v176,v177
v_fma_f32 v161,-v247,v176,v161
v_mfma_f32_16x16x32_bf16 a[76:79],v[132:135],v[36:39],a[76:79]
ds_read_b64_tr_b16 v[106:107],v225 offset:41472
v_fma_f32 v177,v247,v160,v177
v_mfma_f32_16x16x32_bf16 a[92:95],v[128:131],v[40:43],a[92:95]
v_fma_f32 v193,v248,v192,v193
v_fma_f32 v209,v248,v208,v209
v_mfma_f32_16x16x32_bf16 a[92:95],v[132:135],v[44:47],a[92:95]
v_fma_f32 v193,-v249,v208,v193
v_fma_f32 v209,v249,v192,v209
v_mfma_f32_16x16x32_bf16 a[108:111],v[128:131],v[48:51],a[108:111]
ds_read_b64_tr_b16 v[108:109],v225 offset:45056
v_fma_f32 v162,v246,v161,v162
v_mfma_f32_16x16x32_bf16 a[108:111],v[132:135],v[52:55],a[108:111]
v_fma_f32 v178,v246,v177,v178
v_fma_f32 v162,-v247,v177,v162
v_mfma_f32_16x16x32_bf16 a[124:127],v[128:131],v[56:59],a[124:127]
v_fma_f32 v178,v247,v161,v178
v_fma_f32 v194,v248,v193,v194
v_mfma_f32_16x16x32_bf16 a[124:127],v[132:135],v[60:63],a[124:127]
ds_read_b64_tr_b16 v[110:111],v225 offset:45568
v_fma_f32 v210,v248,v209,v210
s_waitcnt lgkmcnt(15)
v_mfma_f32_16x16x32_bf16 a[128:131],v[136:139],v[0:3],a[128:131]
v_fma_f32 v194,-v249,v209,v194
v_fma_f32 v210,v249,v193,v210
s_waitcnt lgkmcnt(13)
v_mfma_f32_16x16x32_bf16 a[128:131],v[140:143],v[4:7],a[128:131]
ds_read_b128 v[128:131],v228 offset:12288
ds_read_b64_tr_b16 v[112:113],v226 offset:40960
v_mfma_f32_16x16x32_bf16 a[144:147],v[136:139],v[8:11],a[144:147]
ds_read_b128 v[132:135],v229 offset:12288
v_fma_f32 v163,v246,v162,v163
v_mfma_f32_16x16x32_bf16 a[144:147],v[140:143],v[12:15],a[144:147]
v_fma_f32 v179,v246,v178,v179
v_fma_f32 v163,-v247,v178,v163
v_mfma_f32_16x16x32_bf16 a[160:163],v[136:139],v[16:19],a[160:163]
ds_read_b64_tr_b16 v[114:115],v226 offset:41472
v_fma_f32 v179,v247,v162,v179
v_mfma_f32_16x16x32_bf16 a[160:163],v[140:143],v[20:23],a[160:163]
v_fma_f32 v195,v248,v194,v195
v_fma_f32 v211,v248,v210,v211
v_mfma_f32_16x16x32_bf16 a[176:179],v[136:139],v[24:27],a[176:179]
v_fma_f32 v195,-v249,v210,v195
v_fma_f32 v211,v249,v194,v211
v_mfma_f32_16x16x32_bf16 a[176:179],v[140:143],v[28:31],a[176:179]
ds_read_b64_tr_b16 v[116:117],v226 offset:45056
v_fma_f32 v164,v246,v163,v164
v_mfma_f32_16x16x32_bf16 a[192:195],v[136:139],v[32:35],a[192:195]
v_fma_f32 v180,v246,v179,v180
v_fma_f32 v164,-v247,v179,v164
v_mfma_f32_16x16x32_bf16 a[192:195],v[140:143],v[36:39],a[192:195]
ds_read_b64_tr_b16 v[118:119],v226 offset:45568
v_fma_f32 v180,v247,v163,v180
v_mfma_f32_16x16x32_bf16 a[208:211],v[136:139],v[40:43],a[208:211]
v_fma_f32 v196,v248,v195,v196
v_fma_f32 v212,v248,v211,v212
v_mfma_f32_16x16x32_bf16 a[208:211],v[140:143],v[44:47],a[208:211]
v_fma_f32 v196,-v249,v211,v196
v_fma_f32 v212,v249,v195,v212
v_mfma_f32_16x16x32_bf16 a[224:227],v[136:139],v[48:51],a[224:227]
ds_read_b64_tr_b16 v[120:121],v227 offset:40960
v_fma_f32 v165,v246,v164,v165
v_mfma_f32_16x16x32_bf16 a[224:227],v[140:143],v[52:55],a[224:227]
v_fma_f32 v181,v246,v180,v181
v_fma_f32 v165,-v247,v180,v165
v_mfma_f32_16x16x32_bf16 a[240:243],v[136:139],v[56:59],a[240:243]
v_fma_f32 v181,v247,v164,v181
v_fma_f32 v197,v248,v196,v197
v_mfma_f32_16x16x32_bf16 a[240:243],v[140:143],v[60:63],a[240:243]
ds_read_b64_tr_b16 v[122:123],v227 offset:41472
v_fma_f32 v213,v248,v212,v213
s_waitcnt lgkmcnt(15)
v_mfma_f32_16x16x32_bf16 a[132:135],v[144:147],v[0:3],a[132:135]
ds_read_b128 v[136:139],v228 offset:14336
ds_read_b128 v[140:143],v229 offset:14336
s_waitcnt lgkmcnt(15)
v_mfma_f32_16x16x32_bf16 a[132:135],v[148:151],v[4:7],a[132:135]
ds_read_b64_tr_b16 v[124:125],v227 offset:45056
v_fma_f32 v197,-v249,v212,v197
v_mfma_f32_16x16x32_bf16 a[148:151],v[144:147],v[8:11],a[148:151]
v_fma_f32 v213,v249,v196,v213
v_fma_f32 v166,v246,v165,v166
v_mfma_f32_16x16x32_bf16 a[148:151],v[148:151],v[12:15],a[148:151]
v_fma_f32 v182,v246,v181,v182
v_fma_f32 v166,-v247,v181,v166
v_mfma_f32_16x16x32_bf16 a[164:167],v[144:147],v[16:19],a[164:167]
ds_read_b64_tr_b16 v[126:127],v227 offset:45568
v_fma_f32 v182,v247,v165,v182
v_mfma_f32_16x16x32_bf16 a[164:167],v[148:151],v[20:23],a[164:167]
v_fma_f32 v198,v248,v197,v198
v_fma_f32 v214,v248,v213,v214
v_mfma_f32_16x16x32_bf16 a[180:183],v[144:147],v[24:27],a[180:183]
v_fma_f32 v198,-v249,v213,v198
v_fma_f32 v214,v249,v197,v214
v_mfma_f32_16x16x32_bf16 a[180:183],v[148:151],v[28:31],a[180:183]
v_fma_f32 v167,v246,v166,v167
v_fma_f32 v183,v246,v182,v183
v_mfma_f32_16x16x32_bf16 a[196:199],v[144:147],v[32:35],a[196:199]
v_fma_f32 v167,-v247,v182,v167
v_fma_f32 v183,v247,v166,v183
v_mfma_f32_16x16x32_bf16 a[196:199],v[148:151],v[36:39],a[196:199]
v_fma_f32 v199,v248,v198,v199
v_fma_f32 v215,v248,v214,v215
v_mfma_f32_16x16x32_bf16 a[212:215],v[144:147],v[40:43],a[212:215]
v_fma_f32 v199,-v249,v214,v199
v_fma_f32 v215,v249,v198,v215
v_mfma_f32_16x16x32_bf16 a[212:215],v[148:151],v[44:47],a[212:215]
v_fma_f32 v168,v246,v167,v168
v_fma_f32 v184,v246,v183,v184
v_mfma_f32_16x16x32_bf16 a[228:231],v[144:147],v[48:51],a[228:231]
v_fma_f32 v168,-v247,v183,v168
v_fma_f32 v184,v247,v167,v184
v_mfma_f32_16x16x32_bf16 a[228:231],v[148:151],v[52:55],a[228:231]
v_fma_f32 v200,v248,v199,v200
v_fma_f32 v216,v248,v215,v216
v_mfma_f32_16x16x32_bf16 a[244:247],v[144:147],v[56:59],a[244:247]
v_fma_f32 v200,-v249,v215,v200
v_fma_f32 v216,v249,v199,v216
v_mfma_f32_16x16x32_bf16 a[244:247],v[148:151],v[60:63],a[244:247]
s_waitcnt vmcnt(0) lgkmcnt(0)
s_barrier
v_mfma_f32_16x16x32_bf16 a[136:139],v[128:131],v[0:3],a[136:139]
s_add_u32 m0,s70,32768
s_nop 0
buffer_load_dwordx4 v230,s[48:51],s66 offen offset:0 lds
v_mfma_f32_16x16x32_bf16 a[136:139],v[132:135],v[4:7],a[136:139]
ds_read_b128 v[144:147],v228 offset:32768
v_fma_f32 v169,v246,v168,v169
v_mfma_f32_16x16x32_bf16 a[152:155],v[128:131],v[8:11],a[152:155]
ds_read_b128 v[148:151],v229 offset:32768
v_fma_f32 v185,v246,v184,v185
v_mfma_f32_16x16x32_bf16 a[152:155],v[132:135],v[12:15],a[152:155]
v_fma_f32 v169,-v247,v184,v169
v_fma_f32 v185,v247,v168,v185
v_mfma_f32_16x16x32_bf16 a[168:171],v[128:131],v[16:19],a[168:171]
buffer_load_dwordx4 v231,s[48:51],s66 offen offset:1024 lds
v_fma_f32 v201,v248,v200,v201
v_mfma_f32_16x16x32_bf16 a[168:171],v[132:135],v[20:23],a[168:171]
v_fma_f32 v217,v248,v216,v217
v_fma_f32 v201,-v249,v216,v201
v_mfma_f32_16x16x32_bf16 a[184:187],v[128:131],v[24:27],a[184:187]
v_fma_f32 v217,v249,v200,v217
v_fma_f32 v170,v246,v169,v170
v_mfma_f32_16x16x32_bf16 a[184:187],v[132:135],v[28:31],a[184:187]
v_fma_f32 v186,v246,v185,v186
v_fma_f32 v170,-v247,v185,v170
v_mfma_f32_16x16x32_bf16 a[200:203],v[128:131],v[32:35],a[200:203]
buffer_load_dwordx4 v232,s[48:51],s66 offen offset:2048 lds
v_fma_f32 v186,v247,v169,v186
v_mfma_f32_16x16x32_bf16 a[200:203],v[132:135],v[36:39],a[200:203]
v_fma_f32 v202,v248,v201,v202
v_fma_f32 v218,v248,v217,v218
v_mfma_f32_16x16x32_bf16 a[216:219],v[128:131],v[40:43],a[216:219]
v_fma_f32 v202,-v249,v217,v202
v_fma_f32 v218,v249,v201,v218
v_mfma_f32_16x16x32_bf16 a[216:219],v[132:135],v[44:47],a[216:219]
v_fma_f32 v171,v246,v170,v171
v_fma_f32 v187,v246,v186,v187
v_mfma_f32_16x16x32_bf16 a[232:235],v[128:131],v[48:51],a[232:235]
buffer_load_dwordx4 v233,s[48:51],s66 offen offset:3072 lds
v_fma_f32 v171,-v247,v186,v171
v_mfma_f32_16x16x32_bf16 a[232:235],v[132:135],v[52:55],a[232:235]
v_fma_f32 v187,v247,v170,v187
v_fma_f32 v203,v248,v202,v203
v_mfma_f32_16x16x32_bf16 a[248:251],v[128:131],v[56:59],a[248:251]
v_fma_f32 v219,v248,v218,v219
v_fma_f32 v203,-v249,v218,v203
v_mfma_f32_16x16x32_bf16 a[248:251],v[132:135],v[60:63],a[248:251]
v_fma_f32 v219,v249,v202,v219
v_fma_f32 v172,v246,v171,v172
v_mfma_f32_16x16x32_bf16 a[140:143],v[136:139],v[0:3],a[140:143]
s_add_u32 m0,s70,36864
s_nop 0
buffer_load_dwordx4 v230,s[48:51],s67 offen offset:0 lds
v_mfma_f32_16x16x32_bf16 a[140:143],v[140:143],v[4:7],a[140:143]
ds_read_b128 v[128:131],v228 offset:34816
v_fma_f32 v188,v246,v187,v188
v_mfma_f32_16x16x32_bf16 a[156:159],v[136:139],v[8:11],a[156:159]
ds_read_b128 v[132:135],v229 offset:34816
v_fma_f32 v172,-v247,v187,v172
v_mfma_f32_16x16x32_bf16 a[156:159],v[140:143],v[12:15],a[156:159]
v_fma_f32 v188,v247,v171,v188
v_fma_f32 v204,v248,v203,v204
v_mfma_f32_16x16x32_bf16 a[172:175],v[136:139],v[16:19],a[172:175]
buffer_load_dwordx4 v231,s[48:51],s67 offen offset:1024 lds
v_fma_f32 v220,v248,v219,v220
v_mfma_f32_16x16x32_bf16 a[172:175],v[140:143],v[20:23],a[172:175]
v_fma_f32 v204,-v249,v219,v204
v_fma_f32 v220,v249,v203,v220
v_mfma_f32_16x16x32_bf16 a[188:191],v[136:139],v[24:27],a[188:191]
v_fma_f32 v173,v246,v172,v173
v_fma_f32 v189,v246,v188,v189
v_mfma_f32_16x16x32_bf16 a[188:191],v[140:143],v[28:31],a[188:191]
v_fma_f32 v173,-v247,v188,v173
v_fma_f32 v189,v247,v172,v189
v_mfma_f32_16x16x32_bf16 a[204:207],v[136:139],v[32:35],a[204:207]
buffer_load_dwordx4 v232,s[48:51],s67 offen offset:2048 lds
v_fma_f32 v205,v248,v204,v205
v_mfma_f32_16x16x32_bf16 a[204:207],v[140:143],v[36:39],a[204:207]
v_fma_f32 v221,v248,v220,v221
v_fma_f32 v205,-v249,v220,v205
v_mfma_f32_16x16x32_bf16 a[220:223],v[136:139],v[40:43],a[220:223]
v_fma_f32 v221,v249,v204,v221
v_fma_f32 v174,v246,v173,v174
v_mfma_f32_16x16x32_bf16 a[220:223],v[140:143],v[44:47],a[220:223]
v_fma_f32 v190,v246,v189,v190
v_fma_f32 v174,-v247,v189,v174
v_mfma_f32_16x16x32_bf16 a[236:239],v[136:139],v[48:51],a[236:239]
buffer_load_dwordx4 v233,s[48:51],s67 offen offset:3072 lds
v_fma_f32 v190,v247,v173,v190
v_mfma_f32_16x16x32_bf16 a[236:239],v[140:143],v[52:55],a[236:239]
v_fma_f32 v206,v248,v205,v206
v_fma_f32 v222,v248,v221,v222
v_mfma_f32_16x16x32_bf16 a[252:255],v[136:139],v[56:59],a[252:255]
v_fma_f32 v206,-v249,v221,v206
v_fma_f32 v222,v249,v205,v222
v_mfma_f32_16x16x32_bf16 a[252:255],v[140:143],v[60:63],a[252:255]
v_fma_f32 v175,v246,v174,v175
v_fma_f32 v191,v246,v190,v191
s_waitcnt lgkmcnt(3)
v_mfma_f32_16x16x32_bf16 a[0:3],v[144:147],v[64:67],a[0:3]
s_add_u32 s66,s66,128
s_add_u32 s67,s67,128
s_waitcnt lgkmcnt(2)
v_mfma_f32_16x16x32_bf16 a[0:3],v[148:151],v[68:71],a[0:3]
ds_read_b128 v[136:139],v228 offset:36864
v_fma_f32 v175,-v247,v190,v175
v_mfma_f32_16x16x32_bf16 a[16:19],v[144:147],v[72:75],a[16:19]
ds_read_b128 v[140:143],v229 offset:36864
v_fma_f32 v191,v247,v174,v191
v_mfma_f32_16x16x32_bf16 a[16:19],v[148:151],v[76:79],a[16:19]
s_add_u32 m0,s71,131072
s_nop 0
buffer_load_dwordx4 v234,s[52:55],s68 offen offset:0 lds
v_mfma_f32_16x16x32_bf16 a[32:35],v[144:147],v[80:83],a[32:35]
v_fma_f32 v207,v248,v206,v207
v_fma_f32 v223,v248,v222,v223
v_mfma_f32_16x16x32_bf16 a[32:35],v[148:151],v[84:87],a[32:35]
v_fma_f32 v207,-v249,v222,v207
v_fma_f32 v223,v249,v206,v223
v_mfma_f32_16x16x32_bf16 a[48:51],v[144:147],v[88:91],a[48:51]
v_cvt_pk_bf16_f32 v160,v160,v161
v_cvt_pk_bf16_f32 v161,v162,v163
v_mfma_f32_16x16x32_bf16 a[48:51],v[148:151],v[92:95],a[48:51]
buffer_load_dwordx4 v235,s[52:55],s68 offen offset:1024 lds
v_cvt_pk_bf16_f32 v162,v164,v165
v_mfma_f32_16x16x32_bf16 a[64:67],v[144:147],v[96:99],a[64:67]
v_cvt_pk_bf16_f32 v163,v166,v167
v_cvt_pk_bf16_f32 v164,v168,v169
v_mfma_f32_16x16x32_bf16 a[64:67],v[148:151],v[100:103],a[64:67]
v_cvt_pk_bf16_f32 v165,v170,v171
v_cvt_pk_bf16_f32 v166,v172,v173
v_mfma_f32_16x16x32_bf16 a[80:83],v[144:147],v[104:107],a[80:83]
v_cvt_pk_bf16_f32 v167,v174,v175
buffer_store_dwordx4 v[160:163],v242,s[36:39],s85 offen offset:0 sc1
v_mfma_f32_16x16x32_bf16 a[80:83],v[148:151],v[108:111],a[80:83]
buffer_load_dwordx4 v236,s[52:55],s68 offen offset:2048 lds
v_mfma_f32_16x16x32_bf16 a[96:99],v[144:147],v[112:115],a[96:99]
v_mfma_f32_16x16x32_bf16 a[96:99],v[148:151],v[116:119],a[96:99]
buffer_store_dwordx4 v[164:167],v239,s[36:39],s85 offen offset:1024 sc1
v_mfma_f32_16x16x32_bf16 a[112:115],v[144:147],v[120:123],a[112:115]
v_mfma_f32_16x16x32_bf16 a[112:115],v[148:151],v[124:127],a[112:115]
buffer_load_dwordx4 v237,s[52:55],s68 offen offset:3072 lds
s_waitcnt lgkmcnt(3)
v_mfma_f32_16x16x32_bf16 a[4:7],v[128:131],v[64:67],a[4:7]
v_cvt_pk_bf16_f32 v176,-v176,-v177
v_cvt_pk_bf16_f32 v177,-v178,-v179
s_waitcnt lgkmcnt(2)
v_mfma_f32_16x16x32_bf16 a[4:7],v[132:135],v[68:71],a[4:7]
ds_read_b128 v[144:147],v228 offset:38912
v_cvt_pk_bf16_f32 v178,-v180,-v181
v_mfma_f32_16x16x32_bf16 a[20:23],v[128:131],v[72:75],a[20:23]
ds_read_b128 v[148:151],v229 offset:38912
v_cvt_pk_bf16_f32 v179,-v182,-v183
v_mfma_f32_16x16x32_bf16 a[20:23],v[132:135],v[76:79],a[20:23]
s_add_u32 s68,s68,524288
v_cvt_pk_bf16_f32 v180,-v184,-v185
v_mfma_f32_16x16x32_bf16 a[36:39],v[128:131],v[80:83],a[36:39]
s_add_u32 m0,s70,65536
s_nop 0
)"
R"(buffer_load_dwordx4 v230,s[48:51],s66 offen offset:0 lds
v_mfma_f32_16x16x32_bf16 a[36:39],v[132:135],v[84:87],a[36:39]
v_cvt_pk_bf16_f32 v181,-v186,-v187
v_cvt_pk_bf16_f32 v182,-v188,-v189
v_mfma_f32_16x16x32_bf16 a[52:55],v[128:131],v[88:91],a[52:55]
v_cvt_pk_bf16_f32 v183,-v190,-v191
buffer_store_dwordx4 v[176:179],v242,s[36:39],s86 offen offset:0 sc1
v_mfma_f32_16x16x32_bf16 a[52:55],v[132:135],v[92:95],a[52:55]
v_mfma_f32_16x16x32_bf16 a[68:71],v[128:131],v[96:99],a[68:71]
buffer_load_dwordx4 v231,s[48:51],s66 offen offset:1024 lds
v_mfma_f32_16x16x32_bf16 a[68:71],v[132:135],v[100:103],a[68:71]
buffer_store_dwordx4 v[180:183],v239,s[36:39],s86 offen offset:1024 sc1
v_mfma_f32_16x16x32_bf16 a[84:87],v[128:131],v[104:107],a[84:87]
v_mfma_f32_16x16x32_bf16 a[84:87],v[132:135],v[108:111],a[84:87]
v_mfma_f32_16x16x32_bf16 a[100:103],v[128:131],v[112:115],a[100:103]
buffer_load_dwordx4 v232,s[48:51],s66 offen offset:2048 lds
v_cvt_pk_bf16_f32 v192,v192,v193
v_mfma_f32_16x16x32_bf16 a[100:103],v[132:135],v[116:119],a[100:103]
v_cvt_pk_bf16_f32 v193,v194,v195
v_cvt_pk_bf16_f32 v194,v196,v197
v_mfma_f32_16x16x32_bf16 a[116:119],v[128:131],v[120:123],a[116:119]
v_cvt_pk_bf16_f32 v195,v198,v199
v_cvt_pk_bf16_f32 v196,v200,v201
v_mfma_f32_16x16x32_bf16 a[116:119],v[132:135],v[124:127],a[116:119]
v_cvt_pk_bf16_f32 v197,v202,v203
v_cvt_pk_bf16_f32 v198,v204,v205
s_waitcnt lgkmcnt(3)
v_mfma_f32_16x16x32_bf16 a[8:11],v[136:139],v[64:67],a[8:11]
buffer_load_dwordx4 v233,s[48:51],s66 offen offset:3072 lds
v_cvt_pk_bf16_f32 v199,v206,v207
s_waitcnt lgkmcnt(2)
v_mfma_f32_16x16x32_bf16 a[8:11],v[140:143],v[68:71],a[8:11]
ds_read_b128 v[128:131],v228 offset:40960
buffer_store_dwordx4 v[192:195],v242,s[36:39],s85 offen offset:256 sc1
v_mfma_f32_16x16x32_bf16 a[24:27],v[136:139],v[72:75],a[24:27]
ds_read_b128 v[132:135],v229 offset:40960
v_mfma_f32_16x16x32_bf16 a[24:27],v[140:143],v[76:79],a[24:27]
v_mfma_f32_16x16x32_bf16 a[40:43],v[136:139],v[80:83],a[40:43]
s_add_u32 m0,s70,69632
s_nop 0
buffer_load_dwordx4 v230,s[48:51],s67 offen offset:0 lds
v_mfma_f32_16x16x32_bf16 a[40:43],v[140:143],v[84:87],a[40:43]
buffer_store_dwordx4 v[196:199],v239,s[36:39],s85 offen offset:1280 sc1
v_mfma_f32_16x16x32_bf16 a[56:59],v[136:139],v[88:91],a[56:59]
v_mfma_f32_16x16x32_bf16 a[56:59],v[140:143],v[92:95],a[56:59]
v_mfma_f32_16x16x32_bf16 a[72:75],v[136:139],v[96:99],a[72:75]
buffer_load_dwordx4 v231,s[48:51],s67 offen offset:1024 lds
v_cvt_pk_bf16_f32 v208,-v208,-v209
v_mfma_f32_16x16x32_bf16 a[72:75],v[140:143],v[100:103],a[72:75]
v_cvt_pk_bf16_f32 v209,-v210,-v211
v_cvt_pk_bf16_f32 v210,-v212,-v213
v_mfma_f32_16x16x32_bf16 a[88:91],v[136:139],v[104:107],a[88:91]
v_cvt_pk_bf16_f32 v211,-v214,-v215
v_cvt_pk_bf16_f32 v212,-v216,-v217
v_mfma_f32_16x16x32_bf16 a[88:91],v[140:143],v[108:111],a[88:91]
v_cvt_pk_bf16_f32 v213,-v218,-v219
v_cvt_pk_bf16_f32 v214,-v220,-v221
v_mfma_f32_16x16x32_bf16 a[104:107],v[136:139],v[112:115],a[104:107]
buffer_load_dwordx4 v232,s[48:51],s67 offen offset:2048 lds
v_cvt_pk_bf16_f32 v215,-v222,-v223
v_mfma_f32_16x16x32_bf16 a[104:107],v[140:143],v[116:119],a[104:107]
buffer_store_dwordx4 v[208:211],v242,s[36:39],s86 offen offset:256 sc1
v_mfma_f32_16x16x32_bf16 a[120:123],v[136:139],v[120:123],a[120:123]
v_mfma_f32_16x16x32_bf16 a[120:123],v[140:143],v[124:127],a[120:123]
s_waitcnt lgkmcnt(3)
v_mfma_f32_16x16x32_bf16 a[12:15],v[144:147],v[64:67],a[12:15]
buffer_load_dwordx4 v233,s[48:51],s67 offen offset:3072 lds
buffer_store_dwordx4 v[212:215],v239,s[36:39],s86 offen offset:1280 sc1
s_waitcnt lgkmcnt(2)
v_mfma_f32_16x16x32_bf16 a[12:15],v[148:151],v[68:71],a[12:15]
ds_read_b128 v[136:139],v228 offset:43008
v_mfma_f32_16x16x32_bf16 a[28:31],v[144:147],v[72:75],a[28:31]
ds_read_b128 v[140:143],v229 offset:43008
v_mfma_f32_16x16x32_bf16 a[28:31],v[148:151],v[76:79],a[28:31]
v_mfma_f32_16x16x32_bf16 a[44:47],v[144:147],v[80:83],a[44:47]
s_add_u32 s66,s66,128
s_add_u32 s67,s67,128
v_mfma_f32_16x16x32_bf16 a[44:47],v[148:151],v[84:87],a[44:47]
s_add_u32 m0,s71,147456
s_nop 0
buffer_load_dwordx4 v234,s[52:55],s68 offen offset:0 lds
v_mfma_f32_16x16x32_bf16 a[60:63],v[144:147],v[88:91],a[60:63]
v_mfma_f32_16x16x32_bf16 a[60:63],v[148:151],v[92:95],a[60:63]
v_mfma_f32_16x16x32_bf16 a[76:79],v[144:147],v[96:99],a[76:79]
v_mfma_f32_16x16x32_bf16 a[76:79],v[148:151],v[100:103],a[76:79]
buffer_load_dwordx4 v235,s[52:55],s68 offen offset:1024 lds
v_mfma_f32_16x16x32_bf16 a[92:95],v[144:147],v[104:107],a[92:95]
v_mfma_f32_16x16x32_bf16 a[92:95],v[148:151],v[108:111],a[92:95]
v_mfma_f32_16x16x32_bf16 a[108:111],v[144:147],v[112:115],a[108:111]
v_mfma_f32_16x16x32_bf16 a[108:111],v[148:151],v[116:119],a[108:111]
buffer_load_dwordx4 v236,s[52:55],s68 offen offset:2048 lds
v_mfma_f32_16x16x32_bf16 a[124:127],v[144:147],v[120:123],a[124:127]
v_mfma_f32_16x16x32_bf16 a[124:127],v[148:151],v[124:127],a[124:127]
s_waitcnt lgkmcnt(3)
v_mfma_f32_16x16x32_bf16 a[128:131],v[128:131],v[64:67],a[128:131]
s_waitcnt lgkmcnt(2)
v_mfma_f32_16x16x32_bf16 a[128:131],v[132:135],v[68:71],a[128:131]
ds_read_b128 v[144:147],v228 offset:45056
buffer_load_dwordx4 v237,s[52:55],s68 offen offset:3072 lds
v_mfma_f32_16x16x32_bf16 a[144:147],v[128:131],v[72:75],a[144:147]
ds_read_b128 v[148:151],v229 offset:45056
v_accvgpr_read_b32 v0,a0
v_mfma_f32_16x16x32_bf16 a[144:147],v[132:135],v[76:79],a[144:147]
v_accvgpr_read_b32 v1,a1
v_accvgpr_read_b32 v2,a2
v_mfma_f32_16x16x32_bf16 a[160:163],v[128:131],v[80:83],a[160:163]
v_accvgpr_read_b32 v3,a3
v_accvgpr_read_b32 v4,a4
v_mfma_f32_16x16x32_bf16 a[160:163],v[132:135],v[84:87],a[160:163]
s_add_u32 s68,s68,524288
v_accvgpr_read_b32 v5,a5
v_mfma_f32_16x16x32_bf16 a[176:179],v[128:131],v[88:91],a[176:179]
v_accvgpr_read_b32 v6,a6
v_accvgpr_read_b32 v7,a7
v_mfma_f32_16x16x32_bf16 a[176:179],v[132:135],v[92:95],a[176:179]
v_cvt_pk_bf16_f32 v0,v0,v1
v_cvt_pk_bf16_f32 v1,v2,v3
v_mfma_f32_16x16x32_bf16 a[192:195],v[128:131],v[96:99],a[192:195]
v_cvt_pk_bf16_f32 v2,v4,v5
v_cvt_pk_bf16_f32 v3,v6,v7
v_mfma_f32_16x16x32_bf16 a[192:195],v[132:135],v[100:103],a[192:195]
s_add_u32 s87,s72,0
buffer_store_dwordx4 v[0:3],v255,s[40:43],s87 offen offset:0 sc1
v_mfma_f32_16x16x32_bf16 a[208:211],v[128:131],v[104:107],a[208:211]
v_accvgpr_read_b32 v8,a16
v_accvgpr_read_b32 v9,a17
v_mfma_f32_16x16x32_bf16 a[208:211],v[132:135],v[108:111],a[208:211]
v_accvgpr_read_b32 v10,a18
v_accvgpr_read_b32 v11,a19
v_mfma_f32_16x16x32_bf16 a[224:227],v[128:131],v[112:115],a[224:227]
v_accvgpr_read_b32 v12,a20
v_accvgpr_read_b32 v13,a21
v_mfma_f32_16x16x32_bf16 a[224:227],v[132:135],v[116:119],a[224:227]
v_accvgpr_read_b32 v14,a22
v_accvgpr_read_b32 v15,a23
v_mfma_f32_16x16x32_bf16 a[240:243],v[128:131],v[120:123],a[240:243]
v_cvt_pk_bf16_f32 v8,v8,v9
v_cvt_pk_bf16_f32 v9,v10,v11
v_mfma_f32_16x16x32_bf16 a[240:243],v[132:135],v[124:127],a[240:243]
v_cvt_pk_bf16_f32 v10,v12,v13
v_cvt_pk_bf16_f32 v11,v14,v15
s_waitcnt lgkmcnt(3)
v_mfma_f32_16x16x32_bf16 a[132:135],v[136:139],v[64:67],a[132:135]
ds_read_b128 v[128:131],v228 offset:47104
ds_read_b128 v[132:135],v229 offset:47104
s_waitcnt lgkmcnt(4)
v_mfma_f32_16x16x32_bf16 a[132:135],v[140:143],v[68:71],a[132:135]
s_add_u32 s87,s72,0
buffer_store_dwordx4 v[8:11],v255,s[40:43],s87 offen offset:2048 sc1
v_mfma_f32_16x16x32_bf16 a[148:151],v[136:139],v[72:75],a[148:151]
v_accvgpr_read_b32 v16,a32
v_accvgpr_read_b32 v17,a33
v_mfma_f32_16x16x32_bf16 a[148:151],v[140:143],v[76:79],a[148:151]
v_accvgpr_read_b32 v18,a34
v_accvgpr_read_b32 v19,a35
v_mfma_f32_16x16x32_bf16 a[164:167],v[136:139],v[80:83],a[164:167]
v_accvgpr_read_b32 v20,a36
v_accvgpr_read_b32 v21,a37
v_mfma_f32_16x16x32_bf16 a[164:167],v[140:143],v[84:87],a[164:167]
v_accvgpr_read_b32 v22,a38
v_accvgpr_read_b32 v23,a39
v_mfma_f32_16x16x32_bf16 a[180:183],v[136:139],v[88:91],a[180:183]
v_cvt_pk_bf16_f32 v16,v16,v17
v_cvt_pk_bf16_f32 v17,v18,v19
v_mfma_f32_16x16x32_bf16 a[180:183],v[140:143],v[92:95],a[180:183]
v_cvt_pk_bf16_f32 v18,v20,v21
v_cvt_pk_bf16_f32 v19,v22,v23
v_mfma_f32_16x16x32_bf16 a[196:199],v[136:139],v[96:99],a[196:199]
s_add_u32 s87,s72,4096
buffer_store_dwordx4 v[16:19],v255,s[40:43],s87 offen offset:0 sc1
v_mfma_f32_16x16x32_bf16 a[196:199],v[140:143],v[100:103],a[196:199]
v_accvgpr_read_b32 v24,a48
v_accvgpr_read_b32 v25,a49
v_mfma_f32_16x16x32_bf16 a[212:215],v[136:139],v[104:107],a[212:215]
v_accvgpr_read_b32 v26,a50
v_accvgpr_read_b32 v27,a51
v_mfma_f32_16x16x32_bf16 a[212:215],v[140:143],v[108:111],a[212:215]
v_accvgpr_read_b32 v28,a52
v_accvgpr_read_b32 v29,a53
v_mfma_f32_16x16x32_bf16 a[228:231],v[136:139],v[112:115],a[228:231]
v_accvgpr_read_b32 v30,a54
v_accvgpr_read_b32 v31,a55
v_mfma_f32_16x16x32_bf16 a[228:231],v[140:143],v[116:119],a[228:231]
v_cvt_pk_bf16_f32 v24,v24,v25
v_cvt_pk_bf16_f32 v25,v26,v27
v_mfma_f32_16x16x32_bf16 a[244:247],v[136:139],v[120:123],a[244:247]
v_cvt_pk_bf16_f32 v26,v28,v29
v_cvt_pk_bf16_f32 v27,v30,v31
v_mfma_f32_16x16x32_bf16 a[244:247],v[140:143],v[124:127],a[244:247]
s_add_u32 s87,s72,4096
buffer_store_dwordx4 v[24:27],v255,s[40:43],s87 offen offset:2048 sc1
s_waitcnt lgkmcnt(3)
v_mfma_f32_16x16x32_bf16 a[136:139],v[144:147],v[64:67],a[136:139]
v_accvgpr_read_b32 v0,a64
v_accvgpr_read_b32 v1,a65
s_waitcnt lgkmcnt(2)
v_mfma_f32_16x16x32_bf16 a[136:139],v[148:151],v[68:71],a[136:139]
v_accvgpr_read_b32 v2,a66
v_accvgpr_read_b32 v3,a67
v_mfma_f32_16x16x32_bf16 a[152:155],v[144:147],v[72:75],a[152:155]
v_accvgpr_read_b32 v4,a68
v_accvgpr_read_b32 v5,a69
v_mfma_f32_16x16x32_bf16 a[152:155],v[148:151],v[76:79],a[152:155]
v_accvgpr_read_b32 v6,a70
v_accvgpr_read_b32 v7,a71
v_mfma_f32_16x16x32_bf16 a[168:171],v[144:147],v[80:83],a[168:171]
v_cvt_pk_bf16_f32 v0,v0,v1
v_cvt_pk_bf16_f32 v1,v2,v3
v_mfma_f32_16x16x32_bf16 a[168:171],v[148:151],v[84:87],a[168:171]
v_cvt_pk_bf16_f32 v2,v4,v5
v_cvt_pk_bf16_f32 v3,v6,v7
v_mfma_f32_16x16x32_bf16 a[184:187],v[144:147],v[88:91],a[184:187]
s_add_u32 s87,s72,8192
buffer_store_dwordx4 v[0:3],v255,s[40:43],s87 offen offset:0 sc1
v_mfma_f32_16x16x32_bf16 a[184:187],v[148:151],v[92:95],a[184:187]
v_accvgpr_read_b32 v8,a80
v_accvgpr_read_b32 v9,a81
v_mfma_f32_16x16x32_bf16 a[200:203],v[144:147],v[96:99],a[200:203]
s_and_b32 s60,s56,1
s_lshl_b32 s60,s60,14
v_mfma_f32_16x16x32_bf16 a[200:203],v[148:151],v[100:103],a[200:203]
s_sub_u32 s60,98304,s60
v_add_u32 v224,s60,v224
v_mfma_f32_16x16x32_bf16 a[216:219],v[144:147],v[104:107],a[216:219]
v_add_u32 v225,s60,v225
v_add_u32 v226,s60,v226
v_mfma_f32_16x16x32_bf16 a[216:219],v[148:151],v[108:111],a[216:219]
v_add_u32 v227,s60,v227
s_lshr_b32 s60,s56,1
v_mfma_f32_16x16x32_bf16 a[232:235],v[144:147],v[112:115],a[232:235]
s_lshl_b32 s60,s60,14
s_lshl_b32 s61,s56,13
v_mfma_f32_16x16x32_bf16 a[232:235],v[148:151],v[116:119],a[232:235]
s_sub_u32 s60,s60,s61
v_subrev_u32 v240,s60,v228
v_mfma_f32_16x16x32_bf16 a[248:251],v[144:147],v[120:123],a[248:251]
v_subrev_u32 v238,0x10000,v240
v_subrev_u32 v241,s60,v229
v_mfma_f32_16x16x32_bf16 a[248:251],v[148:151],v[124:127],a[248:251]
v_subrev_u32 v239,0x10000,v241
v_accvgpr_read_b32 v10,a82
s_waitcnt lgkmcnt(1)
v_mfma_f32_16x16x32_bf16 a[140:143],v[128:131],v[64:67],a[140:143]
v_accvgpr_read_b32 v11,a83
v_accvgpr_read_b32 v12,a84
s_waitcnt lgkmcnt(0)
v_mfma_f32_16x16x32_bf16 a[140:143],v[132:135],v[68:71],a[140:143]
v_accvgpr_read_b32 v13,a85
v_accvgpr_read_b32 v14,a86
v_mfma_f32_16x16x32_bf16 a[156:159],v[128:131],v[72:75],a[156:159]
v_accvgpr_read_b32 v15,a87
v_cvt_pk_bf16_f32 v8,v8,v9
v_mfma_f32_16x16x32_bf16 a[156:159],v[132:135],v[76:79],a[156:159]
v_cvt_pk_bf16_f32 v9,v10,v11
v_cvt_pk_bf16_f32 v10,v12,v13
v_mfma_f32_16x16x32_bf16 a[172:175],v[128:131],v[80:83],a[172:175]
v_cvt_pk_bf16_f32 v11,v14,v15
s_add_u32 s87,s72,8192
v_mfma_f32_16x16x32_bf16 a[172:175],v[132:135],v[84:87],a[172:175]
buffer_store_dwordx4 v[8:11],v255,s[40:43],s87 offen offset:2048 sc1
v_mfma_f32_16x16x32_bf16 a[188:191],v[128:131],v[88:91],a[188:191]
v_accvgpr_read_b32 v16,a96
v_accvgpr_read_b32 v17,a97
v_mfma_f32_16x16x32_bf16 a[188:191],v[132:135],v[92:95],a[188:191]
v_accvgpr_read_b32 v18,a98
v_accvgpr_read_b32 v19,a99
v_mfma_f32_16x16x32_bf16 a[204:207],v[128:131],v[96:99],a[204:207]
v_accvgpr_read_b32 v20,a100
v_accvgpr_read_b32 v21,a101
v_mfma_f32_16x16x32_bf16 a[204:207],v[132:135],v[100:103],a[204:207]
v_accvgpr_read_b32 v22,a102
v_accvgpr_read_b32 v23,a103
v_mfma_f32_16x16x32_bf16 a[220:223],v[128:131],v[104:107],a[220:223]
v_cvt_pk_bf16_f32 v16,v16,v17
v_cvt_pk_bf16_f32 v17,v18,v19
v_mfma_f32_16x16x32_bf16 a[220:223],v[132:135],v[108:111],a[220:223]
v_cvt_pk_bf16_f32 v18,v20,v21
v_cvt_pk_bf16_f32 v19,v22,v23
v_mfma_f32_16x16x32_bf16 a[236:239],v[128:131],v[112:115],a[236:239]
s_add_u32 s87,s72,12288
buffer_store_dwordx4 v[16:19],v255,s[40:43],s87 offen offset:0 sc1
v_mfma_f32_16x16x32_bf16 a[236:239],v[132:135],v[116:119],a[236:239]
v_accvgpr_read_b32 v24,a112
v_accvgpr_read_b32 v25,a113
v_mfma_f32_16x16x32_bf16 a[252:255],v[128:131],v[120:123],a[252:255]
v_accvgpr_read_b32 v26,a114
v_accvgpr_read_b32 v27,a115
v_mfma_f32_16x16x32_bf16 a[252:255],v[132:135],v[124:127],a[252:255]
v_accvgpr_read_b32 v28,a116
v_accvgpr_read_b32 v29,a117
v_accvgpr_read_b32 v30,a118
v_accvgpr_read_b32 v31,a119
v_cvt_pk_bf16_f32 v24,v24,v25
v_cvt_pk_bf16_f32 v25,v26,v27
v_cvt_pk_bf16_f32 v26,v28,v29
v_cvt_pk_bf16_f32 v27,v30,v31
s_add_u32 s87,s72,12288
buffer_store_dwordx4 v[24:27],v255,s[40:43],s87 offen offset:2048 sc1
s_waitcnt lgkmcnt(0)
s_barrier
L_KVE_%=:
s_add_u32 m0,s70,0
s_nop 0
buffer_load_dwordx4 v230,s[48:51],s66 offen offset:0 lds
buffer_load_dwordx4 v231,s[48:51],s66 offen offset:1024 lds
buffer_load_dwordx4 v232,s[48:51],s66 offen offset:2048 lds
buffer_load_dwordx4 v233,s[48:51],s66 offen offset:3072 lds
s_add_u32 m0,s70,4096
s_nop 0
buffer_load_dwordx4 v230,s[48:51],s67 offen offset:0 lds
buffer_load_dwordx4 v231,s[48:51],s67 offen offset:1024 lds
buffer_load_dwordx4 v232,s[48:51],s67 offen offset:2048 lds
buffer_load_dwordx4 v233,s[48:51],s67 offen offset:3072 lds
s_add_u32 s66,s66,128
s_add_u32 s67,s67,128
s_add_u32 m0,s71,98304
s_nop 0
buffer_load_dwordx4 v234,s[52:55],s68 offen offset:0 lds
buffer_load_dwordx4 v235,s[52:55],s68 offen offset:1024 lds
buffer_load_dwordx4 v236,s[52:55],s68 offen offset:2048 lds
buffer_load_dwordx4 v237,s[52:55],s68 offen offset:3072 lds
s_add_u32 s68,s68,524288
s_cmp_lg_u32 s65,0
s_cbranch_scc1 L_NBURST_%=
s_nop 3
v_accvgpr_read_b32 v0,a8
v_accvgpr_read_b32 v1,a9
v_accvgpr_read_b32 v2,a10
v_accvgpr_read_b32 v3,a11
v_accvgpr_read_b32 v4,a12
v_accvgpr_read_b32 v5,a13
v_accvgpr_read_b32 v6,a14
v_accvgpr_read_b32 v7,a15
v_cvt_pk_bf16_f32 v0,v0,v1
v_cvt_pk_bf16_f32 v1,v2,v3
v_cvt_pk_bf16_f32 v2,v4,v5
v_cvt_pk_bf16_f32 v3,v6,v7
s_add_u32 s87,s72,0
buffer_store_dwordx4 v[0:3],v255,s[40:43],s87 offen offset:1024 sc1
v_accvgpr_read_b32 v8,a24
v_accvgpr_read_b32 v9,a25
v_accvgpr_read_b32 v10,a26
v_accvgpr_read_b32 v11,a27
v_accvgpr_read_b32 v12,a28
v_accvgpr_read_b32 v13,a29
v_accvgpr_read_b32 v14,a30
v_accvgpr_read_b32 v15,a31
v_cvt_pk_bf16_f32 v8,v8,v9
v_cvt_pk_bf16_f32 v9,v10,v11
v_cvt_pk_bf16_f32 v10,v12,v13
v_cvt_pk_bf16_f32 v11,v14,v15
s_add_u32 s87,s72,0
buffer_store_dwordx4 v[8:11],v255,s[40:43],s87 offen offset:3072 sc1
v_accvgpr_read_b32 v16,a40
v_accvgpr_read_b32 v17,a41
v_accvgpr_read_b32 v18,a42
v_accvgpr_read_b32 v19,a43
v_accvgpr_read_b32 v20,a44
v_accvgpr_read_b32 v21,a45
v_accvgpr_read_b32 v22,a46
v_accvgpr_read_b32 v23,a47
v_cvt_pk_bf16_f32 v16,v16,v17
v_cvt_pk_bf16_f32 v17,v18,v19
v_cvt_pk_bf16_f32 v18,v20,v21
v_cvt_pk_bf16_f32 v19,v22,v23
s_add_u32 s87,s72,4096
buffer_store_dwordx4 v[16:19],v255,s[40:43],s87 offen offset:1024 sc1
v_accvgpr_read_b32 v24,a56
v_accvgpr_read_b32 v25,a57
v_accvgpr_read_b32 v26,a58
v_accvgpr_read_b32 v27,a59
v_accvgpr_read_b32 v28,a60
v_accvgpr_read_b32 v29,a61
v_accvgpr_read_b32 v30,a62
v_accvgpr_read_b32 v31,a63
v_cvt_pk_bf16_f32 v24,v24,v25
v_cvt_pk_bf16_f32 v25,v26,v27
v_cvt_pk_bf16_f32 v26,v28,v29
v_cvt_pk_bf16_f32 v27,v30,v31
s_add_u32 s87,s72,4096
buffer_store_dwordx4 v[24:27],v255,s[40:43],s87 offen offset:3072 sc1
v_accvgpr_read_b32 v32,a72
v_accvgpr_read_b32 v33,a73
v_accvgpr_read_b32 v34,a74
v_accvgpr_read_b32 v35,a75
v_accvgpr_read_b32 v36,a76
v_accvgpr_read_b32 v37,a77
v_accvgpr_read_b32 v38,a78
v_accvgpr_read_b32 v39,a79
v_cvt_pk_bf16_f32 v32,v32,v33
v_cvt_pk_bf16_f32 v33,v34,v35
v_cvt_pk_bf16_f32 v34,v36,v37
v_cvt_pk_bf16_f32 v35,v38,v39
s_add_u32 s87,s72,8192
buffer_store_dwordx4 v[32:35],v255,s[40:43],s87 offen offset:1024 sc1
v_accvgpr_read_b32 v40,a88
v_accvgpr_read_b32 v41,a89
v_accvgpr_read_b32 v42,a90
v_accvgpr_read_b32 v43,a91
v_accvgpr_read_b32 v44,a92
)"
R"(v_accvgpr_read_b32 v45,a93
v_accvgpr_read_b32 v46,a94
v_accvgpr_read_b32 v47,a95
v_cvt_pk_bf16_f32 v40,v40,v41
v_cvt_pk_bf16_f32 v41,v42,v43
v_cvt_pk_bf16_f32 v42,v44,v45
v_cvt_pk_bf16_f32 v43,v46,v47
s_add_u32 s87,s72,8192
buffer_store_dwordx4 v[40:43],v255,s[40:43],s87 offen offset:3072 sc1
v_accvgpr_read_b32 v48,a104
v_accvgpr_read_b32 v49,a105
v_accvgpr_read_b32 v50,a106
v_accvgpr_read_b32 v51,a107
v_accvgpr_read_b32 v52,a108
v_accvgpr_read_b32 v53,a109
v_accvgpr_read_b32 v54,a110
v_accvgpr_read_b32 v55,a111
v_cvt_pk_bf16_f32 v48,v48,v49
v_cvt_pk_bf16_f32 v49,v50,v51
v_cvt_pk_bf16_f32 v50,v52,v53
v_cvt_pk_bf16_f32 v51,v54,v55
s_add_u32 s87,s72,12288
buffer_store_dwordx4 v[48:51],v255,s[40:43],s87 offen offset:1024 sc1
v_accvgpr_read_b32 v56,a120
v_accvgpr_read_b32 v57,a121
v_accvgpr_read_b32 v58,a122
v_accvgpr_read_b32 v59,a123
v_accvgpr_read_b32 v60,a124
v_accvgpr_read_b32 v61,a125
v_accvgpr_read_b32 v62,a126
v_accvgpr_read_b32 v63,a127
v_cvt_pk_bf16_f32 v56,v56,v57
v_cvt_pk_bf16_f32 v57,v58,v59
v_cvt_pk_bf16_f32 v58,v60,v61
v_cvt_pk_bf16_f32 v59,v62,v63
s_add_u32 s87,s72,12288
buffer_store_dwordx4 v[56:59],v255,s[40:43],s87 offen offset:3072 sc1
v_accvgpr_read_b32 v0,a128
v_accvgpr_read_b32 v1,a129
v_accvgpr_read_b32 v2,a130
v_accvgpr_read_b32 v3,a131
v_accvgpr_read_b32 v4,a132
v_accvgpr_read_b32 v5,a133
v_accvgpr_read_b32 v6,a134
v_accvgpr_read_b32 v7,a135
v_cvt_pk_bf16_f32 v0,v0,v1
v_cvt_pk_bf16_f32 v1,v2,v3
v_cvt_pk_bf16_f32 v2,v4,v5
v_cvt_pk_bf16_f32 v3,v6,v7
s_add_u32 s87,s72,32768
buffer_store_dwordx4 v[0:3],v255,s[40:43],s87 offen offset:0 sc1
v_accvgpr_read_b32 v8,a136
v_accvgpr_read_b32 v9,a137
v_accvgpr_read_b32 v10,a138
v_accvgpr_read_b32 v11,a139
v_accvgpr_read_b32 v12,a140
v_accvgpr_read_b32 v13,a141
v_accvgpr_read_b32 v14,a142
v_accvgpr_read_b32 v15,a143
v_cvt_pk_bf16_f32 v8,v8,v9
v_cvt_pk_bf16_f32 v9,v10,v11
v_cvt_pk_bf16_f32 v10,v12,v13
v_cvt_pk_bf16_f32 v11,v14,v15
s_add_u32 s87,s72,32768
buffer_store_dwordx4 v[8:11],v255,s[40:43],s87 offen offset:1024 sc1
v_accvgpr_read_b32 v16,a144
v_accvgpr_read_b32 v17,a145
v_accvgpr_read_b32 v18,a146
v_accvgpr_read_b32 v19,a147
v_accvgpr_read_b32 v20,a148
v_accvgpr_read_b32 v21,a149
v_accvgpr_read_b32 v22,a150
v_accvgpr_read_b32 v23,a151
v_cvt_pk_bf16_f32 v16,v16,v17
v_cvt_pk_bf16_f32 v17,v18,v19
v_cvt_pk_bf16_f32 v18,v20,v21
v_cvt_pk_bf16_f32 v19,v22,v23
s_add_u32 s87,s72,32768
buffer_store_dwordx4 v[16:19],v255,s[40:43],s87 offen offset:2048 sc1
v_accvgpr_read_b32 v24,a152
v_accvgpr_read_b32 v25,a153
v_accvgpr_read_b32 v26,a154
v_accvgpr_read_b32 v27,a155
v_accvgpr_read_b32 v28,a156
v_accvgpr_read_b32 v29,a157
v_accvgpr_read_b32 v30,a158
v_accvgpr_read_b32 v31,a159
v_cvt_pk_bf16_f32 v24,v24,v25
v_cvt_pk_bf16_f32 v25,v26,v27
v_cvt_pk_bf16_f32 v26,v28,v29
v_cvt_pk_bf16_f32 v27,v30,v31
s_add_u32 s87,s72,32768
buffer_store_dwordx4 v[24:27],v255,s[40:43],s87 offen offset:3072 sc1
v_accvgpr_read_b32 v32,a160
v_accvgpr_read_b32 v33,a161
v_accvgpr_read_b32 v34,a162
v_accvgpr_read_b32 v35,a163
v_accvgpr_read_b32 v36,a164
v_accvgpr_read_b32 v37,a165
v_accvgpr_read_b32 v38,a166
v_accvgpr_read_b32 v39,a167
v_cvt_pk_bf16_f32 v32,v32,v33
v_cvt_pk_bf16_f32 v33,v34,v35
v_cvt_pk_bf16_f32 v34,v36,v37
v_cvt_pk_bf16_f32 v35,v38,v39
s_add_u32 s87,s72,36864
buffer_store_dwordx4 v[32:35],v255,s[40:43],s87 offen offset:0 sc1
v_accvgpr_read_b32 v40,a168
v_accvgpr_read_b32 v41,a169
v_accvgpr_read_b32 v42,a170
v_accvgpr_read_b32 v43,a171
v_accvgpr_read_b32 v44,a172
v_accvgpr_read_b32 v45,a173
v_accvgpr_read_b32 v46,a174
v_accvgpr_read_b32 v47,a175
v_cvt_pk_bf16_f32 v40,v40,v41
v_cvt_pk_bf16_f32 v41,v42,v43
v_cvt_pk_bf16_f32 v42,v44,v45
v_cvt_pk_bf16_f32 v43,v46,v47
s_add_u32 s87,s72,36864
buffer_store_dwordx4 v[40:43],v255,s[40:43],s87 offen offset:1024 sc1
v_accvgpr_read_b32 v48,a176
v_accvgpr_read_b32 v49,a177
v_accvgpr_read_b32 v50,a178
v_accvgpr_read_b32 v51,a179
v_accvgpr_read_b32 v52,a180
v_accvgpr_read_b32 v53,a181
v_accvgpr_read_b32 v54,a182
v_accvgpr_read_b32 v55,a183
v_cvt_pk_bf16_f32 v48,v48,v49
v_cvt_pk_bf16_f32 v49,v50,v51
v_cvt_pk_bf16_f32 v50,v52,v53
v_cvt_pk_bf16_f32 v51,v54,v55
s_add_u32 s87,s72,36864
buffer_store_dwordx4 v[48:51],v255,s[40:43],s87 offen offset:2048 sc1
v_accvgpr_read_b32 v56,a184
v_accvgpr_read_b32 v57,a185
v_accvgpr_read_b32 v58,a186
v_accvgpr_read_b32 v59,a187
v_accvgpr_read_b32 v60,a188
v_accvgpr_read_b32 v61,a189
v_accvgpr_read_b32 v62,a190
v_accvgpr_read_b32 v63,a191
v_cvt_pk_bf16_f32 v56,v56,v57
v_cvt_pk_bf16_f32 v57,v58,v59
v_cvt_pk_bf16_f32 v58,v60,v61
v_cvt_pk_bf16_f32 v59,v62,v63
s_add_u32 s87,s72,36864
buffer_store_dwordx4 v[56:59],v255,s[40:43],s87 offen offset:3072 sc1
v_accvgpr_read_b32 v0,a192
v_accvgpr_read_b32 v1,a193
v_accvgpr_read_b32 v2,a194
v_accvgpr_read_b32 v3,a195
v_accvgpr_read_b32 v4,a196
v_accvgpr_read_b32 v5,a197
v_accvgpr_read_b32 v6,a198
v_accvgpr_read_b32 v7,a199
v_cvt_pk_bf16_f32 v0,v0,v1
v_cvt_pk_bf16_f32 v1,v2,v3
v_cvt_pk_bf16_f32 v2,v4,v5
v_cvt_pk_bf16_f32 v3,v6,v7
s_add_u32 s87,s72,40960
buffer_store_dwordx4 v[0:3],v255,s[40:43],s87 offen offset:0 sc1
v_accvgpr_read_b32 v8,a200
v_accvgpr_read_b32 v9,a201
v_accvgpr_read_b32 v10,a202
v_accvgpr_read_b32 v11,a203
v_accvgpr_read_b32 v12,a204
v_accvgpr_read_b32 v13,a205
v_accvgpr_read_b32 v14,a206
v_accvgpr_read_b32 v15,a207
v_cvt_pk_bf16_f32 v8,v8,v9
v_cvt_pk_bf16_f32 v9,v10,v11
v_cvt_pk_bf16_f32 v10,v12,v13
v_cvt_pk_bf16_f32 v11,v14,v15
s_add_u32 s87,s72,40960
buffer_store_dwordx4 v[8:11],v255,s[40:43],s87 offen offset:1024 sc1
v_accvgpr_read_b32 v16,a208
v_accvgpr_read_b32 v17,a209
v_accvgpr_read_b32 v18,a210
v_accvgpr_read_b32 v19,a211
v_accvgpr_read_b32 v20,a212
v_accvgpr_read_b32 v21,a213
v_accvgpr_read_b32 v22,a214
v_accvgpr_read_b32 v23,a215
v_cvt_pk_bf16_f32 v16,v16,v17
v_cvt_pk_bf16_f32 v17,v18,v19
v_cvt_pk_bf16_f32 v18,v20,v21
v_cvt_pk_bf16_f32 v19,v22,v23
s_add_u32 s87,s72,40960
buffer_store_dwordx4 v[16:19],v255,s[40:43],s87 offen offset:2048 sc1
v_accvgpr_read_b32 v24,a216
v_accvgpr_read_b32 v25,a217
v_accvgpr_read_b32 v26,a218
v_accvgpr_read_b32 v27,a219
v_accvgpr_read_b32 v28,a220
v_accvgpr_read_b32 v29,a221
v_accvgpr_read_b32 v30,a222
v_accvgpr_read_b32 v31,a223
v_cvt_pk_bf16_f32 v24,v24,v25
v_cvt_pk_bf16_f32 v25,v26,v27
v_cvt_pk_bf16_f32 v26,v28,v29
v_cvt_pk_bf16_f32 v27,v30,v31
s_add_u32 s87,s72,40960
buffer_store_dwordx4 v[24:27],v255,s[40:43],s87 offen offset:3072 sc1
v_accvgpr_read_b32 v32,a224
v_accvgpr_read_b32 v33,a225
v_accvgpr_read_b32 v34,a226
v_accvgpr_read_b32 v35,a227
v_accvgpr_read_b32 v36,a228
v_accvgpr_read_b32 v37,a229
v_accvgpr_read_b32 v38,a230
v_accvgpr_read_b32 v39,a231
v_cvt_pk_bf16_f32 v32,v32,v33
v_cvt_pk_bf16_f32 v33,v34,v35
v_cvt_pk_bf16_f32 v34,v36,v37
v_cvt_pk_bf16_f32 v35,v38,v39
s_add_u32 s87,s72,45056
buffer_store_dwordx4 v[32:35],v255,s[40:43],s87 offen offset:0 sc1
v_accvgpr_read_b32 v40,a232
v_accvgpr_read_b32 v41,a233
v_accvgpr_read_b32 v42,a234
v_accvgpr_read_b32 v43,a235
v_accvgpr_read_b32 v44,a236
v_accvgpr_read_b32 v45,a237
v_accvgpr_read_b32 v46,a238
v_accvgpr_read_b32 v47,a239
v_cvt_pk_bf16_f32 v40,v40,v41
v_cvt_pk_bf16_f32 v41,v42,v43
v_cvt_pk_bf16_f32 v42,v44,v45
v_cvt_pk_bf16_f32 v43,v46,v47
s_add_u32 s87,s72,45056
buffer_store_dwordx4 v[40:43],v255,s[40:43],s87 offen offset:1024 sc1
v_accvgpr_read_b32 v48,a240
v_accvgpr_read_b32 v49,a241
v_accvgpr_read_b32 v50,a242
v_accvgpr_read_b32 v51,a243
v_accvgpr_read_b32 v52,a244
v_accvgpr_read_b32 v53,a245
v_accvgpr_read_b32 v54,a246
v_accvgpr_read_b32 v55,a247
v_cvt_pk_bf16_f32 v48,v48,v49
v_cvt_pk_bf16_f32 v49,v50,v51
v_cvt_pk_bf16_f32 v50,v52,v53
v_cvt_pk_bf16_f32 v51,v54,v55
s_add_u32 s87,s72,45056
buffer_store_dwordx4 v[48:51],v255,s[40:43],s87 offen offset:2048 sc1
v_accvgpr_read_b32 v56,a248
v_accvgpr_read_b32 v57,a249
v_accvgpr_read_b32 v58,a250
v_accvgpr_read_b32 v59,a251
v_accvgpr_read_b32 v60,a252
v_accvgpr_read_b32 v61,a253
v_accvgpr_read_b32 v62,a254
v_accvgpr_read_b32 v63,a255
v_cvt_pk_bf16_f32 v56,v56,v57
v_cvt_pk_bf16_f32 v57,v58,v59
v_cvt_pk_bf16_f32 v58,v60,v61
v_cvt_pk_bf16_f32 v59,v62,v63
s_add_u32 s87,s72,45056
buffer_store_dwordx4 v[56:59],v255,s[40:43],s87 offen offset:3072 sc1
s_mov_b32 s42,0
L_NBURST_%=:
s_waitcnt vmcnt(25) lgkmcnt(0)
s_barrier
ds_read_b128 v[128:131],v238 offset:32768
ds_read_b128 v[132:135],v239 offset:32768
ds_read_b128 v[136:139],v238 offset:34816
ds_read_b128 v[140:143],v239 offset:34816
ds_read_b128 v[144:147],v238 offset:36864
ds_read_b128 v[148:151],v239 offset:36864
ds_read_b128 v[152:155],v238 offset:38912
ds_read_b128 v[156:159],v239 offset:38912
ds_read_b64_tr_b16 v[192:193],v224 offset:32768
ds_read_b64_tr_b16 v[194:195],v224 offset:33280
ds_read_b64_tr_b16 v[196:197],v224 offset:36864
ds_read_b64_tr_b16 v[198:199],v224 offset:37376
ds_read_b64_tr_b16 v[200:201],v225 offset:32768
ds_read_b64_tr_b16 v[202:203],v225 offset:33280
ds_read_b64_tr_b16 v[204:205],v225 offset:36864
ds_read_b64_tr_b16 v[206:207],v225 offset:37376
s_waitcnt lgkmcnt(0)
s_barrier
v_mfma_f32_16x16x32_bf16 v[0:3],v[192:195],v[128:131],0
v_mfma_f32_16x16x32_bf16 v[0:3],v[196:199],v[132:135],v[0:3]
ds_read_b64_tr_b16 v[208:209],v226 offset:32768
v_mfma_f32_16x16x32_bf16 v[32:35],v[192:195],v[136:139],0
ds_read_b64_tr_b16 v[210:211],v226 offset:33280
ds_read_b128 v[160:163],v240 offset:0
v_mfma_f32_16x16x32_bf16 v[32:35],v[196:199],v[140:143],v[32:35]
ds_read_b64_tr_b16 v[212:213],v226 offset:36864
v_mfma_f32_16x16x32_bf16 v[64:67],v[192:195],v[144:147],0
ds_read_b64_tr_b16 v[214:215],v226 offset:37376
v_mfma_f32_16x16x32_bf16 v[64:67],v[196:199],v[148:151],v[64:67]
s_add_u32 m0,s70,32768
s_nop 0
buffer_load_dwordx4 v230,s[48:51],s66 offen offset:0 lds
v_mfma_f32_16x16x32_bf16 v[96:99],v[192:195],v[152:155],0
ds_read_b128 v[164:167],v241 offset:0
v_mfma_f32_16x16x32_bf16 v[96:99],v[196:199],v[156:159],v[96:99]
v_mfma_f32_16x16x32_bf16 v[4:7],v[200:203],v[128:131],0
v_mfma_f32_16x16x32_bf16 v[4:7],v[204:207],v[132:135],v[4:7]
ds_read_b64_tr_b16 v[216:217],v227 offset:32768
buffer_load_dwordx4 v231,s[48:51],s66 offen offset:1024 lds
v_mfma_f32_16x16x32_bf16 v[36:39],v[200:203],v[136:139],0
ds_read_b64_tr_b16 v[218:219],v227 offset:33280
v_mfma_f32_16x16x32_bf16 v[36:39],v[204:207],v[140:143],v[36:39]
ds_read_b64_tr_b16 v[220:221],v227 offset:36864
ds_read_b128 v[168:171],v240 offset:2048
v_mfma_f32_16x16x32_bf16 v[68:71],v[200:203],v[144:147],0
ds_read_b64_tr_b16 v[222:223],v227 offset:37376
v_mfma_f32_16x16x32_bf16 v[68:71],v[204:207],v[148:151],v[68:71]
buffer_load_dwordx4 v232,s[48:51],s66 offen offset:2048 lds
v_mfma_f32_16x16x32_bf16 v[100:103],v[200:203],v[152:155],0
v_mfma_f32_16x16x32_bf16 v[100:103],v[204:207],v[156:159],v[100:103]
ds_read_b128 v[172:175],v241 offset:2048
s_waitcnt lgkmcnt(10)
v_mfma_f32_16x16x32_bf16 v[8:11],v[208:211],v[128:131],0
s_waitcnt lgkmcnt(7)
v_mfma_f32_16x16x32_bf16 v[8:11],v[212:215],v[132:135],v[8:11]
ds_read_b64_tr_b16 v[192:193],v224 offset:40960
buffer_load_dwordx4 v233,s[48:51],s66 offen offset:3072 lds
v_mfma_f32_16x16x32_bf16 v[40:43],v[208:211],v[136:139],0
ds_read_b64_tr_b16 v[194:195],v224 offset:41472
v_mfma_f32_16x16x32_bf16 v[40:43],v[212:215],v[140:143],v[40:43]
ds_read_b64_tr_b16 v[196:197],v224 offset:45056
v_mfma_f32_16x16x32_bf16 v[72:75],v[208:211],v[144:147],0
ds_read_b64_tr_b16 v[198:199],v224 offset:45568
ds_read_b128 v[176:179],v240 offset:4096
v_mfma_f32_16x16x32_bf16 v[72:75],v[212:215],v[148:151],v[72:75]
s_add_u32 m0,s70,36864
s_nop 0
buffer_load_dwordx4 v230,s[48:51],s67 offen offset:0 lds
v_mfma_f32_16x16x32_bf16 v[104:107],v[208:211],v[152:155],0
v_mfma_f32_16x16x32_bf16 v[104:107],v[212:215],v[156:159],v[104:107]
s_waitcnt lgkmcnt(9)
v_mfma_f32_16x16x32_bf16 v[12:15],v[216:219],v[128:131],0
v_accvgpr_read_b32 v246,a8
v_accvgpr_read_b32 v247,a9
s_waitcnt lgkmcnt(6)
v_mfma_f32_16x16x32_bf16 v[12:15],v[220:223],v[132:135],v[12:15]
ds_read_b64_tr_b16 v[200:201],v225 offset:40960
ds_read_b128 v[180:183],v241 offset:4096
v_mfma_f32_16x16x32_bf16 v[44:47],v[216:219],v[136:139],0
ds_read_b64_tr_b16 v[202:203],v225 offset:41472
buffer_load_dwordx4 v231,s[48:51],s67 offen offset:1024 lds
v_mfma_f32_16x16x32_bf16 v[44:47],v[220:223],v[140:143],v[44:47]
ds_read_b64_tr_b16 v[204:205],v225 offset:45056
v_accvgpr_read_b32 v248,a10
v_mfma_f32_16x16x32_bf16 v[76:79],v[216:219],v[144:147],0
ds_read_b64_tr_b16 v[206:207],v225 offset:45568
v_accvgpr_read_b32 v249,a11
v_mfma_f32_16x16x32_bf16 v[76:79],v[220:223],v[148:151],v[76:79]
ds_read_b128 v[184:187],v240 offset:6144
v_accvgpr_read_b32 v250,a12
v_mfma_f32_16x16x32_bf16 v[108:111],v[216:219],v[152:155],0
buffer_load_dwordx4 v232,s[48:51],s67 offen offset:2048 lds
v_accvgpr_read_b32 v251,a13
v_mfma_f32_16x16x32_bf16 v[108:111],v[220:223],v[156:159],v[108:111]
v_accvgpr_read_b32 v252,a14
v_accvgpr_read_b32 v253,a15
s_waitcnt lgkmcnt(9)
v_mfma_f32_16x16x32_bf16 v[16:19],v[192:195],v[128:131],0
v_cvt_pk_bf16_f32 v246,v246,v247
v_cvt_pk_bf16_f32 v247,v248,v249
s_waitcnt lgkmcnt(7)
v_mfma_f32_16x16x32_bf16 v[16:19],v[196:199],v[132:135],v[16:19]
ds_read_b64_tr_b16 v[208:209],v226 offset:40960
v_cvt_pk_bf16_f32 v248,v250,v251
v_mfma_f32_16x16x32_bf16 v[48:51],v[192:195],v[136:139],0
ds_read_b64_tr_b16 v[210:211],v226 offset:41472
ds_read_b128 v[188:191],v241 offset:6144
v_mfma_f32_16x16x32_bf16 v[48:51],v[196:199],v[140:143],v[48:51]
ds_read_b64_tr_b16 v[212:213],v226 offset:45056
buffer_load_dwordx4 v233,s[48:51],s67 offen offset:3072 lds
v_mfma_f32_16x16x32_bf16 v[80:83],v[192:195],v[144:147],0
ds_read_b64_tr_b16 v[214:215],v226 offset:45568
v_cvt_pk_bf16_f32 v249,v252,v253
v_mfma_f32_16x16x32_bf16 v[80:83],v[196:199],v[148:151],v[80:83]
s_add_u32 s87,s72,0
buffer_store_dwordx4 v[246:249],v255,s[40:43],s87 offen offset:1024 sc1
v_mfma_f32_16x16x32_bf16 v[112:115],v[192:195],v[152:155],0
v_mfma_f32_16x16x32_bf16 v[112:115],v[196:199],v[156:159],v[112:115]
s_add_u32 s66,s66,128
s_add_u32 s67,s67,128
s_waitcnt lgkmcnt(8)
v_mfma_f32_16x16x32_bf16 v[20:23],v[200:203],v[128:131],0
ds_read_b64_tr_b16 v[216:217],v227 offset:40960
ds_read_b64_tr_b16 v[218:219],v227 offset:41472
s_waitcnt lgkmcnt(8)
v_mfma_f32_16x16x32_bf16 v[20:23],v[204:207],v[132:135],v[20:23]
ds_read_b64_tr_b16 v[220:221],v227 offset:45056
ds_read_b64_tr_b16 v[222:223],v227 offset:45568
v_mfma_f32_16x16x32_bf16 v[52:55],v[200:203],v[136:139],0
v_accvgpr_read_b32 v246,a24
v_accvgpr_read_b32 v247,a25
v_mfma_f32_16x16x32_bf16 v[52:55],v[204:207],v[140:143],v[52:55]
v_accvgpr_read_b32 v248,a26
v_accvgpr_read_b32 v249,a27
v_mfma_f32_16x16x32_bf16 v[84:87],v[200:203],v[144:147],0
)"
R"(v_accvgpr_read_b32 v250,a28
v_accvgpr_read_b32 v251,a29
v_mfma_f32_16x16x32_bf16 v[84:87],v[204:207],v[148:151],v[84:87]
v_accvgpr_read_b32 v252,a30
v_accvgpr_read_b32 v253,a31
v_mfma_f32_16x16x32_bf16 v[116:119],v[200:203],v[152:155],0
v_cvt_pk_bf16_f32 v246,v246,v247
v_cvt_pk_bf16_f32 v247,v248,v249
v_mfma_f32_16x16x32_bf16 v[116:119],v[204:207],v[156:159],v[116:119]
s_waitcnt vmcnt(10) lgkmcnt(0)
s_barrier
v_mfma_f32_16x16x32_bf16 v[24:27],v[208:211],v[128:131],0
s_add_u32 m0,s71,131072
s_nop 0
buffer_load_dwordx4 v234,s[52:55],s68 offen offset:0 lds
v_mfma_f32_16x16x32_bf16 v[24:27],v[212:215],v[132:135],v[24:27]
ds_read_b64_tr_b16 v[192:193],v224 offset:49152
v_cvt_pk_bf16_f32 v248,v250,v251
v_mfma_f32_16x16x32_bf16 v[56:59],v[208:211],v[136:139],0
ds_read_b64_tr_b16 v[194:195],v224 offset:49664
v_cvt_pk_bf16_f32 v249,v252,v253
v_mfma_f32_16x16x32_bf16 v[56:59],v[212:215],v[140:143],v[56:59]
ds_read_b64_tr_b16 v[196:197],v224 offset:53248
s_add_u32 s87,s72,0
v_mfma_f32_16x16x32_bf16 v[88:91],v[208:211],v[144:147],0
ds_read_b64_tr_b16 v[198:199],v224 offset:53760
buffer_load_dwordx4 v235,s[52:55],s68 offen offset:1024 lds
v_mfma_f32_16x16x32_bf16 v[88:91],v[212:215],v[148:151],v[88:91]
buffer_store_dwordx4 v[246:249],v255,s[40:43],s87 offen offset:3072 sc1
v_mfma_f32_16x16x32_bf16 v[120:123],v[208:211],v[152:155],0
v_mfma_f32_16x16x32_bf16 v[120:123],v[212:215],v[156:159],v[120:123]
v_accvgpr_read_b32 v246,a40
v_accvgpr_read_b32 v247,a41
v_mfma_f32_16x16x32_bf16 v[28:31],v[216:219],v[128:131],0
buffer_load_dwordx4 v236,s[52:55],s68 offen offset:2048 lds
v_accvgpr_read_b32 v248,a42
v_mfma_f32_16x16x32_bf16 v[28:31],v[220:223],v[132:135],v[28:31]
ds_read_b64_tr_b16 v[200:201],v225 offset:49152
v_accvgpr_read_b32 v249,a43
v_mfma_f32_16x16x32_bf16 v[60:63],v[216:219],v[136:139],0
ds_read_b64_tr_b16 v[202:203],v225 offset:49664
v_accvgpr_read_b32 v250,a44
v_mfma_f32_16x16x32_bf16 v[60:63],v[220:223],v[140:143],v[60:63]
ds_read_b64_tr_b16 v[204:205],v225 offset:53248
v_accvgpr_read_b32 v251,a45
v_mfma_f32_16x16x32_bf16 v[92:95],v[216:219],v[144:147],0
ds_read_b64_tr_b16 v[206:207],v225 offset:53760
buffer_load_dwordx4 v237,s[52:55],s68 offen offset:3072 lds
v_mfma_f32_16x16x32_bf16 v[92:95],v[220:223],v[148:151],v[92:95]
v_accvgpr_read_b32 v252,a46
v_accvgpr_read_b32 v253,a47
v_mfma_f32_16x16x32_bf16 v[124:127],v[216:219],v[152:155],0
v_cvt_pk_bf16_f32 v246,v246,v247
v_cvt_pk_bf16_f32 v247,v248,v249
v_mfma_f32_16x16x32_bf16 v[124:127],v[220:223],v[156:159],v[124:127]
s_add_u32 s68,s68,524288
v_cvt_pk_bf16_f32 v248,v250,v251
s_waitcnt lgkmcnt(6)
v_mfma_f32_16x16x32_bf16 v[0:3],v[192:195],v[160:163],v[0:3]
v_cvt_pk_bf16_f32 v249,v252,v253
s_add_u32 s87,s72,4096
s_waitcnt lgkmcnt(4)
v_mfma_f32_16x16x32_bf16 v[0:3],v[196:199],v[164:167],v[0:3]
ds_read_b64_tr_b16 v[208:209],v226 offset:49152
buffer_store_dwordx4 v[246:249],v255,s[40:43],s87 offen offset:1024 sc1
v_mfma_f32_16x16x32_bf16 v[32:35],v[192:195],v[168:171],v[32:35]
ds_read_b64_tr_b16 v[210:211],v226 offset:49664
ds_read_b128 v[128:131],v238 offset:0
v_mfma_f32_16x16x32_bf16 v[32:35],v[196:199],v[172:175],v[32:35]
ds_read_b64_tr_b16 v[212:213],v226 offset:53248
v_mfma_f32_16x16x32_bf16 v[64:67],v[192:195],v[176:179],v[64:67]
ds_read_b64_tr_b16 v[214:215],v226 offset:53760
v_mfma_f32_16x16x32_bf16 v[64:67],v[196:199],v[180:183],v[64:67]
s_add_u32 m0,s70,65536
s_nop 0
buffer_load_dwordx4 v230,s[48:51],s66 offen offset:0 lds
v_mfma_f32_16x16x32_bf16 v[96:99],v[192:195],v[184:187],v[96:99]
ds_read_b128 v[132:135],v239 offset:0
v_mfma_f32_16x16x32_bf16 v[96:99],v[196:199],v[188:191],v[96:99]
s_waitcnt lgkmcnt(8)
v_mfma_f32_16x16x32_bf16 v[4:7],v[200:203],v[160:163],v[4:7]
s_waitcnt lgkmcnt(6)
v_mfma_f32_16x16x32_bf16 v[4:7],v[204:207],v[164:167],v[4:7]
ds_read_b64_tr_b16 v[216:217],v227 offset:49152
buffer_load_dwordx4 v231,s[48:51],s66 offen offset:1024 lds
v_mfma_f32_16x16x32_bf16 v[36:39],v[200:203],v[168:171],v[36:39]
ds_read_b64_tr_b16 v[218:219],v227 offset:49664
v_mfma_f32_16x16x32_bf16 v[36:39],v[204:207],v[172:175],v[36:39]
ds_read_b64_tr_b16 v[220:221],v227 offset:53248
ds_read_b128 v[136:139],v238 offset:2048
v_mfma_f32_16x16x32_bf16 v[68:71],v[200:203],v[176:179],v[68:71]
ds_read_b64_tr_b16 v[222:223],v227 offset:53760
v_mfma_f32_16x16x32_bf16 v[68:71],v[204:207],v[180:183],v[68:71]
buffer_load_dwordx4 v232,s[48:51],s66 offen offset:2048 lds
v_mfma_f32_16x16x32_bf16 v[100:103],v[200:203],v[184:187],v[100:103]
v_mfma_f32_16x16x32_bf16 v[100:103],v[204:207],v[188:191],v[100:103]
ds_read_b128 v[140:143],v239 offset:2048
s_waitcnt lgkmcnt(10)
v_mfma_f32_16x16x32_bf16 v[8:11],v[208:211],v[160:163],v[8:11]
s_waitcnt lgkmcnt(7)
v_mfma_f32_16x16x32_bf16 v[8:11],v[212:215],v[164:167],v[8:11]
ds_read_b64_tr_b16 v[192:193],v224 offset:57344
buffer_load_dwordx4 v233,s[48:51],s66 offen offset:3072 lds
v_mfma_f32_16x16x32_bf16 v[40:43],v[208:211],v[168:171],v[40:43]
ds_read_b64_tr_b16 v[194:195],v224 offset:57856
v_mfma_f32_16x16x32_bf16 v[40:43],v[212:215],v[172:175],v[40:43]
ds_read_b64_tr_b16 v[196:197],v224 offset:61440
v_mfma_f32_16x16x32_bf16 v[72:75],v[208:211],v[176:179],v[72:75]
ds_read_b64_tr_b16 v[198:199],v224 offset:61952
ds_read_b128 v[144:147],v238 offset:4096
v_mfma_f32_16x16x32_bf16 v[72:75],v[212:215],v[180:183],v[72:75]
s_add_u32 m0,s70,69632
s_nop 0
buffer_load_dwordx4 v230,s[48:51],s67 offen offset:0 lds
v_mfma_f32_16x16x32_bf16 v[104:107],v[208:211],v[184:187],v[104:107]
v_mfma_f32_16x16x32_bf16 v[104:107],v[212:215],v[188:191],v[104:107]
s_waitcnt lgkmcnt(9)
v_mfma_f32_16x16x32_bf16 v[12:15],v[216:219],v[160:163],v[12:15]
v_accvgpr_read_b32 v246,a56
v_accvgpr_read_b32 v247,a57
s_waitcnt lgkmcnt(6)
v_mfma_f32_16x16x32_bf16 v[12:15],v[220:223],v[164:167],v[12:15]
ds_read_b64_tr_b16 v[200:201],v225 offset:57344
ds_read_b128 v[148:151],v239 offset:4096
v_mfma_f32_16x16x32_bf16 v[44:47],v[216:219],v[168:171],v[44:47]
ds_read_b64_tr_b16 v[202:203],v225 offset:57856
buffer_load_dwordx4 v231,s[48:51],s67 offen offset:1024 lds
v_mfma_f32_16x16x32_bf16 v[44:47],v[220:223],v[172:175],v[44:47]
ds_read_b64_tr_b16 v[204:205],v225 offset:61440
v_accvgpr_read_b32 v248,a58
v_mfma_f32_16x16x32_bf16 v[76:79],v[216:219],v[176:179],v[76:79]
ds_read_b64_tr_b16 v[206:207],v225 offset:61952
v_accvgpr_read_b32 v249,a59
v_mfma_f32_16x16x32_bf16 v[76:79],v[220:223],v[180:183],v[76:79]
ds_read_b128 v[152:155],v238 offset:6144
v_accvgpr_read_b32 v250,a60
v_mfma_f32_16x16x32_bf16 v[108:111],v[216:219],v[184:187],v[108:111]
buffer_load_dwordx4 v232,s[48:51],s67 offen offset:2048 lds
v_accvgpr_read_b32 v251,a61
v_mfma_f32_16x16x32_bf16 v[108:111],v[220:223],v[188:191],v[108:111]
v_accvgpr_read_b32 v252,a62
v_accvgpr_read_b32 v253,a63
s_waitcnt lgkmcnt(9)
v_mfma_f32_16x16x32_bf16 v[16:19],v[192:195],v[160:163],v[16:19]
v_cvt_pk_bf16_f32 v246,v246,v247
v_cvt_pk_bf16_f32 v247,v248,v249
s_waitcnt lgkmcnt(7)
v_mfma_f32_16x16x32_bf16 v[16:19],v[196:199],v[164:167],v[16:19]
ds_read_b64_tr_b16 v[208:209],v226 offset:57344
v_cvt_pk_bf16_f32 v248,v250,v251
v_mfma_f32_16x16x32_bf16 v[48:51],v[192:195],v[168:171],v[48:51]
ds_read_b64_tr_b16 v[210:211],v226 offset:57856
ds_read_b128 v[156:159],v239 offset:6144
v_mfma_f32_16x16x32_bf16 v[48:51],v[196:199],v[172:175],v[48:51]
ds_read_b64_tr_b16 v[212:213],v226 offset:61440
buffer_load_dwordx4 v233,s[48:51],s67 offen offset:3072 lds
v_mfma_f32_16x16x32_bf16 v[80:83],v[192:195],v[176:179],v[80:83]
ds_read_b64_tr_b16 v[214:215],v226 offset:61952
v_cvt_pk_bf16_f32 v249,v252,v253
v_mfma_f32_16x16x32_bf16 v[80:83],v[196:199],v[180:183],v[80:83]
s_add_u32 s87,s72,4096
buffer_store_dwordx4 v[246:249],v255,s[40:43],s87 offen offset:3072 sc1
v_mfma_f32_16x16x32_bf16 v[112:115],v[192:195],v[184:187],v[112:115]
v_mfma_f32_16x16x32_bf16 v[112:115],v[196:199],v[188:191],v[112:115]
s_add_u32 s66,s66,128
s_add_u32 s67,s67,128
s_waitcnt lgkmcnt(8)
v_mfma_f32_16x16x32_bf16 v[20:23],v[200:203],v[160:163],v[20:23]
ds_read_b64_tr_b16 v[216:217],v227 offset:57344
ds_read_b64_tr_b16 v[218:219],v227 offset:57856
s_waitcnt lgkmcnt(8)
v_mfma_f32_16x16x32_bf16 v[20:23],v[204:207],v[164:167],v[20:23]
ds_read_b64_tr_b16 v[220:221],v227 offset:61440
ds_read_b64_tr_b16 v[222:223],v227 offset:61952
v_mfma_f32_16x16x32_bf16 v[52:55],v[200:203],v[168:171],v[52:55]
v_accvgpr_read_b32 v246,a72
v_accvgpr_read_b32 v247,a73
v_mfma_f32_16x16x32_bf16 v[52:55],v[204:207],v[172:175],v[52:55]
v_accvgpr_read_b32 v248,a74
v_accvgpr_read_b32 v249,a75
v_mfma_f32_16x16x32_bf16 v[84:87],v[200:203],v[176:179],v[84:87]
v_accvgpr_read_b32 v250,a76
v_accvgpr_read_b32 v251,a77
v_mfma_f32_16x16x32_bf16 v[84:87],v[204:207],v[180:183],v[84:87]
v_accvgpr_read_b32 v252,a78
v_accvgpr_read_b32 v253,a79
v_mfma_f32_16x16x32_bf16 v[116:119],v[200:203],v[184:187],v[116:119]
v_cvt_pk_bf16_f32 v246,v246,v247
v_cvt_pk_bf16_f32 v247,v248,v249
v_mfma_f32_16x16x32_bf16 v[116:119],v[204:207],v[188:191],v[116:119]
s_waitcnt vmcnt(16) lgkmcnt(0)
s_barrier
v_mfma_f32_16x16x32_bf16 v[24:27],v[208:211],v[160:163],v[24:27]
s_add_u32 m0,s71,147456
s_nop 0
buffer_load_dwordx4 v234,s[52:55],s68 offen offset:0 lds
v_mfma_f32_16x16x32_bf16 v[24:27],v[212:215],v[164:167],v[24:27]
ds_read_b64_tr_b16 v[192:193],v224 offset:0
v_cvt_pk_bf16_f32 v248,v250,v251
v_mfma_f32_16x16x32_bf16 v[56:59],v[208:211],v[168:171],v[56:59]
ds_read_b64_tr_b16 v[194:195],v224 offset:512
v_cvt_pk_bf16_f32 v249,v252,v253
v_mfma_f32_16x16x32_bf16 v[56:59],v[212:215],v[172:175],v[56:59]
ds_read_b64_tr_b16 v[196:197],v224 offset:4096
s_add_u32 s87,s72,8192
v_mfma_f32_16x16x32_bf16 v[88:91],v[208:211],v[176:179],v[88:91]
ds_read_b64_tr_b16 v[198:199],v224 offset:4608
buffer_load_dwordx4 v235,s[52:55],s68 offen offset:1024 lds
v_mfma_f32_16x16x32_bf16 v[88:91],v[212:215],v[180:183],v[88:91]
buffer_store_dwordx4 v[246:249],v255,s[40:43],s87 offen offset:1024 sc1
v_mfma_f32_16x16x32_bf16 v[120:123],v[208:211],v[184:187],v[120:123]
v_mfma_f32_16x16x32_bf16 v[120:123],v[212:215],v[188:191],v[120:123]
v_accvgpr_read_b32 v246,a88
v_accvgpr_read_b32 v247,a89
v_mfma_f32_16x16x32_bf16 v[28:31],v[216:219],v[160:163],v[28:31]
buffer_load_dwordx4 v236,s[52:55],s68 offen offset:2048 lds
v_accvgpr_read_b32 v248,a90
v_mfma_f32_16x16x32_bf16 v[28:31],v[220:223],v[164:167],v[28:31]
ds_read_b64_tr_b16 v[200:201],v225 offset:0
v_accvgpr_read_b32 v249,a91
v_mfma_f32_16x16x32_bf16 v[60:63],v[216:219],v[168:171],v[60:63]
ds_read_b64_tr_b16 v[202:203],v225 offset:512
v_accvgpr_read_b32 v250,a92
v_mfma_f32_16x16x32_bf16 v[60:63],v[220:223],v[172:175],v[60:63]
ds_read_b64_tr_b16 v[204:205],v225 offset:4096
v_accvgpr_read_b32 v251,a93
v_mfma_f32_16x16x32_bf16 v[92:95],v[216:219],v[176:179],v[92:95]
ds_read_b64_tr_b16 v[206:207],v225 offset:4608
buffer_load_dwordx4 v237,s[52:55],s68 offen offset:3072 lds
v_mfma_f32_16x16x32_bf16 v[92:95],v[220:223],v[180:183],v[92:95]
v_accvgpr_read_b32 v252,a94
v_accvgpr_read_b32 v253,a95
v_mfma_f32_16x16x32_bf16 v[124:127],v[216:219],v[184:187],v[124:127]
v_cvt_pk_bf16_f32 v246,v246,v247
v_cvt_pk_bf16_f32 v247,v248,v249
v_mfma_f32_16x16x32_bf16 v[124:127],v[220:223],v[188:191],v[124:127]
s_add_u32 s68,s68,524288
v_cvt_pk_bf16_f32 v248,v250,v251
s_waitcnt lgkmcnt(6)
v_mfma_f32_16x16x32_bf16 v[0:3],v[192:195],v[128:131],v[0:3]
v_cvt_pk_bf16_f32 v249,v252,v253
s_add_u32 s87,s72,8192
s_waitcnt lgkmcnt(4)
v_mfma_f32_16x16x32_bf16 v[0:3],v[196:199],v[132:135],v[0:3]
ds_read_b64_tr_b16 v[208:209],v226 offset:0
buffer_store_dwordx4 v[246:249],v255,s[40:43],s87 offen offset:3072 sc1
v_mfma_f32_16x16x32_bf16 v[32:35],v[192:195],v[136:139],v[32:35]
ds_read_b64_tr_b16 v[210:211],v226 offset:512
ds_read_b128 v[160:163],v238 offset:32768
v_mfma_f32_16x16x32_bf16 v[32:35],v[196:199],v[140:143],v[32:35]
ds_read_b64_tr_b16 v[212:213],v226 offset:4096
v_mfma_f32_16x16x32_bf16 v[64:67],v[192:195],v[144:147],v[64:67]
ds_read_b64_tr_b16 v[214:215],v226 offset:4608
v_mfma_f32_16x16x32_bf16 v[64:67],v[196:199],v[148:151],v[64:67]
s_add_u32 m0,s70,0
s_nop 0
buffer_load_dwordx4 v230,s[48:51],s66 offen offset:0 lds
v_mfma_f32_16x16x32_bf16 v[96:99],v[192:195],v[152:155],v[96:99]
ds_read_b128 v[164:167],v239 offset:32768
v_mfma_f32_16x16x32_bf16 v[96:99],v[196:199],v[156:159],v[96:99]
s_waitcnt lgkmcnt(8)
v_mfma_f32_16x16x32_bf16 v[4:7],v[200:203],v[128:131],v[4:7]
s_waitcnt lgkmcnt(6)
v_mfma_f32_16x16x32_bf16 v[4:7],v[204:207],v[132:135],v[4:7]
ds_read_b64_tr_b16 v[216:217],v227 offset:0
buffer_load_dwordx4 v231,s[48:51],s66 offen offset:1024 lds
v_mfma_f32_16x16x32_bf16 v[36:39],v[200:203],v[136:139],v[36:39]
ds_read_b64_tr_b16 v[218:219],v227 offset:512
v_mfma_f32_16x16x32_bf16 v[36:39],v[204:207],v[140:143],v[36:39]
ds_read_b64_tr_b16 v[220:221],v227 offset:4096
ds_read_b128 v[168:171],v238 offset:34816
v_mfma_f32_16x16x32_bf16 v[68:71],v[200:203],v[144:147],v[68:71]
ds_read_b64_tr_b16 v[222:223],v227 offset:4608
v_mfma_f32_16x16x32_bf16 v[68:71],v[204:207],v[148:151],v[68:71]
buffer_load_dwordx4 v232,s[48:51],s66 offen offset:2048 lds
v_mfma_f32_16x16x32_bf16 v[100:103],v[200:203],v[152:155],v[100:103]
v_mfma_f32_16x16x32_bf16 v[100:103],v[204:207],v[156:159],v[100:103]
ds_read_b128 v[172:175],v239 offset:34816
s_waitcnt lgkmcnt(10)
v_mfma_f32_16x16x32_bf16 v[8:11],v[208:211],v[128:131],v[8:11]
s_waitcnt lgkmcnt(7)
v_mfma_f32_16x16x32_bf16 v[8:11],v[212:215],v[132:135],v[8:11]
ds_read_b64_tr_b16 v[192:193],v224 offset:8192
buffer_load_dwordx4 v233,s[48:51],s66 offen offset:3072 lds
v_mfma_f32_16x16x32_bf16 v[40:43],v[208:211],v[136:139],v[40:43]
ds_read_b64_tr_b16 v[194:195],v224 offset:8704
v_mfma_f32_16x16x32_bf16 v[40:43],v[212:215],v[140:143],v[40:43]
ds_read_b64_tr_b16 v[196:197],v224 offset:12288
v_mfma_f32_16x16x32_bf16 v[72:75],v[208:211],v[144:147],v[72:75]
ds_read_b64_tr_b16 v[198:199],v224 offset:12800
ds_read_b128 v[176:179],v238 offset:36864
v_mfma_f32_16x16x32_bf16 v[72:75],v[212:215],v[148:151],v[72:75]
s_add_u32 m0,s70,4096
s_nop 0
buffer_load_dwordx4 v230,s[48:51],s67 offen offset:0 lds
v_mfma_f32_16x16x32_bf16 v[104:107],v[208:211],v[152:155],v[104:107]
v_mfma_f32_16x16x32_bf16 v[104:107],v[212:215],v[156:159],v[104:107]
s_waitcnt lgkmcnt(9)
v_mfma_f32_16x16x32_bf16 v[12:15],v[216:219],v[128:131],v[12:15]
v_accvgpr_read_b32 v246,a104
v_accvgpr_read_b32 v247,a105
s_waitcnt lgkmcnt(6)
v_mfma_f32_16x16x32_bf16 v[12:15],v[220:223],v[132:135],v[12:15]
ds_read_b64_tr_b16 v[200:201],v225 offset:8192
ds_read_b128 v[180:183],v239 offset:36864
v_mfma_f32_16x16x32_bf16 v[44:47],v[216:219],v[136:139],v[44:47]
ds_read_b64_tr_b16 v[202:203],v225 offset:8704
buffer_load_dwordx4 v231,s[48:51],s67 offen offset:1024 lds
v_mfma_f32_16x16x32_bf16 v[44:47],v[220:223],v[140:143],v[44:47]
ds_read_b64_tr_b16 v[204:205],v225 offset:12288
v_accvgpr_read_b32 v248,a106
v_mfma_f32_16x16x32_bf16 v[76:79],v[216:219],v[144:147],v[76:79]
ds_read_b64_tr_b16 v[206:207],v225 offset:12800
v_accvgpr_read_b32 v249,a107
v_mfma_f32_16x16x32_bf16 v[76:79],v[220:223],v[148:151],v[76:79]
ds_read_b128 v[184:187],v238 offset:38912
v_accvgpr_read_b32 v250,a108
v_mfma_f32_16x16x32_bf16 v[108:111],v[216:219],v[152:155],v[108:111]
buffer_load_dwordx4 v232,s[48:51],s67 offen offset:2048 lds
v_accvgpr_read_b32 v251,a109
v_mfma_f32_16x16x32_bf16 v[108:111],v[220:223],v[156:159],v[108:111]
v_accvgpr_read_b32 v252,a110
v_accvgpr_read_b32 v253,a111
s_waitcnt lgkmcnt(9)
v_mfma_f32_16x16x32_bf16 v[16:19],v[192:195],v[128:131],v[16:19]
v_cvt_pk_bf16_f32 v246,v246,v247
v_cvt_pk_bf16_f32 v247,v248,v249
s_waitcnt lgkmcnt(7)
v_mfma_f32_16x16x32_bf16 v[16:19],v[196:199],v[132:135],v[16:19]
ds_read_b64_tr_b16 v[208:209],v226 offset:8192
v_cvt_pk_bf16_f32 v248,v250,v251
v_mfma_f32_16x16x32_bf16 v[48:51],v[192:195],v[136:139],v[48:51]
ds_read_b64_tr_b16 v[210:211],v226 offset:8704
ds_read_b128 v[188:191],v239 offset:38912
v_mfma_f32_16x16x32_bf16 v[48:51],v[196:199],v[140:143],v[48:51]
ds_read_b64_tr_b16 v[212:213],v226 offset:12288
buffer_load_dwordx4 v233,s[48:51],s67 offen offset:3072 lds
v_mfma_f32_16x16x32_bf16 v[80:83],v[192:195],v[144:147],v[80:83]
ds_read_b64_tr_b16 v[214:215],v226 offset:12800
v_cvt_pk_bf16_f32 v249,v252,v253
v_mfma_f32_16x16x32_bf16 v[80:83],v[196:199],v[148:151],v[80:83]
s_add_u32 s87,s72,12288
buffer_store_dwordx4 v[246:249],v255,s[40:43],s87 offen offset:1024 sc1
v_mfma_f32_16x16x32_bf16 v[112:115],v[192:195],v[152:155],v[112:115]
v_mfma_f32_16x16x32_bf16 v[112:115],v[196:199],v[156:159],v[112:115]
s_add_u32 s66,s66,128
s_add_u32 s67,s67,128
s_waitcnt lgkmcnt(8)
v_mfma_f32_16x16x32_bf16 v[20:23],v[200:203],v[128:131],v[20:23]
ds_read_b64_tr_b16 v[216:217],v227 offset:8192
ds_read_b64_tr_b16 v[218:219],v227 offset:8704
s_waitcnt lgkmcnt(8)
v_mfma_f32_16x16x32_bf16 v[20:23],v[204:207],v[132:135],v[20:23]
ds_read_b64_tr_b16 v[220:221],v227 offset:12288
ds_read_b64_tr_b16 v[222:223],v227 offset:12800
v_mfma_f32_16x16x32_bf16 v[52:55],v[200:203],v[136:139],v[52:55]
v_accvgpr_read_b32 v246,a120
v_accvgpr_read_b32 v247,a121
v_mfma_f32_16x16x32_bf16 v[52:55],v[204:207],v[140:143],v[52:55]
v_accvgpr_read_b32 v248,a122
v_accvgpr_read_b32 v249,a123
v_mfma_f32_16x16x32_bf16 v[84:87],v[200:203],v[144:147],v[84:87]
v_accvgpr_read_b32 v250,a124
v_accvgpr_read_b32 v251,a125
v_mfma_f32_16x16x32_bf16 v[84:87],v[204:207],v[148:151],v[84:87]
v_accvgpr_read_b32 v252,a126
v_accvgpr_read_b32 v253,a127
v_mfma_f32_16x16x32_bf16 v[116:119],v[200:203],v[152:155],v[116:119]
v_cvt_pk_bf16_f32 v246,v246,v247
v_cvt_pk_bf16_f32 v247,v248,v249
v_mfma_f32_16x16x32_bf16 v[116:119],v[204:207],v[156:159],v[116:119]
s_waitcnt vmcnt(16) lgkmcnt(0)
s_barrier
s_or_b32 s58,s65,s56
s_cbranch_scc1 L_PUBK_%=
v_mov_b32 v242,1
v_mov_b32 v243,s92
s_mov_b64 exec,1
global_atomic_add v243,v242,s[22:23]
s_mov_b64 exec,-1
L_PUBK_%=:
v_mfma_f32_16x16x32_bf16 v[24:27],v[208:211],v[128:131],v[24:27]
s_add_u32 m0,s71,98304
s_nop 0
buffer_load_dwordx4 v234,s[52:55],s68 offen offset:0 lds
v_mfma_f32_16x16x32_bf16 v[24:27],v[212:215],v[132:135],v[24:27]
ds_read_b64_tr_b16 v[192:193],v224 offset:32768
v_cvt_pk_bf16_f32 v248,v250,v251
v_mfma_f32_16x16x32_bf16 v[56:59],v[208:211],v[136:139],v[56:59]
ds_read_b64_tr_b16 v[194:195],v224 offset:33280
v_cvt_pk_bf16_f32 v249,v252,v253
v_mfma_f32_16x16x32_bf16 v[56:59],v[212:215],v[140:143],v[56:59]
ds_read_b64_tr_b16 v[196:197],v224 offset:36864
s_add_u32 s87,s72,12288
v_mfma_f32_16x16x32_bf16 v[88:91],v[208:211],v[144:147],v[88:91]
ds_read_b64_tr_b16 v[198:199],v224 offset:37376
buffer_load_dwordx4 v235,s[52:55],s68 offen offset:1024 lds
v_mfma_f32_16x16x32_bf16 v[88:91],v[212:215],v[148:151],v[88:91]
buffer_store_dwordx4 v[246:249],v255,s[40:43],s87 offen offset:3072 sc1
v_mfma_f32_16x16x32_bf16 v[120:123],v[208:211],v[152:155],v[120:123]
)"
R"(v_mfma_f32_16x16x32_bf16 v[120:123],v[212:215],v[156:159],v[120:123]
v_accvgpr_read_b32 v246,a128
v_accvgpr_read_b32 v247,a129
v_mfma_f32_16x16x32_bf16 v[28:31],v[216:219],v[128:131],v[28:31]
buffer_load_dwordx4 v236,s[52:55],s68 offen offset:2048 lds
v_accvgpr_read_b32 v248,a130
v_mfma_f32_16x16x32_bf16 v[28:31],v[220:223],v[132:135],v[28:31]
ds_read_b64_tr_b16 v[200:201],v225 offset:32768
v_accvgpr_read_b32 v249,a131
v_mfma_f32_16x16x32_bf16 v[60:63],v[216:219],v[136:139],v[60:63]
ds_read_b64_tr_b16 v[202:203],v225 offset:33280
v_accvgpr_read_b32 v250,a132
v_mfma_f32_16x16x32_bf16 v[60:63],v[220:223],v[140:143],v[60:63]
ds_read_b64_tr_b16 v[204:205],v225 offset:36864
v_accvgpr_read_b32 v251,a133
v_mfma_f32_16x16x32_bf16 v[92:95],v[216:219],v[144:147],v[92:95]
ds_read_b64_tr_b16 v[206:207],v225 offset:37376
buffer_load_dwordx4 v237,s[52:55],s68 offen offset:3072 lds
v_mfma_f32_16x16x32_bf16 v[92:95],v[220:223],v[148:151],v[92:95]
v_accvgpr_read_b32 v252,a134
v_accvgpr_read_b32 v253,a135
v_mfma_f32_16x16x32_bf16 v[124:127],v[216:219],v[152:155],v[124:127]
v_cvt_pk_bf16_f32 v246,v246,v247
v_cvt_pk_bf16_f32 v247,v248,v249
v_mfma_f32_16x16x32_bf16 v[124:127],v[220:223],v[156:159],v[124:127]
s_add_u32 s68,s68,524288
v_cvt_pk_bf16_f32 v248,v250,v251
s_waitcnt lgkmcnt(6)
v_mfma_f32_16x16x32_bf16 v[0:3],v[192:195],v[160:163],v[0:3]
v_cvt_pk_bf16_f32 v249,v252,v253
s_add_u32 s87,s72,32768
s_waitcnt lgkmcnt(4)
v_mfma_f32_16x16x32_bf16 v[0:3],v[196:199],v[164:167],v[0:3]
ds_read_b64_tr_b16 v[208:209],v226 offset:32768
buffer_store_dwordx4 v[246:249],v255,s[40:43],s87 offen offset:0 sc1
v_mfma_f32_16x16x32_bf16 v[32:35],v[192:195],v[168:171],v[32:35]
ds_read_b64_tr_b16 v[210:211],v226 offset:33280
ds_read_b128 v[128:131],v240 offset:0
v_mfma_f32_16x16x32_bf16 v[32:35],v[196:199],v[172:175],v[32:35]
ds_read_b64_tr_b16 v[212:213],v226 offset:36864
v_mfma_f32_16x16x32_bf16 v[64:67],v[192:195],v[176:179],v[64:67]
ds_read_b64_tr_b16 v[214:215],v226 offset:37376
v_mfma_f32_16x16x32_bf16 v[64:67],v[196:199],v[180:183],v[64:67]
s_add_u32 m0,s70,32768
s_nop 0
buffer_load_dwordx4 v230,s[48:51],s66 offen offset:0 lds
v_mfma_f32_16x16x32_bf16 v[96:99],v[192:195],v[184:187],v[96:99]
ds_read_b128 v[132:135],v241 offset:0
v_mfma_f32_16x16x32_bf16 v[96:99],v[196:199],v[188:191],v[96:99]
s_waitcnt lgkmcnt(8)
v_mfma_f32_16x16x32_bf16 v[4:7],v[200:203],v[160:163],v[4:7]
s_waitcnt lgkmcnt(6)
v_mfma_f32_16x16x32_bf16 v[4:7],v[204:207],v[164:167],v[4:7]
ds_read_b64_tr_b16 v[216:217],v227 offset:32768
buffer_load_dwordx4 v231,s[48:51],s66 offen offset:1024 lds
v_mfma_f32_16x16x32_bf16 v[36:39],v[200:203],v[168:171],v[36:39]
ds_read_b64_tr_b16 v[218:219],v227 offset:33280
v_mfma_f32_16x16x32_bf16 v[36:39],v[204:207],v[172:175],v[36:39]
ds_read_b64_tr_b16 v[220:221],v227 offset:36864
ds_read_b128 v[136:139],v240 offset:2048
v_mfma_f32_16x16x32_bf16 v[68:71],v[200:203],v[176:179],v[68:71]
ds_read_b64_tr_b16 v[222:223],v227 offset:37376
v_mfma_f32_16x16x32_bf16 v[68:71],v[204:207],v[180:183],v[68:71]
buffer_load_dwordx4 v232,s[48:51],s66 offen offset:2048 lds
v_mfma_f32_16x16x32_bf16 v[100:103],v[200:203],v[184:187],v[100:103]
v_mfma_f32_16x16x32_bf16 v[100:103],v[204:207],v[188:191],v[100:103]
ds_read_b128 v[140:143],v241 offset:2048
s_waitcnt lgkmcnt(10)
v_mfma_f32_16x16x32_bf16 v[8:11],v[208:211],v[160:163],v[8:11]
s_waitcnt lgkmcnt(7)
v_mfma_f32_16x16x32_bf16 v[8:11],v[212:215],v[164:167],v[8:11]
ds_read_b64_tr_b16 v[192:193],v224 offset:40960
buffer_load_dwordx4 v233,s[48:51],s66 offen offset:3072 lds
v_mfma_f32_16x16x32_bf16 v[40:43],v[208:211],v[168:171],v[40:43]
ds_read_b64_tr_b16 v[194:195],v224 offset:41472
v_mfma_f32_16x16x32_bf16 v[40:43],v[212:215],v[172:175],v[40:43]
ds_read_b64_tr_b16 v[196:197],v224 offset:45056
v_mfma_f32_16x16x32_bf16 v[72:75],v[208:211],v[176:179],v[72:75]
ds_read_b64_tr_b16 v[198:199],v224 offset:45568
ds_read_b128 v[144:147],v240 offset:4096
v_mfma_f32_16x16x32_bf16 v[72:75],v[212:215],v[180:183],v[72:75]
s_add_u32 m0,s70,36864
s_nop 0
buffer_load_dwordx4 v230,s[48:51],s67 offen offset:0 lds
v_mfma_f32_16x16x32_bf16 v[104:107],v[208:211],v[184:187],v[104:107]
v_mfma_f32_16x16x32_bf16 v[104:107],v[212:215],v[188:191],v[104:107]
s_waitcnt lgkmcnt(9)
v_mfma_f32_16x16x32_bf16 v[12:15],v[216:219],v[160:163],v[12:15]
v_accvgpr_read_b32 v246,a136
v_accvgpr_read_b32 v247,a137
s_waitcnt lgkmcnt(6)
v_mfma_f32_16x16x32_bf16 v[12:15],v[220:223],v[164:167],v[12:15]
ds_read_b64_tr_b16 v[200:201],v225 offset:40960
ds_read_b128 v[148:151],v241 offset:4096
v_mfma_f32_16x16x32_bf16 v[44:47],v[216:219],v[168:171],v[44:47]
ds_read_b64_tr_b16 v[202:203],v225 offset:41472
buffer_load_dwordx4 v231,s[48:51],s67 offen offset:1024 lds
v_mfma_f32_16x16x32_bf16 v[44:47],v[220:223],v[172:175],v[44:47]
ds_read_b64_tr_b16 v[204:205],v225 offset:45056
v_accvgpr_read_b32 v248,a138
v_mfma_f32_16x16x32_bf16 v[76:79],v[216:219],v[176:179],v[76:79]
ds_read_b64_tr_b16 v[206:207],v225 offset:45568
v_accvgpr_read_b32 v249,a139
v_mfma_f32_16x16x32_bf16 v[76:79],v[220:223],v[180:183],v[76:79]
ds_read_b128 v[152:155],v240 offset:6144
v_accvgpr_read_b32 v250,a140
v_mfma_f32_16x16x32_bf16 v[108:111],v[216:219],v[184:187],v[108:111]
buffer_load_dwordx4 v232,s[48:51],s67 offen offset:2048 lds
v_accvgpr_read_b32 v251,a141
v_mfma_f32_16x16x32_bf16 v[108:111],v[220:223],v[188:191],v[108:111]
v_accvgpr_read_b32 v252,a142
v_accvgpr_read_b32 v253,a143
s_waitcnt lgkmcnt(9)
v_mfma_f32_16x16x32_bf16 v[16:19],v[192:195],v[160:163],v[16:19]
v_cvt_pk_bf16_f32 v246,v246,v247
v_cvt_pk_bf16_f32 v247,v248,v249
s_waitcnt lgkmcnt(7)
v_mfma_f32_16x16x32_bf16 v[16:19],v[196:199],v[164:167],v[16:19]
ds_read_b64_tr_b16 v[208:209],v226 offset:40960
v_cvt_pk_bf16_f32 v248,v250,v251
v_mfma_f32_16x16x32_bf16 v[48:51],v[192:195],v[168:171],v[48:51]
ds_read_b64_tr_b16 v[210:211],v226 offset:41472
ds_read_b128 v[156:159],v241 offset:6144
v_mfma_f32_16x16x32_bf16 v[48:51],v[196:199],v[172:175],v[48:51]
ds_read_b64_tr_b16 v[212:213],v226 offset:45056
buffer_load_dwordx4 v233,s[48:51],s67 offen offset:3072 lds
v_mfma_f32_16x16x32_bf16 v[80:83],v[192:195],v[176:179],v[80:83]
ds_read_b64_tr_b16 v[214:215],v226 offset:45568
v_cvt_pk_bf16_f32 v249,v252,v253
v_mfma_f32_16x16x32_bf16 v[80:83],v[196:199],v[180:183],v[80:83]
s_add_u32 s87,s72,32768
buffer_store_dwordx4 v[246:249],v255,s[40:43],s87 offen offset:1024 sc1
v_mfma_f32_16x16x32_bf16 v[112:115],v[192:195],v[184:187],v[112:115]
v_mfma_f32_16x16x32_bf16 v[112:115],v[196:199],v[188:191],v[112:115]
s_add_u32 s66,s66,128
s_add_u32 s67,s67,128
s_waitcnt lgkmcnt(8)
v_mfma_f32_16x16x32_bf16 v[20:23],v[200:203],v[160:163],v[20:23]
ds_read_b64_tr_b16 v[216:217],v227 offset:40960
ds_read_b64_tr_b16 v[218:219],v227 offset:41472
s_waitcnt lgkmcnt(8)
v_mfma_f32_16x16x32_bf16 v[20:23],v[204:207],v[164:167],v[20:23]
ds_read_b64_tr_b16 v[220:221],v227 offset:45056
ds_read_b64_tr_b16 v[222:223],v227 offset:45568
v_mfma_f32_16x16x32_bf16 v[52:55],v[200:203],v[168:171],v[52:55]
v_accvgpr_read_b32 v246,a144
v_accvgpr_read_b32 v247,a145
v_mfma_f32_16x16x32_bf16 v[52:55],v[204:207],v[172:175],v[52:55]
v_accvgpr_read_b32 v248,a146
v_accvgpr_read_b32 v249,a147
v_mfma_f32_16x16x32_bf16 v[84:87],v[200:203],v[176:179],v[84:87]
v_accvgpr_read_b32 v250,a148
v_accvgpr_read_b32 v251,a149
v_mfma_f32_16x16x32_bf16 v[84:87],v[204:207],v[180:183],v[84:87]
v_accvgpr_read_b32 v252,a150
v_accvgpr_read_b32 v253,a151
v_mfma_f32_16x16x32_bf16 v[116:119],v[200:203],v[184:187],v[116:119]
v_cvt_pk_bf16_f32 v246,v246,v247
v_cvt_pk_bf16_f32 v247,v248,v249
v_mfma_f32_16x16x32_bf16 v[116:119],v[204:207],v[188:191],v[116:119]
s_waitcnt vmcnt(16) lgkmcnt(0)
s_barrier
v_mfma_f32_16x16x32_bf16 v[24:27],v[208:211],v[160:163],v[24:27]
s_add_u32 m0,s71,131072
s_nop 0
buffer_load_dwordx4 v234,s[52:55],s68 offen offset:0 lds
v_mfma_f32_16x16x32_bf16 v[24:27],v[212:215],v[164:167],v[24:27]
ds_read_b64_tr_b16 v[192:193],v224 offset:49152
v_cvt_pk_bf16_f32 v248,v250,v251
v_mfma_f32_16x16x32_bf16 v[56:59],v[208:211],v[168:171],v[56:59]
ds_read_b64_tr_b16 v[194:195],v224 offset:49664
v_cvt_pk_bf16_f32 v249,v252,v253
v_mfma_f32_16x16x32_bf16 v[56:59],v[212:215],v[172:175],v[56:59]
ds_read_b64_tr_b16 v[196:197],v224 offset:53248
s_add_u32 s87,s72,32768
v_mfma_f32_16x16x32_bf16 v[88:91],v[208:211],v[176:179],v[88:91]
ds_read_b64_tr_b16 v[198:199],v224 offset:53760
buffer_load_dwordx4 v235,s[52:55],s68 offen offset:1024 lds
v_mfma_f32_16x16x32_bf16 v[88:91],v[212:215],v[180:183],v[88:91]
buffer_store_dwordx4 v[246:249],v255,s[40:43],s87 offen offset:2048 sc1
v_mfma_f32_16x16x32_bf16 v[120:123],v[208:211],v[184:187],v[120:123]
v_mfma_f32_16x16x32_bf16 v[120:123],v[212:215],v[188:191],v[120:123]
v_accvgpr_read_b32 v246,a152
v_accvgpr_read_b32 v247,a153
v_mfma_f32_16x16x32_bf16 v[28:31],v[216:219],v[160:163],v[28:31]
buffer_load_dwordx4 v236,s[52:55],s68 offen offset:2048 lds
v_accvgpr_read_b32 v248,a154
v_mfma_f32_16x16x32_bf16 v[28:31],v[220:223],v[164:167],v[28:31]
ds_read_b64_tr_b16 v[200:201],v225 offset:49152
v_accvgpr_read_b32 v249,a155
v_mfma_f32_16x16x32_bf16 v[60:63],v[216:219],v[168:171],v[60:63]
ds_read_b64_tr_b16 v[202:203],v225 offset:49664
v_accvgpr_read_b32 v250,a156
v_mfma_f32_16x16x32_bf16 v[60:63],v[220:223],v[172:175],v[60:63]
ds_read_b64_tr_b16 v[204:205],v225 offset:53248
v_accvgpr_read_b32 v251,a157
v_mfma_f32_16x16x32_bf16 v[92:95],v[216:219],v[176:179],v[92:95]
ds_read_b64_tr_b16 v[206:207],v225 offset:53760
buffer_load_dwordx4 v237,s[52:55],s68 offen offset:3072 lds
v_mfma_f32_16x16x32_bf16 v[92:95],v[220:223],v[180:183],v[92:95]
v_accvgpr_read_b32 v252,a158
v_accvgpr_read_b32 v253,a159
v_mfma_f32_16x16x32_bf16 v[124:127],v[216:219],v[184:187],v[124:127]
v_cvt_pk_bf16_f32 v246,v246,v247
v_cvt_pk_bf16_f32 v247,v248,v249
v_mfma_f32_16x16x32_bf16 v[124:127],v[220:223],v[188:191],v[124:127]
s_add_u32 s68,s68,524288
v_cvt_pk_bf16_f32 v248,v250,v251
s_waitcnt lgkmcnt(6)
v_mfma_f32_16x16x32_bf16 v[0:3],v[192:195],v[128:131],v[0:3]
v_cvt_pk_bf16_f32 v249,v252,v253
s_add_u32 s87,s72,32768
s_waitcnt lgkmcnt(4)
v_mfma_f32_16x16x32_bf16 v[0:3],v[196:199],v[132:135],v[0:3]
ds_read_b64_tr_b16 v[208:209],v226 offset:49152
buffer_store_dwordx4 v[246:249],v255,s[40:43],s87 offen offset:3072 sc1
v_mfma_f32_16x16x32_bf16 v[32:35],v[192:195],v[136:139],v[32:35]
ds_read_b64_tr_b16 v[210:211],v226 offset:49664
ds_read_b128 v[160:163],v238 offset:0
v_mfma_f32_16x16x32_bf16 v[32:35],v[196:199],v[140:143],v[32:35]
ds_read_b64_tr_b16 v[212:213],v226 offset:53248
v_mfma_f32_16x16x32_bf16 v[64:67],v[192:195],v[144:147],v[64:67]
ds_read_b64_tr_b16 v[214:215],v226 offset:53760
v_mfma_f32_16x16x32_bf16 v[64:67],v[196:199],v[148:151],v[64:67]
s_add_u32 m0,s70,65536
s_nop 0
buffer_load_dwordx4 v230,s[48:51],s66 offen offset:0 lds
v_mfma_f32_16x16x32_bf16 v[96:99],v[192:195],v[152:155],v[96:99]
ds_read_b128 v[164:167],v239 offset:0
v_mfma_f32_16x16x32_bf16 v[96:99],v[196:199],v[156:159],v[96:99]
s_waitcnt lgkmcnt(8)
v_mfma_f32_16x16x32_bf16 v[4:7],v[200:203],v[128:131],v[4:7]
s_waitcnt lgkmcnt(6)
v_mfma_f32_16x16x32_bf16 v[4:7],v[204:207],v[132:135],v[4:7]
ds_read_b64_tr_b16 v[216:217],v227 offset:49152
buffer_load_dwordx4 v231,s[48:51],s66 offen offset:1024 lds
v_mfma_f32_16x16x32_bf16 v[36:39],v[200:203],v[136:139],v[36:39]
ds_read_b64_tr_b16 v[218:219],v227 offset:49664
v_mfma_f32_16x16x32_bf16 v[36:39],v[204:207],v[140:143],v[36:39]
ds_read_b64_tr_b16 v[220:221],v227 offset:53248
ds_read_b128 v[168:171],v238 offset:2048
v_mfma_f32_16x16x32_bf16 v[68:71],v[200:203],v[144:147],v[68:71]
ds_read_b64_tr_b16 v[222:223],v227 offset:53760
v_mfma_f32_16x16x32_bf16 v[68:71],v[204:207],v[148:151],v[68:71]
buffer_load_dwordx4 v232,s[48:51],s66 offen offset:2048 lds
v_mfma_f32_16x16x32_bf16 v[100:103],v[200:203],v[152:155],v[100:103]
v_mfma_f32_16x16x32_bf16 v[100:103],v[204:207],v[156:159],v[100:103]
ds_read_b128 v[172:175],v239 offset:2048
s_waitcnt lgkmcnt(10)
v_mfma_f32_16x16x32_bf16 v[8:11],v[208:211],v[128:131],v[8:11]
s_waitcnt lgkmcnt(7)
v_mfma_f32_16x16x32_bf16 v[8:11],v[212:215],v[132:135],v[8:11]
ds_read_b64_tr_b16 v[192:193],v224 offset:57344
buffer_load_dwordx4 v233,s[48:51],s66 offen offset:3072 lds
v_mfma_f32_16x16x32_bf16 v[40:43],v[208:211],v[136:139],v[40:43]
ds_read_b64_tr_b16 v[194:195],v224 offset:57856
v_mfma_f32_16x16x32_bf16 v[40:43],v[212:215],v[140:143],v[40:43]
ds_read_b64_tr_b16 v[196:197],v224 offset:61440
v_mfma_f32_16x16x32_bf16 v[72:75],v[208:211],v[144:147],v[72:75]
ds_read_b64_tr_b16 v[198:199],v224 offset:61952
ds_read_b128 v[176:179],v238 offset:4096
v_mfma_f32_16x16x32_bf16 v[72:75],v[212:215],v[148:151],v[72:75]
s_add_u32 m0,s70,69632
s_nop 0
buffer_load_dwordx4 v230,s[48:51],s67 offen offset:0 lds
v_mfma_f32_16x16x32_bf16 v[104:107],v[208:211],v[152:155],v[104:107]
v_mfma_f32_16x16x32_bf16 v[104:107],v[212:215],v[156:159],v[104:107]
s_waitcnt lgkmcnt(9)
v_mfma_f32_16x16x32_bf16 v[12:15],v[216:219],v[128:131],v[12:15]
v_accvgpr_read_b32 v246,a160
v_accvgpr_read_b32 v247,a161
s_waitcnt lgkmcnt(6)
v_mfma_f32_16x16x32_bf16 v[12:15],v[220:223],v[132:135],v[12:15]
ds_read_b64_tr_b16 v[200:201],v225 offset:57344
ds_read_b128 v[180:183],v239 offset:4096
v_mfma_f32_16x16x32_bf16 v[44:47],v[216:219],v[136:139],v[44:47]
ds_read_b64_tr_b16 v[202:203],v225 offset:57856
buffer_load_dwordx4 v231,s[48:51],s67 offen offset:1024 lds
v_mfma_f32_16x16x32_bf16 v[44:47],v[220:223],v[140:143],v[44:47]
ds_read_b64_tr_b16 v[204:205],v225 offset:61440
v_accvgpr_read_b32 v248,a162
v_mfma_f32_16x16x32_bf16 v[76:79],v[216:219],v[144:147],v[76:79]
ds_read_b64_tr_b16 v[206:207],v225 offset:61952
v_accvgpr_read_b32 v249,a163
v_mfma_f32_16x16x32_bf16 v[76:79],v[220:223],v[148:151],v[76:79]
ds_read_b128 v[184:187],v238 offset:6144
v_accvgpr_read_b32 v250,a164
v_mfma_f32_16x16x32_bf16 v[108:111],v[216:219],v[152:155],v[108:111]
buffer_load_dwordx4 v232,s[48:51],s67 offen offset:2048 lds
v_accvgpr_read_b32 v251,a165
v_mfma_f32_16x16x32_bf16 v[108:111],v[220:223],v[156:159],v[108:111]
v_accvgpr_read_b32 v252,a166
v_accvgpr_read_b32 v253,a167
s_waitcnt lgkmcnt(9)
v_mfma_f32_16x16x32_bf16 v[16:19],v[192:195],v[128:131],v[16:19]
v_cvt_pk_bf16_f32 v246,v246,v247
v_cvt_pk_bf16_f32 v247,v248,v249
s_waitcnt lgkmcnt(7)
v_mfma_f32_16x16x32_bf16 v[16:19],v[196:199],v[132:135],v[16:19]
ds_read_b64_tr_b16 v[208:209],v226 offset:57344
v_cvt_pk_bf16_f32 v248,v250,v251
v_mfma_f32_16x16x32_bf16 v[48:51],v[192:195],v[136:139],v[48:51]
ds_read_b64_tr_b16 v[210:211],v226 offset:57856
ds_read_b128 v[188:191],v239 offset:6144
v_mfma_f32_16x16x32_bf16 v[48:51],v[196:199],v[140:143],v[48:51]
ds_read_b64_tr_b16 v[212:213],v226 offset:61440
buffer_load_dwordx4 v233,s[48:51],s67 offen offset:3072 lds
v_mfma_f32_16x16x32_bf16 v[80:83],v[192:195],v[144:147],v[80:83]
ds_read_b64_tr_b16 v[214:215],v226 offset:61952
v_cvt_pk_bf16_f32 v249,v252,v253
v_mfma_f32_16x16x32_bf16 v[80:83],v[196:199],v[148:151],v[80:83]
s_add_u32 s87,s72,36864
buffer_store_dwordx4 v[246:249],v255,s[40:43],s87 offen offset:0 sc1
v_mfma_f32_16x16x32_bf16 v[112:115],v[192:195],v[152:155],v[112:115]
v_mfma_f32_16x16x32_bf16 v[112:115],v[196:199],v[156:159],v[112:115]
s_add_u32 s66,s66,128
s_add_u32 s67,s67,128
s_waitcnt lgkmcnt(8)
v_mfma_f32_16x16x32_bf16 v[20:23],v[200:203],v[128:131],v[20:23]
ds_read_b64_tr_b16 v[216:217],v227 offset:57344
ds_read_b64_tr_b16 v[218:219],v227 offset:57856
s_waitcnt lgkmcnt(8)
v_mfma_f32_16x16x32_bf16 v[20:23],v[204:207],v[132:135],v[20:23]
ds_read_b64_tr_b16 v[220:221],v227 offset:61440
ds_read_b64_tr_b16 v[222:223],v227 offset:61952
v_mfma_f32_16x16x32_bf16 v[52:55],v[200:203],v[136:139],v[52:55]
v_accvgpr_read_b32 v246,a168
v_accvgpr_read_b32 v247,a169
v_mfma_f32_16x16x32_bf16 v[52:55],v[204:207],v[140:143],v[52:55]
v_accvgpr_read_b32 v248,a170
v_accvgpr_read_b32 v249,a171
v_mfma_f32_16x16x32_bf16 v[84:87],v[200:203],v[144:147],v[84:87]
v_accvgpr_read_b32 v250,a172
v_accvgpr_read_b32 v251,a173
v_mfma_f32_16x16x32_bf16 v[84:87],v[204:207],v[148:151],v[84:87]
v_accvgpr_read_b32 v252,a174
v_accvgpr_read_b32 v253,a175
v_mfma_f32_16x16x32_bf16 v[116:119],v[200:203],v[152:155],v[116:119]
v_cvt_pk_bf16_f32 v246,v246,v247
v_cvt_pk_bf16_f32 v247,v248,v249
v_mfma_f32_16x16x32_bf16 v[116:119],v[204:207],v[156:159],v[116:119]
s_waitcnt vmcnt(16) lgkmcnt(0)
s_barrier
v_mfma_f32_16x16x32_bf16 v[24:27],v[208:211],v[128:131],v[24:27]
s_add_u32 m0,s71,147456
s_nop 0
buffer_load_dwordx4 v234,s[52:55],s68 offen offset:0 lds
v_mfma_f32_16x16x32_bf16 v[24:27],v[212:215],v[132:135],v[24:27]
ds_read_b64_tr_b16 v[192:193],v224 offset:0
v_cvt_pk_bf16_f32 v248,v250,v251
v_mfma_f32_16x16x32_bf16 v[56:59],v[208:211],v[136:139],v[56:59]
ds_read_b64_tr_b16 v[194:195],v224 offset:512
v_cvt_pk_bf16_f32 v249,v252,v253
v_mfma_f32_16x16x32_bf16 v[56:59],v[212:215],v[140:143],v[56:59]
ds_read_b64_tr_b16 v[196:197],v224 offset:4096
s_add_u32 s87,s72,36864
v_mfma_f32_16x16x32_bf16 v[88:91],v[208:211],v[144:147],v[88:91]
ds_read_b64_tr_b16 v[198:199],v224 offset:4608
buffer_load_dwordx4 v235,s[52:55],s68 offen offset:1024 lds
v_mfma_f32_16x16x32_bf16 v[88:91],v[212:215],v[148:151],v[88:91]
buffer_store_dwordx4 v[246:249],v255,s[40:43],s87 offen offset:1024 sc1
v_mfma_f32_16x16x32_bf16 v[120:123],v[208:211],v[152:155],v[120:123]
v_mfma_f32_16x16x32_bf16 v[120:123],v[212:215],v[156:159],v[120:123]
v_accvgpr_read_b32 v246,a176
v_accvgpr_read_b32 v247,a177
v_mfma_f32_16x16x32_bf16 v[28:31],v[216:219],v[128:131],v[28:31]
buffer_load_dwordx4 v236,s[52:55],s68 offen offset:2048 lds
v_accvgpr_read_b32 v248,a178
v_mfma_f32_16x16x32_bf16 v[28:31],v[220:223],v[132:135],v[28:31]
ds_read_b64_tr_b16 v[200:201],v225 offset:0
v_accvgpr_read_b32 v249,a179
v_mfma_f32_16x16x32_bf16 v[60:63],v[216:219],v[136:139],v[60:63]
ds_read_b64_tr_b16 v[202:203],v225 offset:512
v_accvgpr_read_b32 v250,a180
v_mfma_f32_16x16x32_bf16 v[60:63],v[220:223],v[140:143],v[60:63]
ds_read_b64_tr_b16 v[204:205],v225 offset:4096
v_accvgpr_read_b32 v251,a181
v_mfma_f32_16x16x32_bf16 v[92:95],v[216:219],v[144:147],v[92:95]
ds_read_b64_tr_b16 v[206:207],v225 offset:4608
buffer_load_dwordx4 v237,s[52:55],s68 offen offset:3072 lds
v_mfma_f32_16x16x32_bf16 v[92:95],v[220:223],v[148:151],v[92:95]
v_accvgpr_read_b32 v252,a182
v_accvgpr_read_b32 v253,a183
v_mfma_f32_16x16x32_bf16 v[124:127],v[216:219],v[152:155],v[124:127]
v_cvt_pk_bf16_f32 v246,v246,v247
v_cvt_pk_bf16_f32 v247,v248,v249
v_mfma_f32_16x16x32_bf16 v[124:127],v[220:223],v[156:159],v[124:127]
s_add_u32 s68,s68,524288
v_cvt_pk_bf16_f32 v248,v250,v251
s_waitcnt lgkmcnt(6)
v_mfma_f32_16x16x32_bf16 v[0:3],v[192:195],v[160:163],v[0:3]
v_cvt_pk_bf16_f32 v249,v252,v253
s_add_u32 s87,s72,36864
s_waitcnt lgkmcnt(4)
v_mfma_f32_16x16x32_bf16 v[0:3],v[196:199],v[164:167],v[0:3]
ds_read_b64_tr_b16 v[208:209],v226 offset:0
buffer_store_dwordx4 v[246:249],v255,s[40:43],s87 offen offset:2048 sc1
v_mfma_f32_16x16x32_bf16 v[32:35],v[192:195],v[168:171],v[32:35]
ds_read_b64_tr_b16 v[210:211],v226 offset:512
ds_read_b128 v[128:131],v238 offset:32768
)"
R"(v_mfma_f32_16x16x32_bf16 v[32:35],v[196:199],v[172:175],v[32:35]
ds_read_b64_tr_b16 v[212:213],v226 offset:4096
v_mfma_f32_16x16x32_bf16 v[64:67],v[192:195],v[176:179],v[64:67]
ds_read_b64_tr_b16 v[214:215],v226 offset:4608
v_mfma_f32_16x16x32_bf16 v[64:67],v[196:199],v[180:183],v[64:67]
s_add_u32 m0,s70,0
s_nop 0
buffer_load_dwordx4 v230,s[48:51],s66 offen offset:0 lds
v_mfma_f32_16x16x32_bf16 v[96:99],v[192:195],v[184:187],v[96:99]
ds_read_b128 v[132:135],v239 offset:32768
v_mfma_f32_16x16x32_bf16 v[96:99],v[196:199],v[188:191],v[96:99]
s_waitcnt lgkmcnt(8)
v_mfma_f32_16x16x32_bf16 v[4:7],v[200:203],v[160:163],v[4:7]
s_waitcnt lgkmcnt(6)
v_mfma_f32_16x16x32_bf16 v[4:7],v[204:207],v[164:167],v[4:7]
ds_read_b64_tr_b16 v[216:217],v227 offset:0
buffer_load_dwordx4 v231,s[48:51],s66 offen offset:1024 lds
v_mfma_f32_16x16x32_bf16 v[36:39],v[200:203],v[168:171],v[36:39]
ds_read_b64_tr_b16 v[218:219],v227 offset:512
v_mfma_f32_16x16x32_bf16 v[36:39],v[204:207],v[172:175],v[36:39]
ds_read_b64_tr_b16 v[220:221],v227 offset:4096
ds_read_b128 v[136:139],v238 offset:34816
v_mfma_f32_16x16x32_bf16 v[68:71],v[200:203],v[176:179],v[68:71]
ds_read_b64_tr_b16 v[222:223],v227 offset:4608
v_mov_b32 v243,s99
v_mfma_f32_16x16x32_bf16 v[68:71],v[204:207],v[180:183],v[68:71]
buffer_load_dwordx4 v232,s[48:51],s66 offen offset:2048 lds
global_load_dword v242,v243,s[22:23] sc1
v_mfma_f32_16x16x32_bf16 v[100:103],v[200:203],v[184:187],v[100:103]
s_lshl_b32 s60,s77,20
s_add_u32 s60,s60,1048576
v_mfma_f32_16x16x32_bf16 v[100:103],v[204:207],v[188:191],v[100:103]
ds_read_b128 v[140:143],v239 offset:34816
s_mov_b64 s[36:37],s[22:23]
s_waitcnt lgkmcnt(10)
v_mfma_f32_16x16x32_bf16 v[8:11],v[208:211],v[160:163],v[8:11]
s_add_u32 s36,s36,s60
s_addc_u32 s37,s37,0
s_waitcnt lgkmcnt(7)
v_mfma_f32_16x16x32_bf16 v[8:11],v[212:215],v[164:167],v[8:11]
ds_read_b64_tr_b16 v[192:193],v224 offset:8192
buffer_load_dwordx4 v233,s[48:51],s66 offen offset:3072 lds
v_mfma_f32_16x16x32_bf16 v[40:43],v[208:211],v[168:171],v[40:43]
ds_read_b64_tr_b16 v[194:195],v224 offset:8704
s_mov_b32 s38,0x100000
v_mfma_f32_16x16x32_bf16 v[40:43],v[212:215],v[172:175],v[40:43]
ds_read_b64_tr_b16 v[196:197],v224 offset:12288
s_mov_b32 s39,0x20000
v_mfma_f32_16x16x32_bf16 v[72:75],v[208:211],v[176:179],v[72:75]
ds_read_b64_tr_b16 v[198:199],v224 offset:12800
ds_read_b128 v[144:147],v238 offset:36864
v_mfma_f32_16x16x32_bf16 v[72:75],v[212:215],v[180:183],v[72:75]
s_add_u32 m0,s70,4096
s_nop 0
buffer_load_dwordx4 v230,s[48:51],s67 offen offset:0 lds
v_mfma_f32_16x16x32_bf16 v[104:107],v[208:211],v[184:187],v[104:107]
s_lshl_b32 s60,s56,12
v_add_u32 v228,s60,v255
v_mfma_f32_16x16x32_bf16 v[104:107],v[212:215],v[188:191],v[104:107]
s_add_u32 s81,s57,s60
s_add_u32 s82,s81,0x8000
s_waitcnt lgkmcnt(9)
v_mfma_f32_16x16x32_bf16 v[12:15],v[216:219],v[160:163],v[12:15]
v_accvgpr_read_b32 v246,a184
v_accvgpr_read_b32 v247,a185
s_waitcnt lgkmcnt(6)
v_mfma_f32_16x16x32_bf16 v[12:15],v[220:223],v[164:167],v[12:15]
ds_read_b64_tr_b16 v[200:201],v225 offset:8192
ds_read_b128 v[148:151],v239 offset:36864
v_mfma_f32_16x16x32_bf16 v[44:47],v[216:219],v[168:171],v[44:47]
ds_read_b64_tr_b16 v[202:203],v225 offset:8704
buffer_load_dwordx4 v231,s[48:51],s67 offen offset:1024 lds
v_mfma_f32_16x16x32_bf16 v[44:47],v[220:223],v[172:175],v[44:47]
ds_read_b64_tr_b16 v[204:205],v225 offset:12288
v_accvgpr_read_b32 v248,a186
v_mfma_f32_16x16x32_bf16 v[76:79],v[216:219],v[176:179],v[76:79]
ds_read_b64_tr_b16 v[206:207],v225 offset:12800
v_accvgpr_read_b32 v249,a187
v_mfma_f32_16x16x32_bf16 v[76:79],v[220:223],v[180:183],v[76:79]
ds_read_b128 v[152:155],v238 offset:38912
v_accvgpr_read_b32 v250,a188
v_mfma_f32_16x16x32_bf16 v[108:111],v[216:219],v[184:187],v[108:111]
buffer_load_dwordx4 v232,s[48:51],s67 offen offset:2048 lds
v_accvgpr_read_b32 v251,a189
v_mfma_f32_16x16x32_bf16 v[108:111],v[220:223],v[188:191],v[108:111]
v_accvgpr_read_b32 v252,a190
v_accvgpr_read_b32 v253,a191
s_waitcnt lgkmcnt(9)
v_mfma_f32_16x16x32_bf16 v[16:19],v[192:195],v[160:163],v[16:19]
v_cvt_pk_bf16_f32 v246,v246,v247
v_cvt_pk_bf16_f32 v247,v248,v249
s_waitcnt lgkmcnt(7)
v_mfma_f32_16x16x32_bf16 v[16:19],v[196:199],v[164:167],v[16:19]
ds_read_b64_tr_b16 v[208:209],v226 offset:8192
v_cvt_pk_bf16_f32 v248,v250,v251
v_mfma_f32_16x16x32_bf16 v[48:51],v[192:195],v[168:171],v[48:51]
ds_read_b64_tr_b16 v[210:211],v226 offset:8704
ds_read_b128 v[156:159],v239 offset:38912
v_mfma_f32_16x16x32_bf16 v[48:51],v[196:199],v[172:175],v[48:51]
ds_read_b64_tr_b16 v[212:213],v226 offset:12288
buffer_load_dwordx4 v233,s[48:51],s67 offen offset:3072 lds
v_mfma_f32_16x16x32_bf16 v[80:83],v[192:195],v[176:179],v[80:83]
ds_read_b64_tr_b16 v[214:215],v226 offset:12800
v_cvt_pk_bf16_f32 v249,v252,v253
v_mfma_f32_16x16x32_bf16 v[80:83],v[196:199],v[180:183],v[80:83]
s_add_u32 s87,s72,36864
buffer_store_dwordx4 v[246:249],v255,s[40:43],s87 offen offset:3072 sc1
v_mfma_f32_16x16x32_bf16 v[112:115],v[192:195],v[184:187],v[112:115]
v_mfma_f32_16x16x32_bf16 v[112:115],v[196:199],v[188:191],v[112:115]
s_add_u32 s66,s66,128
s_add_u32 s67,s67,128
s_waitcnt lgkmcnt(8)
v_mfma_f32_16x16x32_bf16 v[20:23],v[200:203],v[160:163],v[20:23]
ds_read_b64_tr_b16 v[216:217],v227 offset:8192
ds_read_b64_tr_b16 v[218:219],v227 offset:8704
s_waitcnt lgkmcnt(8)
v_mfma_f32_16x16x32_bf16 v[20:23],v[204:207],v[164:167],v[20:23]
ds_read_b64_tr_b16 v[220:221],v227 offset:12288
ds_read_b64_tr_b16 v[222:223],v227 offset:12800
v_mfma_f32_16x16x32_bf16 v[52:55],v[200:203],v[168:171],v[52:55]
v_accvgpr_read_b32 v246,a192
v_accvgpr_read_b32 v247,a193
v_mfma_f32_16x16x32_bf16 v[52:55],v[204:207],v[172:175],v[52:55]
v_accvgpr_read_b32 v248,a194
v_accvgpr_read_b32 v249,a195
v_mfma_f32_16x16x32_bf16 v[84:87],v[200:203],v[176:179],v[84:87]
v_accvgpr_read_b32 v250,a196
v_accvgpr_read_b32 v251,a197
v_mfma_f32_16x16x32_bf16 v[84:87],v[204:207],v[180:183],v[84:87]
v_accvgpr_read_b32 v252,a198
v_accvgpr_read_b32 v253,a199
v_mfma_f32_16x16x32_bf16 v[116:119],v[200:203],v[184:187],v[116:119]
v_cvt_pk_bf16_f32 v246,v246,v247
v_cvt_pk_bf16_f32 v247,v248,v249
v_mfma_f32_16x16x32_bf16 v[116:119],v[204:207],v[188:191],v[116:119]
s_waitcnt vmcnt(17) lgkmcnt(0)
s_barrier
v_mfma_f32_16x16x32_bf16 v[24:27],v[208:211],v[160:163],v[24:27]
s_add_u32 m0,s71,98304
s_nop 0
buffer_load_dwordx4 v234,s[52:55],s68 offen offset:0 lds
v_mfma_f32_16x16x32_bf16 v[24:27],v[212:215],v[164:167],v[24:27]
ds_read_b64_tr_b16 v[192:193],v224 offset:32768
v_cvt_pk_bf16_f32 v248,v250,v251
v_mfma_f32_16x16x32_bf16 v[56:59],v[208:211],v[168:171],v[56:59]
ds_read_b64_tr_b16 v[194:195],v224 offset:33280
v_cvt_pk_bf16_f32 v249,v252,v253
v_mfma_f32_16x16x32_bf16 v[56:59],v[212:215],v[172:175],v[56:59]
ds_read_b64_tr_b16 v[196:197],v224 offset:36864
s_add_u32 s87,s72,40960
v_mfma_f32_16x16x32_bf16 v[88:91],v[208:211],v[176:179],v[88:91]
ds_read_b64_tr_b16 v[198:199],v224 offset:37376
buffer_load_dwordx4 v235,s[52:55],s68 offen offset:1024 lds
v_mfma_f32_16x16x32_bf16 v[88:91],v[212:215],v[180:183],v[88:91]
buffer_store_dwordx4 v[246:249],v255,s[40:43],s87 offen offset:0 sc1
v_mfma_f32_16x16x32_bf16 v[120:123],v[208:211],v[184:187],v[120:123]
v_mfma_f32_16x16x32_bf16 v[120:123],v[212:215],v[188:191],v[120:123]
v_accvgpr_read_b32 v246,a200
v_accvgpr_read_b32 v247,a201
v_mfma_f32_16x16x32_bf16 v[28:31],v[216:219],v[160:163],v[28:31]
buffer_load_dwordx4 v236,s[52:55],s68 offen offset:2048 lds
v_accvgpr_read_b32 v248,a202
v_mfma_f32_16x16x32_bf16 v[28:31],v[220:223],v[164:167],v[28:31]
ds_read_b64_tr_b16 v[200:201],v225 offset:32768
v_accvgpr_read_b32 v249,a203
v_mfma_f32_16x16x32_bf16 v[60:63],v[216:219],v[168:171],v[60:63]
ds_read_b64_tr_b16 v[202:203],v225 offset:33280
v_accvgpr_read_b32 v250,a204
v_mfma_f32_16x16x32_bf16 v[60:63],v[220:223],v[172:175],v[60:63]
ds_read_b64_tr_b16 v[204:205],v225 offset:36864
v_accvgpr_read_b32 v251,a205
v_mfma_f32_16x16x32_bf16 v[92:95],v[216:219],v[176:179],v[92:95]
ds_read_b64_tr_b16 v[206:207],v225 offset:37376
buffer_load_dwordx4 v237,s[52:55],s68 offen offset:3072 lds
v_mfma_f32_16x16x32_bf16 v[92:95],v[220:223],v[180:183],v[92:95]
v_accvgpr_read_b32 v252,a206
v_accvgpr_read_b32 v253,a207
v_mfma_f32_16x16x32_bf16 v[124:127],v[216:219],v[184:187],v[124:127]
v_cvt_pk_bf16_f32 v246,v246,v247
v_cvt_pk_bf16_f32 v247,v248,v249
v_mfma_f32_16x16x32_bf16 v[124:127],v[220:223],v[188:191],v[124:127]
s_add_u32 s68,s68,524288
v_cvt_pk_bf16_f32 v248,v250,v251
s_waitcnt lgkmcnt(6)
v_mfma_f32_16x16x32_bf16 v[0:3],v[192:195],v[128:131],v[0:3]
v_cvt_pk_bf16_f32 v249,v252,v253
s_add_u32 s87,s72,40960
s_waitcnt lgkmcnt(4)
v_mfma_f32_16x16x32_bf16 v[0:3],v[196:199],v[132:135],v[0:3]
ds_read_b64_tr_b16 v[208:209],v226 offset:32768
buffer_store_dwordx4 v[246:249],v255,s[40:43],s87 offen offset:1024 sc1
v_mfma_f32_16x16x32_bf16 v[32:35],v[192:195],v[136:139],v[32:35]
ds_read_b64_tr_b16 v[210:211],v226 offset:33280
ds_read_b128 v[160:163],v240 offset:0
v_mfma_f32_16x16x32_bf16 v[32:35],v[196:199],v[140:143],v[32:35]
ds_read_b64_tr_b16 v[212:213],v226 offset:36864
v_mfma_f32_16x16x32_bf16 v[64:67],v[192:195],v[144:147],v[64:67]
ds_read_b64_tr_b16 v[214:215],v226 offset:37376
v_mfma_f32_16x16x32_bf16 v[64:67],v[196:199],v[148:151],v[64:67]
s_add_u32 m0,s70,32768
s_nop 0
buffer_load_dwordx4 v230,s[48:51],s66 offen offset:0 lds
v_mfma_f32_16x16x32_bf16 v[96:99],v[192:195],v[152:155],v[96:99]
ds_read_b128 v[164:167],v241 offset:0
v_mfma_f32_16x16x32_bf16 v[96:99],v[196:199],v[156:159],v[96:99]
s_waitcnt lgkmcnt(8)
v_mfma_f32_16x16x32_bf16 v[4:7],v[200:203],v[128:131],v[4:7]
s_waitcnt lgkmcnt(6)
v_mfma_f32_16x16x32_bf16 v[4:7],v[204:207],v[132:135],v[4:7]
ds_read_b64_tr_b16 v[216:217],v227 offset:32768
buffer_load_dwordx4 v231,s[48:51],s66 offen offset:1024 lds
v_mfma_f32_16x16x32_bf16 v[36:39],v[200:203],v[136:139],v[36:39]
ds_read_b64_tr_b16 v[218:219],v227 offset:33280
v_mfma_f32_16x16x32_bf16 v[36:39],v[204:207],v[140:143],v[36:39]
ds_read_b64_tr_b16 v[220:221],v227 offset:36864
ds_read_b128 v[168:171],v240 offset:2048
v_mfma_f32_16x16x32_bf16 v[68:71],v[200:203],v[144:147],v[68:71]
ds_read_b64_tr_b16 v[222:223],v227 offset:37376
v_mfma_f32_16x16x32_bf16 v[68:71],v[204:207],v[148:151],v[68:71]
buffer_load_dwordx4 v232,s[48:51],s66 offen offset:2048 lds
v_mfma_f32_16x16x32_bf16 v[100:103],v[200:203],v[152:155],v[100:103]
v_mfma_f32_16x16x32_bf16 v[100:103],v[204:207],v[156:159],v[100:103]
ds_read_b128 v[172:175],v241 offset:2048
s_waitcnt lgkmcnt(10)
v_mfma_f32_16x16x32_bf16 v[8:11],v[208:211],v[128:131],v[8:11]
s_waitcnt lgkmcnt(7)
v_mfma_f32_16x16x32_bf16 v[8:11],v[212:215],v[132:135],v[8:11]
ds_read_b64_tr_b16 v[192:193],v224 offset:40960
buffer_load_dwordx4 v233,s[48:51],s66 offen offset:3072 lds
v_mfma_f32_16x16x32_bf16 v[40:43],v[208:211],v[136:139],v[40:43]
ds_read_b64_tr_b16 v[194:195],v224 offset:41472
v_mfma_f32_16x16x32_bf16 v[40:43],v[212:215],v[140:143],v[40:43]
ds_read_b64_tr_b16 v[196:197],v224 offset:45056
v_mfma_f32_16x16x32_bf16 v[72:75],v[208:211],v[144:147],v[72:75]
ds_read_b64_tr_b16 v[198:199],v224 offset:45568
ds_read_b128 v[176:179],v240 offset:4096
v_mfma_f32_16x16x32_bf16 v[72:75],v[212:215],v[148:151],v[72:75]
s_add_u32 m0,s70,36864
s_nop 0
buffer_load_dwordx4 v230,s[48:51],s67 offen offset:0 lds
v_mfma_f32_16x16x32_bf16 v[104:107],v[208:211],v[152:155],v[104:107]
v_mfma_f32_16x16x32_bf16 v[104:107],v[212:215],v[156:159],v[104:107]
s_waitcnt lgkmcnt(9)
v_mfma_f32_16x16x32_bf16 v[12:15],v[216:219],v[128:131],v[12:15]
v_accvgpr_read_b32 v246,a208
v_accvgpr_read_b32 v247,a209
s_waitcnt lgkmcnt(6)
v_mfma_f32_16x16x32_bf16 v[12:15],v[220:223],v[132:135],v[12:15]
ds_read_b64_tr_b16 v[200:201],v225 offset:40960
ds_read_b128 v[180:183],v241 offset:4096
v_mfma_f32_16x16x32_bf16 v[44:47],v[216:219],v[136:139],v[44:47]
ds_read_b64_tr_b16 v[202:203],v225 offset:41472
buffer_load_dwordx4 v231,s[48:51],s67 offen offset:1024 lds
v_mfma_f32_16x16x32_bf16 v[44:47],v[220:223],v[140:143],v[44:47]
ds_read_b64_tr_b16 v[204:205],v225 offset:45056
v_accvgpr_read_b32 v248,a210
v_mfma_f32_16x16x32_bf16 v[76:79],v[216:219],v[144:147],v[76:79]
ds_read_b64_tr_b16 v[206:207],v225 offset:45568
v_accvgpr_read_b32 v249,a211
v_mfma_f32_16x16x32_bf16 v[76:79],v[220:223],v[148:151],v[76:79]
ds_read_b128 v[184:187],v240 offset:6144
v_accvgpr_read_b32 v250,a212
v_mfma_f32_16x16x32_bf16 v[108:111],v[216:219],v[152:155],v[108:111]
buffer_load_dwordx4 v232,s[48:51],s67 offen offset:2048 lds
v_accvgpr_read_b32 v251,a213
v_mfma_f32_16x16x32_bf16 v[108:111],v[220:223],v[156:159],v[108:111]
v_accvgpr_read_b32 v252,a214
v_accvgpr_read_b32 v253,a215
s_waitcnt lgkmcnt(9)
v_mfma_f32_16x16x32_bf16 v[16:19],v[192:195],v[128:131],v[16:19]
v_cvt_pk_bf16_f32 v246,v246,v247
v_cvt_pk_bf16_f32 v247,v248,v249
s_waitcnt lgkmcnt(7)
v_mfma_f32_16x16x32_bf16 v[16:19],v[196:199],v[132:135],v[16:19]
ds_read_b64_tr_b16 v[208:209],v226 offset:40960
v_cvt_pk_bf16_f32 v248,v250,v251
v_mfma_f32_16x16x32_bf16 v[48:51],v[192:195],v[136:139],v[48:51]
ds_read_b64_tr_b16 v[210:211],v226 offset:41472
ds_read_b128 v[188:191],v241 offset:6144
v_mfma_f32_16x16x32_bf16 v[48:51],v[196:199],v[140:143],v[48:51]
ds_read_b64_tr_b16 v[212:213],v226 offset:45056
buffer_load_dwordx4 v233,s[48:51],s67 offen offset:3072 lds
v_mfma_f32_16x16x32_bf16 v[80:83],v[192:195],v[144:147],v[80:83]
ds_read_b64_tr_b16 v[214:215],v226 offset:45568
v_cvt_pk_bf16_f32 v249,v252,v253
v_mfma_f32_16x16x32_bf16 v[80:83],v[196:199],v[148:151],v[80:83]
s_add_u32 s87,s72,40960
buffer_store_dwordx4 v[246:249],v255,s[40:43],s87 offen offset:2048 sc1
v_mfma_f32_16x16x32_bf16 v[112:115],v[192:195],v[152:155],v[112:115]
v_mfma_f32_16x16x32_bf16 v[112:115],v[196:199],v[156:159],v[112:115]
s_add_u32 s66,s66,128
s_add_u32 s67,s67,128
s_waitcnt lgkmcnt(8)
v_mfma_f32_16x16x32_bf16 v[20:23],v[200:203],v[128:131],v[20:23]
ds_read_b64_tr_b16 v[216:217],v227 offset:40960
ds_read_b64_tr_b16 v[218:219],v227 offset:41472
s_waitcnt lgkmcnt(8)
v_mfma_f32_16x16x32_bf16 v[20:23],v[204:207],v[132:135],v[20:23]
ds_read_b64_tr_b16 v[220:221],v227 offset:45056
ds_read_b64_tr_b16 v[222:223],v227 offset:45568
v_mfma_f32_16x16x32_bf16 v[52:55],v[200:203],v[136:139],v[52:55]
v_accvgpr_read_b32 v246,a216
v_accvgpr_read_b32 v247,a217
v_mfma_f32_16x16x32_bf16 v[52:55],v[204:207],v[140:143],v[52:55]
v_accvgpr_read_b32 v248,a218
v_accvgpr_read_b32 v249,a219
v_mfma_f32_16x16x32_bf16 v[84:87],v[200:203],v[144:147],v[84:87]
v_accvgpr_read_b32 v250,a220
v_accvgpr_read_b32 v251,a221
v_mfma_f32_16x16x32_bf16 v[84:87],v[204:207],v[148:151],v[84:87]
v_accvgpr_read_b32 v252,a222
v_accvgpr_read_b32 v253,a223
v_mfma_f32_16x16x32_bf16 v[116:119],v[200:203],v[152:155],v[116:119]
v_cvt_pk_bf16_f32 v246,v246,v247
v_cvt_pk_bf16_f32 v247,v248,v249
v_mfma_f32_16x16x32_bf16 v[116:119],v[204:207],v[156:159],v[116:119]
s_waitcnt vmcnt(16) lgkmcnt(0)
s_cmp_lg_u32 s56,0
s_cbranch_scc1 L_PXK_%=
v_mov_b32 v243,s99
s_mov_b32 s95,0
s_branch L_PCK_%=
L_PLK_%=:
global_load_dword v242,v243,s[22:23] sc1
s_waitcnt vmcnt(0)
L_PCK_%=:
v_readfirstlane_b32 s58,v242
s_nop 3
s_cmp_ge_u32 s58,6
s_cbranch_scc1 L_PDK_%=
s_sleep 2
s_add_u32 s95,s95,1
s_cmp_lt_u32 s95,0x20000
s_cbranch_scc1 L_PLK_%=
L_PDK_%=:
buffer_inv sc1
L_PXK_%=:
s_barrier
v_mfma_f32_16x16x32_bf16 v[24:27],v[208:211],v[128:131],v[24:27]
s_add_u32 m0,s71,131072
s_nop 0
buffer_load_dwordx4 v234,s[52:55],s68 offen offset:0 lds
v_mfma_f32_16x16x32_bf16 v[24:27],v[212:215],v[132:135],v[24:27]
ds_read_b64_tr_b16 v[192:193],v224 offset:49152
v_cvt_pk_bf16_f32 v248,v250,v251
v_mfma_f32_16x16x32_bf16 v[56:59],v[208:211],v[136:139],v[56:59]
ds_read_b64_tr_b16 v[194:195],v224 offset:49664
v_cvt_pk_bf16_f32 v249,v252,v253
v_mfma_f32_16x16x32_bf16 v[56:59],v[212:215],v[140:143],v[56:59]
ds_read_b64_tr_b16 v[196:197],v224 offset:53248
s_add_u32 s87,s72,40960
v_mfma_f32_16x16x32_bf16 v[88:91],v[208:211],v[144:147],v[88:91]
ds_read_b64_tr_b16 v[198:199],v224 offset:53760
buffer_load_dwordx4 v235,s[52:55],s68 offen offset:1024 lds
v_mfma_f32_16x16x32_bf16 v[88:91],v[212:215],v[148:151],v[88:91]
buffer_store_dwordx4 v[246:249],v255,s[40:43],s87 offen offset:3072 sc1
v_mfma_f32_16x16x32_bf16 v[120:123],v[208:211],v[152:155],v[120:123]
v_mfma_f32_16x16x32_bf16 v[120:123],v[212:215],v[156:159],v[120:123]
v_accvgpr_read_b32 v246,a224
v_accvgpr_read_b32 v247,a225
v_mfma_f32_16x16x32_bf16 v[28:31],v[216:219],v[128:131],v[28:31]
buffer_load_dwordx4 v236,s[52:55],s68 offen offset:2048 lds
v_accvgpr_read_b32 v248,a226
v_mfma_f32_16x16x32_bf16 v[28:31],v[220:223],v[132:135],v[28:31]
ds_read_b64_tr_b16 v[200:201],v225 offset:49152
v_accvgpr_read_b32 v249,a227
v_mfma_f32_16x16x32_bf16 v[60:63],v[216:219],v[136:139],v[60:63]
ds_read_b64_tr_b16 v[202:203],v225 offset:49664
v_accvgpr_read_b32 v250,a228
v_mfma_f32_16x16x32_bf16 v[60:63],v[220:223],v[140:143],v[60:63]
ds_read_b64_tr_b16 v[204:205],v225 offset:53248
v_accvgpr_read_b32 v251,a229
v_mfma_f32_16x16x32_bf16 v[92:95],v[216:219],v[144:147],v[92:95]
ds_read_b64_tr_b16 v[206:207],v225 offset:53760
buffer_load_dwordx4 v237,s[52:55],s68 offen offset:3072 lds
v_mfma_f32_16x16x32_bf16 v[92:95],v[220:223],v[148:151],v[92:95]
v_accvgpr_read_b32 v252,a230
v_accvgpr_read_b32 v253,a231
v_mfma_f32_16x16x32_bf16 v[124:127],v[216:219],v[152:155],v[124:127]
v_cvt_pk_bf16_f32 v246,v246,v247
v_cvt_pk_bf16_f32 v247,v248,v249
v_mfma_f32_16x16x32_bf16 v[124:127],v[220:223],v[156:159],v[124:127]
s_add_u32 s68,s68,524288
v_cvt_pk_bf16_f32 v248,v250,v251
s_waitcnt lgkmcnt(6)
v_mfma_f32_16x16x32_bf16 v[0:3],v[192:195],v[160:163],v[0:3]
v_cvt_pk_bf16_f32 v249,v252,v253
s_add_u32 s87,s72,45056
s_waitcnt lgkmcnt(4)
v_mfma_f32_16x16x32_bf16 v[0:3],v[196:199],v[164:167],v[0:3]
ds_read_b64_tr_b16 v[208:209],v226 offset:49152
buffer_store_dwordx4 v[246:249],v255,s[40:43],s87 offen offset:0 sc1
v_mfma_f32_16x16x32_bf16 v[32:35],v[192:195],v[168:171],v[32:35]
ds_read_b64_tr_b16 v[210:211],v226 offset:49664
ds_read_b128 v[128:131],v238 offset:0
v_mfma_f32_16x16x32_bf16 v[32:35],v[196:199],v[172:175],v[32:35]
ds_read_b64_tr_b16 v[212:213],v226 offset:53248
v_mfma_f32_16x16x32_bf16 v[64:67],v[192:195],v[176:179],v[64:67]
ds_read_b64_tr_b16 v[214:215],v226 offset:53760
v_mfma_f32_16x16x32_bf16 v[64:67],v[196:199],v[180:183],v[64:67]
)"
R"(v_mfma_f32_16x16x32_bf16 v[96:99],v[192:195],v[184:187],v[96:99]
ds_read_b128 v[132:135],v239 offset:0
v_mfma_f32_16x16x32_bf16 v[96:99],v[196:199],v[188:191],v[96:99]
s_waitcnt lgkmcnt(8)
v_mfma_f32_16x16x32_bf16 v[4:7],v[200:203],v[160:163],v[4:7]
s_waitcnt lgkmcnt(6)
v_mfma_f32_16x16x32_bf16 v[4:7],v[204:207],v[164:167],v[4:7]
ds_read_b64_tr_b16 v[216:217],v227 offset:49152
v_mfma_f32_16x16x32_bf16 v[36:39],v[200:203],v[168:171],v[36:39]
ds_read_b64_tr_b16 v[218:219],v227 offset:49664
v_mfma_f32_16x16x32_bf16 v[36:39],v[204:207],v[172:175],v[36:39]
ds_read_b64_tr_b16 v[220:221],v227 offset:53248
ds_read_b128 v[136:139],v238 offset:2048
v_mfma_f32_16x16x32_bf16 v[68:71],v[200:203],v[176:179],v[68:71]
ds_read_b64_tr_b16 v[222:223],v227 offset:53760
v_mfma_f32_16x16x32_bf16 v[68:71],v[204:207],v[180:183],v[68:71]
v_mfma_f32_16x16x32_bf16 v[100:103],v[200:203],v[184:187],v[100:103]
v_mfma_f32_16x16x32_bf16 v[100:103],v[204:207],v[188:191],v[100:103]
ds_read_b128 v[140:143],v239 offset:2048
s_waitcnt lgkmcnt(10)
v_mfma_f32_16x16x32_bf16 v[8:11],v[208:211],v[160:163],v[8:11]
s_waitcnt lgkmcnt(7)
v_mfma_f32_16x16x32_bf16 v[8:11],v[212:215],v[164:167],v[8:11]
ds_read_b64_tr_b16 v[192:193],v224 offset:57344
v_mfma_f32_16x16x32_bf16 v[40:43],v[208:211],v[168:171],v[40:43]
ds_read_b64_tr_b16 v[194:195],v224 offset:57856
v_mfma_f32_16x16x32_bf16 v[40:43],v[212:215],v[172:175],v[40:43]
ds_read_b64_tr_b16 v[196:197],v224 offset:61440
v_mfma_f32_16x16x32_bf16 v[72:75],v[208:211],v[176:179],v[72:75]
ds_read_b64_tr_b16 v[198:199],v224 offset:61952
ds_read_b128 v[144:147],v238 offset:4096
v_mfma_f32_16x16x32_bf16 v[72:75],v[212:215],v[180:183],v[72:75]
v_mfma_f32_16x16x32_bf16 v[104:107],v[208:211],v[184:187],v[104:107]
v_mfma_f32_16x16x32_bf16 v[104:107],v[212:215],v[188:191],v[104:107]
s_waitcnt lgkmcnt(9)
v_mfma_f32_16x16x32_bf16 v[12:15],v[216:219],v[160:163],v[12:15]
v_accvgpr_read_b32 v246,a232
v_accvgpr_read_b32 v247,a233
s_waitcnt lgkmcnt(6)
v_mfma_f32_16x16x32_bf16 v[12:15],v[220:223],v[164:167],v[12:15]
ds_read_b64_tr_b16 v[200:201],v225 offset:57344
ds_read_b128 v[148:151],v239 offset:4096
v_mfma_f32_16x16x32_bf16 v[44:47],v[216:219],v[168:171],v[44:47]
ds_read_b64_tr_b16 v[202:203],v225 offset:57856
v_accvgpr_read_b32 v248,a234
v_mfma_f32_16x16x32_bf16 v[44:47],v[220:223],v[172:175],v[44:47]
ds_read_b64_tr_b16 v[204:205],v225 offset:61440
v_accvgpr_read_b32 v249,a235
v_mfma_f32_16x16x32_bf16 v[76:79],v[216:219],v[176:179],v[76:79]
ds_read_b64_tr_b16 v[206:207],v225 offset:61952
v_accvgpr_read_b32 v250,a236
v_mfma_f32_16x16x32_bf16 v[76:79],v[220:223],v[180:183],v[76:79]
ds_read_b128 v[152:155],v238 offset:6144
v_accvgpr_read_b32 v251,a237
v_mfma_f32_16x16x32_bf16 v[108:111],v[216:219],v[184:187],v[108:111]
v_accvgpr_read_b32 v252,a238
v_accvgpr_read_b32 v253,a239
v_mfma_f32_16x16x32_bf16 v[108:111],v[220:223],v[188:191],v[108:111]
v_cvt_pk_bf16_f32 v246,v246,v247
v_cvt_pk_bf16_f32 v247,v248,v249
s_waitcnt lgkmcnt(9)
v_mfma_f32_16x16x32_bf16 v[16:19],v[192:195],v[160:163],v[16:19]
v_cvt_pk_bf16_f32 v248,v250,v251
v_cvt_pk_bf16_f32 v249,v252,v253
s_waitcnt lgkmcnt(7)
v_mfma_f32_16x16x32_bf16 v[16:19],v[196:199],v[164:167],v[16:19]
ds_read_b64_tr_b16 v[208:209],v226 offset:57344
s_add_u32 s87,s72,45056
v_mfma_f32_16x16x32_bf16 v[48:51],v[192:195],v[168:171],v[48:51]
ds_read_b64_tr_b16 v[210:211],v226 offset:57856
ds_read_b128 v[156:159],v239 offset:6144
v_mfma_f32_16x16x32_bf16 v[48:51],v[196:199],v[172:175],v[48:51]
ds_read_b64_tr_b16 v[212:213],v226 offset:61440
buffer_store_dwordx4 v[246:249],v255,s[40:43],s87 offen offset:1024 sc1
v_mfma_f32_16x16x32_bf16 v[80:83],v[192:195],v[176:179],v[80:83]
ds_read_b64_tr_b16 v[214:215],v226 offset:61952
v_mfma_f32_16x16x32_bf16 v[80:83],v[196:199],v[180:183],v[80:83]
v_accvgpr_read_b32 v246,a240
v_accvgpr_read_b32 v247,a241
v_mfma_f32_16x16x32_bf16 v[112:115],v[192:195],v[184:187],v[112:115]
v_accvgpr_read_b32 v248,a242
v_accvgpr_read_b32 v249,a243
v_mfma_f32_16x16x32_bf16 v[112:115],v[196:199],v[188:191],v[112:115]
v_accvgpr_read_b32 v250,a244
v_accvgpr_read_b32 v251,a245
s_waitcnt lgkmcnt(8)
v_mfma_f32_16x16x32_bf16 v[20:23],v[200:203],v[160:163],v[20:23]
ds_read_b64_tr_b16 v[216:217],v227 offset:57344
ds_read_b64_tr_b16 v[218:219],v227 offset:57856
s_waitcnt lgkmcnt(8)
v_mfma_f32_16x16x32_bf16 v[20:23],v[204:207],v[164:167],v[20:23]
ds_read_b64_tr_b16 v[220:221],v227 offset:61440
ds_read_b64_tr_b16 v[222:223],v227 offset:61952
v_mfma_f32_16x16x32_bf16 v[52:55],v[200:203],v[168:171],v[52:55]
v_accvgpr_read_b32 v252,a246
v_accvgpr_read_b32 v253,a247
v_mfma_f32_16x16x32_bf16 v[52:55],v[204:207],v[172:175],v[52:55]
v_cvt_pk_bf16_f32 v246,v246,v247
v_cvt_pk_bf16_f32 v247,v248,v249
v_mfma_f32_16x16x32_bf16 v[84:87],v[200:203],v[176:179],v[84:87]
v_cvt_pk_bf16_f32 v248,v250,v251
v_cvt_pk_bf16_f32 v249,v252,v253
v_mfma_f32_16x16x32_bf16 v[84:87],v[204:207],v[180:183],v[84:87]
s_add_u32 s87,s72,45056
buffer_store_dwordx4 v[246:249],v255,s[40:43],s87 offen offset:2048 sc1
v_mfma_f32_16x16x32_bf16 v[116:119],v[200:203],v[184:187],v[116:119]
v_mfma_f32_16x16x32_bf16 v[116:119],v[204:207],v[188:191],v[116:119]
s_waitcnt vmcnt(8)
s_waitcnt vmcnt(8) lgkmcnt(0)
s_barrier
v_mfma_f32_16x16x32_bf16 v[24:27],v[208:211],v[160:163],v[24:27]
s_mov_b32 m0,s81
s_mov_b32 s80,0x0
buffer_load_dwordx4 v228,s[36:39],s80 offen offset:0 lds
v_mfma_f32_16x16x32_bf16 v[24:27],v[212:215],v[164:167],v[24:27]
ds_read_b64_tr_b16 v[192:193],v224 offset:0
v_accvgpr_read_b32 v246,a248
v_mfma_f32_16x16x32_bf16 v[56:59],v[208:211],v[168:171],v[56:59]
ds_read_b64_tr_b16 v[194:195],v224 offset:512
v_accvgpr_read_b32 v247,a249
v_mfma_f32_16x16x32_bf16 v[56:59],v[212:215],v[172:175],v[56:59]
ds_read_b64_tr_b16 v[196:197],v224 offset:4096
v_accvgpr_read_b32 v248,a250
v_mfma_f32_16x16x32_bf16 v[88:91],v[208:211],v[176:179],v[88:91]
ds_read_b64_tr_b16 v[198:199],v224 offset:4608
buffer_load_dwordx4 v228,s[36:39],s80 offen offset:1024 lds
v_mfma_f32_16x16x32_bf16 v[88:91],v[212:215],v[180:183],v[88:91]
v_accvgpr_read_b32 v249,a251
v_accvgpr_read_b32 v250,a252
v_mfma_f32_16x16x32_bf16 v[120:123],v[208:211],v[184:187],v[120:123]
v_accvgpr_read_b32 v251,a253
v_accvgpr_read_b32 v252,a254
v_mfma_f32_16x16x32_bf16 v[120:123],v[212:215],v[188:191],v[120:123]
v_accvgpr_read_b32 v253,a255
v_cvt_pk_bf16_f32 v246,v246,v247
v_mfma_f32_16x16x32_bf16 v[28:31],v[216:219],v[160:163],v[28:31]
buffer_load_dwordx4 v228,s[36:39],s80 offen offset:2048 lds
v_cvt_pk_bf16_f32 v247,v248,v249
v_mfma_f32_16x16x32_bf16 v[28:31],v[220:223],v[164:167],v[28:31]
ds_read_b64_tr_b16 v[200:201],v225 offset:0
v_cvt_pk_bf16_f32 v248,v250,v251
v_mfma_f32_16x16x32_bf16 v[60:63],v[216:219],v[168:171],v[60:63]
ds_read_b64_tr_b16 v[202:203],v225 offset:512
v_cvt_pk_bf16_f32 v249,v252,v253
v_mfma_f32_16x16x32_bf16 v[60:63],v[220:223],v[172:175],v[60:63]
ds_read_b64_tr_b16 v[204:205],v225 offset:4096
s_add_u32 s87,s72,45056
v_mfma_f32_16x16x32_bf16 v[92:95],v[216:219],v[176:179],v[92:95]
ds_read_b64_tr_b16 v[206:207],v225 offset:4608
buffer_load_dwordx4 v228,s[36:39],s80 offen offset:3072 lds
v_mfma_f32_16x16x32_bf16 v[92:95],v[220:223],v[180:183],v[92:95]
buffer_store_dwordx4 v[246:249],v255,s[40:43],s87 offen offset:3072 sc1
v_mfma_f32_16x16x32_bf16 v[124:127],v[216:219],v[184:187],v[124:127]
v_mfma_f32_16x16x32_bf16 v[124:127],v[220:223],v[188:191],v[124:127]
s_waitcnt lgkmcnt(6)
v_mfma_f32_16x16x32_bf16 v[0:3],v[192:195],v[128:131],v[0:3]
s_add_u32 m0,s81,0x10000
s_mov_b32 s80,0x4000
buffer_load_dwordx4 v228,s[36:39],s80 offen offset:0 lds
s_waitcnt lgkmcnt(4)
v_mfma_f32_16x16x32_bf16 v[0:3],v[196:199],v[132:135],v[0:3]
ds_read_b64_tr_b16 v[208:209],v226 offset:0
v_mfma_f32_16x16x32_bf16 v[32:35],v[192:195],v[136:139],v[32:35]
ds_read_b64_tr_b16 v[210:211],v226 offset:512
ds_read_b128 v[160:163],v238 offset:32768
v_mfma_f32_16x16x32_bf16 v[32:35],v[196:199],v[140:143],v[32:35]
ds_read_b64_tr_b16 v[212:213],v226 offset:4096
v_mfma_f32_16x16x32_bf16 v[64:67],v[192:195],v[144:147],v[64:67]
ds_read_b64_tr_b16 v[214:215],v226 offset:4608
buffer_load_dwordx4 v228,s[36:39],s80 offen offset:1024 lds
v_mfma_f32_16x16x32_bf16 v[64:67],v[196:199],v[148:151],v[64:67]
v_mfma_f32_16x16x32_bf16 v[96:99],v[192:195],v[152:155],v[96:99]
ds_read_b128 v[164:167],v239 offset:32768
v_mfma_f32_16x16x32_bf16 v[96:99],v[196:199],v[156:159],v[96:99]
s_waitcnt lgkmcnt(8)
v_mfma_f32_16x16x32_bf16 v[4:7],v[200:203],v[128:131],v[4:7]
buffer_load_dwordx4 v228,s[36:39],s80 offen offset:2048 lds
s_waitcnt lgkmcnt(6)
v_mfma_f32_16x16x32_bf16 v[4:7],v[204:207],v[132:135],v[4:7]
ds_read_b64_tr_b16 v[216:217],v227 offset:0
v_mfma_f32_16x16x32_bf16 v[36:39],v[200:203],v[136:139],v[36:39]
ds_read_b64_tr_b16 v[218:219],v227 offset:512
v_mfma_f32_16x16x32_bf16 v[36:39],v[204:207],v[140:143],v[36:39]
ds_read_b64_tr_b16 v[220:221],v227 offset:4096
ds_read_b128 v[168:171],v238 offset:34816
v_mfma_f32_16x16x32_bf16 v[68:71],v[200:203],v[144:147],v[68:71]
ds_read_b64_tr_b16 v[222:223],v227 offset:4608
buffer_load_dwordx4 v228,s[36:39],s80 offen offset:3072 lds
v_mfma_f32_16x16x32_bf16 v[68:71],v[204:207],v[148:151],v[68:71]
v_mfma_f32_16x16x32_bf16 v[100:103],v[200:203],v[152:155],v[100:103]
v_mfma_f32_16x16x32_bf16 v[100:103],v[204:207],v[156:159],v[100:103]
ds_read_b128 v[172:175],v239 offset:34816
s_waitcnt lgkmcnt(10)
v_mfma_f32_16x16x32_bf16 v[8:11],v[208:211],v[128:131],v[8:11]
s_waitcnt lgkmcnt(7)
v_mfma_f32_16x16x32_bf16 v[8:11],v[212:215],v[132:135],v[8:11]
ds_read_b64_tr_b16 v[192:193],v224 offset:8192
v_mfma_f32_16x16x32_bf16 v[40:43],v[208:211],v[136:139],v[40:43]
ds_read_b64_tr_b16 v[194:195],v224 offset:8704
v_mfma_f32_16x16x32_bf16 v[40:43],v[212:215],v[140:143],v[40:43]
ds_read_b64_tr_b16 v[196:197],v224 offset:12288
v_mfma_f32_16x16x32_bf16 v[72:75],v[208:211],v[144:147],v[72:75]
ds_read_b64_tr_b16 v[198:199],v224 offset:12800
ds_read_b128 v[176:179],v238 offset:36864
v_mfma_f32_16x16x32_bf16 v[72:75],v[212:215],v[148:151],v[72:75]
v_mfma_f32_16x16x32_bf16 v[104:107],v[208:211],v[152:155],v[104:107]
v_mfma_f32_16x16x32_bf16 v[104:107],v[212:215],v[156:159],v[104:107]
s_waitcnt lgkmcnt(9)
v_mfma_f32_16x16x32_bf16 v[12:15],v[216:219],v[128:131],v[12:15]
s_waitcnt lgkmcnt(6)
v_mfma_f32_16x16x32_bf16 v[12:15],v[220:223],v[132:135],v[12:15]
ds_read_b64_tr_b16 v[200:201],v225 offset:8192
ds_read_b128 v[180:183],v239 offset:36864
v_mfma_f32_16x16x32_bf16 v[44:47],v[216:219],v[136:139],v[44:47]
ds_read_b64_tr_b16 v[202:203],v225 offset:8704
v_mfma_f32_16x16x32_bf16 v[44:47],v[220:223],v[140:143],v[44:47]
ds_read_b64_tr_b16 v[204:205],v225 offset:12288
v_mfma_f32_16x16x32_bf16 v[76:79],v[216:219],v[144:147],v[76:79]
ds_read_b64_tr_b16 v[206:207],v225 offset:12800
v_mfma_f32_16x16x32_bf16 v[76:79],v[220:223],v[148:151],v[76:79]
ds_read_b128 v[184:187],v238 offset:38912
v_mfma_f32_16x16x32_bf16 v[108:111],v[216:219],v[152:155],v[108:111]
v_mfma_f32_16x16x32_bf16 v[108:111],v[220:223],v[156:159],v[108:111]
s_waitcnt lgkmcnt(9)
v_mfma_f32_16x16x32_bf16 v[16:19],v[192:195],v[128:131],v[16:19]
s_waitcnt lgkmcnt(7)
v_mfma_f32_16x16x32_bf16 v[16:19],v[196:199],v[132:135],v[16:19]
ds_read_b64_tr_b16 v[208:209],v226 offset:8192
v_mfma_f32_16x16x32_bf16 v[48:51],v[192:195],v[136:139],v[48:51]
ds_read_b64_tr_b16 v[210:211],v226 offset:8704
ds_read_b128 v[188:191],v239 offset:38912
v_mfma_f32_16x16x32_bf16 v[48:51],v[196:199],v[140:143],v[48:51]
ds_read_b64_tr_b16 v[212:213],v226 offset:12288
v_mfma_f32_16x16x32_bf16 v[80:83],v[192:195],v[144:147],v[80:83]
ds_read_b64_tr_b16 v[214:215],v226 offset:12800
v_mfma_f32_16x16x32_bf16 v[80:83],v[196:199],v[148:151],v[80:83]
v_mfma_f32_16x16x32_bf16 v[112:115],v[192:195],v[152:155],v[112:115]
v_mfma_f32_16x16x32_bf16 v[112:115],v[196:199],v[156:159],v[112:115]
s_waitcnt lgkmcnt(8)
v_mfma_f32_16x16x32_bf16 v[20:23],v[200:203],v[128:131],v[20:23]
ds_read_b64_tr_b16 v[216:217],v227 offset:8192
ds_read_b64_tr_b16 v[218:219],v227 offset:8704
s_waitcnt lgkmcnt(8)
v_mfma_f32_16x16x32_bf16 v[20:23],v[204:207],v[132:135],v[20:23]
ds_read_b64_tr_b16 v[220:221],v227 offset:12288
ds_read_b64_tr_b16 v[222:223],v227 offset:12800
v_mfma_f32_16x16x32_bf16 v[52:55],v[200:203],v[136:139],v[52:55]
v_mfma_f32_16x16x32_bf16 v[52:55],v[204:207],v[140:143],v[52:55]
v_mfma_f32_16x16x32_bf16 v[84:87],v[200:203],v[144:147],v[84:87]
v_mfma_f32_16x16x32_bf16 v[84:87],v[204:207],v[148:151],v[84:87]
v_mfma_f32_16x16x32_bf16 v[116:119],v[200:203],v[152:155],v[116:119]
v_mfma_f32_16x16x32_bf16 v[116:119],v[204:207],v[156:159],v[116:119]
s_waitcnt vmcnt(12) lgkmcnt(0)
s_barrier
v_mfma_f32_16x16x32_bf16 v[24:27],v[208:211],v[128:131],v[24:27]
s_mov_b32 m0,s82
s_mov_b32 s80,0x8000
buffer_load_dwordx4 v228,s[36:39],s80 offen offset:0 lds
v_mfma_f32_16x16x32_bf16 v[24:27],v[212:215],v[132:135],v[24:27]
ds_read_b64_tr_b16 v[192:193],v224 offset:32768
v_mfma_f32_16x16x32_bf16 v[56:59],v[208:211],v[136:139],v[56:59]
ds_read_b64_tr_b16 v[194:195],v224 offset:33280
v_mfma_f32_16x16x32_bf16 v[56:59],v[212:215],v[140:143],v[56:59]
ds_read_b64_tr_b16 v[196:197],v224 offset:36864
v_mfma_f32_16x16x32_bf16 v[88:91],v[208:211],v[144:147],v[88:91]
ds_read_b64_tr_b16 v[198:199],v224 offset:37376
buffer_load_dwordx4 v228,s[36:39],s80 offen offset:1024 lds
v_mfma_f32_16x16x32_bf16 v[88:91],v[212:215],v[148:151],v[88:91]
v_mfma_f32_16x16x32_bf16 v[120:123],v[208:211],v[152:155],v[120:123]
v_mfma_f32_16x16x32_bf16 v[120:123],v[212:215],v[156:159],v[120:123]
v_mfma_f32_16x16x32_bf16 v[28:31],v[216:219],v[128:131],v[28:31]
buffer_load_dwordx4 v228,s[36:39],s80 offen offset:2048 lds
v_mfma_f32_16x16x32_bf16 v[28:31],v[220:223],v[132:135],v[28:31]
ds_read_b64_tr_b16 v[200:201],v225 offset:32768
v_mfma_f32_16x16x32_bf16 v[60:63],v[216:219],v[136:139],v[60:63]
ds_read_b64_tr_b16 v[202:203],v225 offset:33280
v_mfma_f32_16x16x32_bf16 v[60:63],v[220:223],v[140:143],v[60:63]
ds_read_b64_tr_b16 v[204:205],v225 offset:36864
v_mfma_f32_16x16x32_bf16 v[92:95],v[216:219],v[144:147],v[92:95]
ds_read_b64_tr_b16 v[206:207],v225 offset:37376
buffer_load_dwordx4 v228,s[36:39],s80 offen offset:3072 lds
v_mfma_f32_16x16x32_bf16 v[92:95],v[220:223],v[148:151],v[92:95]
v_mfma_f32_16x16x32_bf16 v[124:127],v[216:219],v[152:155],v[124:127]
v_mfma_f32_16x16x32_bf16 v[124:127],v[220:223],v[156:159],v[124:127]
s_waitcnt lgkmcnt(6)
v_mfma_f32_16x16x32_bf16 v[0:3],v[192:195],v[160:163],v[0:3]
s_add_u32 m0,s82,0x10000
s_mov_b32 s80,0xc000
buffer_load_dwordx4 v228,s[36:39],s80 offen offset:0 lds
s_waitcnt lgkmcnt(4)
v_mfma_f32_16x16x32_bf16 v[0:3],v[196:199],v[164:167],v[0:3]
ds_read_b64_tr_b16 v[208:209],v226 offset:32768
v_mfma_f32_16x16x32_bf16 v[32:35],v[192:195],v[168:171],v[32:35]
ds_read_b64_tr_b16 v[210:211],v226 offset:33280
v_mfma_f32_16x16x32_bf16 v[32:35],v[196:199],v[172:175],v[32:35]
ds_read_b64_tr_b16 v[212:213],v226 offset:36864
v_mfma_f32_16x16x32_bf16 v[64:67],v[192:195],v[176:179],v[64:67]
ds_read_b64_tr_b16 v[214:215],v226 offset:37376
buffer_load_dwordx4 v228,s[36:39],s80 offen offset:1024 lds
v_mfma_f32_16x16x32_bf16 v[64:67],v[196:199],v[180:183],v[64:67]
v_mfma_f32_16x16x32_bf16 v[96:99],v[192:195],v[184:187],v[96:99]
v_mfma_f32_16x16x32_bf16 v[96:99],v[196:199],v[188:191],v[96:99]
s_waitcnt lgkmcnt(6)
v_mfma_f32_16x16x32_bf16 v[4:7],v[200:203],v[160:163],v[4:7]
buffer_load_dwordx4 v228,s[36:39],s80 offen offset:2048 lds
s_waitcnt lgkmcnt(4)
v_mfma_f32_16x16x32_bf16 v[4:7],v[204:207],v[164:167],v[4:7]
ds_read_b64_tr_b16 v[216:217],v227 offset:32768
v_mfma_f32_16x16x32_bf16 v[36:39],v[200:203],v[168:171],v[36:39]
ds_read_b64_tr_b16 v[218:219],v227 offset:33280
v_mfma_f32_16x16x32_bf16 v[36:39],v[204:207],v[172:175],v[36:39]
ds_read_b64_tr_b16 v[220:221],v227 offset:36864
v_mfma_f32_16x16x32_bf16 v[68:71],v[200:203],v[176:179],v[68:71]
ds_read_b64_tr_b16 v[222:223],v227 offset:37376
buffer_load_dwordx4 v228,s[36:39],s80 offen offset:3072 lds
v_mfma_f32_16x16x32_bf16 v[68:71],v[204:207],v[180:183],v[68:71]
v_mfma_f32_16x16x32_bf16 v[100:103],v[200:203],v[184:187],v[100:103]
v_mfma_f32_16x16x32_bf16 v[100:103],v[204:207],v[188:191],v[100:103]
s_waitcnt lgkmcnt(6)
v_mfma_f32_16x16x32_bf16 v[8:11],v[208:211],v[160:163],v[8:11]
s_waitcnt lgkmcnt(4)
v_mfma_f32_16x16x32_bf16 v[8:11],v[212:215],v[164:167],v[8:11]
ds_read_b64_tr_b16 v[192:193],v224 offset:40960
v_mfma_f32_16x16x32_bf16 v[40:43],v[208:211],v[168:171],v[40:43]
ds_read_b64_tr_b16 v[194:195],v224 offset:41472
v_mfma_f32_16x16x32_bf16 v[40:43],v[212:215],v[172:175],v[40:43]
ds_read_b64_tr_b16 v[196:197],v224 offset:45056
v_mfma_f32_16x16x32_bf16 v[72:75],v[208:211],v[176:179],v[72:75]
ds_read_b64_tr_b16 v[198:199],v224 offset:45568
v_mfma_f32_16x16x32_bf16 v[72:75],v[212:215],v[180:183],v[72:75]
v_mfma_f32_16x16x32_bf16 v[104:107],v[208:211],v[184:187],v[104:107]
v_mfma_f32_16x16x32_bf16 v[104:107],v[212:215],v[188:191],v[104:107]
s_waitcnt lgkmcnt(6)
v_mfma_f32_16x16x32_bf16 v[12:15],v[216:219],v[160:163],v[12:15]
s_waitcnt lgkmcnt(4)
v_mfma_f32_16x16x32_bf16 v[12:15],v[220:223],v[164:167],v[12:15]
ds_read_b64_tr_b16 v[200:201],v225 offset:40960
v_mfma_f32_16x16x32_bf16 v[44:47],v[216:219],v[168:171],v[44:47]
ds_read_b64_tr_b16 v[202:203],v225 offset:41472
v_mfma_f32_16x16x32_bf16 v[44:47],v[220:223],v[172:175],v[44:47]
ds_read_b64_tr_b16 v[204:205],v225 offset:45056
v_mfma_f32_16x16x32_bf16 v[76:79],v[216:219],v[176:179],v[76:79]
ds_read_b64_tr_b16 v[206:207],v225 offset:45568
v_mfma_f32_16x16x32_bf16 v[76:79],v[220:223],v[180:183],v[76:79]
v_mfma_f32_16x16x32_bf16 v[108:111],v[216:219],v[184:187],v[108:111]
v_mfma_f32_16x16x32_bf16 v[108:111],v[220:223],v[188:191],v[108:111]
s_waitcnt lgkmcnt(6)
v_mfma_f32_16x16x32_bf16 v[16:19],v[192:195],v[160:163],v[16:19]
s_waitcnt lgkmcnt(4)
v_mfma_f32_16x16x32_bf16 v[16:19],v[196:199],v[164:167],v[16:19]
ds_read_b64_tr_b16 v[208:209],v226 offset:40960
v_mfma_f32_16x16x32_bf16 v[48:51],v[192:195],v[168:171],v[48:51]
ds_read_b64_tr_b16 v[210:211],v226 offset:41472
v_mfma_f32_16x16x32_bf16 v[48:51],v[196:199],v[172:175],v[48:51]
ds_read_b64_tr_b16 v[212:213],v226 offset:45056
v_mfma_f32_16x16x32_bf16 v[80:83],v[192:195],v[176:179],v[80:83]
ds_read_b64_tr_b16 v[214:215],v226 offset:45568
v_mfma_f32_16x16x32_bf16 v[80:83],v[196:199],v[180:183],v[80:83]
v_mfma_f32_16x16x32_bf16 v[112:115],v[192:195],v[184:187],v[112:115]
v_mfma_f32_16x16x32_bf16 v[112:115],v[196:199],v[188:191],v[112:115]
s_waitcnt lgkmcnt(6)
v_mfma_f32_16x16x32_bf16 v[20:23],v[200:203],v[160:163],v[20:23]
ds_read_b64_tr_b16 v[216:217],v227 offset:40960
ds_read_b64_tr_b16 v[218:219],v227 offset:41472
s_waitcnt lgkmcnt(6)
v_mfma_f32_16x16x32_bf16 v[20:23],v[204:207],v[164:167],v[20:23]
ds_read_b64_tr_b16 v[220:221],v227 offset:45056
ds_read_b64_tr_b16 v[222:223],v227 offset:45568
v_mfma_f32_16x16x32_bf16 v[52:55],v[200:203],v[168:171],v[52:55]
v_mfma_f32_16x16x32_bf16 v[52:55],v[204:207],v[172:175],v[52:55]
v_mfma_f32_16x16x32_bf16 v[84:87],v[200:203],v[176:179],v[84:87]
v_mfma_f32_16x16x32_bf16 v[84:87],v[204:207],v[180:183],v[84:87]
v_mfma_f32_16x16x32_bf16 v[116:119],v[200:203],v[184:187],v[116:119]
v_mfma_f32_16x16x32_bf16 v[116:119],v[204:207],v[188:191],v[116:119]
s_waitcnt lgkmcnt(6)
v_mfma_f32_16x16x32_bf16 v[24:27],v[208:211],v[160:163],v[24:27]
s_waitcnt lgkmcnt(4)
v_mfma_f32_16x16x32_bf16 v[24:27],v[212:215],v[164:167],v[24:27]
v_mfma_f32_16x16x32_bf16 v[56:59],v[208:211],v[168:171],v[56:59]
v_mfma_f32_16x16x32_bf16 v[56:59],v[212:215],v[172:175],v[56:59]
v_mfma_f32_16x16x32_bf16 v[88:91],v[208:211],v[176:179],v[88:91]
v_mfma_f32_16x16x32_bf16 v[88:91],v[212:215],v[180:183],v[88:91]
v_mfma_f32_16x16x32_bf16 v[120:123],v[208:211],v[184:187],v[120:123]
v_mfma_f32_16x16x32_bf16 v[120:123],v[212:215],v[188:191],v[120:123]
s_waitcnt lgkmcnt(2)
v_mfma_f32_16x16x32_bf16 v[28:31],v[216:219],v[160:163],v[28:31]
s_waitcnt lgkmcnt(0)
v_mfma_f32_16x16x32_bf16 v[28:31],v[220:223],v[164:167],v[28:31]
v_mfma_f32_16x16x32_bf16 v[60:63],v[216:219],v[168:171],v[60:63]
v_mfma_f32_16x16x32_bf16 v[60:63],v[220:223],v[172:175],v[60:63]
v_mfma_f32_16x16x32_bf16 v[92:95],v[216:219],v[176:179],v[92:95]
v_mfma_f32_16x16x32_bf16 v[92:95],v[220:223],v[180:183],v[92:95]
v_mfma_f32_16x16x32_bf16 v[124:127],v[216:219],v[184:187],v[124:127]
v_mfma_f32_16x16x32_bf16 v[124:127],v[220:223],v[188:191],v[124:127]
s_waitcnt lgkmcnt(0)
s_barrier
s_mov_b32 s72,s92
s_and_b32 s58,s56,1
s_lshl_b32 s58,s58,13
s_lshr_b32 s59,s56,1
)"
R"(s_lshl_b32 s59,s59,15
s_add_u32 s58,s58,s59
s_add_u32 s58,s58,s57
s_add_u32 s58,s58,0x4000
v_add_u32 v248,s58,v255
v_xor_b32 v236,0x2000,v248
v_mov_b32 v246,0x3e0293ee
v_pk_mul_f32 v[0:1],v[0:1],v[246:247] op_sel:[0,0] op_sel_hi:[1,0]
v_pk_mul_f32 v[2:3],v[2:3],v[246:247] op_sel:[0,0] op_sel_hi:[1,0]
v_pk_mul_f32 v[4:5],v[4:5],v[246:247] op_sel:[0,0] op_sel_hi:[1,0]
v_pk_mul_f32 v[6:7],v[6:7],v[246:247] op_sel:[0,0] op_sel_hi:[1,0]
v_cvt_pk_bf16_f32 v0,v0,v1
v_cvt_pk_bf16_f32 v1,v2,v3
v_cvt_pk_bf16_f32 v2,v4,v5
v_cvt_pk_bf16_f32 v3,v6,v7
v_accvgpr_write_b32 a144,v0
v_accvgpr_write_b32 a145,v1
v_accvgpr_write_b32 a146,v2
v_accvgpr_write_b32 a147,v3
ds_write_b128 v248,v[0:3] offset:0
v_pk_mul_f32 v[8:9],v[8:9],v[246:247] op_sel:[0,0] op_sel_hi:[1,0]
v_pk_mul_f32 v[10:11],v[10:11],v[246:247] op_sel:[0,0] op_sel_hi:[1,0]
v_pk_mul_f32 v[12:13],v[12:13],v[246:247] op_sel:[0,0] op_sel_hi:[1,0]
v_pk_mul_f32 v[14:15],v[14:15],v[246:247] op_sel:[0,0] op_sel_hi:[1,0]
v_cvt_pk_bf16_f32 v8,v8,v9
v_cvt_pk_bf16_f32 v9,v10,v11
v_cvt_pk_bf16_f32 v10,v12,v13
v_cvt_pk_bf16_f32 v11,v14,v15
v_accvgpr_write_b32 a148,v8
v_accvgpr_write_b32 a149,v9
v_accvgpr_write_b32 a150,v10
v_accvgpr_write_b32 a151,v11
ds_write_b128 v248,v[8:11] offset:1024
v_pk_mul_f32 v[16:17],v[16:17],v[246:247] op_sel:[0,0] op_sel_hi:[1,0]
v_pk_mul_f32 v[18:19],v[18:19],v[246:247] op_sel:[0,0] op_sel_hi:[1,0]
v_pk_mul_f32 v[20:21],v[20:21],v[246:247] op_sel:[0,0] op_sel_hi:[1,0]
v_pk_mul_f32 v[22:23],v[22:23],v[246:247] op_sel:[0,0] op_sel_hi:[1,0]
v_cvt_pk_bf16_f32 v16,v16,v17
v_cvt_pk_bf16_f32 v17,v18,v19
v_cvt_pk_bf16_f32 v18,v20,v21
v_cvt_pk_bf16_f32 v19,v22,v23
v_accvgpr_write_b32 a152,v16
v_accvgpr_write_b32 a153,v17
v_accvgpr_write_b32 a154,v18
v_accvgpr_write_b32 a155,v19
ds_write_b128 v248,v[16:19] offset:2048
v_pk_mul_f32 v[24:25],v[24:25],v[246:247] op_sel:[0,0] op_sel_hi:[1,0]
v_pk_mul_f32 v[26:27],v[26:27],v[246:247] op_sel:[0,0] op_sel_hi:[1,0]
v_pk_mul_f32 v[28:29],v[28:29],v[246:247] op_sel:[0,0] op_sel_hi:[1,0]
v_pk_mul_f32 v[30:31],v[30:31],v[246:247] op_sel:[0,0] op_sel_hi:[1,0]
v_cvt_pk_bf16_f32 v24,v24,v25
v_cvt_pk_bf16_f32 v25,v26,v27
v_cvt_pk_bf16_f32 v26,v28,v29
v_cvt_pk_bf16_f32 v27,v30,v31
v_accvgpr_write_b32 a156,v24
v_accvgpr_write_b32 a157,v25
v_accvgpr_write_b32 a158,v26
v_accvgpr_write_b32 a159,v27
ds_write_b128 v248,v[24:27] offset:3072
v_pk_mul_f32 v[32:33],v[32:33],v[246:247] op_sel:[0,0] op_sel_hi:[1,0]
v_pk_mul_f32 v[34:35],v[34:35],v[246:247] op_sel:[0,0] op_sel_hi:[1,0]
v_pk_mul_f32 v[36:37],v[36:37],v[246:247] op_sel:[0,0] op_sel_hi:[1,0]
v_pk_mul_f32 v[38:39],v[38:39],v[246:247] op_sel:[0,0] op_sel_hi:[1,0]
v_cvt_pk_bf16_f32 v32,v32,v33
v_cvt_pk_bf16_f32 v33,v34,v35
v_cvt_pk_bf16_f32 v34,v36,v37
v_cvt_pk_bf16_f32 v35,v38,v39
v_accvgpr_write_b32 a160,v32
v_accvgpr_write_b32 a161,v33
v_accvgpr_write_b32 a162,v34
v_accvgpr_write_b32 a163,v35
ds_write_b128 v248,v[32:35] offset:4096
v_pk_mul_f32 v[40:41],v[40:41],v[246:247] op_sel:[0,0] op_sel_hi:[1,0]
v_pk_mul_f32 v[42:43],v[42:43],v[246:247] op_sel:[0,0] op_sel_hi:[1,0]
v_pk_mul_f32 v[44:45],v[44:45],v[246:247] op_sel:[0,0] op_sel_hi:[1,0]
v_pk_mul_f32 v[46:47],v[46:47],v[246:247] op_sel:[0,0] op_sel_hi:[1,0]
v_cvt_pk_bf16_f32 v40,v40,v41
v_cvt_pk_bf16_f32 v41,v42,v43
v_cvt_pk_bf16_f32 v42,v44,v45
v_cvt_pk_bf16_f32 v43,v46,v47
v_accvgpr_write_b32 a164,v40
v_accvgpr_write_b32 a165,v41
v_accvgpr_write_b32 a166,v42
v_accvgpr_write_b32 a167,v43
ds_write_b128 v248,v[40:43] offset:5120
v_pk_mul_f32 v[48:49],v[48:49],v[246:247] op_sel:[0,0] op_sel_hi:[1,0]
v_pk_mul_f32 v[50:51],v[50:51],v[246:247] op_sel:[0,0] op_sel_hi:[1,0]
v_pk_mul_f32 v[52:53],v[52:53],v[246:247] op_sel:[0,0] op_sel_hi:[1,0]
v_pk_mul_f32 v[54:55],v[54:55],v[246:247] op_sel:[0,0] op_sel_hi:[1,0]
v_cvt_pk_bf16_f32 v48,v48,v49
v_cvt_pk_bf16_f32 v49,v50,v51
v_cvt_pk_bf16_f32 v50,v52,v53
v_cvt_pk_bf16_f32 v51,v54,v55
v_accvgpr_write_b32 a168,v48
v_accvgpr_write_b32 a169,v49
v_accvgpr_write_b32 a170,v50
v_accvgpr_write_b32 a171,v51
ds_write_b128 v248,v[48:51] offset:6144
v_pk_mul_f32 v[56:57],v[56:57],v[246:247] op_sel:[0,0] op_sel_hi:[1,0]
v_pk_mul_f32 v[58:59],v[58:59],v[246:247] op_sel:[0,0] op_sel_hi:[1,0]
v_pk_mul_f32 v[60:61],v[60:61],v[246:247] op_sel:[0,0] op_sel_hi:[1,0]
v_pk_mul_f32 v[62:63],v[62:63],v[246:247] op_sel:[0,0] op_sel_hi:[1,0]
v_cvt_pk_bf16_f32 v56,v56,v57
v_cvt_pk_bf16_f32 v57,v58,v59
v_cvt_pk_bf16_f32 v58,v60,v61
v_cvt_pk_bf16_f32 v59,v62,v63
v_accvgpr_write_b32 a172,v56
v_accvgpr_write_b32 a173,v57
v_accvgpr_write_b32 a174,v58
v_accvgpr_write_b32 a175,v59
ds_write_b128 v248,v[56:59] offset:7168
v_pk_mul_f32 v[64:65],v[64:65],v[246:247] op_sel:[0,0] op_sel_hi:[1,0]
v_pk_mul_f32 v[66:67],v[66:67],v[246:247] op_sel:[0,0] op_sel_hi:[1,0]
v_pk_mul_f32 v[68:69],v[68:69],v[246:247] op_sel:[0,0] op_sel_hi:[1,0]
v_pk_mul_f32 v[70:71],v[70:71],v[246:247] op_sel:[0,0] op_sel_hi:[1,0]
v_cvt_pk_bf16_f32 v64,v64,v65
v_cvt_pk_bf16_f32 v65,v66,v67
v_cvt_pk_bf16_f32 v66,v68,v69
v_cvt_pk_bf16_f32 v67,v70,v71
v_accvgpr_write_b32 a176,v64
v_accvgpr_write_b32 a177,v65
v_accvgpr_write_b32 a178,v66
v_accvgpr_write_b32 a179,v67
v_pk_mul_f32 v[72:73],v[72:73],v[246:247] op_sel:[0,0] op_sel_hi:[1,0]
v_pk_mul_f32 v[74:75],v[74:75],v[246:247] op_sel:[0,0] op_sel_hi:[1,0]
v_pk_mul_f32 v[76:77],v[76:77],v[246:247] op_sel:[0,0] op_sel_hi:[1,0]
v_pk_mul_f32 v[78:79],v[78:79],v[246:247] op_sel:[0,0] op_sel_hi:[1,0]
v_cvt_pk_bf16_f32 v72,v72,v73
v_cvt_pk_bf16_f32 v73,v74,v75
v_cvt_pk_bf16_f32 v74,v76,v77
v_cvt_pk_bf16_f32 v75,v78,v79
v_accvgpr_write_b32 a180,v72
v_accvgpr_write_b32 a181,v73
v_accvgpr_write_b32 a182,v74
v_accvgpr_write_b32 a183,v75
v_pk_mul_f32 v[80:81],v[80:81],v[246:247] op_sel:[0,0] op_sel_hi:[1,0]
v_pk_mul_f32 v[82:83],v[82:83],v[246:247] op_sel:[0,0] op_sel_hi:[1,0]
v_pk_mul_f32 v[84:85],v[84:85],v[246:247] op_sel:[0,0] op_sel_hi:[1,0]
v_pk_mul_f32 v[86:87],v[86:87],v[246:247] op_sel:[0,0] op_sel_hi:[1,0]
v_cvt_pk_bf16_f32 v80,v80,v81
v_cvt_pk_bf16_f32 v81,v82,v83
v_cvt_pk_bf16_f32 v82,v84,v85
v_cvt_pk_bf16_f32 v83,v86,v87
v_accvgpr_write_b32 a184,v80
v_accvgpr_write_b32 a185,v81
v_accvgpr_write_b32 a186,v82
v_accvgpr_write_b32 a187,v83
v_pk_mul_f32 v[88:89],v[88:89],v[246:247] op_sel:[0,0] op_sel_hi:[1,0]
v_pk_mul_f32 v[90:91],v[90:91],v[246:247] op_sel:[0,0] op_sel_hi:[1,0]
v_pk_mul_f32 v[92:93],v[92:93],v[246:247] op_sel:[0,0] op_sel_hi:[1,0]
v_pk_mul_f32 v[94:95],v[94:95],v[246:247] op_sel:[0,0] op_sel_hi:[1,0]
v_cvt_pk_bf16_f32 v88,v88,v89
v_cvt_pk_bf16_f32 v89,v90,v91
v_cvt_pk_bf16_f32 v90,v92,v93
v_cvt_pk_bf16_f32 v91,v94,v95
v_accvgpr_write_b32 a188,v88
v_accvgpr_write_b32 a189,v89
v_accvgpr_write_b32 a190,v90
v_accvgpr_write_b32 a191,v91
v_pk_mul_f32 v[96:97],v[96:97],v[246:247] op_sel:[0,0] op_sel_hi:[1,0]
v_pk_mul_f32 v[98:99],v[98:99],v[246:247] op_sel:[0,0] op_sel_hi:[1,0]
v_pk_mul_f32 v[100:101],v[100:101],v[246:247] op_sel:[0,0] op_sel_hi:[1,0]
v_pk_mul_f32 v[102:103],v[102:103],v[246:247] op_sel:[0,0] op_sel_hi:[1,0]
v_cvt_pk_bf16_f32 v96,v96,v97
v_cvt_pk_bf16_f32 v97,v98,v99
v_cvt_pk_bf16_f32 v98,v100,v101
v_cvt_pk_bf16_f32 v99,v102,v103
v_accvgpr_write_b32 a192,v96
v_accvgpr_write_b32 a193,v97
v_accvgpr_write_b32 a194,v98
v_accvgpr_write_b32 a195,v99
v_pk_mul_f32 v[104:105],v[104:105],v[246:247] op_sel:[0,0] op_sel_hi:[1,0]
v_pk_mul_f32 v[106:107],v[106:107],v[246:247] op_sel:[0,0] op_sel_hi:[1,0]
v_pk_mul_f32 v[108:109],v[108:109],v[246:247] op_sel:[0,0] op_sel_hi:[1,0]
v_pk_mul_f32 v[110:111],v[110:111],v[246:247] op_sel:[0,0] op_sel_hi:[1,0]
v_cvt_pk_bf16_f32 v104,v104,v105
v_cvt_pk_bf16_f32 v105,v106,v107
v_cvt_pk_bf16_f32 v106,v108,v109
v_cvt_pk_bf16_f32 v107,v110,v111
v_accvgpr_write_b32 a196,v104
v_accvgpr_write_b32 a197,v105
v_accvgpr_write_b32 a198,v106
v_accvgpr_write_b32 a199,v107
v_pk_mul_f32 v[112:113],v[112:113],v[246:247] op_sel:[0,0] op_sel_hi:[1,0]
v_pk_mul_f32 v[114:115],v[114:115],v[246:247] op_sel:[0,0] op_sel_hi:[1,0]
v_pk_mul_f32 v[116:117],v[116:117],v[246:247] op_sel:[0,0] op_sel_hi:[1,0]
v_pk_mul_f32 v[118:119],v[118:119],v[246:247] op_sel:[0,0] op_sel_hi:[1,0]
v_cvt_pk_bf16_f32 v112,v112,v113
v_cvt_pk_bf16_f32 v113,v114,v115
v_cvt_pk_bf16_f32 v114,v116,v117
v_cvt_pk_bf16_f32 v115,v118,v119
v_accvgpr_write_b32 a200,v112
v_accvgpr_write_b32 a201,v113
v_accvgpr_write_b32 a202,v114
v_accvgpr_write_b32 a203,v115
v_pk_mul_f32 v[120:121],v[120:121],v[246:247] op_sel:[0,0] op_sel_hi:[1,0]
v_pk_mul_f32 v[122:123],v[122:123],v[246:247] op_sel:[0,0] op_sel_hi:[1,0]
v_pk_mul_f32 v[124:125],v[124:125],v[246:247] op_sel:[0,0] op_sel_hi:[1,0]
v_pk_mul_f32 v[126:127],v[126:127],v[246:247] op_sel:[0,0] op_sel_hi:[1,0]
v_cvt_pk_bf16_f32 v120,v120,v121
v_cvt_pk_bf16_f32 v121,v122,v123
v_cvt_pk_bf16_f32 v122,v124,v125
v_cvt_pk_bf16_f32 v123,v126,v127
v_accvgpr_write_b32 a204,v120
v_accvgpr_write_b32 a205,v121
v_accvgpr_write_b32 a206,v122
v_accvgpr_write_b32 a207,v123
s_mov_b64 s[40:41],s[22:23]
s_add_u32 s40,s40,34603008
s_addc_u32 s41,s41,0
s_mov_b32 s42,0x1000000
s_mov_b32 s43,0x20000
s_lshl_b32 s79,s78,1
s_add_u32 s79,s79,2
s_mov_b32 s80,0x10000
s_lshl_b32 s58,s77,15
s_lshl_b32 s59,s56,11
s_add_u32 s58,s58,s59
s_mul_i32 s85,s78,1048576
s_add_u32 s85,s85,s58
s_sub_u32 s86,15,s78
s_mul_i32 s86,s86,1048576
s_add_u32 s86,s86,s58
v_add_u32 v224,s57,v255
v_mov_b32 v225,v224
v_add_u32 v226,65536,v224
v_mov_b32 v227,v226
s_and_b32 s83,s56,1
s_lshl_b32 s83,s83,14
v_and_b32 v246,15,v254
v_lshrrev_b32 v247,4,v254
v_lshlrev_b32 v247,2,v247
v_sub_u32 v229,v246,v247
v_lshlrev_b32 v234,2,v254
s_lshl_b32 s58,s56,9
s_add_u32 s58,s58,s57
s_add_u32 s58,s58,131072
v_add_u32 v234,s58,v234
s_mov_b64 s[88:89],0
s_mov_b64 s[90:91],0
v_mov_b32 v246,0x3f803f80
v_mov_b32 v235,0x5d800000
v_mov_b32 v250,0
v_mov_b32 v251,0
v_mov_b32 v252,0
v_mov_b32 v253,0
v_accvgpr_write_b32 a240,v246
v_accvgpr_write_b32 a244,0
v_accvgpr_write_b32 a241,v246
v_accvgpr_write_b32 a245,0
v_accvgpr_write_b32 a242,v246
v_accvgpr_write_b32 a246,0
v_accvgpr_write_b32 a243,v246
v_accvgpr_write_b32 a247,0
s_lshl_b32 s58,s78,7
s_lshl_b32 s59,s56,4
s_add_u32 s58,s58,s59
v_add_u32 v231,s58,v229
v_cvt_f32_i32 v231,v231
v_mul_f32 v231,0x71800000,v231
v_add_f32 v16,0x71000000,v231
v_min_f32 v16,v16,0
v_add_f32 v17,0xf1000000,v231
v_min_f32 v17,v17,0
v_add_f32 v18,0xf1c00000,v231
v_min_f32 v18,v18,0
v_add_f32 v19,0xf2200000,v231
v_min_f32 v19,v19,0
v_add_f32 v20,0xf3780000,v231
v_min_f32 v20,v20,0
v_add_f32 v21,0xf3840000,v231
v_min_f32 v21,v21,0
v_add_f32 v22,0xf38c0000,v231
v_min_f32 v22,v22,0
v_add_f32 v23,0xf3940000,v231
v_min_f32 v23,v23,0
v_add_f32 v48,0xf3fc0000,v231
v_min_f32 v48,v48,0
v_add_f32 v49,0xf4020000,v231
v_min_f32 v49,v49,0
v_add_f32 v50,0xf4060000,v231
v_min_f32 v50,v50,0
v_add_f32 v51,0xf40a0000,v231
v_min_f32 v51,v51,0
v_add_f32 v52,0xf43e0000,v231
v_min_f32 v52,v52,0
v_add_f32 v53,0xf4420000,v231
v_min_f32 v53,v53,0
v_add_f32 v54,0xf4460000,v231
v_min_f32 v54,v54,0
v_add_f32 v55,0xf44a0000,v231
v_min_f32 v55,v55,0
s_waitcnt vmcnt(12) lgkmcnt(0)
s_barrier
ds_read_b128 v[144:147],v224 offset:0
ds_read_b128 v[148:151],v224 offset:1024
ds_read_b128 v[152:155],v224 offset:2048
ds_read_b128 v[156:159],v224 offset:3072
ds_read_b128 v[160:163],v224 offset:4096
ds_read_b128 v[164:167],v224 offset:5120
ds_read_b128 v[168:171],v224 offset:6144
ds_read_b128 v[172:175],v224 offset:7168
ds_read_b128 v[176:179],v224 offset:8192
ds_read_b128 v[180:183],v224 offset:9216
ds_read_b128 v[184:187],v224 offset:10240
ds_read_b128 v[188:191],v224 offset:11264
ds_read_b128 v[192:195],v224 offset:12288
ds_read_b128 v[196:199],v224 offset:13312
ds_read_b128 v[200:203],v224 offset:14336
ds_read_b128 v[204:207],v224 offset:15360
ds_read_b128 a[208:211],v236 offset:0
ds_read_b128 a[212:215],v236 offset:1024
ds_read_b128 a[216:219],v236 offset:2048
ds_read_b128 a[220:223],v236 offset:3072
ds_read_b128 a[224:227],v236 offset:4096
ds_read_b128 a[228:231],v236 offset:5120
ds_read_b128 a[232:235],v236 offset:6144
ds_read_b128 a[236:239],v236 offset:7168
s_waitcnt lgkmcnt(15)
v_mfma_f32_16x16x32_bf16 v[0:3],v[144:147],a[144:147],0
v_mfma_f32_16x16x32_bf16 v[0:3],v[148:151],a[148:151],v[0:3]
v_mfma_f32_16x16x32_bf16 v[0:3],v[152:155],a[152:155],v[0:3]
v_mfma_f32_16x16x32_bf16 v[0:3],v[156:159],a[156:159],v[0:3]
v_mfma_f32_16x16x32_bf16 v[8:11],v[144:147],a[160:163],0
v_mfma_f32_16x16x32_bf16 v[8:11],v[148:151],a[164:167],v[8:11]
v_mfma_f32_16x16x32_bf16 v[8:11],v[152:155],a[168:171],v[8:11]
v_mfma_f32_16x16x32_bf16 v[8:11],v[156:159],a[172:175],v[8:11]
v_mfma_f32_16x16x32_bf16 v[16:19],v[144:147],a[176:179],v[16:19]
v_mfma_f32_16x16x32_bf16 v[16:19],v[148:151],a[180:183],v[16:19]
v_mfma_f32_16x16x32_bf16 v[16:19],v[152:155],a[184:187],v[16:19]
v_mfma_f32_16x16x32_bf16 v[16:19],v[156:159],a[188:191],v[16:19]
v_max3_f32 v128,v0,v1,v2
v_max_f32 v128,v128,v3
v_mfma_f32_16x16x32_bf16 v[24:27],v[144:147],a[192:195],0
v_mov_b32 v129,v128
v_mfma_f32_16x16x32_bf16 v[24:27],v[148:151],a[196:199],v[24:27]
v_mfma_f32_16x16x32_bf16 v[24:27],v[152:155],a[200:203],v[24:27]
v_permlane32_swap_b32 v128,v129
v_max_f32 v128,v128,v129
v_max3_f32 v132,v8,v9,v10
v_mfma_f32_16x16x32_bf16 v[24:27],v[156:159],a[204:207],v[24:27]
v_mov_b32 v129,v128
v_max_f32 v132,v132,v11
s_waitcnt lgkmcnt(15)
v_mfma_f32_16x16x32_bf16 v[4:7],v[160:163],a[144:147],0
v_mov_b32 v133,v132
v_mfma_f32_16x16x32_bf16 v[4:7],v[164:167],a[148:151],v[4:7]
v_permlane16_swap_b32 v128,v129
v_max_f32 v128,v128,v129
v_mfma_f32_16x16x32_bf16 v[4:7],v[168:171],a[152:155],v[4:7]
v_sub_f32 v128,0,v128
v_mov_b32 v129,v128
v_permlane32_swap_b32 v132,v133
v_max3_f32 v136,v16,v17,v18
v_mfma_f32_16x16x32_bf16 v[4:7],v[172:175],a[156:159],v[4:7]
v_mov_b32 v130,v128
v_mov_b32 v131,v128
v_max_f32 v132,v132,v133
v_max_f32 v136,v136,v19
v_mfma_f32_16x16x32_bf16 v[12:15],v[160:163],a[160:163],0
v_mov_b32 v133,v132
v_mov_b32 v137,v136
v_mfma_f32_16x16x32_bf16 v[12:15],v[164:167],a[164:167],v[12:15]
ds_write_b32 v234,v128 offset:0
v_add_f32 v0,v0,v128
v_mfma_f32_16x16x32_bf16 v[12:15],v[168:171],a[168:171],v[12:15]
v_permlane16_swap_b32 v132,v133
v_max_f32 v132,v132,v133
v_permlane32_swap_b32 v136,v137
v_max3_f32 v140,v24,v25,v26
v_mfma_f32_16x16x32_bf16 v[12:15],v[172:175],a[172:175],v[12:15]
v_sub_f32 v132,0,v132
v_mov_b32 v133,v132
v_max_f32 v136,v136,v137
v_max_f32 v140,v140,v27
v_mfma_f32_16x16x32_bf16 v[20:23],v[160:163],a[176:179],v[20:23]
v_mov_b32 v134,v132
v_mov_b32 v135,v132
v_mov_b32 v137,v136
v_mov_b32 v141,v140
v_mfma_f32_16x16x32_bf16 v[20:23],v[164:167],a[180:183],v[20:23]
ds_write_b32 v234,v132 offset:256
v_add_f32 v1,v1,v128
v_mfma_f32_16x16x32_bf16 v[20:23],v[168:171],a[184:187],v[20:23]
v_permlane16_swap_b32 v136,v137
v_max_f32 v136,v136,v137
v_permlane32_swap_b32 v140,v141
v_mfma_f32_16x16x32_bf16 v[20:23],v[172:175],a[188:191],v[20:23]
v_sub_f32 v136,0,v136
v_mov_b32 v137,v136
v_max_f32 v140,v140,v141
v_mfma_f32_16x16x32_bf16 v[28:31],v[160:163],a[192:195],0
v_mov_b32 v138,v136
v_mov_b32 v139,v136
v_mov_b32 v141,v140
v_mfma_f32_16x16x32_bf16 v[28:31],v[164:167],a[196:199],v[28:31]
v_exp_f32 v0,v0
v_mfma_f32_16x16x32_bf16 v[28:31],v[168:171],a[200:203],v[28:31]
v_permlane16_swap_b32 v140,v141
v_max_f32 v140,v140,v141
)"
R"(v_add_f32 v250,v250,v0
v_mfma_f32_16x16x32_bf16 v[28:31],v[172:175],a[204:207],v[28:31]
v_sub_f32 v140,0,v140
s_waitcnt lgkmcnt(12)
v_mfma_f32_16x16x32_bf16 v[32:35],v[176:179],a[144:147],v[128:131]
v_mov_b32 v141,v140
v_mov_b32 v142,v140
v_mfma_f32_16x16x32_bf16 v[32:35],v[180:183],a[148:151],v[32:35]
v_mov_b32 v143,v140
v_add_f32 v2,v2,v128
v_mfma_f32_16x16x32_bf16 v[32:35],v[184:187],a[152:155],v[32:35]
v_exp_f32 v1,v1
v_mfma_f32_16x16x32_bf16 v[32:35],v[188:191],a[156:159],v[32:35]
v_exp_f32 v2,v2
v_add_f32 v251,v251,v1
v_mfma_f32_16x16x32_bf16 v[40:43],v[176:179],a[160:163],v[132:135]
v_add_f32 v3,v3,v128
v_add_f32 v8,v8,v132
v_add_f32 v250,v250,v2
v_mfma_f32_16x16x32_bf16 v[40:43],v[180:183],a[164:167],v[40:43]
v_exp_f32 v3,v3
v_cvt_pk_bf16_f32 v0,v0,v1
v_mfma_f32_16x16x32_bf16 v[40:43],v[184:187],a[168:171],v[40:43]
v_exp_f32 v8,v8
v_add_f32 v251,v251,v3
v_mfma_f32_16x16x32_bf16 v[40:43],v[188:191],a[172:175],v[40:43]
v_add_f32 v9,v9,v132
v_add_f32 v10,v10,v132
v_add_f32 v252,v252,v8
v_mfma_f32_16x16x32_bf16 v[48:51],v[176:179],a[176:179],v[48:51]
v_exp_f32 v9,v9
v_cvt_pk_bf16_f32 v1,v2,v3
v_mfma_f32_16x16x32_bf16 v[48:51],v[180:183],a[180:183],v[48:51]
v_exp_f32 v10,v10
v_add_f32 v253,v253,v9
v_mfma_f32_16x16x32_bf16 v[48:51],v[184:187],a[184:187],v[48:51]
v_add_f32 v11,v11,v132
v_add_f32 v16,v16,v136
v_add_f32 v252,v252,v10
v_mfma_f32_16x16x32_bf16 v[48:51],v[188:191],a[188:191],v[48:51]
v_exp_f32 v11,v11
v_cvt_pk_bf16_f32 v8,v8,v9
v_mfma_f32_16x16x32_bf16 v[56:59],v[176:179],a[192:195],v[140:143]
v_exp_f32 v16,v16
v_add_f32 v253,v253,v11
v_mfma_f32_16x16x32_bf16 v[56:59],v[180:183],a[196:199],v[56:59]
v_add_f32 v17,v17,v136
v_add_f32 v18,v18,v136
v_cvt_pk_bf16_f32 v9,v10,v11
v_mfma_f32_16x16x32_bf16 v[56:59],v[184:187],a[200:203],v[56:59]
v_exp_f32 v17,v17
v_mfma_f32_16x16x32_bf16 v[56:59],v[188:191],a[204:207],v[56:59]
v_add_f32 v19,v19,v136
v_cvt_pk_bf16_f32 v16,v16,v17
s_waitcnt lgkmcnt(8)
v_mfma_f32_16x16x32_bf16 v[36:39],v[192:195],a[144:147],v[128:131]
v_exp_f32 v18,v18
v_mfma_f32_16x16x32_bf16 v[36:39],v[196:199],a[148:151],v[36:39]
v_exp_f32 v19,v19
v_mfma_f32_16x16x32_bf16 v[36:39],v[200:203],a[152:155],v[36:39]
v_add_f32 v24,v24,v140
v_add_f32 v25,v25,v140
v_cvt_pk_bf16_f32 v17,v18,v19
v_mfma_f32_16x16x32_bf16 v[36:39],v[204:207],a[156:159],v[36:39]
v_exp_f32 v24,v24
v_mfma_f32_16x16x32_bf16 v[44:47],v[192:195],a[160:163],v[132:135]
v_exp_f32 v25,v25
v_mfma_f32_16x16x32_bf16 v[44:47],v[196:199],a[164:167],v[44:47]
v_add_f32 v26,v26,v140
v_add_f32 v27,v27,v140
v_cvt_pk_bf16_f32 v24,v24,v25
v_mfma_f32_16x16x32_bf16 v[44:47],v[200:203],a[168:171],v[44:47]
v_exp_f32 v26,v26
v_mfma_f32_16x16x32_bf16 v[44:47],v[204:207],a[172:175],v[44:47]
v_exp_f32 v27,v27
v_mfma_f32_16x16x32_bf16 v[52:55],v[192:195],a[176:179],v[52:55]
v_add_f32 v4,v4,v128
v_add_f32 v5,v5,v128
v_cvt_pk_bf16_f32 v25,v26,v27
v_mfma_f32_16x16x32_bf16 v[52:55],v[196:199],a[180:183],v[52:55]
v_exp_f32 v4,v4
v_mfma_f32_16x16x32_bf16 v[52:55],v[200:203],a[184:187],v[52:55]
v_exp_f32 v5,v5
v_add_f32 v250,v250,v4
v_mfma_f32_16x16x32_bf16 v[52:55],v[204:207],a[188:191],v[52:55]
v_add_f32 v6,v6,v128
v_add_f32 v7,v7,v128
v_exp_f32 v36,v36
v_mfma_f32_16x16x32_bf16 v[60:63],v[192:195],a[192:195],v[140:143]
v_exp_f32 v6,v6
v_exp_f32 v37,v37
v_mfma_f32_16x16x32_bf16 v[60:63],v[196:199],a[196:199],v[60:63]
v_exp_f32 v7,v7
v_exp_f32 v38,v38
v_mfma_f32_16x16x32_bf16 v[60:63],v[200:203],a[200:203],v[60:63]
v_add_f32 v12,v12,v132
v_add_f32 v13,v13,v132
v_exp_f32 v39,v39
v_mfma_f32_16x16x32_bf16 v[60:63],v[204:207],a[204:207],v[60:63]
v_exp_f32 v12,v12
v_exp_f32 v44,v44
v_mfma_f32_16x16x32_bf16 a[0:3],a[244:247],a[244:247],0
v_exp_f32 v13,v13
v_exp_f32 v45,v45
v_mfma_f32_16x16x32_bf16 a[4:7],a[244:247],a[244:247],0
v_add_f32 v14,v14,v132
v_add_f32 v15,v15,v132
v_exp_f32 v46,v46
v_mfma_f32_16x16x32_bf16 a[8:11],a[244:247],a[244:247],0
v_exp_f32 v14,v14
v_exp_f32 v47,v47
v_mfma_f32_16x16x32_bf16 a[12:15],a[244:247],a[244:247],0
v_exp_f32 v15,v15
v_add_f32 v52,v52,v136
v_mfma_f32_16x16x32_bf16 a[16:19],a[244:247],a[244:247],0
v_add_f32 v20,v20,v136
v_add_f32 v21,v21,v136
v_exp_f32 v52,v52
v_mfma_f32_16x16x32_bf16 a[20:23],a[244:247],a[244:247],0
v_exp_f32 v20,v20
v_add_f32 v53,v53,v136
v_mfma_f32_16x16x32_bf16 a[24:27],a[244:247],a[244:247],0
v_exp_f32 v21,v21
v_exp_f32 v53,v53
v_mfma_f32_16x16x32_bf16 a[28:31],a[244:247],a[244:247],0
v_add_f32 v22,v22,v136
v_add_f32 v23,v23,v136
v_add_f32 v54,v54,v136
v_cvt_pk_bf16_f32 v18,v20,v21
v_mfma_f32_16x16x32_bf16 a[32:35],a[244:247],a[244:247],0
v_exp_f32 v22,v22
v_exp_f32 v54,v54
v_mfma_f32_16x16x32_bf16 a[36:39],a[244:247],a[244:247],0
v_exp_f32 v23,v23
v_add_f32 v55,v55,v136
v_mfma_f32_16x16x32_bf16 a[40:43],a[244:247],a[244:247],0
v_add_f32 v28,v28,v140
v_add_f32 v29,v29,v140
v_exp_f32 v55,v55
v_mfma_f32_16x16x32_bf16 a[44:47],a[244:247],a[244:247],0
v_exp_f32 v28,v28
v_add_f32 v251,v251,v5
v_cvt_pk_bf16_f32 v19,v22,v23
v_mfma_f32_16x16x32_bf16 a[48:51],a[244:247],a[244:247],0
v_exp_f32 v29,v29
v_add_f32 v250,v250,v36
v_cvt_pk_bf16_f32 v2,v4,v5
v_mfma_f32_16x16x32_bf16 a[52:55],a[244:247],a[244:247],0
v_add_f32 v30,v30,v140
v_add_f32 v31,v31,v140
v_add_f32 v250,v250,v6
v_cvt_pk_bf16_f32 v26,v28,v29
v_mfma_f32_16x16x32_bf16 a[56:59],a[244:247],a[244:247],0
v_exp_f32 v30,v30
v_add_f32 v251,v251,v37
v_mfma_f32_16x16x32_bf16 a[60:63],a[244:247],a[244:247],0
v_exp_f32 v31,v31
v_add_f32 v251,v251,v7
v_mfma_f32_16x16x32_bf16 a[64:67],a[244:247],a[244:247],0
v_exp_f32 v32,v32
v_add_f32 v250,v250,v38
v_cvt_pk_bf16_f32 v3,v6,v7
v_mfma_f32_16x16x32_bf16 a[68:71],a[244:247],a[244:247],0
v_exp_f32 v33,v33
v_add_f32 v251,v251,v39
v_cvt_pk_bf16_f32 v27,v30,v31
v_mfma_f32_16x16x32_bf16 a[72:75],a[244:247],a[244:247],0
v_exp_f32 v34,v34
v_add_f32 v252,v252,v12
v_mfma_f32_16x16x32_bf16 a[76:79],a[244:247],a[244:247],0
v_exp_f32 v35,v35
v_add_f32 v252,v252,v44
v_mfma_f32_16x16x32_bf16 a[80:83],a[244:247],a[244:247],0
v_exp_f32 v40,v40
v_add_f32 v253,v253,v13
v_mfma_f32_16x16x32_bf16 a[84:87],a[244:247],a[244:247],0
v_exp_f32 v41,v41
v_add_f32 v253,v253,v45
v_cvt_pk_bf16_f32 v10,v12,v13
v_mfma_f32_16x16x32_bf16 a[88:91],a[244:247],a[244:247],0
v_exp_f32 v42,v42
v_add_f32 v252,v252,v46
v_mfma_f32_16x16x32_bf16 a[92:95],a[244:247],a[244:247],0
v_exp_f32 v43,v43
v_add_f32 v252,v252,v14
v_mfma_f32_16x16x32_bf16 a[96:99],a[244:247],a[244:247],0
v_add_f32 v48,v48,v136
v_add_f32 v49,v49,v136
v_add_f32 v253,v253,v47
v_mfma_f32_16x16x32_bf16 a[100:103],a[244:247],a[244:247],0
v_exp_f32 v48,v48
v_add_f32 v253,v253,v15
v_mfma_f32_16x16x32_bf16 a[104:107],a[244:247],a[244:247],0
v_exp_f32 v49,v49
v_add_f32 v250,v250,v32
v_cvt_pk_bf16_f32 v11,v14,v15
v_mfma_f32_16x16x32_bf16 a[108:111],a[244:247],a[244:247],0
v_add_f32 v50,v50,v136
v_add_f32 v51,v51,v136
v_add_f32 v251,v251,v33
v_cvt_pk_bf16_f32 v48,v48,v49
v_mfma_f32_16x16x32_bf16 a[112:115],a[244:247],a[244:247],0
v_exp_f32 v50,v50
v_add_f32 v250,v250,v34
v_cvt_pk_bf16_f32 v32,v32,v33
v_mfma_f32_16x16x32_bf16 a[116:119],a[244:247],a[244:247],0
v_exp_f32 v51,v51
v_add_f32 v251,v251,v35
v_mfma_f32_16x16x32_bf16 a[120:123],a[244:247],a[244:247],0
v_exp_f32 v56,v56
v_add_f32 v252,v252,v40
v_cvt_pk_bf16_f32 v33,v34,v35
v_cvt_pk_bf16_f32 v49,v50,v51
v_mfma_f32_16x16x32_bf16 a[124:127],a[244:247],a[244:247],0
v_exp_f32 v57,v57
v_add_f32 v253,v253,v41
v_cvt_pk_bf16_f32 v34,v36,v37
v_mfma_f32_16x16x32_bf16 a[136:139],a[244:247],a[244:247],0
v_exp_f32 v58,v58
v_add_f32 v252,v252,v42
v_cvt_pk_bf16_f32 v35,v38,v39
v_cvt_pk_bf16_f32 v56,v56,v57
v_mfma_f32_16x16x32_bf16 a[140:143],a[244:247],a[244:247],0
v_exp_f32 v59,v59
v_add_f32 v253,v253,v43
v_cvt_pk_bf16_f32 v40,v40,v41
v_cvt_pk_bf16_f32 v41,v42,v43
v_cvt_pk_bf16_f32 v42,v44,v45
v_cvt_pk_bf16_f32 v43,v46,v47
v_cvt_pk_bf16_f32 v57,v58,v59
s_load_dwordx2 s[44:45],%2,0x60
s_load_dwordx4 s[92:95],%2,0x68
s_waitcnt vmcnt(0) lgkmcnt(0)
s_barrier
s_xor_b32 s58,s65,1
s_or_b32 s58,s58,s56
s_cbranch_scc1 L_PUBL_%=
v_mov_b32 v242,1
v_mov_b32 v243,s72
s_mov_b64 exec,1
global_atomic_add v243,v242,s[22:23]
s_mov_b64 exec,-1
L_PUBL_%=:
v_xor_b32 v246,0x200,v234
ds_read_b32 v232,v246
ds_read_b32 v233,v246 offset:256
ds_read_b128 v[144:147],v224 offset:32768
ds_read_b128 v[148:151],v224 offset:33792
ds_read_b128 v[152:155],v224 offset:34816
ds_read_b128 v[156:159],v224 offset:35840
ds_read_b128 v[160:163],v226 offset:0
ds_read_b128 v[164:167],v226 offset:1024
ds_read_b128 v[168:171],v226 offset:2048
ds_read_b128 v[172:175],v226 offset:3072
s_mov_b32 s84,0
s_lshl_b32 s62,s56,8
v_mov_b32 v238,s99
s_sub_u32 s87,s79,2
s_cmp_eq_u32 s87,0
s_cbranch_scc1 L_EM2_%=
L_E1_%=:
s_waitcnt lgkmcnt(4)
v_mfma_f32_16x16x32_bf16 v[64:67],v[144:147],a[144:147],v[128:131]
v_exp_f32 v60,v60
v_mfma_f32_16x16x32_bf16 v[64:67],v[148:151],a[148:151],v[64:67]
v_exp_f32 v61,v61
v_mfma_f32_16x16x32_bf16 v[64:67],v[152:155],a[152:155],v[64:67]
v_exp_f32 v62,v62
v_mfma_f32_16x16x32_bf16 v[64:67],v[156:159],a[156:159],v[64:67]
v_exp_f32 v63,v63
v_mfma_f32_16x16x32_bf16 v[72:75],v[144:147],a[160:163],v[132:135]
v_cvt_pk_bf16_f32 v50,v52,v53
v_cvt_pk_bf16_f32 v51,v54,v55
v_mfma_f32_16x16x32_bf16 v[72:75],v[148:151],a[164:167],v[72:75]
v_cvt_pk_bf16_f32 v58,v60,v61
v_cvt_pk_bf16_f32 v59,v62,v63
v_mfma_f32_16x16x32_bf16 v[72:75],v[152:155],a[168:171],v[72:75]
ds_read_b128 v[176:179],v224 offset:36864
ds_read_b128 v[180:183],v224 offset:37888
v_mfma_f32_16x16x32_bf16 v[72:75],v[156:159],a[172:175],v[72:75]
ds_read_b128 v[184:187],v224 offset:38912
ds_read_b128 v[188:191],v224 offset:39936
v_mfma_f32_16x16x32_bf16 v[80:83],v[144:147],a[176:179],v[136:139]
v_mfma_f32_16x16x32_bf16 v[80:83],v[148:151],a[180:183],v[80:83]
s_mov_b32 m0,s81
v_mfma_f32_16x16x32_bf16 v[80:83],v[152:155],a[184:187],v[80:83]
buffer_load_dwordx4 v228,s[36:39],s80 offen offset:0 lds
v_mfma_f32_16x16x32_bf16 v[80:83],v[156:159],a[188:191],v[80:83]
v_exp_f32 v64,v64
v_mfma_f32_16x16x32_bf16 v[88:91],v[144:147],a[192:195],v[140:143]
v_add_f32 v250,v250,v64
v_mfma_f32_16x16x32_bf16 v[88:91],v[148:151],a[196:199],v[88:91]
v_exp_f32 v65,v65
v_mfma_f32_16x16x32_bf16 v[88:91],v[152:155],a[200:203],v[88:91]
v_add_f32 v251,v251,v65
v_mfma_f32_16x16x32_bf16 v[88:91],v[156:159],a[204:207],v[88:91]
v_cvt_pk_bf16_f32 v64,v64,v65
s_waitcnt lgkmcnt(4)
v_mfma_f32_16x16x32_bf16 a[0:3],v[160:163],v[0:3],a[0:3]
v_exp_f32 v66,v66
v_mfma_f32_16x16x32_bf16 a[0:3],v[164:167],v[32:35],a[0:3]
ds_read_b128 v[192:195],v226 offset:4096
v_add_f32 v250,v250,v66
v_mfma_f32_16x16x32_bf16 a[16:19],v[168:171],v[0:3],a[16:19]
buffer_load_dwordx4 v228,s[36:39],s80 offen offset:1024 lds
v_mfma_f32_16x16x32_bf16 a[16:19],v[172:175],v[32:35],a[16:19]
ds_read_b128 v[196:199],v226 offset:5120
v_mfma_f32_16x16x32_bf16 a[4:7],v[160:163],v[8:11],a[4:7]
v_exp_f32 v67,v67
v_mfma_f32_16x16x32_bf16 a[4:7],v[164:167],v[40:43],a[4:7]
ds_read_b128 v[200:203],v226 offset:6144
v_add_f32 v251,v251,v67
v_mfma_f32_16x16x32_bf16 a[20:23],v[168:171],v[8:11],a[20:23]
v_exp_f32 v72,v72
v_mfma_f32_16x16x32_bf16 a[20:23],v[172:175],v[40:43],a[20:23]
ds_read_b128 v[204:207],v226 offset:7168
v_add_f32 v252,v252,v72
v_mfma_f32_16x16x32_bf16 a[8:11],v[160:163],v[16:19],a[8:11]
v_exp_f32 v73,v73
v_mfma_f32_16x16x32_bf16 a[8:11],v[164:167],v[48:51],a[8:11]
v_add_f32 v253,v253,v73
v_cvt_pk_bf16_f32 v65,v66,v67
v_mfma_f32_16x16x32_bf16 a[24:27],v[168:171],v[16:19],a[24:27]
buffer_load_dwordx4 v228,s[36:39],s80 offen offset:2048 lds
v_cvt_pk_bf16_f32 v72,v72,v73
v_mfma_f32_16x16x32_bf16 a[24:27],v[172:175],v[48:51],a[24:27]
v_exp_f32 v74,v74
v_mfma_f32_16x16x32_bf16 a[12:15],v[160:163],v[24:27],a[12:15]
v_add_f32 v252,v252,v74
v_mfma_f32_16x16x32_bf16 a[12:15],v[164:167],v[56:59],a[12:15]
v_exp_f32 v75,v75
v_mfma_f32_16x16x32_bf16 a[28:31],v[168:171],v[24:27],a[28:31]
v_add_f32 v253,v253,v75
v_mfma_f32_16x16x32_bf16 a[28:31],v[172:175],v[56:59],a[28:31]
v_cvt_pk_bf16_f32 v73,v74,v75
s_waitcnt lgkmcnt(4)
v_mfma_f32_16x16x32_bf16 v[68:71],v[176:179],a[144:147],v[128:131]
v_exp_f32 v80,v80
v_mfma_f32_16x16x32_bf16 v[68:71],v[180:183],a[148:151],v[68:71]
ds_read_b128 v[144:147],v224 offset:40960
v_mfma_f32_16x16x32_bf16 v[68:71],v[184:187],a[152:155],v[68:71]
buffer_load_dwordx4 v228,s[36:39],s80 offen offset:3072 lds
v_mfma_f32_16x16x32_bf16 v[68:71],v[188:191],a[156:159],v[68:71]
ds_read_b128 v[148:151],v224 offset:41984
v_mfma_f32_16x16x32_bf16 v[76:79],v[176:179],a[160:163],v[132:135]
v_exp_f32 v81,v81
v_mfma_f32_16x16x32_bf16 v[76:79],v[180:183],a[164:167],v[76:79]
ds_read_b128 v[152:155],v224 offset:43008
v_cvt_pk_bf16_f32 v80,v80,v81
v_mfma_f32_16x16x32_bf16 v[76:79],v[184:187],a[168:171],v[76:79]
v_exp_f32 v82,v82
v_mfma_f32_16x16x32_bf16 v[76:79],v[188:191],a[172:175],v[76:79]
ds_read_b128 v[156:159],v224 offset:44032
v_mfma_f32_16x16x32_bf16 v[84:87],v[176:179],a[176:179],v[136:139]
v_exp_f32 v83,v83
v_mfma_f32_16x16x32_bf16 v[84:87],v[180:183],a[180:183],v[84:87]
v_exp_f32 v68,v68
v_mfma_f32_16x16x32_bf16 v[84:87],v[184:187],a[184:187],v[84:87]
v_exp_f32 v88,v88
v_mfma_f32_16x16x32_bf16 v[84:87],v[188:191],a[188:191],v[84:87]
v_exp_f32 v69,v69
v_mfma_f32_16x16x32_bf16 v[92:95],v[176:179],a[192:195],v[140:143]
v_exp_f32 v89,v89
v_mfma_f32_16x16x32_bf16 v[92:95],v[180:183],a[196:199],v[92:95]
v_exp_f32 v70,v70
v_mfma_f32_16x16x32_bf16 v[92:95],v[184:187],a[200:203],v[92:95]
v_exp_f32 v90,v90
v_mfma_f32_16x16x32_bf16 v[92:95],v[188:191],a[204:207],v[92:95]
v_add_f32 v250,v250,v68
s_waitcnt lgkmcnt(4)
v_mfma_f32_16x16x32_bf16 a[32:35],v[192:195],v[0:3],a[32:35]
v_exp_f32 v91,v91
v_mfma_f32_16x16x32_bf16 a[32:35],v[196:199],v[32:35],a[32:35]
ds_read_b128 v[160:163],v226 offset:8192
v_add_f32 v251,v251,v69
v_mfma_f32_16x16x32_bf16 a[48:51],v[200:203],v[0:3],a[48:51]
v_exp_f32 v71,v71
v_mfma_f32_16x16x32_bf16 a[48:51],v[204:207],v[32:35],a[48:51]
ds_read_b128 v[164:167],v226 offset:9216
v_add_f32 v250,v250,v70
v_mfma_f32_16x16x32_bf16 a[36:39],v[192:195],v[8:11],a[36:39]
v_exp_f32 v76,v76
v_mfma_f32_16x16x32_bf16 a[36:39],v[196:199],v[40:43],a[36:39]
ds_read_b128 v[168:171],v226 offset:10240
v_add_f32 v251,v251,v71
v_mfma_f32_16x16x32_bf16 a[52:55],v[200:203],v[8:11],a[52:55]
v_exp_f32 v77,v77
v_mfma_f32_16x16x32_bf16 a[52:55],v[204:207],v[40:43],a[52:55]
ds_read_b128 v[172:175],v226 offset:11264
v_add_f32 v252,v252,v76
v_mfma_f32_16x16x32_bf16 a[40:43],v[192:195],v[16:19],a[40:43]
v_exp_f32 v78,v78
v_mfma_f32_16x16x32_bf16 a[40:43],v[196:199],v[48:51],a[40:43]
v_add_f32 v253,v253,v77
v_add_f32 v252,v252,v78
v_mfma_f32_16x16x32_bf16 a[56:59],v[200:203],v[16:19],a[56:59]
v_exp_f32 v79,v79
v_mfma_f32_16x16x32_bf16 a[56:59],v[204:207],v[48:51],a[56:59]
v_add_f32 v253,v253,v79
v_cvt_pk_bf16_f32 v66,v68,v69
)"
R"(v_mfma_f32_16x16x32_bf16 a[44:47],v[192:195],v[24:27],a[44:47]
v_exp_f32 v84,v84
v_mfma_f32_16x16x32_bf16 a[44:47],v[196:199],v[56:59],a[44:47]
v_cvt_pk_bf16_f32 v67,v70,v71
v_cvt_pk_bf16_f32 v74,v76,v77
v_mfma_f32_16x16x32_bf16 a[60:63],v[200:203],v[24:27],a[60:63]
v_exp_f32 v85,v85
v_mfma_f32_16x16x32_bf16 a[60:63],v[204:207],v[56:59],a[60:63]
v_cvt_pk_bf16_f32 v75,v78,v79
v_cvt_pk_bf16_f32 v81,v82,v83
v_mfma_f32_16x16x32_bf16 a[136:139],a[240:243],v[16:19],a[136:139]
v_exp_f32 v86,v86
v_mfma_f32_16x16x32_bf16 a[136:139],a[240:243],v[48:51],a[136:139]
v_cvt_pk_bf16_f32 v82,v84,v85
s_waitcnt lgkmcnt(4)
v_mfma_f32_16x16x32_bf16 v[96:99],v[144:147],a[144:147],v[128:131]
v_exp_f32 v87,v87
v_mfma_f32_16x16x32_bf16 v[96:99],v[148:151],a[148:151],v[96:99]
ds_read_b128 v[176:179],v224 offset:45056
v_cvt_pk_bf16_f32 v83,v86,v87
v_mfma_f32_16x16x32_bf16 v[96:99],v[152:155],a[152:155],v[96:99]
v_exp_f32 v92,v92
v_mfma_f32_16x16x32_bf16 v[96:99],v[156:159],a[156:159],v[96:99]
ds_read_b128 v[180:183],v224 offset:46080
v_cvt_pk_bf16_f32 v88,v88,v89
v_mfma_f32_16x16x32_bf16 v[104:107],v[144:147],a[160:163],v[132:135]
v_exp_f32 v93,v93
v_mfma_f32_16x16x32_bf16 v[104:107],v[148:151],a[164:167],v[104:107]
ds_read_b128 v[184:187],v224 offset:47104
v_cvt_pk_bf16_f32 v89,v90,v91
v_mfma_f32_16x16x32_bf16 v[104:107],v[152:155],a[168:171],v[104:107]
v_exp_f32 v94,v94
v_mfma_f32_16x16x32_bf16 v[104:107],v[156:159],a[172:175],v[104:107]
ds_read_b128 v[188:191],v224 offset:48128
v_cvt_pk_bf16_f32 v90,v92,v93
v_mfma_f32_16x16x32_bf16 v[112:115],v[144:147],a[176:179],v[136:139]
v_exp_f32 v95,v95
v_mfma_f32_16x16x32_bf16 v[112:115],v[148:151],a[180:183],v[112:115]
v_exp_f32 v96,v96
v_mfma_f32_16x16x32_bf16 v[112:115],v[152:155],a[184:187],v[112:115]
v_add_f32 v250,v250,v96
v_cvt_pk_bf16_f32 v91,v94,v95
v_mfma_f32_16x16x32_bf16 v[112:115],v[156:159],a[188:191],v[112:115]
v_exp_f32 v97,v97
v_mfma_f32_16x16x32_bf16 v[120:123],v[144:147],a[192:195],v[140:143]
v_add_f32 v251,v251,v97
v_mfma_f32_16x16x32_bf16 v[120:123],v[148:151],a[196:199],v[120:123]
v_exp_f32 v98,v98
v_mfma_f32_16x16x32_bf16 v[120:123],v[152:155],a[200:203],v[120:123]
v_add_f32 v250,v250,v98
v_cvt_pk_bf16_f32 v96,v96,v97
v_mfma_f32_16x16x32_bf16 v[120:123],v[156:159],a[204:207],v[120:123]
s_waitcnt lgkmcnt(4)
v_mfma_f32_16x16x32_bf16 a[64:67],v[160:163],v[0:3],a[64:67]
v_exp_f32 v99,v99
v_mfma_f32_16x16x32_bf16 a[64:67],v[164:167],v[32:35],a[64:67]
ds_read_b128 v[192:195],v226 offset:12288
v_add_f32 v251,v251,v99
v_mfma_f32_16x16x32_bf16 a[80:83],v[168:171],v[0:3],a[80:83]
v_exp_f32 v104,v104
v_mfma_f32_16x16x32_bf16 a[80:83],v[172:175],v[32:35],a[80:83]
ds_read_b128 v[196:199],v226 offset:13312
v_add_f32 v252,v252,v104
v_mfma_f32_16x16x32_bf16 a[68:71],v[160:163],v[8:11],a[68:71]
v_exp_f32 v105,v105
v_mfma_f32_16x16x32_bf16 a[68:71],v[164:167],v[40:43],a[68:71]
ds_read_b128 v[200:203],v226 offset:14336
v_add_f32 v253,v253,v105
v_mfma_f32_16x16x32_bf16 a[84:87],v[168:171],v[8:11],a[84:87]
v_exp_f32 v106,v106
v_mfma_f32_16x16x32_bf16 a[84:87],v[172:175],v[40:43],a[84:87]
ds_read_b128 v[204:207],v226 offset:15360
v_add_f32 v252,v252,v106
v_mfma_f32_16x16x32_bf16 a[72:75],v[160:163],v[16:19],a[72:75]
v_exp_f32 v107,v107
v_mfma_f32_16x16x32_bf16 a[72:75],v[164:167],v[48:51],a[72:75]
v_add_f32 v253,v253,v107
v_cvt_pk_bf16_f32 v97,v98,v99
v_mfma_f32_16x16x32_bf16 a[88:91],v[168:171],v[16:19],a[88:91]
v_exp_f32 v112,v112
v_mfma_f32_16x16x32_bf16 a[88:91],v[172:175],v[48:51],a[88:91]
v_cvt_pk_bf16_f32 v104,v104,v105
v_mfma_f32_16x16x32_bf16 a[76:79],v[160:163],v[24:27],a[76:79]
v_exp_f32 v113,v113
v_mfma_f32_16x16x32_bf16 a[76:79],v[164:167],v[56:59],a[76:79]
v_cvt_pk_bf16_f32 v105,v106,v107
v_cvt_pk_bf16_f32 v112,v112,v113
v_mfma_f32_16x16x32_bf16 a[92:95],v[168:171],v[24:27],a[92:95]
v_exp_f32 v114,v114
v_mfma_f32_16x16x32_bf16 a[92:95],v[172:175],v[56:59],a[92:95]
s_waitcnt vmcnt(0) lgkmcnt(0)
s_barrier
v_mfma_f32_16x16x32_bf16 v[100:103],v[176:179],a[144:147],v[128:131]
s_add_u32 m0,s81,0x10000
s_add_u32 s59,s80,0x4000
v_mfma_f32_16x16x32_bf16 v[100:103],v[180:183],a[148:151],v[100:103]
ds_read_b128 v[144:147],v225 offset:0
buffer_load_dwordx4 v228,s[36:39],s59 offen offset:0 lds
v_mfma_f32_16x16x32_bf16 v[100:103],v[184:187],a[152:155],v[100:103]
v_exp_f32 v115,v115
v_mfma_f32_16x16x32_bf16 v[100:103],v[188:191],a[156:159],v[100:103]
ds_read_b128 v[148:151],v225 offset:1024
v_cvt_pk_bf16_f32 v113,v114,v115
v_mfma_f32_16x16x32_bf16 v[108:111],v[176:179],a[160:163],v[132:135]
v_exp_f32 v120,v120
v_mfma_f32_16x16x32_bf16 v[108:111],v[180:183],a[164:167],v[108:111]
ds_read_b128 v[152:155],v225 offset:2048
v_mfma_f32_16x16x32_bf16 v[108:111],v[184:187],a[168:171],v[108:111]
v_exp_f32 v121,v121
v_mfma_f32_16x16x32_bf16 v[108:111],v[188:191],a[172:175],v[108:111]
ds_read_b128 v[156:159],v225 offset:3072
v_cvt_pk_bf16_f32 v120,v120,v121
v_mfma_f32_16x16x32_bf16 v[116:119],v[176:179],a[176:179],v[136:139]
buffer_load_dwordx4 v228,s[36:39],s59 offen offset:1024 lds
v_mfma_f32_16x16x32_bf16 v[116:119],v[180:183],a[180:183],v[116:119]
v_exp_f32 v122,v122
v_mfma_f32_16x16x32_bf16 v[116:119],v[184:187],a[184:187],v[116:119]
v_exp_f32 v100,v100
v_mfma_f32_16x16x32_bf16 v[116:119],v[188:191],a[188:191],v[116:119]
v_exp_f32 v123,v123
v_mfma_f32_16x16x32_bf16 v[124:127],v[176:179],a[192:195],v[140:143]
v_exp_f32 v101,v101
v_mfma_f32_16x16x32_bf16 v[124:127],v[180:183],a[196:199],v[124:127]
v_exp_f32 v102,v102
v_mfma_f32_16x16x32_bf16 v[124:127],v[184:187],a[200:203],v[124:127]
v_exp_f32 v103,v103
v_mfma_f32_16x16x32_bf16 v[124:127],v[188:191],a[204:207],v[124:127]
buffer_load_dwordx4 v228,s[36:39],s59 offen offset:2048 lds
v_add_f32 v250,v250,v100
v_mfma_f32_16x16x32_bf16 a[96:99],v[192:195],v[0:3],a[96:99]
v_exp_f32 v108,v108
v_mfma_f32_16x16x32_bf16 a[96:99],v[196:199],v[32:35],a[96:99]
ds_read_b128 v[160:163],v227 offset:32768
v_add_f32 v251,v251,v101
v_mfma_f32_16x16x32_bf16 a[112:115],v[200:203],v[0:3],a[112:115]
v_exp_f32 v109,v109
v_mfma_f32_16x16x32_bf16 a[112:115],v[204:207],v[32:35],a[112:115]
ds_read_b128 v[164:167],v227 offset:33792
v_add_f32 v250,v250,v102
v_mfma_f32_16x16x32_bf16 a[100:103],v[192:195],v[8:11],a[100:103]
v_exp_f32 v110,v110
v_mfma_f32_16x16x32_bf16 a[100:103],v[196:199],v[40:43],a[100:103]
ds_read_b128 v[168:171],v227 offset:34816
v_add_f32 v251,v251,v103
v_mfma_f32_16x16x32_bf16 a[116:119],v[200:203],v[8:11],a[116:119]
buffer_load_dwordx4 v228,s[36:39],s59 offen offset:3072 lds
v_add_f32 v252,v252,v108
v_mfma_f32_16x16x32_bf16 a[116:119],v[204:207],v[40:43],a[116:119]
ds_read_b128 v[172:175],v227 offset:35840
v_add_f32 v253,v253,v109
v_mfma_f32_16x16x32_bf16 a[104:107],v[192:195],v[16:19],a[104:107]
v_exp_f32 v111,v111
v_mfma_f32_16x16x32_bf16 a[104:107],v[196:199],v[48:51],a[104:107]
v_exp_f32 v116,v116
v_mfma_f32_16x16x32_bf16 a[120:123],v[200:203],v[16:19],a[120:123]
v_exp_f32 v117,v117
v_mfma_f32_16x16x32_bf16 a[120:123],v[204:207],v[48:51],a[120:123]
v_exp_f32 v118,v118
v_mfma_f32_16x16x32_bf16 a[108:111],v[192:195],v[24:27],a[108:111]
v_exp_f32 v119,v119
v_mfma_f32_16x16x32_bf16 a[108:111],v[196:199],v[56:59],a[108:111]
v_add_f32 v252,v252,v110
v_add_f32 v253,v253,v111
v_mfma_f32_16x16x32_bf16 a[124:127],v[200:203],v[24:27],a[124:127]
v_cvt_pk_bf16_f32 v98,v100,v101
v_cvt_pk_bf16_f32 v106,v108,v109
v_mfma_f32_16x16x32_bf16 a[124:127],v[204:207],v[56:59],a[124:127]
v_cvt_pk_bf16_f32 v99,v102,v103
v_cvt_pk_bf16_f32 v107,v110,v111
v_mfma_f32_16x16x32_bf16 a[140:143],a[240:243],v[24:27],a[140:143]
v_cvt_pk_bf16_f32 v121,v122,v123
v_mfma_f32_16x16x32_bf16 a[140:143],a[240:243],v[56:59],a[140:143]
s_add_u32 s80,s80,0x8000
s_add_u32 s62,s62,1
s_cmp_lg_u32 s62,3
s_cbranch_scc1 L_HAE1_%=
global_load_dword v237,v238,s[22:23] offset:128 sc1
L_HAE1_%=:
s_sub_u32 s87,s87,2
s_cmp_eq_u32 s87,0
s_cbranch_scc1 L_OM2_%=
s_waitcnt lgkmcnt(4)
v_mfma_f32_16x16x32_bf16 v[0:3],v[144:147],a[144:147],v[128:131]
v_exp_f32 v124,v124
v_mfma_f32_16x16x32_bf16 v[0:3],v[148:151],a[148:151],v[0:3]
v_exp_f32 v125,v125
v_mfma_f32_16x16x32_bf16 v[0:3],v[152:155],a[152:155],v[0:3]
v_exp_f32 v126,v126
v_mfma_f32_16x16x32_bf16 v[0:3],v[156:159],a[156:159],v[0:3]
v_exp_f32 v127,v127
v_mfma_f32_16x16x32_bf16 v[8:11],v[144:147],a[160:163],v[132:135]
v_cvt_pk_bf16_f32 v114,v116,v117
v_cvt_pk_bf16_f32 v115,v118,v119
v_mfma_f32_16x16x32_bf16 v[8:11],v[148:151],a[164:167],v[8:11]
v_cvt_pk_bf16_f32 v122,v124,v125
v_cvt_pk_bf16_f32 v123,v126,v127
v_mfma_f32_16x16x32_bf16 v[8:11],v[152:155],a[168:171],v[8:11]
ds_read_b128 v[176:179],v224 offset:4096
ds_read_b128 v[180:183],v224 offset:5120
v_mfma_f32_16x16x32_bf16 v[8:11],v[156:159],a[172:175],v[8:11]
ds_read_b128 v[184:187],v224 offset:6144
ds_read_b128 v[188:191],v224 offset:7168
v_mfma_f32_16x16x32_bf16 v[16:19],v[144:147],a[176:179],v[136:139]
v_mfma_f32_16x16x32_bf16 v[16:19],v[148:151],a[180:183],v[16:19]
s_mov_b32 m0,s82
v_mfma_f32_16x16x32_bf16 v[16:19],v[152:155],a[184:187],v[16:19]
buffer_load_dwordx4 v228,s[36:39],s80 offen offset:0 lds
v_mfma_f32_16x16x32_bf16 v[16:19],v[156:159],a[188:191],v[16:19]
v_exp_f32 v0,v0
v_mfma_f32_16x16x32_bf16 v[24:27],v[144:147],a[192:195],v[140:143]
v_add_f32 v250,v250,v0
v_mfma_f32_16x16x32_bf16 v[24:27],v[148:151],a[196:199],v[24:27]
v_exp_f32 v1,v1
v_mfma_f32_16x16x32_bf16 v[24:27],v[152:155],a[200:203],v[24:27]
v_add_f32 v251,v251,v1
v_mfma_f32_16x16x32_bf16 v[24:27],v[156:159],a[204:207],v[24:27]
v_cvt_pk_bf16_f32 v0,v0,v1
s_waitcnt lgkmcnt(4)
v_mfma_f32_16x16x32_bf16 a[0:3],v[160:163],v[64:67],a[0:3]
v_exp_f32 v2,v2
v_mfma_f32_16x16x32_bf16 a[0:3],v[164:167],v[96:99],a[0:3]
ds_read_b128 v[192:195],v226 offset:36864
v_add_f32 v250,v250,v2
v_mfma_f32_16x16x32_bf16 a[16:19],v[168:171],v[64:67],a[16:19]
buffer_load_dwordx4 v228,s[36:39],s80 offen offset:1024 lds
v_mfma_f32_16x16x32_bf16 a[16:19],v[172:175],v[96:99],a[16:19]
ds_read_b128 v[196:199],v226 offset:37888
v_mfma_f32_16x16x32_bf16 a[4:7],v[160:163],v[72:75],a[4:7]
v_exp_f32 v3,v3
v_mfma_f32_16x16x32_bf16 a[4:7],v[164:167],v[104:107],a[4:7]
ds_read_b128 v[200:203],v226 offset:38912
v_add_f32 v251,v251,v3
v_mfma_f32_16x16x32_bf16 a[20:23],v[168:171],v[72:75],a[20:23]
v_exp_f32 v8,v8
v_mfma_f32_16x16x32_bf16 a[20:23],v[172:175],v[104:107],a[20:23]
ds_read_b128 v[204:207],v226 offset:39936
v_add_f32 v252,v252,v8
v_mfma_f32_16x16x32_bf16 a[8:11],v[160:163],v[80:83],a[8:11]
v_exp_f32 v9,v9
v_mfma_f32_16x16x32_bf16 a[8:11],v[164:167],v[112:115],a[8:11]
v_add_f32 v253,v253,v9
v_cvt_pk_bf16_f32 v1,v2,v3
v_mfma_f32_16x16x32_bf16 a[24:27],v[168:171],v[80:83],a[24:27]
buffer_load_dwordx4 v228,s[36:39],s80 offen offset:2048 lds
v_cvt_pk_bf16_f32 v8,v8,v9
v_mfma_f32_16x16x32_bf16 a[24:27],v[172:175],v[112:115],a[24:27]
v_exp_f32 v10,v10
v_mfma_f32_16x16x32_bf16 a[12:15],v[160:163],v[88:91],a[12:15]
v_add_f32 v252,v252,v10
v_mfma_f32_16x16x32_bf16 a[12:15],v[164:167],v[120:123],a[12:15]
v_exp_f32 v11,v11
v_mfma_f32_16x16x32_bf16 a[28:31],v[168:171],v[88:91],a[28:31]
v_add_f32 v253,v253,v11
v_mfma_f32_16x16x32_bf16 a[28:31],v[172:175],v[120:123],a[28:31]
v_cvt_pk_bf16_f32 v9,v10,v11
s_waitcnt lgkmcnt(4)
v_mfma_f32_16x16x32_bf16 v[4:7],v[176:179],a[144:147],v[128:131]
v_exp_f32 v16,v16
v_mfma_f32_16x16x32_bf16 v[4:7],v[180:183],a[148:151],v[4:7]
ds_read_b128 v[144:147],v224 offset:8192
v_mfma_f32_16x16x32_bf16 v[4:7],v[184:187],a[152:155],v[4:7]
buffer_load_dwordx4 v228,s[36:39],s80 offen offset:3072 lds
v_mfma_f32_16x16x32_bf16 v[4:7],v[188:191],a[156:159],v[4:7]
ds_read_b128 v[148:151],v224 offset:9216
v_mfma_f32_16x16x32_bf16 v[12:15],v[176:179],a[160:163],v[132:135]
v_exp_f32 v17,v17
v_mfma_f32_16x16x32_bf16 v[12:15],v[180:183],a[164:167],v[12:15]
ds_read_b128 v[152:155],v224 offset:10240
v_cvt_pk_bf16_f32 v16,v16,v17
v_mfma_f32_16x16x32_bf16 v[12:15],v[184:187],a[168:171],v[12:15]
v_exp_f32 v18,v18
v_mfma_f32_16x16x32_bf16 v[12:15],v[188:191],a[172:175],v[12:15]
ds_read_b128 v[156:159],v224 offset:11264
v_mfma_f32_16x16x32_bf16 v[20:23],v[176:179],a[176:179],v[136:139]
v_exp_f32 v19,v19
v_mfma_f32_16x16x32_bf16 v[20:23],v[180:183],a[180:183],v[20:23]
v_exp_f32 v4,v4
v_mfma_f32_16x16x32_bf16 v[20:23],v[184:187],a[184:187],v[20:23]
v_exp_f32 v24,v24
v_mfma_f32_16x16x32_bf16 v[20:23],v[188:191],a[188:191],v[20:23]
v_exp_f32 v5,v5
v_mfma_f32_16x16x32_bf16 v[28:31],v[176:179],a[192:195],v[140:143]
v_exp_f32 v25,v25
v_mfma_f32_16x16x32_bf16 v[28:31],v[180:183],a[196:199],v[28:31]
v_exp_f32 v6,v6
v_mfma_f32_16x16x32_bf16 v[28:31],v[184:187],a[200:203],v[28:31]
v_exp_f32 v26,v26
v_mfma_f32_16x16x32_bf16 v[28:31],v[188:191],a[204:207],v[28:31]
v_add_f32 v250,v250,v4
s_waitcnt lgkmcnt(4)
v_mfma_f32_16x16x32_bf16 a[32:35],v[192:195],v[64:67],a[32:35]
v_exp_f32 v27,v27
v_mfma_f32_16x16x32_bf16 a[32:35],v[196:199],v[96:99],a[32:35]
ds_read_b128 v[160:163],v226 offset:40960
v_add_f32 v251,v251,v5
v_mfma_f32_16x16x32_bf16 a[48:51],v[200:203],v[64:67],a[48:51]
v_exp_f32 v7,v7
v_mfma_f32_16x16x32_bf16 a[48:51],v[204:207],v[96:99],a[48:51]
ds_read_b128 v[164:167],v226 offset:41984
v_add_f32 v250,v250,v6
v_mfma_f32_16x16x32_bf16 a[36:39],v[192:195],v[72:75],a[36:39]
v_exp_f32 v12,v12
v_mfma_f32_16x16x32_bf16 a[36:39],v[196:199],v[104:107],a[36:39]
ds_read_b128 v[168:171],v226 offset:43008
v_add_f32 v251,v251,v7
v_mfma_f32_16x16x32_bf16 a[52:55],v[200:203],v[72:75],a[52:55]
v_exp_f32 v13,v13
v_mfma_f32_16x16x32_bf16 a[52:55],v[204:207],v[104:107],a[52:55]
ds_read_b128 v[172:175],v226 offset:44032
v_add_f32 v252,v252,v12
v_mfma_f32_16x16x32_bf16 a[40:43],v[192:195],v[80:83],a[40:43]
v_exp_f32 v14,v14
v_mfma_f32_16x16x32_bf16 a[40:43],v[196:199],v[112:115],a[40:43]
v_add_f32 v253,v253,v13
v_add_f32 v252,v252,v14
v_mfma_f32_16x16x32_bf16 a[56:59],v[200:203],v[80:83],a[56:59]
v_exp_f32 v15,v15
v_mfma_f32_16x16x32_bf16 a[56:59],v[204:207],v[112:115],a[56:59]
v_add_f32 v253,v253,v15
v_cvt_pk_bf16_f32 v2,v4,v5
v_mfma_f32_16x16x32_bf16 a[44:47],v[192:195],v[88:91],a[44:47]
v_exp_f32 v20,v20
v_mfma_f32_16x16x32_bf16 a[44:47],v[196:199],v[120:123],a[44:47]
v_cvt_pk_bf16_f32 v3,v6,v7
v_cvt_pk_bf16_f32 v10,v12,v13
v_mfma_f32_16x16x32_bf16 a[60:63],v[200:203],v[88:91],a[60:63]
v_exp_f32 v21,v21
v_mfma_f32_16x16x32_bf16 a[60:63],v[204:207],v[120:123],a[60:63]
v_cvt_pk_bf16_f32 v11,v14,v15
v_cvt_pk_bf16_f32 v17,v18,v19
v_mfma_f32_16x16x32_bf16 a[136:139],a[240:243],v[80:83],a[136:139]
v_exp_f32 v22,v22
v_mfma_f32_16x16x32_bf16 a[136:139],a[240:243],v[112:115],a[136:139]
v_cvt_pk_bf16_f32 v18,v20,v21
s_waitcnt lgkmcnt(4)
v_mfma_f32_16x16x32_bf16 v[32:35],v[144:147],a[144:147],v[128:131]
v_exp_f32 v23,v23
v_mfma_f32_16x16x32_bf16 v[32:35],v[148:151],a[148:151],v[32:35]
ds_read_b128 v[176:179],v224 offset:12288
v_cvt_pk_bf16_f32 v19,v22,v23
v_mfma_f32_16x16x32_bf16 v[32:35],v[152:155],a[152:155],v[32:35]
v_exp_f32 v28,v28
v_mfma_f32_16x16x32_bf16 v[32:35],v[156:159],a[156:159],v[32:35]
ds_read_b128 v[180:183],v224 offset:13312
v_cvt_pk_bf16_f32 v24,v24,v25
v_mfma_f32_16x16x32_bf16 v[40:43],v[144:147],a[160:163],v[132:135]
v_exp_f32 v29,v29
v_mfma_f32_16x16x32_bf16 v[40:43],v[148:151],a[164:167],v[40:43]
ds_read_b128 v[184:187],v224 offset:14336
v_cvt_pk_bf16_f32 v25,v26,v27
v_mfma_f32_16x16x32_bf16 v[40:43],v[152:155],a[168:171],v[40:43]
v_exp_f32 v30,v30
v_mfma_f32_16x16x32_bf16 v[40:43],v[156:159],a[172:175],v[40:43]
ds_read_b128 v[188:191],v224 offset:15360
v_cvt_pk_bf16_f32 v26,v28,v29
v_mfma_f32_16x16x32_bf16 v[48:51],v[144:147],a[176:179],v[136:139]
v_exp_f32 v31,v31
v_mfma_f32_16x16x32_bf16 v[48:51],v[148:151],a[180:183],v[48:51]
v_exp_f32 v32,v32
v_mfma_f32_16x16x32_bf16 v[48:51],v[152:155],a[184:187],v[48:51]
v_add_f32 v250,v250,v32
v_cvt_pk_bf16_f32 v27,v30,v31
v_mfma_f32_16x16x32_bf16 v[48:51],v[156:159],a[188:191],v[48:51]
v_exp_f32 v33,v33
v_mfma_f32_16x16x32_bf16 v[56:59],v[144:147],a[192:195],v[140:143]
v_add_f32 v251,v251,v33
v_mfma_f32_16x16x32_bf16 v[56:59],v[148:151],a[196:199],v[56:59]
v_exp_f32 v34,v34
v_mfma_f32_16x16x32_bf16 v[56:59],v[152:155],a[200:203],v[56:59]
v_add_f32 v250,v250,v34
v_cvt_pk_bf16_f32 v32,v32,v33
v_mfma_f32_16x16x32_bf16 v[56:59],v[156:159],a[204:207],v[56:59]
s_waitcnt lgkmcnt(4)
v_mfma_f32_16x16x32_bf16 a[64:67],v[160:163],v[64:67],a[64:67]
v_exp_f32 v35,v35
v_mfma_f32_16x16x32_bf16 a[64:67],v[164:167],v[96:99],a[64:67]
ds_read_b128 v[192:195],v226 offset:45056
v_add_f32 v251,v251,v35
v_mfma_f32_16x16x32_bf16 a[80:83],v[168:171],v[64:67],a[80:83]
v_exp_f32 v40,v40
v_mfma_f32_16x16x32_bf16 a[80:83],v[172:175],v[96:99],a[80:83]
ds_read_b128 v[196:199],v226 offset:46080
v_add_f32 v252,v252,v40
v_mfma_f32_16x16x32_bf16 a[68:71],v[160:163],v[72:75],a[68:71]
v_exp_f32 v41,v41
v_mfma_f32_16x16x32_bf16 a[68:71],v[164:167],v[104:107],a[68:71]
ds_read_b128 v[200:203],v226 offset:47104
v_add_f32 v253,v253,v41
v_mfma_f32_16x16x32_bf16 a[84:87],v[168:171],v[72:75],a[84:87]
v_exp_f32 v42,v42
v_mfma_f32_16x16x32_bf16 a[84:87],v[172:175],v[104:107],a[84:87]
ds_read_b128 v[204:207],v226 offset:48128
v_add_f32 v252,v252,v42
v_mfma_f32_16x16x32_bf16 a[72:75],v[160:163],v[80:83],a[72:75]
v_exp_f32 v43,v43
v_mfma_f32_16x16x32_bf16 a[72:75],v[164:167],v[112:115],a[72:75]
v_add_f32 v253,v253,v43
v_cvt_pk_bf16_f32 v33,v34,v35
v_mfma_f32_16x16x32_bf16 a[88:91],v[168:171],v[80:83],a[88:91]
v_exp_f32 v48,v48
)"
R"(v_mfma_f32_16x16x32_bf16 a[88:91],v[172:175],v[112:115],a[88:91]
v_cvt_pk_bf16_f32 v40,v40,v41
v_mfma_f32_16x16x32_bf16 a[76:79],v[160:163],v[88:91],a[76:79]
v_exp_f32 v49,v49
v_mfma_f32_16x16x32_bf16 a[76:79],v[164:167],v[120:123],a[76:79]
v_cvt_pk_bf16_f32 v41,v42,v43
v_cvt_pk_bf16_f32 v48,v48,v49
v_mfma_f32_16x16x32_bf16 a[92:95],v[168:171],v[88:91],a[92:95]
v_exp_f32 v50,v50
v_mfma_f32_16x16x32_bf16 a[92:95],v[172:175],v[120:123],a[92:95]
s_waitcnt vmcnt(0) lgkmcnt(0)
s_barrier
v_mfma_f32_16x16x32_bf16 v[36:39],v[176:179],a[144:147],v[128:131]
s_add_u32 m0,s82,0x10000
s_add_u32 s59,s80,0x4000
v_mfma_f32_16x16x32_bf16 v[36:39],v[180:183],a[148:151],v[36:39]
ds_read_b128 v[144:147],v225 offset:32768
buffer_load_dwordx4 v228,s[36:39],s59 offen offset:0 lds
v_mfma_f32_16x16x32_bf16 v[36:39],v[184:187],a[152:155],v[36:39]
v_exp_f32 v51,v51
v_mfma_f32_16x16x32_bf16 v[36:39],v[188:191],a[156:159],v[36:39]
ds_read_b128 v[148:151],v225 offset:33792
v_cvt_pk_bf16_f32 v49,v50,v51
v_mfma_f32_16x16x32_bf16 v[44:47],v[176:179],a[160:163],v[132:135]
v_exp_f32 v56,v56
v_mfma_f32_16x16x32_bf16 v[44:47],v[180:183],a[164:167],v[44:47]
ds_read_b128 v[152:155],v225 offset:34816
v_mfma_f32_16x16x32_bf16 v[44:47],v[184:187],a[168:171],v[44:47]
v_exp_f32 v57,v57
v_mfma_f32_16x16x32_bf16 v[44:47],v[188:191],a[172:175],v[44:47]
ds_read_b128 v[156:159],v225 offset:35840
v_cvt_pk_bf16_f32 v56,v56,v57
v_mfma_f32_16x16x32_bf16 v[52:55],v[176:179],a[176:179],v[136:139]
buffer_load_dwordx4 v228,s[36:39],s59 offen offset:1024 lds
v_mfma_f32_16x16x32_bf16 v[52:55],v[180:183],a[180:183],v[52:55]
v_exp_f32 v58,v58
v_mfma_f32_16x16x32_bf16 v[52:55],v[184:187],a[184:187],v[52:55]
v_exp_f32 v36,v36
v_mfma_f32_16x16x32_bf16 v[52:55],v[188:191],a[188:191],v[52:55]
v_exp_f32 v59,v59
v_mfma_f32_16x16x32_bf16 v[60:63],v[176:179],a[192:195],v[140:143]
v_exp_f32 v37,v37
v_mfma_f32_16x16x32_bf16 v[60:63],v[180:183],a[196:199],v[60:63]
v_exp_f32 v38,v38
v_mfma_f32_16x16x32_bf16 v[60:63],v[184:187],a[200:203],v[60:63]
v_exp_f32 v39,v39
v_mfma_f32_16x16x32_bf16 v[60:63],v[188:191],a[204:207],v[60:63]
buffer_load_dwordx4 v228,s[36:39],s59 offen offset:2048 lds
v_add_f32 v250,v250,v36
v_mfma_f32_16x16x32_bf16 a[96:99],v[192:195],v[64:67],a[96:99]
v_exp_f32 v44,v44
v_mfma_f32_16x16x32_bf16 a[96:99],v[196:199],v[96:99],a[96:99]
ds_read_b128 v[160:163],v227 offset:0
v_add_f32 v251,v251,v37
v_mfma_f32_16x16x32_bf16 a[112:115],v[200:203],v[64:67],a[112:115]
v_exp_f32 v45,v45
v_mfma_f32_16x16x32_bf16 a[112:115],v[204:207],v[96:99],a[112:115]
ds_read_b128 v[164:167],v227 offset:1024
v_add_f32 v250,v250,v38
v_mfma_f32_16x16x32_bf16 a[100:103],v[192:195],v[72:75],a[100:103]
v_exp_f32 v46,v46
v_mfma_f32_16x16x32_bf16 a[100:103],v[196:199],v[104:107],a[100:103]
ds_read_b128 v[168:171],v227 offset:2048
v_add_f32 v251,v251,v39
v_mfma_f32_16x16x32_bf16 a[116:119],v[200:203],v[72:75],a[116:119]
buffer_load_dwordx4 v228,s[36:39],s59 offen offset:3072 lds
v_add_f32 v252,v252,v44
v_mfma_f32_16x16x32_bf16 a[116:119],v[204:207],v[104:107],a[116:119]
ds_read_b128 v[172:175],v227 offset:3072
v_add_f32 v253,v253,v45
v_mfma_f32_16x16x32_bf16 a[104:107],v[192:195],v[80:83],a[104:107]
v_exp_f32 v47,v47
v_mfma_f32_16x16x32_bf16 a[104:107],v[196:199],v[112:115],a[104:107]
v_exp_f32 v52,v52
v_mfma_f32_16x16x32_bf16 a[120:123],v[200:203],v[80:83],a[120:123]
v_exp_f32 v53,v53
v_mfma_f32_16x16x32_bf16 a[120:123],v[204:207],v[112:115],a[120:123]
v_exp_f32 v54,v54
v_mfma_f32_16x16x32_bf16 a[108:111],v[192:195],v[88:91],a[108:111]
v_exp_f32 v55,v55
v_mfma_f32_16x16x32_bf16 a[108:111],v[196:199],v[120:123],a[108:111]
v_add_f32 v252,v252,v46
v_add_f32 v253,v253,v47
v_mfma_f32_16x16x32_bf16 a[124:127],v[200:203],v[88:91],a[124:127]
v_cvt_pk_bf16_f32 v34,v36,v37
v_cvt_pk_bf16_f32 v42,v44,v45
v_mfma_f32_16x16x32_bf16 a[124:127],v[204:207],v[120:123],a[124:127]
v_cvt_pk_bf16_f32 v35,v38,v39
v_cvt_pk_bf16_f32 v43,v46,v47
v_mfma_f32_16x16x32_bf16 a[140:143],a[240:243],v[88:91],a[140:143]
v_cvt_pk_bf16_f32 v57,v58,v59
v_mfma_f32_16x16x32_bf16 a[140:143],a[240:243],v[120:123],a[140:143]
s_add_u32 s80,s80,0x8000
s_add_u32 s62,s62,1
s_cmp_lg_u32 s62,4
s_cbranch_scc1 L_HBO1_%=
s_mov_b32 s59,0
L_HPO1_%=:
v_readfirstlane_b32 s58,v237
s_nop 3
s_cmp_ge_u32 s58,10
s_cbranch_scc1 L_HDO1_%=
s_sleep 2
global_load_dword v237,v238,s[22:23] offset:128 sc1
s_add_u32 s59,s59,1
s_cmp_lt_u32 s59,0x20000
s_waitcnt vmcnt(0)
s_cbranch_scc1 L_HPO1_%=
L_HDO1_%=:
buffer_inv sc1
L_HBO1_%=:
s_branch L_E1_%=
L_OM2_%=:
s_lshl_b32 s58,s56,4
v_add_u32 v231,s58,v229
v_cvt_f32_i32 v231,v231
v_mul_f32 v231,0x71800000,v231
s_waitcnt lgkmcnt(4)
v_mfma_f32_16x16x32_bf16 v[0:3],v[144:147],a[144:147],v[128:131]
v_exp_f32 v124,v124
v_add_f32 v17,0xf1000000,v231
v_min_f32 v17,v17,v137
v_mfma_f32_16x16x32_bf16 v[0:3],v[148:151],a[148:151],v[0:3]
v_exp_f32 v125,v125
v_add_f32 v18,0xf1c00000,v231
v_min_f32 v18,v18,v138
v_mfma_f32_16x16x32_bf16 v[0:3],v[152:155],a[152:155],v[0:3]
v_exp_f32 v126,v126
v_add_f32 v19,0xf2200000,v231
v_min_f32 v19,v19,v139
v_mfma_f32_16x16x32_bf16 v[0:3],v[156:159],a[156:159],v[0:3]
v_exp_f32 v127,v127
v_mfma_f32_16x16x32_bf16 v[8:11],v[144:147],a[160:163],v[132:135]
v_cvt_pk_bf16_f32 v114,v116,v117
v_cvt_pk_bf16_f32 v115,v118,v119
v_mfma_f32_16x16x32_bf16 v[8:11],v[148:151],a[164:167],v[8:11]
v_cvt_pk_bf16_f32 v122,v124,v125
v_cvt_pk_bf16_f32 v123,v126,v127
v_mfma_f32_16x16x32_bf16 v[8:11],v[152:155],a[168:171],v[8:11]
v_add_f32 v16,0x71000000,v231
v_min_f32 v16,v16,v136
v_mfma_f32_16x16x32_bf16 v[8:11],v[156:159],a[172:175],v[8:11]
ds_read_b128 v[176:179],v224 offset:4096
ds_read_b128 v[180:183],v224 offset:5120
v_mfma_f32_16x16x32_bf16 v[16:19],v[144:147],a[176:179],v[16:19]
ds_read_b128 v[184:187],v224 offset:6144
ds_read_b128 v[188:191],v224 offset:7168
v_mfma_f32_16x16x32_bf16 v[16:19],v[148:151],a[180:183],v[16:19]
s_mov_b32 m0,s82
v_exp_f32 v0,v0
v_mfma_f32_16x16x32_bf16 v[16:19],v[152:155],a[184:187],v[16:19]
v_add_f32 v20,0xf3780000,v231
v_min_f32 v20,v20,v136
v_mfma_f32_16x16x32_bf16 v[16:19],v[156:159],a[188:191],v[16:19]
v_add_f32 v21,0xf3840000,v231
v_min_f32 v21,v21,v137
v_mfma_f32_16x16x32_bf16 v[24:27],v[144:147],a[192:195],v[140:143]
v_add_f32 v22,0xf38c0000,v231
v_min_f32 v22,v22,v138
v_mfma_f32_16x16x32_bf16 v[24:27],v[148:151],a[196:199],v[24:27]
v_add_f32 v23,0xf3940000,v231
v_min_f32 v23,v23,v139
v_mfma_f32_16x16x32_bf16 v[24:27],v[152:155],a[200:203],v[24:27]
buffer_load_dwordx4 v228,s[36:39],s80 offen offset:0 lds
v_add_f32 v250,v250,v0
v_mfma_f32_16x16x32_bf16 v[24:27],v[156:159],a[204:207],v[24:27]
s_waitcnt lgkmcnt(4)
v_mfma_f32_16x16x32_bf16 a[0:3],v[160:163],v[64:67],a[0:3]
v_exp_f32 v1,v1
v_mfma_f32_16x16x32_bf16 a[0:3],v[164:167],v[96:99],a[0:3]
ds_read_b128 v[192:195],v226 offset:36864
v_add_f32 v251,v251,v1
v_mfma_f32_16x16x32_bf16 a[16:19],v[168:171],v[64:67],a[16:19]
v_exp_f32 v2,v2
v_mfma_f32_16x16x32_bf16 a[16:19],v[172:175],v[96:99],a[16:19]
ds_read_b128 v[196:199],v226 offset:37888
v_add_f32 v250,v250,v2
v_mfma_f32_16x16x32_bf16 a[4:7],v[160:163],v[72:75],a[4:7]
v_exp_f32 v3,v3
v_mfma_f32_16x16x32_bf16 a[4:7],v[164:167],v[104:107],a[4:7]
ds_read_b128 v[200:203],v226 offset:38912
v_add_f32 v251,v251,v3
v_mfma_f32_16x16x32_bf16 a[20:23],v[168:171],v[72:75],a[20:23]
buffer_load_dwordx4 v228,s[36:39],s80 offen offset:1024 lds
v_cvt_pk_bf16_f32 v0,v0,v1
v_mfma_f32_16x16x32_bf16 a[20:23],v[172:175],v[104:107],a[20:23]
ds_read_b128 v[204:207],v226 offset:39936
v_cvt_pk_bf16_f32 v1,v2,v3
v_mfma_f32_16x16x32_bf16 a[8:11],v[160:163],v[80:83],a[8:11]
v_exp_f32 v8,v8
v_mfma_f32_16x16x32_bf16 a[8:11],v[164:167],v[112:115],a[8:11]
v_add_f32 v252,v252,v8
v_mfma_f32_16x16x32_bf16 a[24:27],v[168:171],v[80:83],a[24:27]
v_exp_f32 v9,v9
v_mfma_f32_16x16x32_bf16 a[24:27],v[172:175],v[112:115],a[24:27]
v_add_f32 v253,v253,v9
v_mfma_f32_16x16x32_bf16 a[12:15],v[160:163],v[88:91],a[12:15]
v_exp_f32 v10,v10
v_mfma_f32_16x16x32_bf16 a[12:15],v[164:167],v[120:123],a[12:15]
v_add_f32 v252,v252,v10
v_cvt_pk_bf16_f32 v8,v8,v9
v_mfma_f32_16x16x32_bf16 a[28:31],v[168:171],v[88:91],a[28:31]
buffer_load_dwordx4 v228,s[36:39],s80 offen offset:2048 lds
v_mfma_f32_16x16x32_bf16 a[28:31],v[172:175],v[120:123],a[28:31]
s_waitcnt lgkmcnt(4)
v_mfma_f32_16x16x32_bf16 v[4:7],v[176:179],a[144:147],v[128:131]
v_exp_f32 v11,v11
v_mfma_f32_16x16x32_bf16 v[4:7],v[180:183],a[148:151],v[4:7]
ds_read_b128 v[144:147],v224 offset:8192
v_add_f32 v253,v253,v11
v_mfma_f32_16x16x32_bf16 v[4:7],v[184:187],a[152:155],v[4:7]
v_exp_f32 v16,v16
v_mfma_f32_16x16x32_bf16 v[4:7],v[188:191],a[156:159],v[4:7]
ds_read_b128 v[148:151],v224 offset:9216
v_cvt_pk_bf16_f32 v9,v10,v11
v_mfma_f32_16x16x32_bf16 v[12:15],v[176:179],a[160:163],v[132:135]
v_exp_f32 v17,v17
v_mfma_f32_16x16x32_bf16 v[12:15],v[180:183],a[164:167],v[12:15]
ds_read_b128 v[152:155],v224 offset:10240
v_cvt_pk_bf16_f32 v16,v16,v17
v_mfma_f32_16x16x32_bf16 v[12:15],v[184:187],a[168:171],v[12:15]
buffer_load_dwordx4 v228,s[36:39],s80 offen offset:3072 lds
v_mfma_f32_16x16x32_bf16 v[12:15],v[188:191],a[172:175],v[12:15]
ds_read_b128 v[156:159],v224 offset:11264
v_mfma_f32_16x16x32_bf16 v[20:23],v[176:179],a[176:179],v[20:23]
v_exp_f32 v18,v18
v_mfma_f32_16x16x32_bf16 v[20:23],v[180:183],a[180:183],v[20:23]
v_exp_f32 v4,v4
v_mfma_f32_16x16x32_bf16 v[20:23],v[184:187],a[184:187],v[20:23]
v_exp_f32 v19,v19
v_mfma_f32_16x16x32_bf16 v[20:23],v[188:191],a[188:191],v[20:23]
v_exp_f32 v5,v5
v_mfma_f32_16x16x32_bf16 v[28:31],v[176:179],a[192:195],v[140:143]
v_add_f32 v48,0xf3fc0000,v231
v_min_f32 v48,v48,v136
v_mfma_f32_16x16x32_bf16 v[28:31],v[180:183],a[196:199],v[28:31]
v_add_f32 v49,0xf4020000,v231
v_min_f32 v49,v49,v137
v_mfma_f32_16x16x32_bf16 v[28:31],v[184:187],a[200:203],v[28:31]
v_add_f32 v50,0xf4060000,v231
v_min_f32 v50,v50,v138
v_mfma_f32_16x16x32_bf16 v[28:31],v[188:191],a[204:207],v[28:31]
v_exp_f32 v6,v6
s_waitcnt lgkmcnt(4)
v_mfma_f32_16x16x32_bf16 a[32:35],v[192:195],v[64:67],a[32:35]
v_add_f32 v51,0xf40a0000,v231
v_min_f32 v51,v51,v139
v_mfma_f32_16x16x32_bf16 a[32:35],v[196:199],v[96:99],a[32:35]
ds_read_b128 v[160:163],v226 offset:40960
v_exp_f32 v7,v7
v_mfma_f32_16x16x32_bf16 a[48:51],v[200:203],v[64:67],a[48:51]
v_exp_f32 v24,v24
v_mfma_f32_16x16x32_bf16 a[48:51],v[204:207],v[96:99],a[48:51]
ds_read_b128 v[164:167],v226 offset:41984
v_add_f32 v250,v250,v4
v_mfma_f32_16x16x32_bf16 a[36:39],v[192:195],v[72:75],a[36:39]
v_exp_f32 v25,v25
v_mfma_f32_16x16x32_bf16 a[36:39],v[196:199],v[104:107],a[36:39]
ds_read_b128 v[168:171],v226 offset:43008
v_add_f32 v251,v251,v5
v_mfma_f32_16x16x32_bf16 a[52:55],v[200:203],v[72:75],a[52:55]
v_exp_f32 v26,v26
v_mfma_f32_16x16x32_bf16 a[52:55],v[204:207],v[104:107],a[52:55]
ds_read_b128 v[172:175],v226 offset:44032
v_add_f32 v250,v250,v6
v_mfma_f32_16x16x32_bf16 a[40:43],v[192:195],v[80:83],a[40:43]
v_exp_f32 v27,v27
v_mfma_f32_16x16x32_bf16 a[40:43],v[196:199],v[112:115],a[40:43]
v_exp_f32 v12,v12
v_mfma_f32_16x16x32_bf16 a[56:59],v[200:203],v[80:83],a[56:59]
v_add_f32 v251,v251,v7
v_add_f32 v252,v252,v12
v_mfma_f32_16x16x32_bf16 a[56:59],v[204:207],v[112:115],a[56:59]
v_exp_f32 v13,v13
v_mfma_f32_16x16x32_bf16 a[44:47],v[192:195],v[88:91],a[44:47]
v_add_f32 v253,v253,v13
v_cvt_pk_bf16_f32 v2,v4,v5
v_mfma_f32_16x16x32_bf16 a[44:47],v[196:199],v[120:123],a[44:47]
v_exp_f32 v14,v14
v_mfma_f32_16x16x32_bf16 a[60:63],v[200:203],v[88:91],a[60:63]
v_add_f32 v252,v252,v14
v_cvt_pk_bf16_f32 v3,v6,v7
v_mfma_f32_16x16x32_bf16 a[60:63],v[204:207],v[120:123],a[60:63]
v_exp_f32 v15,v15
v_mfma_f32_16x16x32_bf16 a[136:139],a[240:243],v[80:83],a[136:139]
v_add_f32 v253,v253,v15
v_cvt_pk_bf16_f32 v10,v12,v13
v_mfma_f32_16x16x32_bf16 a[136:139],a[240:243],v[112:115],a[136:139]
v_cvt_pk_bf16_f32 v11,v14,v15
s_waitcnt lgkmcnt(4)
v_mfma_f32_16x16x32_bf16 v[32:35],v[144:147],a[144:147],v[128:131]
v_exp_f32 v20,v20
v_mfma_f32_16x16x32_bf16 v[32:35],v[148:151],a[148:151],v[32:35]
ds_read_b128 v[176:179],v224 offset:12288
v_cvt_pk_bf16_f32 v17,v18,v19
v_mfma_f32_16x16x32_bf16 v[32:35],v[152:155],a[152:155],v[32:35]
v_exp_f32 v21,v21
v_mfma_f32_16x16x32_bf16 v[32:35],v[156:159],a[156:159],v[32:35]
ds_read_b128 v[180:183],v224 offset:13312
v_cvt_pk_bf16_f32 v18,v20,v21
v_mfma_f32_16x16x32_bf16 v[40:43],v[144:147],a[160:163],v[132:135]
v_exp_f32 v22,v22
v_mfma_f32_16x16x32_bf16 v[40:43],v[148:151],a[164:167],v[40:43]
ds_read_b128 v[184:187],v224 offset:14336
v_cvt_pk_bf16_f32 v24,v24,v25
v_mfma_f32_16x16x32_bf16 v[40:43],v[152:155],a[168:171],v[40:43]
v_exp_f32 v23,v23
v_mfma_f32_16x16x32_bf16 v[40:43],v[156:159],a[172:175],v[40:43]
ds_read_b128 v[188:191],v224 offset:15360
v_cvt_pk_bf16_f32 v19,v22,v23
v_mfma_f32_16x16x32_bf16 v[48:51],v[144:147],a[176:179],v[48:51]
v_exp_f32 v28,v28
v_mfma_f32_16x16x32_bf16 v[48:51],v[148:151],a[180:183],v[48:51]
v_exp_f32 v32,v32
v_mfma_f32_16x16x32_bf16 v[48:51],v[152:155],a[184:187],v[48:51]
v_add_f32 v52,0xf43e0000,v231
v_min_f32 v52,v52,v136
v_mfma_f32_16x16x32_bf16 v[48:51],v[156:159],a[188:191],v[48:51]
v_add_f32 v53,0xf4420000,v231
v_min_f32 v53,v53,v137
v_mfma_f32_16x16x32_bf16 v[56:59],v[144:147],a[192:195],v[140:143]
v_add_f32 v54,0xf4460000,v231
v_min_f32 v54,v54,v138
v_mfma_f32_16x16x32_bf16 v[56:59],v[148:151],a[196:199],v[56:59]
v_add_f32 v55,0xf44a0000,v231
v_min_f32 v55,v55,v139
v_mfma_f32_16x16x32_bf16 v[56:59],v[152:155],a[200:203],v[56:59]
v_exp_f32 v29,v29
v_mfma_f32_16x16x32_bf16 v[56:59],v[156:159],a[204:207],v[56:59]
v_exp_f32 v33,v33
s_waitcnt lgkmcnt(4)
v_mfma_f32_16x16x32_bf16 a[64:67],v[160:163],v[64:67],a[64:67]
v_exp_f32 v30,v30
v_mfma_f32_16x16x32_bf16 a[64:67],v[164:167],v[96:99],a[64:67]
ds_read_b128 v[192:195],v226 offset:45056
v_add_f32 v250,v250,v32
v_mfma_f32_16x16x32_bf16 a[80:83],v[168:171],v[64:67],a[80:83]
v_exp_f32 v31,v31
v_mfma_f32_16x16x32_bf16 a[80:83],v[172:175],v[96:99],a[80:83]
ds_read_b128 v[196:199],v226 offset:46080
v_add_f32 v251,v251,v33
v_mfma_f32_16x16x32_bf16 a[68:71],v[160:163],v[72:75],a[68:71]
v_exp_f32 v34,v34
v_mfma_f32_16x16x32_bf16 a[68:71],v[164:167],v[104:107],a[68:71]
ds_read_b128 v[200:203],v226 offset:47104
v_add_f32 v250,v250,v34
v_mfma_f32_16x16x32_bf16 a[84:87],v[168:171],v[72:75],a[84:87]
v_exp_f32 v35,v35
v_mfma_f32_16x16x32_bf16 a[84:87],v[172:175],v[104:107],a[84:87]
ds_read_b128 v[204:207],v226 offset:48128
v_add_f32 v251,v251,v35
v_mfma_f32_16x16x32_bf16 a[72:75],v[160:163],v[80:83],a[72:75]
v_exp_f32 v40,v40
v_mfma_f32_16x16x32_bf16 a[72:75],v[164:167],v[112:115],a[72:75]
v_add_f32 v252,v252,v40
v_cvt_pk_bf16_f32 v25,v26,v27
v_mfma_f32_16x16x32_bf16 a[88:91],v[168:171],v[80:83],a[88:91]
v_exp_f32 v41,v41
v_mfma_f32_16x16x32_bf16 a[88:91],v[172:175],v[112:115],a[88:91]
v_add_f32 v253,v253,v41
v_cvt_pk_bf16_f32 v26,v28,v29
v_mfma_f32_16x16x32_bf16 a[76:79],v[160:163],v[88:91],a[76:79]
v_exp_f32 v42,v42
v_mfma_f32_16x16x32_bf16 a[76:79],v[164:167],v[120:123],a[76:79]
v_add_f32 v252,v252,v42
v_cvt_pk_bf16_f32 v27,v30,v31
v_mfma_f32_16x16x32_bf16 a[92:95],v[168:171],v[88:91],a[92:95]
v_exp_f32 v43,v43
v_mfma_f32_16x16x32_bf16 a[92:95],v[172:175],v[120:123],a[92:95]
s_waitcnt vmcnt(0) lgkmcnt(0)
s_barrier
v_mfma_f32_16x16x32_bf16 v[36:39],v[176:179],a[144:147],v[128:131]
s_add_u32 m0,s82,0x10000
s_add_u32 s59,s80,0x4000
v_mfma_f32_16x16x32_bf16 v[36:39],v[180:183],a[148:151],v[36:39]
ds_read_b128 v[144:147],v225 offset:32768
buffer_load_dwordx4 v228,s[36:39],s59 offen offset:0 lds
v_mfma_f32_16x16x32_bf16 v[36:39],v[184:187],a[152:155],v[36:39]
v_exp_f32 v48,v48
v_mfma_f32_16x16x32_bf16 v[36:39],v[188:191],a[156:159],v[36:39]
ds_read_b128 v[148:151],v225 offset:33792
v_add_f32 v253,v253,v43
v_mfma_f32_16x16x32_bf16 v[44:47],v[176:179],a[160:163],v[132:135]
v_exp_f32 v49,v49
v_mfma_f32_16x16x32_bf16 v[44:47],v[180:183],a[164:167],v[44:47]
ds_read_b128 v[152:155],v225 offset:34816
v_cvt_pk_bf16_f32 v32,v32,v33
v_cvt_pk_bf16_f32 v48,v48,v49
v_mfma_f32_16x16x32_bf16 v[44:47],v[184:187],a[168:171],v[44:47]
v_exp_f32 v50,v50
v_mfma_f32_16x16x32_bf16 v[44:47],v[188:191],a[172:175],v[44:47]
ds_read_b128 v[156:159],v225 offset:35840
v_cvt_pk_bf16_f32 v33,v34,v35
v_mfma_f32_16x16x32_bf16 v[52:55],v[176:179],a[176:179],v[52:55]
buffer_load_dwordx4 v228,s[36:39],s59 offen offset:1024 lds
v_cvt_pk_bf16_f32 v40,v40,v41
v_mfma_f32_16x16x32_bf16 v[52:55],v[180:183],a[180:183],v[52:55]
v_exp_f32 v51,v51
v_mfma_f32_16x16x32_bf16 v[52:55],v[184:187],a[184:187],v[52:55]
v_exp_f32 v36,v36
)"
R"(v_cvt_pk_bf16_f32 v49,v50,v51
v_mfma_f32_16x16x32_bf16 v[52:55],v[188:191],a[188:191],v[52:55]
v_exp_f32 v56,v56
v_mfma_f32_16x16x32_bf16 v[60:63],v[176:179],a[192:195],v[140:143]
v_exp_f32 v37,v37
v_mfma_f32_16x16x32_bf16 v[60:63],v[180:183],a[196:199],v[60:63]
v_exp_f32 v57,v57
v_mfma_f32_16x16x32_bf16 v[60:63],v[184:187],a[200:203],v[60:63]
v_exp_f32 v38,v38
v_cvt_pk_bf16_f32 v56,v56,v57
v_mfma_f32_16x16x32_bf16 v[60:63],v[188:191],a[204:207],v[60:63]
buffer_load_dwordx4 v228,s[36:39],s59 offen offset:2048 lds
v_add_f32 v250,v250,v36
v_mfma_f32_16x16x32_bf16 a[96:99],v[192:195],v[64:67],a[96:99]
v_exp_f32 v58,v58
v_mfma_f32_16x16x32_bf16 a[96:99],v[196:199],v[96:99],a[96:99]
ds_read_b128 v[160:163],v227 offset:0
v_add_f32 v251,v251,v37
v_mfma_f32_16x16x32_bf16 a[112:115],v[200:203],v[64:67],a[112:115]
v_exp_f32 v59,v59
v_mfma_f32_16x16x32_bf16 a[112:115],v[204:207],v[96:99],a[112:115]
ds_read_b128 v[164:167],v227 offset:1024
v_add_f32 v250,v250,v38
v_cvt_pk_bf16_f32 v57,v58,v59
v_mfma_f32_16x16x32_bf16 a[100:103],v[192:195],v[72:75],a[100:103]
v_exp_f32 v39,v39
v_mfma_f32_16x16x32_bf16 a[100:103],v[196:199],v[104:107],a[100:103]
ds_read_b128 v[168:171],v227 offset:2048
v_add_f32 v251,v251,v39
v_mfma_f32_16x16x32_bf16 a[116:119],v[200:203],v[72:75],a[116:119]
buffer_load_dwordx4 v228,s[36:39],s59 offen offset:3072 lds
v_cvt_pk_bf16_f32 v34,v36,v37
v_mfma_f32_16x16x32_bf16 a[116:119],v[204:207],v[104:107],a[116:119]
ds_read_b128 v[172:175],v227 offset:3072
v_cvt_pk_bf16_f32 v35,v38,v39
v_mfma_f32_16x16x32_bf16 a[104:107],v[192:195],v[80:83],a[104:107]
v_exp_f32 v44,v44
v_mfma_f32_16x16x32_bf16 a[104:107],v[196:199],v[112:115],a[104:107]
v_exp_f32 v45,v45
v_mfma_f32_16x16x32_bf16 a[120:123],v[200:203],v[80:83],a[120:123]
v_exp_f32 v46,v46
v_mfma_f32_16x16x32_bf16 a[120:123],v[204:207],v[112:115],a[120:123]
v_exp_f32 v47,v47
v_add_f32 v252,v252,v46
v_mfma_f32_16x16x32_bf16 a[108:111],v[192:195],v[88:91],a[108:111]
v_exp_f32 v52,v52
v_add_f32 v253,v253,v47
v_mfma_f32_16x16x32_bf16 a[108:111],v[196:199],v[120:123],a[108:111]
v_exp_f32 v53,v53
v_mfma_f32_16x16x32_bf16 a[124:127],v[200:203],v[88:91],a[124:127]
v_exp_f32 v54,v54
v_mfma_f32_16x16x32_bf16 a[124:127],v[204:207],v[120:123],a[124:127]
v_exp_f32 v55,v55
v_mfma_f32_16x16x32_bf16 a[140:143],a[240:243],v[88:91],a[140:143]
v_add_f32 v252,v252,v44
v_add_f32 v253,v253,v45
v_mfma_f32_16x16x32_bf16 a[140:143],a[240:243],v[120:123],a[140:143]
v_cvt_pk_bf16_f32 v41,v42,v43
v_cvt_pk_bf16_f32 v42,v44,v45
v_cvt_pk_bf16_f32 v43,v46,v47
s_add_u32 s80,s80,0x8000
s_add_u32 s62,s62,1
s_cmp_lg_u32 s62,4
s_cbranch_scc1 L_HBOM_%=
s_mov_b32 s59,0
L_HPOM_%=:
v_readfirstlane_b32 s58,v237
s_nop 3
s_cmp_ge_u32 s58,10
s_cbranch_scc1 L_HDOM_%=
s_sleep 2
global_load_dword v237,v238,s[22:23] offset:128 sc1
s_add_u32 s59,s59,1
s_cmp_lt_u32 s59,0x20000
s_waitcnt vmcnt(0)
s_cbranch_scc1 L_HPOM_%=
L_HDOM_%=:
buffer_inv sc1
L_HBOM_%=:
L_EM2_%=:
v_add_u32 v225,s83,v225
s_lshl_b32 s58,s56,4
s_sub_u32 s58,s58,64
v_add_u32 v231,s58,v229
v_cvt_f32_i32 v231,v231
v_mul_f32 v231,0x71800000,v231
s_waitcnt lgkmcnt(4)
v_mfma_f32_16x16x32_bf16 v[64:67],v[144:147],a[144:147],v[128:131]
v_exp_f32 v60,v60
v_add_f32 v89,0x747e0000,v231
v_min_f32 v89,v89,v141
v_mfma_f32_16x16x32_bf16 v[64:67],v[148:151],a[148:151],v[64:67]
v_exp_f32 v61,v61
v_add_f32 v90,0x747a0000,v231
v_min_f32 v90,v90,v142
v_mfma_f32_16x16x32_bf16 v[64:67],v[152:155],a[152:155],v[64:67]
v_exp_f32 v62,v62
v_add_f32 v91,0x74760000,v231
v_min_f32 v91,v91,v143
v_mfma_f32_16x16x32_bf16 v[64:67],v[156:159],a[156:159],v[64:67]
v_exp_f32 v63,v63
ds_read_b128 v[176:179],v224 offset:36864
v_mfma_f32_16x16x32_bf16 v[72:75],v[144:147],a[160:163],v[132:135]
v_cvt_pk_bf16_f32 v50,v52,v53
v_cvt_pk_bf16_f32 v51,v54,v55
ds_read_b128 v[180:183],v224 offset:37888
v_mfma_f32_16x16x32_bf16 v[72:75],v[148:151],a[164:167],v[72:75]
v_cvt_pk_bf16_f32 v58,v60,v61
v_cvt_pk_bf16_f32 v59,v62,v63
ds_read_b128 v[184:187],v224 offset:38912
v_mfma_f32_16x16x32_bf16 v[72:75],v[152:155],a[168:171],v[72:75]
v_add_f32 v88,0x74810000,v231
v_min_f32 v88,v88,v140
v_mfma_f32_16x16x32_bf16 v[72:75],v[156:159],a[172:175],v[72:75]
v_add_f32 v92,0x74420000,v231
v_min_f32 v92,v92,v140
ds_read_b128 v[188:191],v224 offset:39936
v_mfma_f32_16x16x32_bf16 v[88:91],v[144:147],a[192:195],v[88:91]
v_add_f32 v93,0x743e0000,v231
v_min_f32 v93,v93,v141
v_mfma_f32_16x16x32_bf16 v[88:91],v[148:151],a[196:199],v[88:91]
v_add_f32 v94,0x743a0000,v231
v_min_f32 v94,v94,v142
v_exp_f32 v64,v64
v_mfma_f32_16x16x32_bf16 v[88:91],v[152:155],a[200:203],v[88:91]
v_add_f32 v95,0x74360000,v231
v_min_f32 v95,v95,v143
v_mfma_f32_16x16x32_bf16 v[88:91],v[156:159],a[204:207],v[88:91]
s_mov_b32 m0,s81
v_exp_f32 v65,v65
s_waitcnt lgkmcnt(4)
v_mfma_f32_16x16x32_bf16 a[0:3],v[160:163],v[0:3],a[0:3]
buffer_load_dwordx4 v228,s[36:39],s80 offen offset:0 lds
v_mov_b32 v80,0
v_mfma_f32_16x16x32_bf16 a[0:3],v[164:167],v[32:35],a[0:3]
ds_read_b128 v[192:195],v226 offset:4096
v_mov_b32 v81,0
v_exp_f32 v66,v66
v_mfma_f32_16x16x32_bf16 a[16:19],v[168:171],v[0:3],a[16:19]
v_mov_b32 v82,0
v_mov_b32 v83,0
v_mfma_f32_16x16x32_bf16 a[16:19],v[172:175],v[32:35],a[16:19]
ds_read_b128 v[196:199],v226 offset:5120
v_mov_b32 v84,0
v_exp_f32 v67,v67
v_mfma_f32_16x16x32_bf16 a[4:7],v[160:163],v[8:11],a[4:7]
v_mov_b32 v85,0
v_mov_b32 v86,0
v_mfma_f32_16x16x32_bf16 a[4:7],v[164:167],v[40:43],a[4:7]
ds_read_b128 v[200:203],v226 offset:6144
v_mov_b32 v87,0
v_mfma_f32_16x16x32_bf16 a[20:23],v[168:171],v[8:11],a[20:23]
buffer_load_dwordx4 v228,s[36:39],s80 offen offset:1024 lds
v_mov_b32 v112,0
v_mfma_f32_16x16x32_bf16 a[20:23],v[172:175],v[40:43],a[20:23]
ds_read_b128 v[204:207],v226 offset:7168
v_mov_b32 v113,0
v_mfma_f32_16x16x32_bf16 a[8:11],v[160:163],v[16:19],a[8:11]
v_mov_b32 v114,0
v_mov_b32 v115,0
v_mfma_f32_16x16x32_bf16 a[8:11],v[164:167],v[48:51],a[8:11]
v_mov_b32 v116,0
v_mov_b32 v117,0
v_mfma_f32_16x16x32_bf16 a[24:27],v[168:171],v[16:19],a[24:27]
v_mov_b32 v118,0
v_mov_b32 v119,0
v_mfma_f32_16x16x32_bf16 a[24:27],v[172:175],v[48:51],a[24:27]
v_exp_f32 v72,v72
v_mfma_f32_16x16x32_bf16 a[12:15],v[160:163],v[24:27],a[12:15]
buffer_load_dwordx4 v228,s[36:39],s80 offen offset:2048 lds
v_add_f32 v250,v250,v64
v_mfma_f32_16x16x32_bf16 a[12:15],v[164:167],v[56:59],a[12:15]
v_exp_f32 v73,v73
v_mfma_f32_16x16x32_bf16 a[28:31],v[168:171],v[24:27],a[28:31]
v_add_f32 v251,v251,v65
v_add_f32 v250,v250,v66
v_mfma_f32_16x16x32_bf16 a[28:31],v[172:175],v[56:59],a[28:31]
v_add_f32 v251,v251,v67
s_waitcnt lgkmcnt(4)
v_mfma_f32_16x16x32_bf16 v[68:71],v[176:179],a[144:147],v[128:131]
v_exp_f32 v74,v74
v_mfma_f32_16x16x32_bf16 v[68:71],v[180:183],a[148:151],v[68:71]
ds_read_b128 v[144:147],v224 offset:40960
v_add_f32 v252,v252,v72
v_mfma_f32_16x16x32_bf16 v[68:71],v[184:187],a[152:155],v[68:71]
buffer_load_dwordx4 v228,s[36:39],s80 offen offset:3072 lds
v_add_f32 v253,v253,v73
v_mfma_f32_16x16x32_bf16 v[68:71],v[188:191],a[156:159],v[68:71]
ds_read_b128 v[148:151],v224 offset:41984
v_add_f32 v252,v252,v74
v_mfma_f32_16x16x32_bf16 v[76:79],v[176:179],a[160:163],v[132:135]
v_exp_f32 v75,v75
v_mfma_f32_16x16x32_bf16 v[76:79],v[180:183],a[164:167],v[76:79]
ds_read_b128 v[152:155],v224 offset:43008
v_add_f32 v253,v253,v75
v_mfma_f32_16x16x32_bf16 v[76:79],v[184:187],a[168:171],v[76:79]
s_add_u32 m0,s81,0x4000
s_add_u32 s58,s80,0x8000
v_mfma_f32_16x16x32_bf16 v[76:79],v[188:191],a[172:175],v[76:79]
ds_read_b128 v[156:159],v224 offset:44032
buffer_load_dwordx4 v228,s[36:39],s58 offen offset:0 lds
v_mfma_f32_16x16x32_bf16 v[92:95],v[176:179],a[192:195],v[92:95]
v_add_f32 v120,0x74020000,v231
v_min_f32 v120,v120,v140
v_mfma_f32_16x16x32_bf16 v[92:95],v[180:183],a[196:199],v[92:95]
v_add_f32 v121,0x73fc0000,v231
v_min_f32 v121,v121,v141
v_mfma_f32_16x16x32_bf16 v[92:95],v[184:187],a[200:203],v[92:95]
v_add_f32 v122,0x73f40000,v231
v_min_f32 v122,v122,v142
v_mfma_f32_16x16x32_bf16 v[92:95],v[188:191],a[204:207],v[92:95]
v_exp_f32 v88,v88
s_waitcnt lgkmcnt(4)
v_mfma_f32_16x16x32_bf16 a[32:35],v[192:195],v[0:3],a[32:35]
v_add_f32 v123,0x73ec0000,v231
v_min_f32 v123,v123,v143
v_mfma_f32_16x16x32_bf16 a[32:35],v[196:199],v[32:35],a[32:35]
ds_read_b128 v[160:163],v226 offset:8192
buffer_load_dwordx4 v228,s[36:39],s58 offen offset:1024 lds
v_mfma_f32_16x16x32_bf16 a[48:51],v[200:203],v[0:3],a[48:51]
v_exp_f32 v89,v89
v_mfma_f32_16x16x32_bf16 a[48:51],v[204:207],v[32:35],a[48:51]
ds_read_b128 v[164:167],v226 offset:9216
v_exp_f32 v68,v68
v_mfma_f32_16x16x32_bf16 a[36:39],v[192:195],v[8:11],a[36:39]
v_exp_f32 v90,v90
v_mfma_f32_16x16x32_bf16 a[36:39],v[196:199],v[40:43],a[36:39]
ds_read_b128 v[168:171],v226 offset:10240
v_add_f32 v250,v250,v68
v_mfma_f32_16x16x32_bf16 a[52:55],v[200:203],v[8:11],a[52:55]
v_exp_f32 v91,v91
v_mfma_f32_16x16x32_bf16 a[52:55],v[204:207],v[40:43],a[52:55]
ds_read_b128 v[172:175],v226 offset:11264
buffer_load_dwordx4 v228,s[36:39],s58 offen offset:2048 lds
v_mfma_f32_16x16x32_bf16 a[40:43],v[192:195],v[16:19],a[40:43]
v_exp_f32 v69,v69
v_mfma_f32_16x16x32_bf16 a[40:43],v[196:199],v[48:51],a[40:43]
v_add_f32 v251,v251,v69
v_cvt_pk_bf16_f32 v64,v64,v65
v_mfma_f32_16x16x32_bf16 a[56:59],v[200:203],v[16:19],a[56:59]
v_exp_f32 v70,v70
v_mfma_f32_16x16x32_bf16 a[56:59],v[204:207],v[48:51],a[56:59]
v_add_f32 v250,v250,v70
v_cvt_pk_bf16_f32 v65,v66,v67
v_mfma_f32_16x16x32_bf16 a[44:47],v[192:195],v[24:27],a[44:47]
v_exp_f32 v71,v71
v_mfma_f32_16x16x32_bf16 a[44:47],v[196:199],v[56:59],a[44:47]
buffer_load_dwordx4 v228,s[36:39],s58 offen offset:3072 lds
v_add_f32 v251,v251,v71
v_mfma_f32_16x16x32_bf16 a[60:63],v[200:203],v[24:27],a[60:63]
v_exp_f32 v76,v76
v_mfma_f32_16x16x32_bf16 a[60:63],v[204:207],v[56:59],a[60:63]
v_add_f32 v252,v252,v76
v_cvt_pk_bf16_f32 v66,v68,v69
v_mfma_f32_16x16x32_bf16 a[136:139],a[240:243],v[16:19],a[136:139]
v_exp_f32 v77,v77
v_mfma_f32_16x16x32_bf16 a[136:139],a[240:243],v[48:51],a[136:139]
v_add_f32 v253,v253,v77
s_waitcnt lgkmcnt(4)
v_mfma_f32_16x16x32_bf16 v[96:99],v[144:147],a[144:147],v[128:131]
v_exp_f32 v78,v78
v_mfma_f32_16x16x32_bf16 v[96:99],v[148:151],a[148:151],v[96:99]
ds_read_b128 v[176:179],v224 offset:45056
v_add_f32 v252,v252,v78
v_mfma_f32_16x16x32_bf16 v[96:99],v[152:155],a[152:155],v[96:99]
v_exp_f32 v79,v79
v_mfma_f32_16x16x32_bf16 v[96:99],v[156:159],a[156:159],v[96:99]
ds_read_b128 v[180:183],v224 offset:46080
v_add_f32 v253,v253,v79
v_mfma_f32_16x16x32_bf16 v[104:107],v[144:147],a[160:163],v[132:135]
v_exp_f32 v92,v92
v_mfma_f32_16x16x32_bf16 v[104:107],v[148:151],a[164:167],v[104:107]
ds_read_b128 v[184:187],v224 offset:47104
v_cvt_pk_bf16_f32 v67,v70,v71
v_mfma_f32_16x16x32_bf16 v[104:107],v[152:155],a[168:171],v[104:107]
v_add_f32 v124,0x73840000,v231
v_min_f32 v124,v124,v140
v_mfma_f32_16x16x32_bf16 v[104:107],v[156:159],a[172:175],v[104:107]
v_add_f32 v125,0x73780000,v231
v_min_f32 v125,v125,v141
ds_read_b128 v[188:191],v224 offset:48128
v_mfma_f32_16x16x32_bf16 v[120:123],v[144:147],a[192:195],v[120:123]
v_add_f32 v126,0x73680000,v231
v_min_f32 v126,v126,v142
v_mfma_f32_16x16x32_bf16 v[120:123],v[148:151],a[196:199],v[120:123]
v_add_f32 v127,0x73580000,v231
v_min_f32 v127,v127,v143
v_mfma_f32_16x16x32_bf16 v[120:123],v[152:155],a[200:203],v[120:123]
v_exp_f32 v93,v93
v_mfma_f32_16x16x32_bf16 v[120:123],v[156:159],a[204:207],v[120:123]
v_exp_f32 v96,v96
s_waitcnt lgkmcnt(4)
v_mfma_f32_16x16x32_bf16 a[64:67],v[160:163],v[0:3],a[64:67]
v_exp_f32 v94,v94
v_mfma_f32_16x16x32_bf16 a[64:67],v[164:167],v[32:35],a[64:67]
ds_read_b128 v[192:195],v226 offset:12288
v_add_f32 v250,v250,v96
v_mfma_f32_16x16x32_bf16 a[80:83],v[168:171],v[0:3],a[80:83]
v_exp_f32 v95,v95
v_mfma_f32_16x16x32_bf16 a[80:83],v[172:175],v[32:35],a[80:83]
ds_read_b128 v[196:199],v226 offset:13312
v_cvt_pk_bf16_f32 v72,v72,v73
v_mfma_f32_16x16x32_bf16 a[68:71],v[160:163],v[8:11],a[68:71]
v_exp_f32 v97,v97
v_mfma_f32_16x16x32_bf16 a[68:71],v[164:167],v[40:43],a[68:71]
ds_read_b128 v[200:203],v226 offset:14336
v_add_f32 v251,v251,v97
v_mfma_f32_16x16x32_bf16 a[84:87],v[168:171],v[8:11],a[84:87]
v_exp_f32 v98,v98
v_mfma_f32_16x16x32_bf16 a[84:87],v[172:175],v[40:43],a[84:87]
ds_read_b128 v[204:207],v226 offset:15360
v_add_f32 v250,v250,v98
v_mfma_f32_16x16x32_bf16 a[72:75],v[160:163],v[16:19],a[72:75]
v_exp_f32 v99,v99
v_mfma_f32_16x16x32_bf16 a[72:75],v[164:167],v[48:51],a[72:75]
v_add_f32 v251,v251,v99
v_cvt_pk_bf16_f32 v73,v74,v75
v_mfma_f32_16x16x32_bf16 a[88:91],v[168:171],v[16:19],a[88:91]
v_exp_f32 v104,v104
v_mfma_f32_16x16x32_bf16 a[88:91],v[172:175],v[48:51],a[88:91]
v_add_f32 v252,v252,v104
v_cvt_pk_bf16_f32 v74,v76,v77
v_mfma_f32_16x16x32_bf16 a[76:79],v[160:163],v[24:27],a[76:79]
v_exp_f32 v105,v105
v_mfma_f32_16x16x32_bf16 a[76:79],v[164:167],v[56:59],a[76:79]
v_add_f32 v253,v253,v105
v_cvt_pk_bf16_f32 v75,v78,v79
v_mfma_f32_16x16x32_bf16 a[92:95],v[168:171],v[24:27],a[92:95]
v_exp_f32 v106,v106
v_cvt_pk_bf16_f32 v104,v104,v105
v_mfma_f32_16x16x32_bf16 a[92:95],v[172:175],v[56:59],a[92:95]
s_waitcnt vmcnt(0) lgkmcnt(0)
s_barrier
v_mfma_f32_16x16x32_bf16 v[100:103],v[176:179],a[144:147],v[128:131]
s_add_u32 m0,s81,0x10000
s_add_u32 s59,s80,0x4000
v_mfma_f32_16x16x32_bf16 v[100:103],v[180:183],a[148:151],v[100:103]
ds_read_b128 v[144:147],v225 offset:0
buffer_load_dwordx4 v228,s[36:39],s59 offen offset:0 lds
v_mfma_f32_16x16x32_bf16 v[100:103],v[184:187],a[152:155],v[100:103]
v_exp_f32 v107,v107
v_mfma_f32_16x16x32_bf16 v[100:103],v[188:191],a[156:159],v[100:103]
ds_read_b128 v[148:151],v225 offset:1024
v_add_f32 v252,v252,v106
v_mfma_f32_16x16x32_bf16 v[108:111],v[176:179],a[160:163],v[132:135]
buffer_load_dwordx4 v228,s[36:39],s59 offen offset:1024 lds
v_add_f32 v253,v253,v107
v_mfma_f32_16x16x32_bf16 v[108:111],v[180:183],a[164:167],v[108:111]
ds_read_b128 v[152:155],v225 offset:2048
v_cvt_pk_bf16_f32 v88,v88,v89
v_cvt_pk_bf16_f32 v105,v106,v107
v_mfma_f32_16x16x32_bf16 v[108:111],v[184:187],a[168:171],v[108:111]
v_exp_f32 v120,v120
v_mfma_f32_16x16x32_bf16 v[108:111],v[188:191],a[172:175],v[108:111]
ds_read_b128 v[156:159],v225 offset:3072
buffer_load_dwordx4 v228,s[36:39],s59 offen offset:2048 lds
v_mfma_f32_16x16x32_bf16 v[124:127],v[176:179],a[192:195],v[124:127]
v_exp_f32 v121,v121
v_mfma_f32_16x16x32_bf16 v[124:127],v[180:183],a[196:199],v[124:127]
v_exp_f32 v100,v100
v_cvt_pk_bf16_f32 v120,v120,v121
v_mfma_f32_16x16x32_bf16 v[124:127],v[184:187],a[200:203],v[124:127]
buffer_load_dwordx4 v228,s[36:39],s59 offen offset:3072 lds
v_exp_f32 v101,v101
v_mfma_f32_16x16x32_bf16 v[124:127],v[188:191],a[204:207],v[124:127]
s_add_u32 m0,s81,0x14000
s_add_u32 s58,s80,0xc000
v_mfma_f32_16x16x32_bf16 a[96:99],v[192:195],v[0:3],a[96:99]
buffer_load_dwordx4 v228,s[36:39],s58 offen offset:0 lds
v_exp_f32 v102,v102
v_mfma_f32_16x16x32_bf16 a[96:99],v[196:199],v[32:35],a[96:99]
ds_read_b128 v[160:163],v227 offset:32768
v_exp_f32 v103,v103
v_mfma_f32_16x16x32_bf16 a[112:115],v[200:203],v[0:3],a[112:115]
v_exp_f32 v122,v122
v_mfma_f32_16x16x32_bf16 a[112:115],v[204:207],v[32:35],a[112:115]
ds_read_b128 v[164:167],v227 offset:33792
buffer_load_dwordx4 v228,s[36:39],s58 offen offset:1024 lds
v_mfma_f32_16x16x32_bf16 a[100:103],v[192:195],v[8:11],a[100:103]
v_exp_f32 v123,v123
v_mfma_f32_16x16x32_bf16 a[100:103],v[196:199],v[40:43],a[100:103]
ds_read_b128 v[168:171],v227 offset:34816
v_add_f32 v250,v250,v100
v_cvt_pk_bf16_f32 v121,v122,v123
v_mfma_f32_16x16x32_bf16 a[116:119],v[200:203],v[8:11],a[116:119]
buffer_load_dwordx4 v228,s[36:39],s58 offen offset:2048 lds
v_add_f32 v251,v251,v101
v_mfma_f32_16x16x32_bf16 a[116:119],v[204:207],v[40:43],a[116:119]
ds_read_b128 v[172:175],v227 offset:35840
v_add_f32 v250,v250,v102
v_mfma_f32_16x16x32_bf16 a[104:107],v[192:195],v[16:19],a[104:107]
v_exp_f32 v108,v108
v_mfma_f32_16x16x32_bf16 a[104:107],v[196:199],v[48:51],a[104:107]
buffer_load_dwordx4 v228,s[36:39],s58 offen offset:3072 lds
v_add_f32 v251,v251,v103
v_mfma_f32_16x16x32_bf16 a[120:123],v[200:203],v[16:19],a[120:123]
v_exp_f32 v109,v109
v_mfma_f32_16x16x32_bf16 a[120:123],v[204:207],v[48:51],a[120:123]
v_exp_f32 v110,v110
v_mfma_f32_16x16x32_bf16 a[108:111],v[192:195],v[24:27],a[108:111]
)"
R"(v_exp_f32 v111,v111
v_mfma_f32_16x16x32_bf16 a[108:111],v[196:199],v[56:59],a[108:111]
v_add_f32 v252,v252,v108
v_add_f32 v253,v253,v109
v_mfma_f32_16x16x32_bf16 a[124:127],v[200:203],v[24:27],a[124:127]
v_add_f32 v252,v252,v110
v_add_f32 v253,v253,v111
v_cvt_pk_bf16_f32 v106,v108,v109
v_mfma_f32_16x16x32_bf16 a[124:127],v[204:207],v[56:59],a[124:127]
v_cvt_pk_bf16_f32 v89,v90,v91
v_cvt_pk_bf16_f32 v96,v96,v97
v_cvt_pk_bf16_f32 v107,v110,v111
v_mfma_f32_16x16x32_bf16 a[140:143],a[240:243],v[24:27],a[140:143]
v_cvt_pk_bf16_f32 v90,v92,v93
v_cvt_pk_bf16_f32 v97,v98,v99
v_mfma_f32_16x16x32_bf16 a[140:143],a[240:243],v[56:59],a[140:143]
v_cvt_pk_bf16_f32 v91,v94,v95
v_cvt_pk_bf16_f32 v98,v100,v101
v_cvt_pk_bf16_f32 v99,v102,v103
s_add_u32 s80,s80,0x10000
s_mov_b32 s46,0x500000
s_mov_b32 s47,0x20000
s_lshl_b32 s58,%3,2
s_add_u32 s58,s58,s56
s_mul_i32 s59,s58,5120
v_min_u32 v243,39,v254
v_lshlrev_b32 v243,7,v243
buffer_load_dword v244,v243,s[44:47],s59 offen
s_cmp_lt_u32 s58,100
s_cselect_b64 s[48:49],s[92:93],s[94:95]
s_cselect_b32 s59,0,100
s_mov_b32 s50,0xc8000
s_mov_b32 s51,0x20000
s_sub_u32 s58,s58,s59
s_lshl_b32 s58,s58,13
v_lshlrev_b32 v243,7,v254
buffer_load_dword v244,v243,s[48:51],s58 offen
v_accvgpr_mov_b32 a176,a208
v_accvgpr_mov_b32 a177,a209
v_accvgpr_mov_b32 a178,a210
v_accvgpr_mov_b32 a179,a211
v_accvgpr_mov_b32 a180,a212
v_accvgpr_mov_b32 a181,a213
v_accvgpr_mov_b32 a182,a214
v_accvgpr_mov_b32 a183,a215
v_accvgpr_mov_b32 a184,a216
v_accvgpr_mov_b32 a185,a217
v_accvgpr_mov_b32 a186,a218
v_accvgpr_mov_b32 a187,a219
v_accvgpr_mov_b32 a188,a220
v_accvgpr_mov_b32 a189,a221
v_accvgpr_mov_b32 a190,a222
v_accvgpr_mov_b32 a191,a223
v_accvgpr_mov_b32 a192,a224
v_accvgpr_mov_b32 a193,a225
v_accvgpr_mov_b32 a194,a226
v_accvgpr_mov_b32 a195,a227
v_accvgpr_mov_b32 a196,a228
v_accvgpr_mov_b32 a197,a229
v_accvgpr_mov_b32 a198,a230
v_accvgpr_mov_b32 a199,a231
v_accvgpr_mov_b32 a200,a232
v_accvgpr_mov_b32 a201,a233
v_accvgpr_mov_b32 a202,a234
v_accvgpr_mov_b32 a203,a235
v_accvgpr_mov_b32 a204,a236
v_accvgpr_mov_b32 a205,a237
v_accvgpr_mov_b32 a206,a238
v_accvgpr_mov_b32 a207,a239
v_mov_b32 v136,v232
v_mov_b32 v137,v232
v_mov_b32 v138,v232
v_mov_b32 v139,v232
v_mov_b32 v140,v233
v_mov_b32 v141,v233
v_mov_b32 v142,v233
v_mov_b32 v143,v233
v_add_u32 v224,s83,v224
v_add_u32 v227,s83,v227
s_nop 0
s_add_u32 s62,s62,1
s_cmp_lg_u32 s62,3
s_cbranch_scc1 L_HAEM_%=
global_load_dword v237,v238,s[22:23] offset:128 sc1
L_HAEM_%=:
s_sub_u32 s87,16,s79
s_mov_b32 s84,1
s_cmp_eq_u32 s87,0
s_cbranch_scc1 L_OMB_%=
s_waitcnt lgkmcnt(4)
v_mfma_f32_16x16x32_bf16 v[0:3],v[144:147],a[144:147],v[128:131]
v_exp_f32 v124,v124
v_mfma_f32_16x16x32_bf16 v[0:3],v[148:151],a[148:151],v[0:3]
v_exp_f32 v125,v125
v_mfma_f32_16x16x32_bf16 v[0:3],v[152:155],a[152:155],v[0:3]
v_exp_f32 v126,v126
v_mfma_f32_16x16x32_bf16 v[0:3],v[156:159],a[156:159],v[0:3]
v_exp_f32 v127,v127
v_mfma_f32_16x16x32_bf16 v[8:11],v[144:147],a[160:163],v[132:135]
v_cvt_pk_bf16_f32 v114,v116,v117
v_cvt_pk_bf16_f32 v115,v118,v119
v_mfma_f32_16x16x32_bf16 v[8:11],v[148:151],a[164:167],v[8:11]
v_cvt_pk_bf16_f32 v122,v124,v125
v_cvt_pk_bf16_f32 v123,v126,v127
v_mfma_f32_16x16x32_bf16 v[8:11],v[152:155],a[168:171],v[8:11]
ds_read_b128 v[176:179],v224 offset:4096
ds_read_b128 v[180:183],v224 offset:5120
v_mfma_f32_16x16x32_bf16 v[8:11],v[156:159],a[172:175],v[8:11]
ds_read_b128 v[184:187],v224 offset:6144
ds_read_b128 v[188:191],v224 offset:7168
v_mfma_f32_16x16x32_bf16 v[16:19],v[144:147],a[176:179],v[136:139]
v_mfma_f32_16x16x32_bf16 v[16:19],v[148:151],a[180:183],v[16:19]
s_mov_b32 m0,s82
v_mfma_f32_16x16x32_bf16 v[16:19],v[152:155],a[184:187],v[16:19]
buffer_load_dwordx4 v228,s[36:39],s80 offen offset:0 lds
v_mfma_f32_16x16x32_bf16 v[16:19],v[156:159],a[188:191],v[16:19]
v_exp_f32 v0,v0
v_mfma_f32_16x16x32_bf16 v[24:27],v[144:147],a[192:195],v[140:143]
v_add_f32 v250,v250,v0
v_mfma_f32_16x16x32_bf16 v[24:27],v[148:151],a[196:199],v[24:27]
v_exp_f32 v1,v1
v_mfma_f32_16x16x32_bf16 v[24:27],v[152:155],a[200:203],v[24:27]
v_add_f32 v251,v251,v1
v_mfma_f32_16x16x32_bf16 v[24:27],v[156:159],a[204:207],v[24:27]
v_cvt_pk_bf16_f32 v0,v0,v1
s_waitcnt lgkmcnt(4)
v_mfma_f32_16x16x32_bf16 a[0:3],v[160:163],v[64:67],a[0:3]
buffer_load_dwordx4 v228,s[36:39],s80 offen offset:1024 lds
v_mfma_f32_16x16x32_bf16 a[0:3],v[164:167],v[96:99],a[0:3]
ds_read_b128 v[192:195],v226 offset:36864
v_mfma_f32_16x16x32_bf16 a[16:19],v[168:171],v[64:67],a[16:19]
v_exp_f32 v2,v2
v_mfma_f32_16x16x32_bf16 a[16:19],v[172:175],v[96:99],a[16:19]
ds_read_b128 v[196:199],v226 offset:37888
v_add_f32 v250,v250,v2
v_mfma_f32_16x16x32_bf16 a[4:7],v[160:163],v[72:75],a[4:7]
v_exp_f32 v3,v3
v_mfma_f32_16x16x32_bf16 a[4:7],v[164:167],v[104:107],a[4:7]
ds_read_b128 v[200:203],v226 offset:38912
v_add_f32 v251,v251,v3
v_mfma_f32_16x16x32_bf16 a[20:23],v[168:171],v[72:75],a[20:23]
buffer_load_dwordx4 v228,s[36:39],s80 offen offset:2048 lds
v_cvt_pk_bf16_f32 v1,v2,v3
v_mfma_f32_16x16x32_bf16 a[20:23],v[172:175],v[104:107],a[20:23]
ds_read_b128 v[204:207],v226 offset:39936
v_mfma_f32_16x16x32_bf16 a[12:15],v[160:163],v[88:91],a[12:15]
v_exp_f32 v8,v8
v_mfma_f32_16x16x32_bf16 a[12:15],v[164:167],v[120:123],a[12:15]
v_add_f32 v252,v252,v8
v_mfma_f32_16x16x32_bf16 a[28:31],v[168:171],v[88:91],a[28:31]
v_exp_f32 v9,v9
v_mfma_f32_16x16x32_bf16 a[28:31],v[172:175],v[120:123],a[28:31]
v_add_f32 v253,v253,v9
s_waitcnt lgkmcnt(4)
v_mfma_f32_16x16x32_bf16 v[4:7],v[176:179],a[144:147],v[128:131]
buffer_load_dwordx4 v228,s[36:39],s80 offen offset:3072 lds
v_cvt_pk_bf16_f32 v8,v8,v9
v_mfma_f32_16x16x32_bf16 v[4:7],v[180:183],a[148:151],v[4:7]
ds_read_b128 v[144:147],v224 offset:8192
v_mfma_f32_16x16x32_bf16 v[4:7],v[184:187],a[152:155],v[4:7]
v_exp_f32 v10,v10
v_mfma_f32_16x16x32_bf16 v[4:7],v[188:191],a[156:159],v[4:7]
ds_read_b128 v[148:151],v224 offset:9216
v_add_f32 v252,v252,v10
v_mfma_f32_16x16x32_bf16 v[12:15],v[176:179],a[160:163],v[132:135]
s_add_u32 m0,s82,0x4000
s_add_u32 s58,s80,0x8000
v_mfma_f32_16x16x32_bf16 v[12:15],v[180:183],a[164:167],v[12:15]
ds_read_b128 v[152:155],v224 offset:10240
buffer_load_dwordx4 v228,s[36:39],s58 offen offset:0 lds
v_mfma_f32_16x16x32_bf16 v[12:15],v[184:187],a[168:171],v[12:15]
v_exp_f32 v11,v11
v_mfma_f32_16x16x32_bf16 v[12:15],v[188:191],a[172:175],v[12:15]
ds_read_b128 v[156:159],v224 offset:11264
v_add_f32 v253,v253,v11
v_mfma_f32_16x16x32_bf16 v[20:23],v[176:179],a[176:179],v[136:139]
v_exp_f32 v16,v16
v_mfma_f32_16x16x32_bf16 v[20:23],v[180:183],a[180:183],v[20:23]
v_exp_f32 v4,v4
v_mfma_f32_16x16x32_bf16 v[20:23],v[184:187],a[184:187],v[20:23]
v_exp_f32 v17,v17
v_mfma_f32_16x16x32_bf16 v[20:23],v[188:191],a[188:191],v[20:23]
buffer_load_dwordx4 v228,s[36:39],s58 offen offset:1024 lds
v_add_f32 v250,v250,v4
v_mfma_f32_16x16x32_bf16 v[28:31],v[176:179],a[192:195],v[140:143]
v_exp_f32 v18,v18
v_mfma_f32_16x16x32_bf16 v[28:31],v[180:183],a[196:199],v[28:31]
v_exp_f32 v5,v5
v_mfma_f32_16x16x32_bf16 v[28:31],v[184:187],a[200:203],v[28:31]
v_exp_f32 v19,v19
v_mfma_f32_16x16x32_bf16 v[28:31],v[188:191],a[204:207],v[28:31]
v_exp_f32 v6,v6
s_waitcnt lgkmcnt(4)
v_mfma_f32_16x16x32_bf16 a[32:35],v[192:195],v[64:67],a[32:35]
v_exp_f32 v24,v24
v_mfma_f32_16x16x32_bf16 a[32:35],v[196:199],v[96:99],a[32:35]
ds_read_b128 v[160:163],v226 offset:40960
buffer_load_dwordx4 v228,s[36:39],s58 offen offset:2048 lds
v_mfma_f32_16x16x32_bf16 a[48:51],v[200:203],v[64:67],a[48:51]
v_exp_f32 v25,v25
v_mfma_f32_16x16x32_bf16 a[48:51],v[204:207],v[96:99],a[48:51]
ds_read_b128 v[164:167],v226 offset:41984
v_exp_f32 v7,v7
v_mfma_f32_16x16x32_bf16 a[36:39],v[192:195],v[72:75],a[36:39]
v_exp_f32 v26,v26
v_cvt_pk_bf16_f32 v24,v24,v25
v_mfma_f32_16x16x32_bf16 a[36:39],v[196:199],v[104:107],a[36:39]
ds_read_b128 v[168:171],v226 offset:43008
v_add_f32 v251,v251,v5
v_mfma_f32_16x16x32_bf16 a[52:55],v[200:203],v[72:75],a[52:55]
v_exp_f32 v27,v27
v_mfma_f32_16x16x32_bf16 a[52:55],v[204:207],v[104:107],a[52:55]
ds_read_b128 v[172:175],v226 offset:44032
buffer_load_dwordx4 v228,s[36:39],s58 offen offset:3072 lds
v_cvt_pk_bf16_f32 v25,v26,v27
v_mfma_f32_16x16x32_bf16 a[44:47],v[192:195],v[88:91],a[44:47]
v_exp_f32 v12,v12
v_mfma_f32_16x16x32_bf16 a[44:47],v[196:199],v[120:123],a[44:47]
v_add_f32 v250,v250,v6
v_add_f32 v251,v251,v7
v_mfma_f32_16x16x32_bf16 a[60:63],v[200:203],v[88:91],a[60:63]
v_exp_f32 v13,v13
v_mfma_f32_16x16x32_bf16 a[60:63],v[204:207],v[120:123],a[60:63]
v_add_f32 v252,v252,v12
s_waitcnt lgkmcnt(4)
v_mfma_f32_16x16x32_bf16 v[32:35],v[144:147],a[144:147],v[128:131]
v_exp_f32 v14,v14
v_mfma_f32_16x16x32_bf16 v[32:35],v[148:151],a[148:151],v[32:35]
ds_read_b128 v[176:179],v224 offset:12288
v_add_f32 v253,v253,v13
v_mfma_f32_16x16x32_bf16 v[32:35],v[152:155],a[152:155],v[32:35]
v_exp_f32 v15,v15
v_mfma_f32_16x16x32_bf16 v[32:35],v[156:159],a[156:159],v[32:35]
ds_read_b128 v[180:183],v224 offset:13312
v_add_f32 v252,v252,v14
v_mfma_f32_16x16x32_bf16 v[40:43],v[144:147],a[160:163],v[132:135]
v_exp_f32 v20,v20
v_mfma_f32_16x16x32_bf16 v[40:43],v[148:151],a[164:167],v[40:43]
ds_read_b128 v[184:187],v224 offset:14336
v_add_f32 v253,v253,v15
v_mfma_f32_16x16x32_bf16 v[40:43],v[152:155],a[168:171],v[40:43]
v_exp_f32 v21,v21
v_mfma_f32_16x16x32_bf16 v[40:43],v[156:159],a[172:175],v[40:43]
ds_read_b128 v[188:191],v224 offset:15360
v_cvt_pk_bf16_f32 v2,v4,v5
v_mfma_f32_16x16x32_bf16 v[48:51],v[144:147],a[176:179],v[136:139]
v_exp_f32 v22,v22
v_mfma_f32_16x16x32_bf16 v[48:51],v[148:151],a[180:183],v[48:51]
v_exp_f32 v32,v32
v_mfma_f32_16x16x32_bf16 v[48:51],v[152:155],a[184:187],v[48:51]
v_exp_f32 v23,v23
v_mfma_f32_16x16x32_bf16 v[48:51],v[156:159],a[188:191],v[48:51]
v_exp_f32 v33,v33
v_mfma_f32_16x16x32_bf16 v[56:59],v[144:147],a[192:195],v[140:143]
v_exp_f32 v28,v28
v_mfma_f32_16x16x32_bf16 v[56:59],v[148:151],a[196:199],v[56:59]
v_exp_f32 v34,v34
v_mfma_f32_16x16x32_bf16 v[56:59],v[152:155],a[200:203],v[56:59]
v_exp_f32 v29,v29
v_mfma_f32_16x16x32_bf16 v[56:59],v[156:159],a[204:207],v[56:59]
v_add_f32 v250,v250,v32
v_cvt_pk_bf16_f32 v26,v28,v29
s_waitcnt lgkmcnt(4)
v_mfma_f32_16x16x32_bf16 a[64:67],v[160:163],v[64:67],a[64:67]
v_exp_f32 v30,v30
v_mfma_f32_16x16x32_bf16 a[64:67],v[164:167],v[96:99],a[64:67]
ds_read_b128 v[192:195],v226 offset:45056
v_add_f32 v251,v251,v33
v_mfma_f32_16x16x32_bf16 a[80:83],v[168:171],v[64:67],a[80:83]
v_exp_f32 v31,v31
v_cvt_pk_bf16_f32 v32,v32,v33
v_mfma_f32_16x16x32_bf16 a[80:83],v[172:175],v[96:99],a[80:83]
ds_read_b128 v[196:199],v226 offset:46080
v_add_f32 v250,v250,v34
v_cvt_pk_bf16_f32 v27,v30,v31
v_mfma_f32_16x16x32_bf16 a[68:71],v[160:163],v[72:75],a[68:71]
v_exp_f32 v35,v35
v_mfma_f32_16x16x32_bf16 a[68:71],v[164:167],v[104:107],a[68:71]
ds_read_b128 v[200:203],v226 offset:47104
v_add_f32 v251,v251,v35
v_mfma_f32_16x16x32_bf16 a[84:87],v[168:171],v[72:75],a[84:87]
v_exp_f32 v40,v40
v_cvt_pk_bf16_f32 v33,v34,v35
v_mfma_f32_16x16x32_bf16 a[84:87],v[172:175],v[104:107],a[84:87]
ds_read_b128 v[204:207],v226 offset:48128
v_add_f32 v252,v252,v40
v_mfma_f32_16x16x32_bf16 a[76:79],v[160:163],v[88:91],a[76:79]
v_exp_f32 v41,v41
v_mfma_f32_16x16x32_bf16 a[76:79],v[164:167],v[120:123],a[76:79]
v_add_f32 v253,v253,v41
v_cvt_pk_bf16_f32 v3,v6,v7
v_mfma_f32_16x16x32_bf16 a[92:95],v[168:171],v[88:91],a[92:95]
v_exp_f32 v42,v42
v_cvt_pk_bf16_f32 v40,v40,v41
v_mfma_f32_16x16x32_bf16 a[92:95],v[172:175],v[120:123],a[92:95]
s_waitcnt vmcnt(0) lgkmcnt(0)
s_barrier
v_mfma_f32_16x16x32_bf16 v[36:39],v[176:179],a[144:147],v[128:131]
s_add_u32 m0,s82,0x10000
s_add_u32 s59,s80,0x4000
v_mfma_f32_16x16x32_bf16 v[36:39],v[180:183],a[148:151],v[36:39]
ds_read_b128 v[144:147],v225 offset:32768
buffer_load_dwordx4 v228,s[36:39],s59 offen offset:0 lds
v_mfma_f32_16x16x32_bf16 v[36:39],v[184:187],a[152:155],v[36:39]
v_exp_f32 v43,v43
v_mfma_f32_16x16x32_bf16 v[36:39],v[188:191],a[156:159],v[36:39]
ds_read_b128 v[148:151],v225 offset:33792
v_add_f32 v252,v252,v42
v_mfma_f32_16x16x32_bf16 v[44:47],v[176:179],a[160:163],v[132:135]
buffer_load_dwordx4 v228,s[36:39],s59 offen offset:1024 lds
v_add_f32 v253,v253,v43
v_mfma_f32_16x16x32_bf16 v[44:47],v[180:183],a[164:167],v[44:47]
ds_read_b128 v[152:155],v225 offset:34816
v_cvt_pk_bf16_f32 v9,v10,v11
v_cvt_pk_bf16_f32 v41,v42,v43
v_mfma_f32_16x16x32_bf16 v[44:47],v[184:187],a[168:171],v[44:47]
v_exp_f32 v48,v48
v_mfma_f32_16x16x32_bf16 v[44:47],v[188:191],a[172:175],v[44:47]
ds_read_b128 v[156:159],v225 offset:35840
buffer_load_dwordx4 v228,s[36:39],s59 offen offset:2048 lds
v_mfma_f32_16x16x32_bf16 v[52:55],v[176:179],a[176:179],v[136:139]
v_exp_f32 v49,v49
v_mfma_f32_16x16x32_bf16 v[52:55],v[180:183],a[180:183],v[52:55]
v_exp_f32 v36,v36
v_cvt_pk_bf16_f32 v48,v48,v49
v_mfma_f32_16x16x32_bf16 v[52:55],v[184:187],a[184:187],v[52:55]
buffer_load_dwordx4 v228,s[36:39],s59 offen offset:3072 lds
v_add_f32 v250,v250,v36
v_mfma_f32_16x16x32_bf16 v[52:55],v[188:191],a[188:191],v[52:55]
s_add_u32 m0,s82,0x14000
s_add_u32 s58,s80,0xc000
v_mfma_f32_16x16x32_bf16 v[60:63],v[176:179],a[192:195],v[140:143]
buffer_load_dwordx4 v228,s[36:39],s58 offen offset:0 lds
v_cvt_pk_bf16_f32 v10,v12,v13
v_mfma_f32_16x16x32_bf16 v[60:63],v[180:183],a[196:199],v[60:63]
v_exp_f32 v50,v50
v_mfma_f32_16x16x32_bf16 v[60:63],v[184:187],a[200:203],v[60:63]
v_exp_f32 v37,v37
v_mfma_f32_16x16x32_bf16 v[60:63],v[188:191],a[204:207],v[60:63]
buffer_load_dwordx4 v228,s[36:39],s58 offen offset:1024 lds
v_exp_f32 v38,v38
v_mfma_f32_16x16x32_bf16 a[96:99],v[192:195],v[64:67],a[96:99]
v_exp_f32 v51,v51
v_mfma_f32_16x16x32_bf16 a[96:99],v[196:199],v[96:99],a[96:99]
ds_read_b128 v[160:163],v227 offset:0
v_exp_f32 v39,v39
v_cvt_pk_bf16_f32 v49,v50,v51
v_mfma_f32_16x16x32_bf16 a[112:115],v[200:203],v[64:67],a[112:115]
buffer_load_dwordx4 v228,s[36:39],s58 offen offset:2048 lds
v_exp_f32 v44,v44
v_mfma_f32_16x16x32_bf16 a[112:115],v[204:207],v[96:99],a[112:115]
ds_read_b128 v[164:167],v227 offset:1024
v_exp_f32 v45,v45
v_mfma_f32_16x16x32_bf16 a[100:103],v[192:195],v[72:75],a[100:103]
v_exp_f32 v56,v56
v_add_f32 v251,v251,v39
v_mfma_f32_16x16x32_bf16 a[100:103],v[196:199],v[104:107],a[100:103]
ds_read_b128 v[168:171],v227 offset:2048
buffer_load_dwordx4 v228,s[36:39],s58 offen offset:3072 lds
v_add_f32 v252,v252,v44
v_mfma_f32_16x16x32_bf16 a[116:119],v[200:203],v[72:75],a[116:119]
v_exp_f32 v57,v57
v_exp_f32 v53,v53
v_mfma_f32_16x16x32_bf16 a[116:119],v[204:207],v[104:107],a[116:119]
ds_read_b128 v[172:175],v227 offset:3072
v_exp_f32 v52,v52
v_cvt_pk_bf16_f32 v56,v56,v57
v_mfma_f32_16x16x32_bf16 a[108:111],v[192:195],v[88:91],a[108:111]
v_exp_f32 v58,v58
v_exp_f32 v54,v54
v_mfma_f32_16x16x32_bf16 a[108:111],v[196:199],v[120:123],a[108:111]
v_exp_f32 v59,v59
v_exp_f32 v55,v55
v_mfma_f32_16x16x32_bf16 a[124:127],v[200:203],v[88:91],a[124:127]
v_exp_f32 v46,v46
v_add_f32 v253,v253,v45
v_cvt_pk_bf16_f32 v57,v58,v59
v_mfma_f32_16x16x32_bf16 a[124:127],v[204:207],v[120:123],a[124:127]
v_exp_f32 v47,v47
v_add_f32 v252,v252,v46
v_cvt_pk_bf16_f32 v42,v44,v45
v_mfma_f32_16x16x32_bf16 a[140:143],a[240:243],v[88:91],a[140:143]
v_add_f32 v251,v251,v37
v_add_f32 v250,v250,v38
v_add_f32 v253,v253,v47
v_mfma_f32_16x16x32_bf16 a[140:143],a[240:243],v[120:123],a[140:143]
v_cvt_pk_bf16_f32 v11,v14,v15
v_cvt_pk_bf16_f32 v16,v16,v17
v_cvt_pk_bf16_f32 v17,v18,v19
v_cvt_pk_bf16_f32 v18,v20,v21
v_cvt_pk_bf16_f32 v19,v22,v23
v_cvt_pk_bf16_f32 v34,v36,v37
v_cvt_pk_bf16_f32 v35,v38,v39
v_cvt_pk_bf16_f32 v43,v46,v47
s_add_u32 s80,s80,0x10000
s_add_u32 s62,s62,1
s_cmp_lg_u32 s62,4
s_cbranch_scc1 L_HBOF_%=
s_mov_b32 s59,0
L_HPOF_%=:
)"
R"(v_readfirstlane_b32 s58,v237
s_nop 3
s_cmp_ge_u32 s58,10
s_cbranch_scc1 L_HDOF_%=
s_sleep 2
global_load_dword v237,v238,s[22:23] offset:128 sc1
s_add_u32 s59,s59,1
s_cmp_lt_u32 s59,0x20000
s_waitcnt vmcnt(0)
s_cbranch_scc1 L_HPOF_%=
L_HDOF_%=:
buffer_inv sc1
L_HBOF_%=:
s_branch L_FIN_%=
L_O2_%=:
s_waitcnt lgkmcnt(4)
v_mfma_f32_16x16x32_bf16 v[0:3],v[144:147],a[144:147],v[128:131]
v_exp_f32 v124,v124
v_mfma_f32_16x16x32_bf16 v[0:3],v[148:151],a[148:151],v[0:3]
v_exp_f32 v125,v125
v_mfma_f32_16x16x32_bf16 v[0:3],v[152:155],a[152:155],v[0:3]
v_exp_f32 v126,v126
v_mfma_f32_16x16x32_bf16 v[0:3],v[156:159],a[156:159],v[0:3]
v_exp_f32 v127,v127
v_mfma_f32_16x16x32_bf16 v[8:11],v[144:147],a[160:163],v[132:135]
v_cvt_pk_bf16_f32 v114,v116,v117
v_cvt_pk_bf16_f32 v115,v118,v119
v_mfma_f32_16x16x32_bf16 v[8:11],v[148:151],a[164:167],v[8:11]
v_cvt_pk_bf16_f32 v122,v124,v125
v_cvt_pk_bf16_f32 v123,v126,v127
v_mfma_f32_16x16x32_bf16 v[8:11],v[152:155],a[168:171],v[8:11]
ds_read_b128 v[176:179],v224 offset:4096
ds_read_b128 v[180:183],v224 offset:5120
v_mfma_f32_16x16x32_bf16 v[8:11],v[156:159],a[172:175],v[8:11]
ds_read_b128 v[184:187],v224 offset:6144
ds_read_b128 v[188:191],v224 offset:7168
v_mfma_f32_16x16x32_bf16 v[16:19],v[144:147],a[176:179],v[136:139]
v_mfma_f32_16x16x32_bf16 v[16:19],v[148:151],a[180:183],v[16:19]
s_mov_b32 m0,s82
v_mfma_f32_16x16x32_bf16 v[16:19],v[152:155],a[184:187],v[16:19]
buffer_load_dwordx4 v228,s[36:39],s80 offen offset:0 lds
v_mfma_f32_16x16x32_bf16 v[16:19],v[156:159],a[188:191],v[16:19]
v_exp_f32 v0,v0
v_mfma_f32_16x16x32_bf16 v[24:27],v[144:147],a[192:195],v[140:143]
v_add_f32 v250,v250,v0
v_mfma_f32_16x16x32_bf16 v[24:27],v[148:151],a[196:199],v[24:27]
v_exp_f32 v1,v1
v_mfma_f32_16x16x32_bf16 v[24:27],v[152:155],a[200:203],v[24:27]
v_add_f32 v251,v251,v1
v_mfma_f32_16x16x32_bf16 v[24:27],v[156:159],a[204:207],v[24:27]
v_cvt_pk_bf16_f32 v0,v0,v1
s_waitcnt lgkmcnt(4)
v_mfma_f32_16x16x32_bf16 a[0:3],v[160:163],v[64:67],a[0:3]
buffer_load_dwordx4 v228,s[36:39],s80 offen offset:1024 lds
v_mfma_f32_16x16x32_bf16 a[0:3],v[164:167],v[96:99],a[0:3]
ds_read_b128 v[192:195],v226 offset:36864
v_mfma_f32_16x16x32_bf16 a[16:19],v[168:171],v[64:67],a[16:19]
v_exp_f32 v2,v2
v_mfma_f32_16x16x32_bf16 a[16:19],v[172:175],v[96:99],a[16:19]
ds_read_b128 v[196:199],v226 offset:37888
v_add_f32 v250,v250,v2
v_mfma_f32_16x16x32_bf16 a[4:7],v[160:163],v[72:75],a[4:7]
v_exp_f32 v3,v3
v_mfma_f32_16x16x32_bf16 a[4:7],v[164:167],v[104:107],a[4:7]
ds_read_b128 v[200:203],v226 offset:38912
v_add_f32 v251,v251,v3
v_mfma_f32_16x16x32_bf16 a[20:23],v[168:171],v[72:75],a[20:23]
buffer_load_dwordx4 v228,s[36:39],s80 offen offset:2048 lds
v_cvt_pk_bf16_f32 v1,v2,v3
v_mfma_f32_16x16x32_bf16 a[20:23],v[172:175],v[104:107],a[20:23]
ds_read_b128 v[204:207],v226 offset:39936
v_mfma_f32_16x16x32_bf16 a[8:11],v[160:163],v[80:83],a[8:11]
v_exp_f32 v8,v8
v_mfma_f32_16x16x32_bf16 a[8:11],v[164:167],v[112:115],a[8:11]
v_add_f32 v252,v252,v8
v_mfma_f32_16x16x32_bf16 a[24:27],v[168:171],v[80:83],a[24:27]
v_exp_f32 v9,v9
v_mfma_f32_16x16x32_bf16 a[24:27],v[172:175],v[112:115],a[24:27]
v_add_f32 v253,v253,v9
v_mfma_f32_16x16x32_bf16 a[12:15],v[160:163],v[88:91],a[12:15]
buffer_load_dwordx4 v228,s[36:39],s80 offen offset:3072 lds
v_cvt_pk_bf16_f32 v8,v8,v9
v_mfma_f32_16x16x32_bf16 a[12:15],v[164:167],v[120:123],a[12:15]
v_exp_f32 v10,v10
v_mfma_f32_16x16x32_bf16 a[28:31],v[168:171],v[88:91],a[28:31]
v_add_f32 v252,v252,v10
v_mfma_f32_16x16x32_bf16 a[28:31],v[172:175],v[120:123],a[28:31]
s_waitcnt lgkmcnt(4)
v_mfma_f32_16x16x32_bf16 v[4:7],v[176:179],a[144:147],v[128:131]
s_add_u32 m0,s82,0x4000
s_add_u32 s58,s80,0x8000
v_mfma_f32_16x16x32_bf16 v[4:7],v[180:183],a[148:151],v[4:7]
ds_read_b128 v[144:147],v224 offset:8192
buffer_load_dwordx4 v228,s[36:39],s58 offen offset:0 lds
v_mfma_f32_16x16x32_bf16 v[4:7],v[184:187],a[152:155],v[4:7]
v_exp_f32 v11,v11
v_mfma_f32_16x16x32_bf16 v[4:7],v[188:191],a[156:159],v[4:7]
ds_read_b128 v[148:151],v224 offset:9216
v_add_f32 v253,v253,v11
v_mfma_f32_16x16x32_bf16 v[12:15],v[176:179],a[160:163],v[132:135]
v_exp_f32 v16,v16
v_mfma_f32_16x16x32_bf16 v[12:15],v[180:183],a[164:167],v[12:15]
ds_read_b128 v[152:155],v224 offset:10240
v_cvt_pk_bf16_f32 v9,v10,v11
v_mfma_f32_16x16x32_bf16 v[12:15],v[184:187],a[168:171],v[12:15]
v_exp_f32 v17,v17
v_mfma_f32_16x16x32_bf16 v[12:15],v[188:191],a[172:175],v[12:15]
ds_read_b128 v[156:159],v224 offset:11264
buffer_load_dwordx4 v228,s[36:39],s58 offen offset:1024 lds
v_mfma_f32_16x16x32_bf16 v[20:23],v[176:179],a[176:179],v[136:139]
v_exp_f32 v18,v18
v_mfma_f32_16x16x32_bf16 v[20:23],v[180:183],a[180:183],v[20:23]
v_exp_f32 v4,v4
v_mfma_f32_16x16x32_bf16 v[20:23],v[184:187],a[184:187],v[20:23]
v_exp_f32 v19,v19
v_mfma_f32_16x16x32_bf16 v[20:23],v[188:191],a[188:191],v[20:23]
v_exp_f32 v5,v5
v_mfma_f32_16x16x32_bf16 v[28:31],v[176:179],a[192:195],v[140:143]
v_exp_f32 v24,v24
v_mfma_f32_16x16x32_bf16 v[28:31],v[180:183],a[196:199],v[28:31]
buffer_load_dwordx4 v228,s[36:39],s58 offen offset:2048 lds
v_exp_f32 v6,v6
v_mfma_f32_16x16x32_bf16 v[28:31],v[184:187],a[200:203],v[28:31]
v_exp_f32 v25,v25
v_mfma_f32_16x16x32_bf16 v[28:31],v[188:191],a[204:207],v[28:31]
v_add_f32 v250,v250,v4
s_waitcnt lgkmcnt(4)
v_mfma_f32_16x16x32_bf16 a[32:35],v[192:195],v[64:67],a[32:35]
v_exp_f32 v26,v26
v_mfma_f32_16x16x32_bf16 a[32:35],v[196:199],v[96:99],a[32:35]
ds_read_b128 v[160:163],v226 offset:40960
v_add_f32 v251,v251,v5
v_mfma_f32_16x16x32_bf16 a[48:51],v[200:203],v[64:67],a[48:51]
v_exp_f32 v27,v27
v_mfma_f32_16x16x32_bf16 a[48:51],v[204:207],v[96:99],a[48:51]
ds_read_b128 v[164:167],v226 offset:41984
buffer_load_dwordx4 v228,s[36:39],s58 offen offset:3072 lds
v_mfma_f32_16x16x32_bf16 a[36:39],v[192:195],v[72:75],a[36:39]
v_exp_f32 v7,v7
v_mfma_f32_16x16x32_bf16 a[36:39],v[196:199],v[104:107],a[36:39]
ds_read_b128 v[168:171],v226 offset:43008
v_add_f32 v250,v250,v6
v_mfma_f32_16x16x32_bf16 a[52:55],v[200:203],v[72:75],a[52:55]
v_exp_f32 v12,v12
v_mfma_f32_16x16x32_bf16 a[52:55],v[204:207],v[104:107],a[52:55]
ds_read_b128 v[172:175],v226 offset:44032
v_add_f32 v251,v251,v7
v_mfma_f32_16x16x32_bf16 a[40:43],v[192:195],v[80:83],a[40:43]
v_exp_f32 v13,v13
v_mfma_f32_16x16x32_bf16 a[40:43],v[196:199],v[112:115],a[40:43]
v_add_f32 v252,v252,v12
v_add_f32 v253,v253,v13
v_mfma_f32_16x16x32_bf16 a[56:59],v[200:203],v[80:83],a[56:59]
v_exp_f32 v14,v14
v_mfma_f32_16x16x32_bf16 a[56:59],v[204:207],v[112:115],a[56:59]
v_add_f32 v252,v252,v14
v_cvt_pk_bf16_f32 v2,v4,v5
v_mfma_f32_16x16x32_bf16 a[44:47],v[192:195],v[88:91],a[44:47]
v_exp_f32 v15,v15
v_mfma_f32_16x16x32_bf16 a[44:47],v[196:199],v[120:123],a[44:47]
v_add_f32 v253,v253,v15
v_cvt_pk_bf16_f32 v3,v6,v7
v_mfma_f32_16x16x32_bf16 a[60:63],v[200:203],v[88:91],a[60:63]
v_exp_f32 v20,v20
v_mfma_f32_16x16x32_bf16 a[60:63],v[204:207],v[120:123],a[60:63]
v_cvt_pk_bf16_f32 v10,v12,v13
v_cvt_pk_bf16_f32 v16,v16,v17
v_mfma_f32_16x16x32_bf16 a[136:139],a[240:243],v[80:83],a[136:139]
v_exp_f32 v21,v21
v_mfma_f32_16x16x32_bf16 a[136:139],a[240:243],v[112:115],a[136:139]
v_cvt_pk_bf16_f32 v11,v14,v15
s_waitcnt lgkmcnt(4)
v_mfma_f32_16x16x32_bf16 v[32:35],v[144:147],a[144:147],v[128:131]
v_exp_f32 v22,v22
v_mfma_f32_16x16x32_bf16 v[32:35],v[148:151],a[148:151],v[32:35]
ds_read_b128 v[176:179],v224 offset:12288
v_cvt_pk_bf16_f32 v17,v18,v19
v_mfma_f32_16x16x32_bf16 v[32:35],v[152:155],a[152:155],v[32:35]
v_exp_f32 v23,v23
v_mfma_f32_16x16x32_bf16 v[32:35],v[156:159],a[156:159],v[32:35]
ds_read_b128 v[180:183],v224 offset:13312
v_cvt_pk_bf16_f32 v18,v20,v21
v_mfma_f32_16x16x32_bf16 v[40:43],v[144:147],a[160:163],v[132:135]
v_exp_f32 v28,v28
v_mfma_f32_16x16x32_bf16 v[40:43],v[148:151],a[164:167],v[40:43]
ds_read_b128 v[184:187],v224 offset:14336
v_cvt_pk_bf16_f32 v19,v22,v23
v_mfma_f32_16x16x32_bf16 v[40:43],v[152:155],a[168:171],v[40:43]
v_exp_f32 v29,v29
v_mfma_f32_16x16x32_bf16 v[40:43],v[156:159],a[172:175],v[40:43]
ds_read_b128 v[188:191],v224 offset:15360
v_cvt_pk_bf16_f32 v24,v24,v25
v_mfma_f32_16x16x32_bf16 v[48:51],v[144:147],a[176:179],v[136:139]
v_exp_f32 v30,v30
v_mfma_f32_16x16x32_bf16 v[48:51],v[148:151],a[180:183],v[48:51]
v_exp_f32 v32,v32
v_mfma_f32_16x16x32_bf16 v[48:51],v[152:155],a[184:187],v[48:51]
v_exp_f32 v31,v31
v_mfma_f32_16x16x32_bf16 v[48:51],v[156:159],a[188:191],v[48:51]
v_exp_f32 v33,v33
v_mfma_f32_16x16x32_bf16 v[56:59],v[144:147],a[192:195],v[140:143]
v_add_f32 v250,v250,v32
v_add_f32 v251,v251,v33
v_mfma_f32_16x16x32_bf16 v[56:59],v[148:151],a[196:199],v[56:59]
v_exp_f32 v34,v34
v_mfma_f32_16x16x32_bf16 v[56:59],v[152:155],a[200:203],v[56:59]
v_add_f32 v250,v250,v34
v_cvt_pk_bf16_f32 v25,v26,v27
v_mfma_f32_16x16x32_bf16 v[56:59],v[156:159],a[204:207],v[56:59]
v_cvt_pk_bf16_f32 v26,v28,v29
s_waitcnt lgkmcnt(4)
v_mfma_f32_16x16x32_bf16 a[64:67],v[160:163],v[64:67],a[64:67]
v_exp_f32 v35,v35
v_mfma_f32_16x16x32_bf16 a[64:67],v[164:167],v[96:99],a[64:67]
ds_read_b128 v[192:195],v226 offset:45056
v_add_f32 v251,v251,v35
v_mfma_f32_16x16x32_bf16 a[80:83],v[168:171],v[64:67],a[80:83]
v_exp_f32 v40,v40
v_mfma_f32_16x16x32_bf16 a[80:83],v[172:175],v[96:99],a[80:83]
ds_read_b128 v[196:199],v226 offset:46080
v_add_f32 v252,v252,v40
v_mfma_f32_16x16x32_bf16 a[68:71],v[160:163],v[72:75],a[68:71]
v_exp_f32 v41,v41
v_mfma_f32_16x16x32_bf16 a[68:71],v[164:167],v[104:107],a[68:71]
ds_read_b128 v[200:203],v226 offset:47104
v_add_f32 v253,v253,v41
v_mfma_f32_16x16x32_bf16 a[84:87],v[168:171],v[72:75],a[84:87]
v_exp_f32 v42,v42
v_mfma_f32_16x16x32_bf16 a[84:87],v[172:175],v[104:107],a[84:87]
ds_read_b128 v[204:207],v226 offset:48128
v_add_f32 v252,v252,v42
v_mfma_f32_16x16x32_bf16 a[72:75],v[160:163],v[80:83],a[72:75]
v_exp_f32 v43,v43
v_mfma_f32_16x16x32_bf16 a[72:75],v[164:167],v[112:115],a[72:75]
v_add_f32 v253,v253,v43
v_cvt_pk_bf16_f32 v27,v30,v31
v_mfma_f32_16x16x32_bf16 a[88:91],v[168:171],v[80:83],a[88:91]
v_exp_f32 v48,v48
v_mfma_f32_16x16x32_bf16 a[88:91],v[172:175],v[112:115],a[88:91]
v_cvt_pk_bf16_f32 v32,v32,v33
v_cvt_pk_bf16_f32 v40,v40,v41
v_mfma_f32_16x16x32_bf16 a[76:79],v[160:163],v[88:91],a[76:79]
v_exp_f32 v49,v49
v_mfma_f32_16x16x32_bf16 a[76:79],v[164:167],v[120:123],a[76:79]
v_cvt_pk_bf16_f32 v33,v34,v35
v_cvt_pk_bf16_f32 v41,v42,v43
v_mfma_f32_16x16x32_bf16 a[92:95],v[168:171],v[88:91],a[92:95]
v_exp_f32 v50,v50
v_mfma_f32_16x16x32_bf16 a[92:95],v[172:175],v[120:123],a[92:95]
s_waitcnt vmcnt(0) lgkmcnt(0)
s_barrier
v_mfma_f32_16x16x32_bf16 v[36:39],v[176:179],a[144:147],v[128:131]
s_add_u32 m0,s82,0x10000
s_add_u32 s59,s80,0x4000
v_mfma_f32_16x16x32_bf16 v[36:39],v[180:183],a[148:151],v[36:39]
ds_read_b128 v[144:147],v225 offset:32768
buffer_load_dwordx4 v228,s[36:39],s59 offen offset:0 lds
v_mfma_f32_16x16x32_bf16 v[36:39],v[184:187],a[152:155],v[36:39]
v_exp_f32 v51,v51
v_mfma_f32_16x16x32_bf16 v[36:39],v[188:191],a[156:159],v[36:39]
ds_read_b128 v[148:151],v225 offset:33792
v_cvt_pk_bf16_f32 v48,v48,v49
v_mfma_f32_16x16x32_bf16 v[44:47],v[176:179],a[160:163],v[132:135]
v_exp_f32 v56,v56
v_mfma_f32_16x16x32_bf16 v[44:47],v[180:183],a[164:167],v[44:47]
ds_read_b128 v[152:155],v225 offset:34816
buffer_load_dwordx4 v228,s[36:39],s59 offen offset:1024 lds
v_mfma_f32_16x16x32_bf16 v[44:47],v[184:187],a[168:171],v[44:47]
v_exp_f32 v57,v57
v_mfma_f32_16x16x32_bf16 v[44:47],v[188:191],a[172:175],v[44:47]
ds_read_b128 v[156:159],v225 offset:35840
v_cvt_pk_bf16_f32 v49,v50,v51
v_mfma_f32_16x16x32_bf16 v[52:55],v[176:179],a[176:179],v[136:139]
v_exp_f32 v58,v58
v_mfma_f32_16x16x32_bf16 v[52:55],v[180:183],a[180:183],v[52:55]
buffer_load_dwordx4 v228,s[36:39],s59 offen offset:2048 lds
v_cvt_pk_bf16_f32 v56,v56,v57
v_mfma_f32_16x16x32_bf16 v[52:55],v[184:187],a[184:187],v[52:55]
v_exp_f32 v59,v59
v_mfma_f32_16x16x32_bf16 v[52:55],v[188:191],a[188:191],v[52:55]
v_exp_f32 v36,v36
v_mfma_f32_16x16x32_bf16 v[60:63],v[176:179],a[192:195],v[140:143]
v_exp_f32 v37,v37
v_mfma_f32_16x16x32_bf16 v[60:63],v[180:183],a[196:199],v[60:63]
buffer_load_dwordx4 v228,s[36:39],s59 offen offset:3072 lds
v_add_f32 v250,v250,v36
v_mfma_f32_16x16x32_bf16 v[60:63],v[184:187],a[200:203],v[60:63]
v_exp_f32 v38,v38
v_mfma_f32_16x16x32_bf16 v[60:63],v[188:191],a[204:207],v[60:63]
s_add_u32 m0,s82,0x14000
s_add_u32 s58,s80,0xc000
v_mfma_f32_16x16x32_bf16 a[96:99],v[192:195],v[64:67],a[96:99]
buffer_load_dwordx4 v228,s[36:39],s58 offen offset:0 lds
v_add_f32 v251,v251,v37
v_mfma_f32_16x16x32_bf16 a[96:99],v[196:199],v[96:99],a[96:99]
ds_read_b128 v[160:163],v227 offset:0
v_add_f32 v250,v250,v38
v_mfma_f32_16x16x32_bf16 a[112:115],v[200:203],v[64:67],a[112:115]
v_exp_f32 v39,v39
v_mfma_f32_16x16x32_bf16 a[112:115],v[204:207],v[96:99],a[112:115]
ds_read_b128 v[164:167],v227 offset:1024
v_add_f32 v251,v251,v39
v_mfma_f32_16x16x32_bf16 a[100:103],v[192:195],v[72:75],a[100:103]
buffer_load_dwordx4 v228,s[36:39],s58 offen offset:1024 lds
v_cvt_pk_bf16_f32 v34,v36,v37
v_mfma_f32_16x16x32_bf16 a[100:103],v[196:199],v[104:107],a[100:103]
ds_read_b128 v[168:171],v227 offset:2048
v_cvt_pk_bf16_f32 v35,v38,v39
v_mfma_f32_16x16x32_bf16 a[116:119],v[200:203],v[72:75],a[116:119]
v_exp_f32 v44,v44
v_mfma_f32_16x16x32_bf16 a[116:119],v[204:207],v[104:107],a[116:119]
ds_read_b128 v[172:175],v227 offset:3072
v_add_f32 v252,v252,v44
v_mfma_f32_16x16x32_bf16 a[104:107],v[192:195],v[80:83],a[104:107]
buffer_load_dwordx4 v228,s[36:39],s58 offen offset:2048 lds
v_cvt_pk_bf16_f32 v57,v58,v59
v_mfma_f32_16x16x32_bf16 a[104:107],v[196:199],v[112:115],a[104:107]
v_exp_f32 v45,v45
v_mfma_f32_16x16x32_bf16 a[120:123],v[200:203],v[80:83],a[120:123]
v_exp_f32 v46,v46
v_mfma_f32_16x16x32_bf16 a[120:123],v[204:207],v[112:115],a[120:123]
v_exp_f32 v47,v47
v_mfma_f32_16x16x32_bf16 a[108:111],v[192:195],v[88:91],a[108:111]
buffer_load_dwordx4 v228,s[36:39],s58 offen offset:3072 lds
v_exp_f32 v55,v55
v_mfma_f32_16x16x32_bf16 a[108:111],v[196:199],v[120:123],a[108:111]
v_exp_f32 v52,v52
v_add_f32 v253,v253,v47
v_mfma_f32_16x16x32_bf16 a[124:127],v[200:203],v[88:91],a[124:127]
v_exp_f32 v53,v53
v_mfma_f32_16x16x32_bf16 a[124:127],v[204:207],v[120:123],a[124:127]
v_exp_f32 v54,v54
v_mfma_f32_16x16x32_bf16 a[140:143],a[240:243],v[88:91],a[140:143]
v_add_f32 v253,v253,v45
v_add_f32 v252,v252,v46
v_mfma_f32_16x16x32_bf16 a[140:143],a[240:243],v[120:123],a[140:143]
v_cvt_pk_bf16_f32 v42,v44,v45
v_cvt_pk_bf16_f32 v43,v46,v47
s_add_u32 s80,s80,0x10000
s_add_u32 s62,s62,1
s_cmp_lg_u32 s62,4
s_cbranch_scc1 L_HBO2_%=
s_mov_b32 s59,0
L_HPO2_%=:
v_readfirstlane_b32 s58,v237
s_nop 3
s_cmp_ge_u32 s58,10
s_cbranch_scc1 L_HDO2_%=
s_sleep 2
global_load_dword v237,v238,s[22:23] offset:128 sc1
s_add_u32 s59,s59,1
s_cmp_lt_u32 s59,0x20000
s_waitcnt vmcnt(0)
s_cbranch_scc1 L_HPO2_%=
L_HDO2_%=:
buffer_inv sc1
L_HBO2_%=:
L_E2_%=:
s_waitcnt lgkmcnt(4)
v_mfma_f32_16x16x32_bf16 v[64:67],v[144:147],a[144:147],v[128:131]
v_exp_f32 v60,v60
v_mfma_f32_16x16x32_bf16 v[64:67],v[148:151],a[148:151],v[64:67]
v_exp_f32 v61,v61
v_mfma_f32_16x16x32_bf16 v[64:67],v[152:155],a[152:155],v[64:67]
v_exp_f32 v62,v62
v_mfma_f32_16x16x32_bf16 v[64:67],v[156:159],a[156:159],v[64:67]
v_exp_f32 v63,v63
v_mfma_f32_16x16x32_bf16 v[72:75],v[144:147],a[160:163],v[132:135]
v_cvt_pk_bf16_f32 v50,v52,v53
v_cvt_pk_bf16_f32 v51,v54,v55
v_mfma_f32_16x16x32_bf16 v[72:75],v[148:151],a[164:167],v[72:75]
v_cvt_pk_bf16_f32 v58,v60,v61
v_cvt_pk_bf16_f32 v59,v62,v63
v_mfma_f32_16x16x32_bf16 v[72:75],v[152:155],a[168:171],v[72:75]
ds_read_b128 v[176:179],v224 offset:36864
ds_read_b128 v[180:183],v224 offset:37888
v_mfma_f32_16x16x32_bf16 v[72:75],v[156:159],a[172:175],v[72:75]
ds_read_b128 v[184:187],v224 offset:38912
ds_read_b128 v[188:191],v224 offset:39936
v_mfma_f32_16x16x32_bf16 v[80:83],v[144:147],a[176:179],v[136:139]
v_mfma_f32_16x16x32_bf16 v[80:83],v[148:151],a[180:183],v[80:83]
s_mov_b32 m0,s81
v_mfma_f32_16x16x32_bf16 v[80:83],v[152:155],a[184:187],v[80:83]
buffer_load_dwordx4 v228,s[36:39],s80 offen offset:0 lds
v_mfma_f32_16x16x32_bf16 v[80:83],v[156:159],a[188:191],v[80:83]
v_exp_f32 v64,v64
v_mfma_f32_16x16x32_bf16 v[88:91],v[144:147],a[192:195],v[140:143]
v_add_f32 v250,v250,v64
v_mfma_f32_16x16x32_bf16 v[88:91],v[148:151],a[196:199],v[88:91]
v_exp_f32 v65,v65
v_mfma_f32_16x16x32_bf16 v[88:91],v[152:155],a[200:203],v[88:91]
v_add_f32 v251,v251,v65
v_mfma_f32_16x16x32_bf16 v[88:91],v[156:159],a[204:207],v[88:91]
v_cvt_pk_bf16_f32 v64,v64,v65
s_waitcnt lgkmcnt(4)
v_mfma_f32_16x16x32_bf16 a[0:3],v[160:163],v[0:3],a[0:3]
buffer_load_dwordx4 v228,s[36:39],s80 offen offset:1024 lds
v_mfma_f32_16x16x32_bf16 a[0:3],v[164:167],v[32:35],a[0:3]
ds_read_b128 v[192:195],v226 offset:4096
v_mfma_f32_16x16x32_bf16 a[16:19],v[168:171],v[0:3],a[16:19]
)"
R"(v_exp_f32 v66,v66
v_mfma_f32_16x16x32_bf16 a[16:19],v[172:175],v[32:35],a[16:19]
ds_read_b128 v[196:199],v226 offset:5120
v_add_f32 v250,v250,v66
v_mfma_f32_16x16x32_bf16 a[4:7],v[160:163],v[8:11],a[4:7]
v_exp_f32 v67,v67
v_mfma_f32_16x16x32_bf16 a[4:7],v[164:167],v[40:43],a[4:7]
ds_read_b128 v[200:203],v226 offset:6144
v_add_f32 v251,v251,v67
v_mfma_f32_16x16x32_bf16 a[20:23],v[168:171],v[8:11],a[20:23]
buffer_load_dwordx4 v228,s[36:39],s80 offen offset:2048 lds
v_cvt_pk_bf16_f32 v65,v66,v67
v_mfma_f32_16x16x32_bf16 a[20:23],v[172:175],v[40:43],a[20:23]
ds_read_b128 v[204:207],v226 offset:7168
v_mfma_f32_16x16x32_bf16 a[8:11],v[160:163],v[16:19],a[8:11]
v_exp_f32 v72,v72
v_mfma_f32_16x16x32_bf16 a[8:11],v[164:167],v[48:51],a[8:11]
v_add_f32 v252,v252,v72
v_mfma_f32_16x16x32_bf16 a[24:27],v[168:171],v[16:19],a[24:27]
v_exp_f32 v73,v73
v_mfma_f32_16x16x32_bf16 a[24:27],v[172:175],v[48:51],a[24:27]
v_add_f32 v253,v253,v73
v_mfma_f32_16x16x32_bf16 a[12:15],v[160:163],v[24:27],a[12:15]
buffer_load_dwordx4 v228,s[36:39],s80 offen offset:3072 lds
v_cvt_pk_bf16_f32 v72,v72,v73
v_mfma_f32_16x16x32_bf16 a[12:15],v[164:167],v[56:59],a[12:15]
v_exp_f32 v74,v74
v_mfma_f32_16x16x32_bf16 a[28:31],v[168:171],v[24:27],a[28:31]
v_add_f32 v252,v252,v74
v_mfma_f32_16x16x32_bf16 a[28:31],v[172:175],v[56:59],a[28:31]
s_waitcnt lgkmcnt(4)
v_mfma_f32_16x16x32_bf16 v[68:71],v[176:179],a[144:147],v[128:131]
s_add_u32 m0,s81,0x4000
s_add_u32 s58,s80,0x8000
v_mfma_f32_16x16x32_bf16 v[68:71],v[180:183],a[148:151],v[68:71]
ds_read_b128 v[144:147],v224 offset:40960
buffer_load_dwordx4 v228,s[36:39],s58 offen offset:0 lds
v_mfma_f32_16x16x32_bf16 v[68:71],v[184:187],a[152:155],v[68:71]
v_exp_f32 v75,v75
v_mfma_f32_16x16x32_bf16 v[68:71],v[188:191],a[156:159],v[68:71]
ds_read_b128 v[148:151],v224 offset:41984
v_add_f32 v253,v253,v75
v_mfma_f32_16x16x32_bf16 v[76:79],v[176:179],a[160:163],v[132:135]
v_exp_f32 v80,v80
v_mfma_f32_16x16x32_bf16 v[76:79],v[180:183],a[164:167],v[76:79]
ds_read_b128 v[152:155],v224 offset:43008
v_cvt_pk_bf16_f32 v73,v74,v75
v_mfma_f32_16x16x32_bf16 v[76:79],v[184:187],a[168:171],v[76:79]
v_exp_f32 v81,v81
v_mfma_f32_16x16x32_bf16 v[76:79],v[188:191],a[172:175],v[76:79]
ds_read_b128 v[156:159],v224 offset:44032
buffer_load_dwordx4 v228,s[36:39],s58 offen offset:1024 lds
v_mfma_f32_16x16x32_bf16 v[84:87],v[176:179],a[176:179],v[136:139]
v_exp_f32 v82,v82
v_mfma_f32_16x16x32_bf16 v[84:87],v[180:183],a[180:183],v[84:87]
v_exp_f32 v68,v68
v_mfma_f32_16x16x32_bf16 v[84:87],v[184:187],a[184:187],v[84:87]
v_exp_f32 v83,v83
v_mfma_f32_16x16x32_bf16 v[84:87],v[188:191],a[188:191],v[84:87]
v_exp_f32 v69,v69
v_mfma_f32_16x16x32_bf16 v[92:95],v[176:179],a[192:195],v[140:143]
v_exp_f32 v88,v88
v_mfma_f32_16x16x32_bf16 v[92:95],v[180:183],a[196:199],v[92:95]
buffer_load_dwordx4 v228,s[36:39],s58 offen offset:2048 lds
v_exp_f32 v70,v70
v_mfma_f32_16x16x32_bf16 v[92:95],v[184:187],a[200:203],v[92:95]
v_exp_f32 v89,v89
v_mfma_f32_16x16x32_bf16 v[92:95],v[188:191],a[204:207],v[92:95]
v_add_f32 v250,v250,v68
s_waitcnt lgkmcnt(4)
v_mfma_f32_16x16x32_bf16 a[32:35],v[192:195],v[0:3],a[32:35]
v_exp_f32 v90,v90
v_mfma_f32_16x16x32_bf16 a[32:35],v[196:199],v[32:35],a[32:35]
ds_read_b128 v[160:163],v226 offset:8192
v_add_f32 v251,v251,v69
v_mfma_f32_16x16x32_bf16 a[48:51],v[200:203],v[0:3],a[48:51]
v_exp_f32 v91,v91
v_mfma_f32_16x16x32_bf16 a[48:51],v[204:207],v[32:35],a[48:51]
ds_read_b128 v[164:167],v226 offset:9216
buffer_load_dwordx4 v228,s[36:39],s58 offen offset:3072 lds
v_mfma_f32_16x16x32_bf16 a[36:39],v[192:195],v[8:11],a[36:39]
v_exp_f32 v71,v71
v_mfma_f32_16x16x32_bf16 a[36:39],v[196:199],v[40:43],a[36:39]
ds_read_b128 v[168:171],v226 offset:10240
v_add_f32 v250,v250,v70
v_mfma_f32_16x16x32_bf16 a[52:55],v[200:203],v[8:11],a[52:55]
v_exp_f32 v76,v76
v_mfma_f32_16x16x32_bf16 a[52:55],v[204:207],v[40:43],a[52:55]
ds_read_b128 v[172:175],v226 offset:11264
v_add_f32 v251,v251,v71
v_mfma_f32_16x16x32_bf16 a[40:43],v[192:195],v[16:19],a[40:43]
v_exp_f32 v77,v77
v_mfma_f32_16x16x32_bf16 a[40:43],v[196:199],v[48:51],a[40:43]
v_add_f32 v252,v252,v76
v_add_f32 v253,v253,v77
v_mfma_f32_16x16x32_bf16 a[56:59],v[200:203],v[16:19],a[56:59]
v_exp_f32 v78,v78
v_mfma_f32_16x16x32_bf16 a[56:59],v[204:207],v[48:51],a[56:59]
v_add_f32 v252,v252,v78
v_cvt_pk_bf16_f32 v66,v68,v69
v_mfma_f32_16x16x32_bf16 a[44:47],v[192:195],v[24:27],a[44:47]
v_exp_f32 v79,v79
v_mfma_f32_16x16x32_bf16 a[44:47],v[196:199],v[56:59],a[44:47]
v_add_f32 v253,v253,v79
v_cvt_pk_bf16_f32 v67,v70,v71
v_mfma_f32_16x16x32_bf16 a[60:63],v[200:203],v[24:27],a[60:63]
v_exp_f32 v84,v84
v_mfma_f32_16x16x32_bf16 a[60:63],v[204:207],v[56:59],a[60:63]
v_cvt_pk_bf16_f32 v74,v76,v77
v_cvt_pk_bf16_f32 v80,v80,v81
v_mfma_f32_16x16x32_bf16 a[136:139],a[240:243],v[16:19],a[136:139]
v_exp_f32 v85,v85
v_mfma_f32_16x16x32_bf16 a[136:139],a[240:243],v[48:51],a[136:139]
v_cvt_pk_bf16_f32 v75,v78,v79
s_waitcnt lgkmcnt(4)
v_mfma_f32_16x16x32_bf16 v[96:99],v[144:147],a[144:147],v[128:131]
v_exp_f32 v86,v86
v_mfma_f32_16x16x32_bf16 v[96:99],v[148:151],a[148:151],v[96:99]
ds_read_b128 v[176:179],v224 offset:45056
v_cvt_pk_bf16_f32 v81,v82,v83
v_mfma_f32_16x16x32_bf16 v[96:99],v[152:155],a[152:155],v[96:99]
v_exp_f32 v87,v87
v_mfma_f32_16x16x32_bf16 v[96:99],v[156:159],a[156:159],v[96:99]
ds_read_b128 v[180:183],v224 offset:46080
v_cvt_pk_bf16_f32 v82,v84,v85
v_mfma_f32_16x16x32_bf16 v[104:107],v[144:147],a[160:163],v[132:135]
v_exp_f32 v92,v92
v_mfma_f32_16x16x32_bf16 v[104:107],v[148:151],a[164:167],v[104:107]
ds_read_b128 v[184:187],v224 offset:47104
v_cvt_pk_bf16_f32 v83,v86,v87
v_mfma_f32_16x16x32_bf16 v[104:107],v[152:155],a[168:171],v[104:107]
v_exp_f32 v93,v93
v_mfma_f32_16x16x32_bf16 v[104:107],v[156:159],a[172:175],v[104:107]
ds_read_b128 v[188:191],v224 offset:48128
v_cvt_pk_bf16_f32 v88,v88,v89
v_mfma_f32_16x16x32_bf16 v[112:115],v[144:147],a[176:179],v[136:139]
v_exp_f32 v94,v94
v_mfma_f32_16x16x32_bf16 v[112:115],v[148:151],a[180:183],v[112:115]
v_exp_f32 v96,v96
v_mfma_f32_16x16x32_bf16 v[112:115],v[152:155],a[184:187],v[112:115]
v_exp_f32 v95,v95
v_mfma_f32_16x16x32_bf16 v[112:115],v[156:159],a[188:191],v[112:115]
v_exp_f32 v97,v97
v_mfma_f32_16x16x32_bf16 v[120:123],v[144:147],a[192:195],v[140:143]
v_add_f32 v250,v250,v96
v_add_f32 v251,v251,v97
v_mfma_f32_16x16x32_bf16 v[120:123],v[148:151],a[196:199],v[120:123]
v_exp_f32 v98,v98
v_mfma_f32_16x16x32_bf16 v[120:123],v[152:155],a[200:203],v[120:123]
v_add_f32 v250,v250,v98
v_cvt_pk_bf16_f32 v89,v90,v91
v_mfma_f32_16x16x32_bf16 v[120:123],v[156:159],a[204:207],v[120:123]
v_cvt_pk_bf16_f32 v90,v92,v93
s_waitcnt lgkmcnt(4)
v_mfma_f32_16x16x32_bf16 a[64:67],v[160:163],v[0:3],a[64:67]
v_exp_f32 v99,v99
v_mfma_f32_16x16x32_bf16 a[64:67],v[164:167],v[32:35],a[64:67]
ds_read_b128 v[192:195],v226 offset:12288
v_add_f32 v251,v251,v99
v_mfma_f32_16x16x32_bf16 a[80:83],v[168:171],v[0:3],a[80:83]
v_exp_f32 v104,v104
v_mfma_f32_16x16x32_bf16 a[80:83],v[172:175],v[32:35],a[80:83]
ds_read_b128 v[196:199],v226 offset:13312
v_add_f32 v252,v252,v104
v_mfma_f32_16x16x32_bf16 a[68:71],v[160:163],v[8:11],a[68:71]
v_exp_f32 v105,v105
v_mfma_f32_16x16x32_bf16 a[68:71],v[164:167],v[40:43],a[68:71]
ds_read_b128 v[200:203],v226 offset:14336
v_add_f32 v253,v253,v105
v_mfma_f32_16x16x32_bf16 a[84:87],v[168:171],v[8:11],a[84:87]
v_exp_f32 v106,v106
v_mfma_f32_16x16x32_bf16 a[84:87],v[172:175],v[40:43],a[84:87]
ds_read_b128 v[204:207],v226 offset:15360
v_add_f32 v252,v252,v106
v_mfma_f32_16x16x32_bf16 a[72:75],v[160:163],v[16:19],a[72:75]
v_exp_f32 v107,v107
v_mfma_f32_16x16x32_bf16 a[72:75],v[164:167],v[48:51],a[72:75]
v_add_f32 v253,v253,v107
v_cvt_pk_bf16_f32 v91,v94,v95
v_mfma_f32_16x16x32_bf16 a[88:91],v[168:171],v[16:19],a[88:91]
v_exp_f32 v112,v112
v_mfma_f32_16x16x32_bf16 a[88:91],v[172:175],v[48:51],a[88:91]
v_cvt_pk_bf16_f32 v96,v96,v97
v_cvt_pk_bf16_f32 v104,v104,v105
v_mfma_f32_16x16x32_bf16 a[76:79],v[160:163],v[24:27],a[76:79]
v_exp_f32 v113,v113
v_mfma_f32_16x16x32_bf16 a[76:79],v[164:167],v[56:59],a[76:79]
v_cvt_pk_bf16_f32 v97,v98,v99
v_cvt_pk_bf16_f32 v105,v106,v107
v_mfma_f32_16x16x32_bf16 a[92:95],v[168:171],v[24:27],a[92:95]
v_exp_f32 v114,v114
v_mfma_f32_16x16x32_bf16 a[92:95],v[172:175],v[56:59],a[92:95]
s_waitcnt vmcnt(0) lgkmcnt(0)
s_barrier
v_mfma_f32_16x16x32_bf16 v[100:103],v[176:179],a[144:147],v[128:131]
s_add_u32 m0,s81,0x10000
s_add_u32 s59,s80,0x4000
v_mfma_f32_16x16x32_bf16 v[100:103],v[180:183],a[148:151],v[100:103]
ds_read_b128 v[144:147],v225 offset:0
buffer_load_dwordx4 v228,s[36:39],s59 offen offset:0 lds
v_mfma_f32_16x16x32_bf16 v[100:103],v[184:187],a[152:155],v[100:103]
v_exp_f32 v115,v115
v_mfma_f32_16x16x32_bf16 v[100:103],v[188:191],a[156:159],v[100:103]
ds_read_b128 v[148:151],v225 offset:1024
v_cvt_pk_bf16_f32 v112,v112,v113
v_mfma_f32_16x16x32_bf16 v[108:111],v[176:179],a[160:163],v[132:135]
v_exp_f32 v120,v120
v_mfma_f32_16x16x32_bf16 v[108:111],v[180:183],a[164:167],v[108:111]
ds_read_b128 v[152:155],v225 offset:2048
buffer_load_dwordx4 v228,s[36:39],s59 offen offset:1024 lds
v_mfma_f32_16x16x32_bf16 v[108:111],v[184:187],a[168:171],v[108:111]
v_exp_f32 v121,v121
v_mfma_f32_16x16x32_bf16 v[108:111],v[188:191],a[172:175],v[108:111]
ds_read_b128 v[156:159],v225 offset:3072
v_cvt_pk_bf16_f32 v113,v114,v115
v_mfma_f32_16x16x32_bf16 v[116:119],v[176:179],a[176:179],v[136:139]
v_exp_f32 v122,v122
v_mfma_f32_16x16x32_bf16 v[116:119],v[180:183],a[180:183],v[116:119]
buffer_load_dwordx4 v228,s[36:39],s59 offen offset:2048 lds
v_cvt_pk_bf16_f32 v120,v120,v121
v_mfma_f32_16x16x32_bf16 v[116:119],v[184:187],a[184:187],v[116:119]
v_exp_f32 v123,v123
v_mfma_f32_16x16x32_bf16 v[116:119],v[188:191],a[188:191],v[116:119]
v_exp_f32 v100,v100
v_mfma_f32_16x16x32_bf16 v[124:127],v[176:179],a[192:195],v[140:143]
v_exp_f32 v101,v101
v_mfma_f32_16x16x32_bf16 v[124:127],v[180:183],a[196:199],v[124:127]
buffer_load_dwordx4 v228,s[36:39],s59 offen offset:3072 lds
v_add_f32 v250,v250,v100
v_mfma_f32_16x16x32_bf16 v[124:127],v[184:187],a[200:203],v[124:127]
v_exp_f32 v102,v102
v_mfma_f32_16x16x32_bf16 v[124:127],v[188:191],a[204:207],v[124:127]
s_add_u32 m0,s81,0x14000
s_add_u32 s58,s80,0xc000
v_mfma_f32_16x16x32_bf16 a[96:99],v[192:195],v[0:3],a[96:99]
buffer_load_dwordx4 v228,s[36:39],s58 offen offset:0 lds
v_add_f32 v251,v251,v101
v_mfma_f32_16x16x32_bf16 a[96:99],v[196:199],v[32:35],a[96:99]
ds_read_b128 v[160:163],v227 offset:32768
v_add_f32 v250,v250,v102
v_mfma_f32_16x16x32_bf16 a[112:115],v[200:203],v[0:3],a[112:115]
v_exp_f32 v103,v103
v_mfma_f32_16x16x32_bf16 a[112:115],v[204:207],v[32:35],a[112:115]
ds_read_b128 v[164:167],v227 offset:33792
v_add_f32 v251,v251,v103
v_mfma_f32_16x16x32_bf16 a[100:103],v[192:195],v[8:11],a[100:103]
buffer_load_dwordx4 v228,s[36:39],s58 offen offset:1024 lds
v_cvt_pk_bf16_f32 v98,v100,v101
v_mfma_f32_16x16x32_bf16 a[100:103],v[196:199],v[40:43],a[100:103]
ds_read_b128 v[168:171],v227 offset:34816
v_cvt_pk_bf16_f32 v99,v102,v103
v_mfma_f32_16x16x32_bf16 a[116:119],v[200:203],v[8:11],a[116:119]
v_exp_f32 v108,v108
v_mfma_f32_16x16x32_bf16 a[116:119],v[204:207],v[40:43],a[116:119]
ds_read_b128 v[172:175],v227 offset:35840
v_add_f32 v252,v252,v108
v_mfma_f32_16x16x32_bf16 a[104:107],v[192:195],v[16:19],a[104:107]
buffer_load_dwordx4 v228,s[36:39],s58 offen offset:2048 lds
v_cvt_pk_bf16_f32 v121,v122,v123
v_mfma_f32_16x16x32_bf16 a[104:107],v[196:199],v[48:51],a[104:107]
v_exp_f32 v109,v109
v_mfma_f32_16x16x32_bf16 a[120:123],v[200:203],v[16:19],a[120:123]
v_exp_f32 v110,v110
v_mfma_f32_16x16x32_bf16 a[120:123],v[204:207],v[48:51],a[120:123]
v_exp_f32 v111,v111
v_mfma_f32_16x16x32_bf16 a[108:111],v[192:195],v[24:27],a[108:111]
buffer_load_dwordx4 v228,s[36:39],s58 offen offset:3072 lds
v_exp_f32 v119,v119
v_mfma_f32_16x16x32_bf16 a[108:111],v[196:199],v[56:59],a[108:111]
v_exp_f32 v116,v116
v_add_f32 v253,v253,v111
v_mfma_f32_16x16x32_bf16 a[124:127],v[200:203],v[24:27],a[124:127]
v_exp_f32 v117,v117
v_mfma_f32_16x16x32_bf16 a[124:127],v[204:207],v[56:59],a[124:127]
v_exp_f32 v118,v118
v_mfma_f32_16x16x32_bf16 a[140:143],a[240:243],v[24:27],a[140:143]
v_add_f32 v253,v253,v109
v_add_f32 v252,v252,v110
v_mfma_f32_16x16x32_bf16 a[140:143],a[240:243],v[56:59],a[140:143]
v_cvt_pk_bf16_f32 v106,v108,v109
v_cvt_pk_bf16_f32 v107,v110,v111
s_add_u32 s80,s80,0x10000
s_add_u32 s62,s62,1
s_cmp_lg_u32 s62,3
s_cbranch_scc1 L_HAE2_%=
global_load_dword v237,v238,s[22:23] offset:128 sc1
L_HAE2_%=:
s_sub_u32 s87,s87,2
s_cmp_lg_u32 s87,0
s_cbranch_scc1 L_O2_%=
L_OMB_%=:
s_cmp_lg_u32 s83,0
s_cbranch_scc1 L_OMBO_%=
s_lshr_b32 s59,s83,8
s_lshl_b32 s58,s56,4
s_sub_u32 s58,s58,s59
v_add_u32 v230,s58,v229
v_cvt_f32_i32 v230,v230
v_mul_f32 v230,0x71800000,v230
s_lshl_b32 s58,s56,4
s_xor_b32 s58,s58,16
s_sub_u32 s58,s58,s59
v_add_u32 v231,s58,v229
v_cvt_f32_i32 v231,v231
v_mul_f32 v231,0x71800000,v231
s_waitcnt lgkmcnt(4)
v_add_f32 v0,0x71000000,v230
v_min_f32 v0,v0,v128
v_add_f32 v1,0xf1000000,v230
v_min_f32 v1,v1,v129
v_add_f32 v2,0xf1c00000,v230
v_min_f32 v2,v2,v130
v_add_f32 v3,0xf2200000,v230
v_min_f32 v3,v3,v131
s_nop 1
v_mfma_f32_16x16x32_bf16 v[0:3],v[144:147],a[144:147],v[0:3]
v_exp_f32 v124,v124
v_add_f32 v17,0xf1000000,v231
v_min_f32 v17,v17,v137
v_mfma_f32_16x16x32_bf16 v[0:3],v[148:151],a[148:151],v[0:3]
v_exp_f32 v125,v125
v_add_f32 v18,0xf1c00000,v231
v_min_f32 v18,v18,v138
v_mfma_f32_16x16x32_bf16 v[0:3],v[152:155],a[152:155],v[0:3]
v_exp_f32 v126,v126
v_add_f32 v19,0xf2200000,v231
v_min_f32 v19,v19,v139
v_mfma_f32_16x16x32_bf16 v[0:3],v[156:159],a[156:159],v[0:3]
v_exp_f32 v127,v127
ds_read_b128 v[176:179],v224 offset:4096
v_mfma_f32_16x16x32_bf16 v[8:11],v[144:147],a[160:163],v[132:135]
v_cvt_pk_bf16_f32 v114,v116,v117
v_cvt_pk_bf16_f32 v115,v118,v119
ds_read_b128 v[180:183],v224 offset:5120
v_mfma_f32_16x16x32_bf16 v[8:11],v[148:151],a[164:167],v[8:11]
v_cvt_pk_bf16_f32 v122,v124,v125
v_cvt_pk_bf16_f32 v123,v126,v127
ds_read_b128 v[184:187],v224 offset:6144
v_mfma_f32_16x16x32_bf16 v[8:11],v[152:155],a[168:171],v[8:11]
v_add_f32 v16,0x71000000,v231
v_min_f32 v16,v16,v136
v_mfma_f32_16x16x32_bf16 v[8:11],v[156:159],a[172:175],v[8:11]
v_add_f32 v4,0xf3780000,v230
v_min_f32 v4,v4,v128
ds_read_b128 v[188:191],v224 offset:7168
v_mfma_f32_16x16x32_bf16 v[16:19],v[144:147],a[176:179],v[16:19]
v_add_f32 v5,0xf3840000,v230
v_min_f32 v5,v5,v129
v_mfma_f32_16x16x32_bf16 v[16:19],v[148:151],a[180:183],v[16:19]
v_add_f32 v6,0xf38c0000,v230
v_min_f32 v6,v6,v130
v_mfma_f32_16x16x32_bf16 v[16:19],v[152:155],a[184:187],v[16:19]
v_add_f32 v7,0xf3940000,v230
v_min_f32 v7,v7,v131
v_mfma_f32_16x16x32_bf16 v[16:19],v[156:159],a[188:191],v[16:19]
v_add_f32 v20,0xf3780000,v231
v_min_f32 v20,v20,v136
v_mfma_f32_16x16x32_bf16 v[24:27],v[144:147],a[192:195],v[140:143]
v_add_f32 v21,0xf3840000,v231
v_min_f32 v21,v21,v137
v_mfma_f32_16x16x32_bf16 v[24:27],v[148:151],a[196:199],v[24:27]
v_add_f32 v22,0xf38c0000,v231
v_min_f32 v22,v22,v138
v_mfma_f32_16x16x32_bf16 v[24:27],v[152:155],a[200:203],v[24:27]
v_add_f32 v23,0xf3940000,v231
v_min_f32 v23,v23,v139
v_mfma_f32_16x16x32_bf16 v[24:27],v[156:159],a[204:207],v[24:27]
v_exp_f32 v0,v0
s_waitcnt lgkmcnt(4)
v_mfma_f32_16x16x32_bf16 a[0:3],v[160:163],v[64:67],a[0:3]
v_add_f32 v250,v250,v0
v_mfma_f32_16x16x32_bf16 a[0:3],v[164:167],v[96:99],a[0:3]
ds_read_b128 v[192:195],v226 offset:36864
v_mfma_f32_16x16x32_bf16 a[16:19],v[168:171],v[64:67],a[16:19]
v_exp_f32 v1,v1
v_mfma_f32_16x16x32_bf16 a[16:19],v[172:175],v[96:99],a[16:19]
ds_read_b128 v[196:199],v226 offset:37888
v_add_f32 v251,v251,v1
v_mfma_f32_16x16x32_bf16 a[4:7],v[160:163],v[72:75],a[4:7]
v_exp_f32 v2,v2
v_mfma_f32_16x16x32_bf16 a[4:7],v[164:167],v[104:107],a[4:7]
ds_read_b128 v[200:203],v226 offset:38912
v_add_f32 v250,v250,v2
v_mfma_f32_16x16x32_bf16 a[20:23],v[168:171],v[72:75],a[20:23]
v_exp_f32 v3,v3
v_mfma_f32_16x16x32_bf16 a[20:23],v[172:175],v[104:107],a[20:23]
ds_read_b128 v[204:207],v226 offset:39936
v_add_f32 v251,v251,v3
v_mfma_f32_16x16x32_bf16 a[8:11],v[160:163],v[80:83],a[8:11]
v_exp_f32 v8,v8
v_mfma_f32_16x16x32_bf16 a[8:11],v[164:167],v[112:115],a[8:11]
v_add_f32 v252,v252,v8
v_cvt_pk_bf16_f32 v0,v0,v1
v_mfma_f32_16x16x32_bf16 a[24:27],v[168:171],v[80:83],a[24:27]
v_exp_f32 v9,v9
v_mfma_f32_16x16x32_bf16 a[24:27],v[172:175],v[112:115],a[24:27]
v_add_f32 v253,v253,v9
v_cvt_pk_bf16_f32 v1,v2,v3
v_mfma_f32_16x16x32_bf16 a[12:15],v[160:163],v[88:91],a[12:15]
v_exp_f32 v10,v10
)"
R"(v_mfma_f32_16x16x32_bf16 a[12:15],v[164:167],v[120:123],a[12:15]
v_add_f32 v252,v252,v10
v_cvt_pk_bf16_f32 v8,v8,v9
v_mfma_f32_16x16x32_bf16 a[28:31],v[168:171],v[88:91],a[28:31]
v_exp_f32 v11,v11
v_mfma_f32_16x16x32_bf16 a[28:31],v[172:175],v[120:123],a[28:31]
v_add_f32 v253,v253,v11
s_waitcnt lgkmcnt(4)
v_mfma_f32_16x16x32_bf16 v[4:7],v[176:179],a[144:147],v[4:7]
v_exp_f32 v16,v16
v_mfma_f32_16x16x32_bf16 v[4:7],v[180:183],a[148:151],v[4:7]
ds_read_b128 v[144:147],v224 offset:8192
v_cvt_pk_bf16_f32 v9,v10,v11
v_mfma_f32_16x16x32_bf16 v[4:7],v[184:187],a[152:155],v[4:7]
v_exp_f32 v17,v17
v_mfma_f32_16x16x32_bf16 v[4:7],v[188:191],a[156:159],v[4:7]
ds_read_b128 v[148:151],v224 offset:9216
v_cvt_pk_bf16_f32 v16,v16,v17
v_mfma_f32_16x16x32_bf16 v[12:15],v[176:179],a[160:163],v[132:135]
v_add_f32 v32,0xf3fc0000,v230
v_min_f32 v32,v32,v128
v_mfma_f32_16x16x32_bf16 v[12:15],v[180:183],a[164:167],v[12:15]
v_add_f32 v33,0xf4020000,v230
v_min_f32 v33,v33,v129
v_mfma_f32_16x16x32_bf16 v[12:15],v[184:187],a[168:171],v[12:15]
v_add_f32 v34,0xf4060000,v230
v_min_f32 v34,v34,v130
v_mfma_f32_16x16x32_bf16 v[12:15],v[188:191],a[172:175],v[12:15]
v_add_f32 v35,0xf40a0000,v230
v_min_f32 v35,v35,v131
v_mfma_f32_16x16x32_bf16 v[20:23],v[176:179],a[176:179],v[20:23]
ds_read_b128 v[152:155],v224 offset:10240
ds_read_b128 v[156:159],v224 offset:11264
v_mfma_f32_16x16x32_bf16 v[20:23],v[180:183],a[180:183],v[20:23]
v_exp_f32 v18,v18
v_mfma_f32_16x16x32_bf16 v[20:23],v[184:187],a[184:187],v[20:23]
v_exp_f32 v4,v4
v_mfma_f32_16x16x32_bf16 v[20:23],v[188:191],a[188:191],v[20:23]
v_exp_f32 v19,v19
v_mfma_f32_16x16x32_bf16 v[28:31],v[176:179],a[192:195],v[140:143]
v_add_f32 v48,0xf3fc0000,v231
v_min_f32 v48,v48,v136
v_mfma_f32_16x16x32_bf16 v[28:31],v[180:183],a[196:199],v[28:31]
v_add_f32 v49,0xf4020000,v231
v_min_f32 v49,v49,v137
v_mfma_f32_16x16x32_bf16 v[28:31],v[184:187],a[200:203],v[28:31]
v_add_f32 v50,0xf4060000,v231
v_min_f32 v50,v50,v138
v_mfma_f32_16x16x32_bf16 v[28:31],v[188:191],a[204:207],v[28:31]
v_exp_f32 v5,v5
s_waitcnt lgkmcnt(4)
v_mfma_f32_16x16x32_bf16 a[32:35],v[192:195],v[64:67],a[32:35]
v_add_f32 v51,0xf40a0000,v231
v_min_f32 v51,v51,v139
v_mfma_f32_16x16x32_bf16 a[32:35],v[196:199],v[96:99],a[32:35]
ds_read_b128 v[160:163],v226 offset:40960
v_exp_f32 v6,v6
v_mfma_f32_16x16x32_bf16 a[48:51],v[200:203],v[64:67],a[48:51]
v_exp_f32 v24,v24
v_mfma_f32_16x16x32_bf16 a[48:51],v[204:207],v[96:99],a[48:51]
ds_read_b128 v[164:167],v226 offset:41984
v_add_f32 v250,v250,v4
v_mfma_f32_16x16x32_bf16 a[36:39],v[192:195],v[72:75],a[36:39]
v_exp_f32 v25,v25
v_mfma_f32_16x16x32_bf16 a[36:39],v[196:199],v[104:107],a[36:39]
ds_read_b128 v[168:171],v226 offset:43008
v_add_f32 v251,v251,v5
v_mfma_f32_16x16x32_bf16 a[52:55],v[200:203],v[72:75],a[52:55]
v_exp_f32 v26,v26
v_mfma_f32_16x16x32_bf16 a[52:55],v[204:207],v[104:107],a[52:55]
ds_read_b128 v[172:175],v226 offset:44032
v_add_f32 v250,v250,v6
v_mfma_f32_16x16x32_bf16 a[40:43],v[192:195],v[80:83],a[40:43]
v_exp_f32 v27,v27
v_mfma_f32_16x16x32_bf16 a[40:43],v[196:199],v[112:115],a[40:43]
v_exp_f32 v7,v7
v_mfma_f32_16x16x32_bf16 a[56:59],v[200:203],v[80:83],a[56:59]
v_add_f32 v251,v251,v7
v_cvt_pk_bf16_f32 v2,v4,v5
v_mfma_f32_16x16x32_bf16 a[56:59],v[204:207],v[112:115],a[56:59]
v_exp_f32 v12,v12
v_mfma_f32_16x16x32_bf16 a[44:47],v[192:195],v[88:91],a[44:47]
v_add_f32 v252,v252,v12
v_cvt_pk_bf16_f32 v3,v6,v7
v_mfma_f32_16x16x32_bf16 a[44:47],v[196:199],v[120:123],a[44:47]
v_exp_f32 v13,v13
v_mfma_f32_16x16x32_bf16 a[60:63],v[200:203],v[88:91],a[60:63]
v_add_f32 v253,v253,v13
v_cvt_pk_bf16_f32 v17,v18,v19
v_mfma_f32_16x16x32_bf16 a[60:63],v[204:207],v[120:123],a[60:63]
v_exp_f32 v14,v14
v_mfma_f32_16x16x32_bf16 a[136:139],a[240:243],v[80:83],a[136:139]
v_add_f32 v252,v252,v14
v_cvt_pk_bf16_f32 v10,v12,v13
v_mfma_f32_16x16x32_bf16 a[136:139],a[240:243],v[112:115],a[136:139]
v_cvt_pk_bf16_f32 v24,v24,v25
s_waitcnt lgkmcnt(4)
v_mfma_f32_16x16x32_bf16 v[32:35],v[144:147],a[144:147],v[32:35]
v_exp_f32 v15,v15
v_mfma_f32_16x16x32_bf16 v[32:35],v[148:151],a[148:151],v[32:35]
ds_read_b128 v[176:179],v224 offset:12288
v_add_f32 v253,v253,v15
v_mfma_f32_16x16x32_bf16 v[32:35],v[152:155],a[152:155],v[32:35]
v_add_f32 v36,0xf43e0000,v230
v_min_f32 v36,v36,v128
v_mfma_f32_16x16x32_bf16 v[32:35],v[156:159],a[156:159],v[32:35]
v_add_f32 v37,0xf4420000,v230
v_min_f32 v37,v37,v129
v_mfma_f32_16x16x32_bf16 v[40:43],v[144:147],a[160:163],v[132:135]
v_add_f32 v38,0xf4460000,v230
v_min_f32 v38,v38,v130
v_mfma_f32_16x16x32_bf16 v[40:43],v[148:151],a[164:167],v[40:43]
v_add_f32 v39,0xf44a0000,v230
v_min_f32 v39,v39,v131
v_mfma_f32_16x16x32_bf16 v[40:43],v[152:155],a[168:171],v[40:43]
ds_read_b128 v[180:183],v224 offset:13312
ds_read_b128 v[184:187],v224 offset:14336
v_mfma_f32_16x16x32_bf16 v[40:43],v[156:159],a[172:175],v[40:43]
ds_read_b128 v[188:191],v224 offset:15360
v_cvt_pk_bf16_f32 v11,v14,v15
v_mfma_f32_16x16x32_bf16 v[48:51],v[144:147],a[176:179],v[48:51]
v_exp_f32 v20,v20
v_mfma_f32_16x16x32_bf16 v[48:51],v[148:151],a[180:183],v[48:51]
v_exp_f32 v32,v32
v_mfma_f32_16x16x32_bf16 v[48:51],v[152:155],a[184:187],v[48:51]
v_add_f32 v52,0xf43e0000,v231
v_min_f32 v52,v52,v136
v_mfma_f32_16x16x32_bf16 v[48:51],v[156:159],a[188:191],v[48:51]
v_add_f32 v53,0xf4420000,v231
v_min_f32 v53,v53,v137
v_mfma_f32_16x16x32_bf16 v[56:59],v[144:147],a[192:195],v[140:143]
v_add_f32 v54,0xf4460000,v231
v_min_f32 v54,v54,v138
v_mfma_f32_16x16x32_bf16 v[56:59],v[148:151],a[196:199],v[56:59]
v_add_f32 v55,0xf44a0000,v231
v_min_f32 v55,v55,v139
v_mfma_f32_16x16x32_bf16 v[56:59],v[152:155],a[200:203],v[56:59]
v_exp_f32 v21,v21
v_mfma_f32_16x16x32_bf16 v[56:59],v[156:159],a[204:207],v[56:59]
v_exp_f32 v33,v33
s_waitcnt lgkmcnt(4)
v_mfma_f32_16x16x32_bf16 a[64:67],v[160:163],v[64:67],a[64:67]
v_exp_f32 v22,v22
v_mfma_f32_16x16x32_bf16 a[64:67],v[164:167],v[96:99],a[64:67]
ds_read_b128 v[192:195],v226 offset:45056
v_exp_f32 v34,v34
v_mfma_f32_16x16x32_bf16 a[80:83],v[168:171],v[64:67],a[80:83]
v_exp_f32 v23,v23
v_mfma_f32_16x16x32_bf16 a[80:83],v[172:175],v[96:99],a[80:83]
ds_read_b128 v[196:199],v226 offset:46080
v_add_f32 v250,v250,v32
v_mfma_f32_16x16x32_bf16 a[68:71],v[160:163],v[72:75],a[68:71]
v_exp_f32 v28,v28
v_mfma_f32_16x16x32_bf16 a[68:71],v[164:167],v[104:107],a[68:71]
ds_read_b128 v[200:203],v226 offset:47104
v_add_f32 v251,v251,v33
v_mfma_f32_16x16x32_bf16 a[84:87],v[168:171],v[72:75],a[84:87]
v_exp_f32 v29,v29
v_mfma_f32_16x16x32_bf16 a[84:87],v[172:175],v[104:107],a[84:87]
ds_read_b128 v[204:207],v226 offset:48128
v_add_f32 v250,v250,v34
v_mfma_f32_16x16x32_bf16 a[72:75],v[160:163],v[80:83],a[72:75]
v_exp_f32 v30,v30
v_mfma_f32_16x16x32_bf16 a[72:75],v[164:167],v[112:115],a[72:75]
v_exp_f32 v35,v35
v_mfma_f32_16x16x32_bf16 a[88:91],v[168:171],v[80:83],a[88:91]
v_exp_f32 v31,v31
v_mfma_f32_16x16x32_bf16 a[88:91],v[172:175],v[112:115],a[88:91]
v_exp_f32 v40,v40
v_mfma_f32_16x16x32_bf16 a[76:79],v[160:163],v[88:91],a[76:79]
v_add_f32 v251,v251,v35
v_add_f32 v252,v252,v40
v_mfma_f32_16x16x32_bf16 a[76:79],v[164:167],v[120:123],a[76:79]
v_exp_f32 v41,v41
v_mfma_f32_16x16x32_bf16 a[92:95],v[168:171],v[88:91],a[92:95]
v_add_f32 v253,v253,v41
v_cvt_pk_bf16_f32 v18,v20,v21
v_mfma_f32_16x16x32_bf16 a[92:95],v[172:175],v[120:123],a[92:95]
s_waitcnt vmcnt(0) lgkmcnt(0)
s_barrier
v_mfma_f32_16x16x32_bf16 v[36:39],v[176:179],a[144:147],v[36:39]
v_exp_f32 v42,v42
v_mfma_f32_16x16x32_bf16 v[36:39],v[180:183],a[148:151],v[36:39]
v_add_f32 v252,v252,v42
v_cvt_pk_bf16_f32 v19,v22,v23
v_mfma_f32_16x16x32_bf16 v[36:39],v[184:187],a[152:155],v[36:39]
v_exp_f32 v43,v43
v_mfma_f32_16x16x32_bf16 v[36:39],v[188:191],a[156:159],v[36:39]
v_add_f32 v253,v253,v43
v_cvt_pk_bf16_f32 v25,v26,v27
v_mfma_f32_16x16x32_bf16 v[44:47],v[176:179],a[160:163],v[132:135]
v_exp_f32 v48,v48
v_mfma_f32_16x16x32_bf16 v[44:47],v[180:183],a[164:167],v[44:47]
v_cvt_pk_bf16_f32 v26,v28,v29
v_cvt_pk_bf16_f32 v32,v32,v33
v_mfma_f32_16x16x32_bf16 v[44:47],v[184:187],a[168:171],v[44:47]
v_exp_f32 v49,v49
v_mfma_f32_16x16x32_bf16 v[44:47],v[188:191],a[172:175],v[44:47]
v_cvt_pk_bf16_f32 v27,v30,v31
v_cvt_pk_bf16_f32 v33,v34,v35
v_cvt_pk_bf16_f32 v48,v48,v49
v_mfma_f32_16x16x32_bf16 v[52:55],v[176:179],a[176:179],v[52:55]
v_exp_f32 v50,v50
v_mfma_f32_16x16x32_bf16 v[52:55],v[180:183],a[180:183],v[52:55]
v_exp_f32 v36,v36
v_mfma_f32_16x16x32_bf16 v[52:55],v[184:187],a[184:187],v[52:55]
v_exp_f32 v51,v51
v_mfma_f32_16x16x32_bf16 v[52:55],v[188:191],a[188:191],v[52:55]
v_exp_f32 v37,v37
v_cvt_pk_bf16_f32 v49,v50,v51
v_mfma_f32_16x16x32_bf16 v[60:63],v[176:179],a[192:195],v[140:143]
v_exp_f32 v56,v56
v_mfma_f32_16x16x32_bf16 v[60:63],v[180:183],a[196:199],v[60:63]
v_exp_f32 v38,v38
v_mfma_f32_16x16x32_bf16 v[60:63],v[184:187],a[200:203],v[60:63]
v_exp_f32 v57,v57
v_mfma_f32_16x16x32_bf16 v[60:63],v[188:191],a[204:207],v[60:63]
v_exp_f32 v39,v39
v_cvt_pk_bf16_f32 v56,v56,v57
v_mfma_f32_16x16x32_bf16 a[96:99],v[192:195],v[64:67],a[96:99]
v_exp_f32 v58,v58
v_mfma_f32_16x16x32_bf16 a[96:99],v[196:199],v[96:99],a[96:99]
ds_read_b128 v[160:163],v227 offset:0
v_add_f32 v250,v250,v36
v_mfma_f32_16x16x32_bf16 a[112:115],v[200:203],v[64:67],a[112:115]
v_exp_f32 v59,v59
v_mfma_f32_16x16x32_bf16 a[112:115],v[204:207],v[96:99],a[112:115]
ds_read_b128 v[164:167],v227 offset:1024
v_add_f32 v251,v251,v37
v_cvt_pk_bf16_f32 v57,v58,v59
v_mfma_f32_16x16x32_bf16 a[100:103],v[192:195],v[72:75],a[100:103]
v_exp_f32 v44,v44
v_mfma_f32_16x16x32_bf16 a[100:103],v[196:199],v[104:107],a[100:103]
ds_read_b128 v[168:171],v227 offset:2048
v_add_f32 v250,v250,v38
v_mfma_f32_16x16x32_bf16 a[116:119],v[200:203],v[72:75],a[116:119]
v_exp_f32 v45,v45
v_mfma_f32_16x16x32_bf16 a[116:119],v[204:207],v[104:107],a[116:119]
ds_read_b128 v[172:175],v227 offset:3072
v_add_f32 v251,v251,v39
v_mfma_f32_16x16x32_bf16 a[104:107],v[192:195],v[80:83],a[104:107]
v_exp_f32 v46,v46
v_mfma_f32_16x16x32_bf16 a[104:107],v[196:199],v[112:115],a[104:107]
v_exp_f32 v47,v47
v_mfma_f32_16x16x32_bf16 a[120:123],v[200:203],v[80:83],a[120:123]
v_exp_f32 v52,v52
v_mfma_f32_16x16x32_bf16 a[120:123],v[204:207],v[112:115],a[120:123]
v_exp_f32 v53,v53
v_mfma_f32_16x16x32_bf16 a[108:111],v[192:195],v[88:91],a[108:111]
v_exp_f32 v54,v54
v_mfma_f32_16x16x32_bf16 a[108:111],v[196:199],v[120:123],a[108:111]
v_exp_f32 v55,v55
v_mfma_f32_16x16x32_bf16 a[124:127],v[200:203],v[88:91],a[124:127]
v_add_f32 v252,v252,v44
v_add_f32 v253,v253,v45
v_mfma_f32_16x16x32_bf16 a[124:127],v[204:207],v[120:123],a[124:127]
v_add_f32 v252,v252,v46
v_add_f32 v253,v253,v47
v_mfma_f32_16x16x32_bf16 a[140:143],a[240:243],v[88:91],a[140:143]
v_cvt_pk_bf16_f32 v34,v36,v37
v_cvt_pk_bf16_f32 v40,v40,v41
v_mfma_f32_16x16x32_bf16 a[140:143],a[240:243],v[120:123],a[140:143]
v_cvt_pk_bf16_f32 v35,v38,v39
v_cvt_pk_bf16_f32 v41,v42,v43
v_cvt_pk_bf16_f32 v42,v44,v45
v_cvt_pk_bf16_f32 v43,v46,v47
s_branch L_OMBX_%=
L_OMBO_%=:
s_lshr_b32 s59,s83,8
s_lshl_b32 s58,s56,4
s_sub_u32 s58,s58,s59
v_add_u32 v230,s58,v229
v_cvt_f32_i32 v230,v230
v_mul_f32 v230,0x71800000,v230
s_lshl_b32 s58,s56,4
s_xor_b32 s58,s58,16
s_sub_u32 s58,s58,s59
v_add_u32 v231,s58,v229
v_cvt_f32_i32 v231,v231
v_mul_f32 v231,0x71800000,v231
s_waitcnt lgkmcnt(4)
v_add_f32 v8,0x74810000,v230
v_min_f32 v8,v8,v132
v_add_f32 v9,0x747e0000,v230
v_min_f32 v9,v9,v133
v_add_f32 v10,0x747a0000,v230
v_min_f32 v10,v10,v134
v_add_f32 v11,0x74760000,v230
v_min_f32 v11,v11,v135
v_add_f32 v24,0x74810000,v231
v_min_f32 v24,v24,v140
v_add_f32 v25,0x747e0000,v231
v_min_f32 v25,v25,v141
v_add_f32 v26,0x747a0000,v231
v_min_f32 v26,v26,v142
v_add_f32 v27,0x74760000,v231
v_min_f32 v27,v27,v143
s_nop 1
v_mfma_f32_16x16x32_bf16 v[8:11],v[144:147],a[160:163],v[8:11]
v_exp_f32 v124,v124
v_mfma_f32_16x16x32_bf16 v[8:11],v[148:151],a[164:167],v[8:11]
v_exp_f32 v125,v125
ds_read_b128 v[176:179],v224 offset:4096
v_mfma_f32_16x16x32_bf16 v[8:11],v[152:155],a[168:171],v[8:11]
v_exp_f32 v126,v126
ds_read_b128 v[180:183],v224 offset:5120
v_mfma_f32_16x16x32_bf16 v[8:11],v[156:159],a[172:175],v[8:11]
v_exp_f32 v127,v127
ds_read_b128 v[184:187],v224 offset:6144
v_mfma_f32_16x16x32_bf16 v[24:27],v[144:147],a[192:195],v[24:27]
v_cvt_pk_bf16_f32 v114,v116,v117
v_cvt_pk_bf16_f32 v115,v118,v119
ds_read_b128 v[188:191],v224 offset:7168
v_mfma_f32_16x16x32_bf16 v[24:27],v[148:151],a[196:199],v[24:27]
v_cvt_pk_bf16_f32 v122,v124,v125
v_cvt_pk_bf16_f32 v123,v126,v127
v_mfma_f32_16x16x32_bf16 v[24:27],v[152:155],a[200:203],v[24:27]
v_add_f32 v12,0x74420000,v230
v_min_f32 v12,v12,v132
v_mfma_f32_16x16x32_bf16 v[24:27],v[156:159],a[204:207],v[24:27]
s_waitcnt lgkmcnt(4)
v_mfma_f32_16x16x32_bf16 a[0:3],v[160:163],v[64:67],a[0:3]
v_add_f32 v13,0x743e0000,v230
v_min_f32 v13,v13,v133
v_mfma_f32_16x16x32_bf16 a[0:3],v[164:167],v[96:99],a[0:3]
v_add_f32 v14,0x743a0000,v230
v_min_f32 v14,v14,v134
v_exp_f32 v8,v8
v_mfma_f32_16x16x32_bf16 a[16:19],v[168:171],v[64:67],a[16:19]
v_add_f32 v15,0x74360000,v230
v_min_f32 v15,v15,v135
v_mfma_f32_16x16x32_bf16 a[16:19],v[172:175],v[96:99],a[16:19]
v_add_f32 v28,0x74420000,v231
v_min_f32 v28,v28,v140
v_exp_f32 v9,v9
v_mfma_f32_16x16x32_bf16 a[4:7],v[160:163],v[72:75],a[4:7]
v_add_f32 v29,0x743e0000,v231
v_min_f32 v29,v29,v141
v_mfma_f32_16x16x32_bf16 a[4:7],v[164:167],v[104:107],a[4:7]
v_add_f32 v30,0x743a0000,v231
v_min_f32 v30,v30,v142
v_exp_f32 v10,v10
v_mfma_f32_16x16x32_bf16 a[20:23],v[168:171],v[72:75],a[20:23]
v_add_f32 v31,0x74360000,v231
v_min_f32 v31,v31,v143
v_mfma_f32_16x16x32_bf16 a[20:23],v[172:175],v[104:107],a[20:23]
ds_read_b128 v[192:195],v226 offset:36864
ds_read_b128 v[196:199],v226 offset:37888
v_exp_f32 v11,v11
v_mfma_f32_16x16x32_bf16 a[8:11],v[160:163],v[80:83],a[8:11]
ds_read_b128 v[200:203],v226 offset:38912
ds_read_b128 v[204:207],v226 offset:39936
v_mfma_f32_16x16x32_bf16 a[8:11],v[164:167],v[112:115],a[8:11]
v_mov_b32 v0,0
v_mov_b32 v1,0
v_exp_f32 v24,v24
v_mfma_f32_16x16x32_bf16 a[24:27],v[168:171],v[80:83],a[24:27]
v_mov_b32 v2,0
v_mov_b32 v3,0
v_mfma_f32_16x16x32_bf16 a[24:27],v[172:175],v[112:115],a[24:27]
v_mov_b32 v4,0
v_mov_b32 v5,0
v_exp_f32 v25,v25
v_mfma_f32_16x16x32_bf16 a[12:15],v[160:163],v[88:91],a[12:15]
v_add_f32 v40,0x74020000,v230
v_min_f32 v40,v40,v132
v_mfma_f32_16x16x32_bf16 a[12:15],v[164:167],v[120:123],a[12:15]
v_add_f32 v41,0x73fc0000,v230
v_min_f32 v41,v41,v133
v_exp_f32 v26,v26
v_mfma_f32_16x16x32_bf16 a[28:31],v[168:171],v[88:91],a[28:31]
v_add_f32 v42,0x73f40000,v230
v_min_f32 v42,v42,v134
v_mfma_f32_16x16x32_bf16 a[28:31],v[172:175],v[120:123],a[28:31]
v_mov_b32 v6,0
v_exp_f32 v27,v27
s_waitcnt lgkmcnt(4)
v_mfma_f32_16x16x32_bf16 v[12:15],v[176:179],a[160:163],v[12:15]
v_add_f32 v43,0x73ec0000,v230
v_min_f32 v43,v43,v135
v_mfma_f32_16x16x32_bf16 v[12:15],v[180:183],a[164:167],v[12:15]
v_add_f32 v56,0x74020000,v231
v_min_f32 v56,v56,v140
v_mfma_f32_16x16x32_bf16 v[12:15],v[184:187],a[168:171],v[12:15]
v_add_f32 v57,0x73fc0000,v231
v_min_f32 v57,v57,v141
v_mfma_f32_16x16x32_bf16 v[12:15],v[188:191],a[172:175],v[12:15]
v_add_f32 v58,0x73f40000,v231
v_min_f32 v58,v58,v142
ds_read_b128 v[152:155],v224 offset:10240
v_mfma_f32_16x16x32_bf16 v[28:31],v[176:179],a[192:195],v[28:31]
v_add_f32 v59,0x73ec0000,v231
v_min_f32 v59,v59,v143
ds_read_b128 v[156:159],v224 offset:11264
v_mfma_f32_16x16x32_bf16 v[28:31],v[180:183],a[196:199],v[28:31]
ds_read_b128 v[144:147],v224 offset:8192
ds_read_b128 v[148:151],v224 offset:9216
v_mfma_f32_16x16x32_bf16 v[28:31],v[184:187],a[200:203],v[28:31]
v_mov_b32 v7,0
v_mov_b32 v32,0
)"
R"(v_mfma_f32_16x16x32_bf16 v[28:31],v[188:191],a[204:207],v[28:31]
v_mov_b32 v33,0
s_waitcnt lgkmcnt(4)
v_mfma_f32_16x16x32_bf16 a[32:35],v[192:195],v[64:67],a[32:35]
v_mov_b32 v34,0
v_mov_b32 v35,0
v_mfma_f32_16x16x32_bf16 a[32:35],v[196:199],v[96:99],a[32:35]
ds_read_b128 v[160:163],v226 offset:40960
v_mov_b32 v36,0
v_exp_f32 v12,v12
v_mfma_f32_16x16x32_bf16 a[48:51],v[200:203],v[64:67],a[48:51]
v_mov_b32 v37,0
v_mov_b32 v38,0
v_mfma_f32_16x16x32_bf16 a[48:51],v[204:207],v[96:99],a[48:51]
ds_read_b128 v[164:167],v226 offset:41984
v_mov_b32 v39,0
v_exp_f32 v13,v13
v_mfma_f32_16x16x32_bf16 a[36:39],v[192:195],v[72:75],a[36:39]
v_mov_b32 v16,0
v_mov_b32 v17,0
v_mfma_f32_16x16x32_bf16 a[36:39],v[196:199],v[104:107],a[36:39]
ds_read_b128 v[168:171],v226 offset:43008
v_mov_b32 v18,0
v_exp_f32 v14,v14
v_mfma_f32_16x16x32_bf16 a[52:55],v[200:203],v[72:75],a[52:55]
v_mov_b32 v19,0
v_mov_b32 v20,0
v_mfma_f32_16x16x32_bf16 a[52:55],v[204:207],v[104:107],a[52:55]
ds_read_b128 v[172:175],v226 offset:44032
v_mov_b32 v21,0
v_exp_f32 v15,v15
v_mfma_f32_16x16x32_bf16 a[40:43],v[192:195],v[80:83],a[40:43]
v_mov_b32 v22,0
v_mov_b32 v23,0
v_mfma_f32_16x16x32_bf16 a[40:43],v[196:199],v[112:115],a[40:43]
v_mov_b32 v48,0
v_mov_b32 v49,0
v_exp_f32 v28,v28
v_mfma_f32_16x16x32_bf16 a[56:59],v[200:203],v[80:83],a[56:59]
v_mov_b32 v50,0
v_mov_b32 v51,0
v_mfma_f32_16x16x32_bf16 a[56:59],v[204:207],v[112:115],a[56:59]
v_mov_b32 v52,0
v_mov_b32 v53,0
v_exp_f32 v29,v29
v_mfma_f32_16x16x32_bf16 a[44:47],v[192:195],v[88:91],a[44:47]
v_add_f32 v44,0x73840000,v230
v_min_f32 v44,v44,v132
v_mfma_f32_16x16x32_bf16 a[44:47],v[196:199],v[120:123],a[44:47]
v_add_f32 v45,0x73780000,v230
v_min_f32 v45,v45,v133
v_exp_f32 v30,v30
v_mfma_f32_16x16x32_bf16 a[60:63],v[200:203],v[88:91],a[60:63]
v_add_f32 v46,0x73680000,v230
v_min_f32 v46,v46,v134
v_mfma_f32_16x16x32_bf16 a[60:63],v[204:207],v[120:123],a[60:63]
v_add_f32 v47,0x73580000,v230
v_min_f32 v47,v47,v135
v_exp_f32 v31,v31
v_mfma_f32_16x16x32_bf16 a[136:139],a[240:243],v[80:83],a[136:139]
v_add_f32 v60,0x73840000,v231
v_min_f32 v60,v60,v140
v_mfma_f32_16x16x32_bf16 a[136:139],a[240:243],v[112:115],a[136:139]
v_mov_b32 v54,0
s_waitcnt lgkmcnt(4)
v_mfma_f32_16x16x32_bf16 v[40:43],v[144:147],a[160:163],v[40:43]
v_add_f32 v61,0x73780000,v231
v_min_f32 v61,v61,v141
v_mfma_f32_16x16x32_bf16 v[40:43],v[148:151],a[164:167],v[40:43]
v_add_f32 v62,0x73680000,v231
v_min_f32 v62,v62,v142
v_mfma_f32_16x16x32_bf16 v[40:43],v[152:155],a[168:171],v[40:43]
v_add_f32 v63,0x73580000,v231
v_min_f32 v63,v63,v143
v_mfma_f32_16x16x32_bf16 v[40:43],v[156:159],a[172:175],v[40:43]
ds_read_b128 v[176:179],v224 offset:12288
ds_read_b128 v[180:183],v224 offset:13312
v_mfma_f32_16x16x32_bf16 v[56:59],v[144:147],a[192:195],v[56:59]
ds_read_b128 v[184:187],v224 offset:14336
ds_read_b128 v[188:191],v224 offset:15360
v_mfma_f32_16x16x32_bf16 v[56:59],v[148:151],a[196:199],v[56:59]
v_mov_b32 v55,0
v_add_f32 v252,v252,v8
v_mfma_f32_16x16x32_bf16 v[56:59],v[152:155],a[200:203],v[56:59]
v_add_f32 v253,v253,v9
v_add_f32 v252,v252,v10
v_mfma_f32_16x16x32_bf16 v[56:59],v[156:159],a[204:207],v[56:59]
v_add_f32 v253,v253,v11
s_waitcnt lgkmcnt(4)
v_mfma_f32_16x16x32_bf16 a[64:67],v[160:163],v[64:67],a[64:67]
v_add_f32 v252,v252,v12
v_add_f32 v253,v253,v13
v_mfma_f32_16x16x32_bf16 a[64:67],v[164:167],v[96:99],a[64:67]
ds_read_b128 v[192:195],v226 offset:45056
v_add_f32 v252,v252,v14
v_mfma_f32_16x16x32_bf16 a[80:83],v[168:171],v[64:67],a[80:83]
v_exp_f32 v40,v40
v_mfma_f32_16x16x32_bf16 a[80:83],v[172:175],v[96:99],a[80:83]
ds_read_b128 v[196:199],v226 offset:46080
v_add_f32 v253,v253,v15
v_mfma_f32_16x16x32_bf16 a[68:71],v[160:163],v[72:75],a[68:71]
v_exp_f32 v41,v41
v_mfma_f32_16x16x32_bf16 a[68:71],v[164:167],v[104:107],a[68:71]
ds_read_b128 v[200:203],v226 offset:47104
v_add_f32 v252,v252,v40
v_mfma_f32_16x16x32_bf16 a[84:87],v[168:171],v[72:75],a[84:87]
v_exp_f32 v42,v42
v_mfma_f32_16x16x32_bf16 a[84:87],v[172:175],v[104:107],a[84:87]
ds_read_b128 v[204:207],v226 offset:48128
v_add_f32 v253,v253,v41
v_mfma_f32_16x16x32_bf16 a[72:75],v[160:163],v[80:83],a[72:75]
v_exp_f32 v43,v43
v_mfma_f32_16x16x32_bf16 a[72:75],v[164:167],v[112:115],a[72:75]
v_add_f32 v252,v252,v42
v_add_f32 v253,v253,v43
v_mfma_f32_16x16x32_bf16 a[88:91],v[168:171],v[80:83],a[88:91]
v_exp_f32 v56,v56
v_mfma_f32_16x16x32_bf16 a[88:91],v[172:175],v[112:115],a[88:91]
v_cvt_pk_bf16_f32 v8,v8,v9
v_cvt_pk_bf16_f32 v24,v24,v25
v_mfma_f32_16x16x32_bf16 a[76:79],v[160:163],v[88:91],a[76:79]
v_exp_f32 v57,v57
v_mfma_f32_16x16x32_bf16 a[76:79],v[164:167],v[120:123],a[76:79]
v_cvt_pk_bf16_f32 v9,v10,v11
v_cvt_pk_bf16_f32 v25,v26,v27
v_mfma_f32_16x16x32_bf16 a[92:95],v[168:171],v[88:91],a[92:95]
v_exp_f32 v58,v58
v_mfma_f32_16x16x32_bf16 a[92:95],v[172:175],v[120:123],a[92:95]
s_waitcnt vmcnt(0) lgkmcnt(0)
s_barrier
v_mfma_f32_16x16x32_bf16 v[44:47],v[176:179],a[160:163],v[44:47]
v_exp_f32 v59,v59
v_mfma_f32_16x16x32_bf16 v[44:47],v[180:183],a[164:167],v[44:47]
v_cvt_pk_bf16_f32 v10,v12,v13
v_cvt_pk_bf16_f32 v26,v28,v29
v_mfma_f32_16x16x32_bf16 v[44:47],v[184:187],a[168:171],v[44:47]
v_cvt_pk_bf16_f32 v11,v14,v15
v_cvt_pk_bf16_f32 v27,v30,v31
v_mfma_f32_16x16x32_bf16 v[44:47],v[188:191],a[172:175],v[44:47]
v_cvt_pk_bf16_f32 v40,v40,v41
v_cvt_pk_bf16_f32 v56,v56,v57
v_mfma_f32_16x16x32_bf16 v[60:63],v[176:179],a[192:195],v[60:63]
v_cvt_pk_bf16_f32 v41,v42,v43
v_cvt_pk_bf16_f32 v57,v58,v59
v_mfma_f32_16x16x32_bf16 v[60:63],v[180:183],a[196:199],v[60:63]
v_mfma_f32_16x16x32_bf16 v[60:63],v[184:187],a[200:203],v[60:63]
v_mfma_f32_16x16x32_bf16 v[60:63],v[188:191],a[204:207],v[60:63]
v_mfma_f32_16x16x32_bf16 a[96:99],v[192:195],v[64:67],a[96:99]
v_mfma_f32_16x16x32_bf16 a[96:99],v[196:199],v[96:99],a[96:99]
ds_read_b128 v[160:163],v227 offset:0
v_mfma_f32_16x16x32_bf16 a[112:115],v[200:203],v[64:67],a[112:115]
v_exp_f32 v44,v44
v_mfma_f32_16x16x32_bf16 a[112:115],v[204:207],v[96:99],a[112:115]
ds_read_b128 v[164:167],v227 offset:1024
v_add_f32 v252,v252,v44
v_mfma_f32_16x16x32_bf16 a[100:103],v[192:195],v[72:75],a[100:103]
v_exp_f32 v45,v45
v_mfma_f32_16x16x32_bf16 a[100:103],v[196:199],v[104:107],a[100:103]
ds_read_b128 v[168:171],v227 offset:2048
v_add_f32 v253,v253,v45
v_mfma_f32_16x16x32_bf16 a[116:119],v[200:203],v[72:75],a[116:119]
v_exp_f32 v46,v46
v_mfma_f32_16x16x32_bf16 a[116:119],v[204:207],v[104:107],a[116:119]
ds_read_b128 v[172:175],v227 offset:3072
v_add_f32 v252,v252,v46
v_mfma_f32_16x16x32_bf16 a[104:107],v[192:195],v[80:83],a[104:107]
v_exp_f32 v47,v47
v_mfma_f32_16x16x32_bf16 a[104:107],v[196:199],v[112:115],a[104:107]
v_add_f32 v253,v253,v47
v_cvt_pk_bf16_f32 v42,v44,v45
v_mfma_f32_16x16x32_bf16 a[120:123],v[200:203],v[80:83],a[120:123]
v_cvt_pk_bf16_f32 v43,v46,v47
v_mfma_f32_16x16x32_bf16 a[120:123],v[204:207],v[112:115],a[120:123]
v_mfma_f32_16x16x32_bf16 a[108:111],v[192:195],v[88:91],a[108:111]
v_mfma_f32_16x16x32_bf16 a[108:111],v[196:199],v[120:123],a[108:111]
v_mfma_f32_16x16x32_bf16 a[124:127],v[200:203],v[88:91],a[124:127]
v_mfma_f32_16x16x32_bf16 a[124:127],v[204:207],v[120:123],a[124:127]
v_mfma_f32_16x16x32_bf16 a[140:143],a[240:243],v[88:91],a[140:143]
v_mfma_f32_16x16x32_bf16 a[140:143],a[240:243],v[120:123],a[140:143]
L_OMBX_%=:
s_cmp_eq_u32 s84,0
s_cbranch_scc1 L_FINAL_%=
s_mov_b32 s84,2
L_FIN_%=:
s_nop 4
s_add_u32 s59,s85,0
s_add_u32 s58,s85,16384
v_accvgpr_read_b32 v246,a136
v_cmp_nlt_f32 vcc,v246,v235
v_rcp_f32 v246,v246
v_mfma_f32_16x16x32_bf16 a[136:139],a[244:247],a[244:247],0
s_or_b64 s[88:89],s[88:89],vcc
v_accvgpr_read_b32 v176,a8
v_accvgpr_read_b32 v177,a9
v_accvgpr_read_b32 v178,a10
v_accvgpr_read_b32 v179,a11
v_mfma_f32_16x16x32_bf16 a[8:11],a[244:247],a[244:247],0
v_accvgpr_read_b32 v180,a24
v_accvgpr_read_b32 v181,a25
v_accvgpr_read_b32 v182,a26
v_accvgpr_read_b32 v183,a27
v_mfma_f32_16x16x32_bf16 a[24:27],a[244:247],a[244:247],0
v_mul_f32 v176,v176,v246
v_mul_f32 v177,v177,v246
v_mul_f32 v178,v178,v246
v_mul_f32 v179,v179,v246
v_mul_f32 v180,v180,v246
v_mul_f32 v181,v181,v246
v_mul_f32 v182,v182,v246
v_mul_f32 v183,v183,v246
v_cvt_pk_bf16_f32 v184,v176,v177
v_cvt_pk_bf16_f32 v185,v178,v179
v_cvt_pk_bf16_f32 v186,v180,v181
v_cvt_pk_bf16_f32 v187,v182,v183
buffer_store_dwordx4 v[184:187],v255,s[40:43],s59 offen offset:0 sc1
v_accvgpr_read_b32 v192,a40
v_accvgpr_read_b32 v193,a41
v_accvgpr_read_b32 v194,a42
v_accvgpr_read_b32 v195,a43
v_mfma_f32_16x16x32_bf16 a[40:43],a[244:247],a[244:247],0
v_accvgpr_read_b32 v196,a56
v_accvgpr_read_b32 v197,a57
v_accvgpr_read_b32 v198,a58
v_accvgpr_read_b32 v199,a59
v_mfma_f32_16x16x32_bf16 a[56:59],a[244:247],a[244:247],0
v_mul_f32 v192,v192,v246
v_mul_f32 v193,v193,v246
v_mul_f32 v194,v194,v246
v_mul_f32 v195,v195,v246
v_mul_f32 v196,v196,v246
v_mul_f32 v197,v197,v246
v_mul_f32 v198,v198,v246
v_mul_f32 v199,v199,v246
v_cvt_pk_bf16_f32 v200,v192,v193
v_cvt_pk_bf16_f32 v201,v194,v195
v_cvt_pk_bf16_f32 v202,v196,v197
v_cvt_pk_bf16_f32 v203,v198,v199
buffer_store_dwordx4 v[200:203],v255,s[40:43],s59 offen offset:1024 sc1
v_accvgpr_read_b32 v176,a72
v_accvgpr_read_b32 v177,a73
v_accvgpr_read_b32 v178,a74
v_accvgpr_read_b32 v179,a75
v_mfma_f32_16x16x32_bf16 a[72:75],a[244:247],a[244:247],0
v_accvgpr_read_b32 v180,a88
v_accvgpr_read_b32 v181,a89
v_accvgpr_read_b32 v182,a90
v_accvgpr_read_b32 v183,a91
v_mfma_f32_16x16x32_bf16 a[88:91],a[244:247],a[244:247],0
v_mul_f32 v176,v176,v246
v_mul_f32 v177,v177,v246
v_mul_f32 v178,v178,v246
v_mul_f32 v179,v179,v246
v_mul_f32 v180,v180,v246
v_mul_f32 v181,v181,v246
v_mul_f32 v182,v182,v246
v_mul_f32 v183,v183,v246
v_cvt_pk_bf16_f32 v184,v176,v177
v_cvt_pk_bf16_f32 v185,v178,v179
v_cvt_pk_bf16_f32 v186,v180,v181
v_cvt_pk_bf16_f32 v187,v182,v183
buffer_store_dwordx4 v[184:187],v255,s[40:43],s58 offen offset:0 sc1
v_accvgpr_read_b32 v192,a104
v_accvgpr_read_b32 v193,a105
v_accvgpr_read_b32 v194,a106
v_accvgpr_read_b32 v195,a107
v_mfma_f32_16x16x32_bf16 a[104:107],a[244:247],a[244:247],0
v_accvgpr_read_b32 v196,a120
v_accvgpr_read_b32 v197,a121
v_accvgpr_read_b32 v198,a122
v_accvgpr_read_b32 v199,a123
v_mfma_f32_16x16x32_bf16 a[120:123],a[244:247],a[244:247],0
v_mul_f32 v192,v192,v246
v_mul_f32 v193,v193,v246
v_mul_f32 v194,v194,v246
v_mul_f32 v195,v195,v246
v_mul_f32 v196,v196,v246
v_mul_f32 v197,v197,v246
v_mul_f32 v198,v198,v246
v_mul_f32 v199,v199,v246
v_cvt_pk_bf16_f32 v200,v192,v193
v_cvt_pk_bf16_f32 v201,v194,v195
v_cvt_pk_bf16_f32 v202,v196,v197
v_cvt_pk_bf16_f32 v203,v198,v199
buffer_store_dwordx4 v[200:203],v255,s[40:43],s58 offen offset:1024 sc1
s_add_u32 s59,s85,8192
s_add_u32 s58,s85,24576
v_accvgpr_read_b32 v248,a140
v_cmp_nlt_f32 vcc,v248,v235
v_rcp_f32 v248,v248
v_mfma_f32_16x16x32_bf16 a[140:143],a[244:247],a[244:247],0
s_or_b64 s[88:89],s[88:89],vcc
v_accvgpr_read_b32 v176,a12
v_accvgpr_read_b32 v177,a13
v_accvgpr_read_b32 v178,a14
v_accvgpr_read_b32 v179,a15
v_mfma_f32_16x16x32_bf16 a[12:15],a[244:247],a[244:247],0
v_accvgpr_read_b32 v180,a28
v_accvgpr_read_b32 v181,a29
v_accvgpr_read_b32 v182,a30
v_accvgpr_read_b32 v183,a31
v_mfma_f32_16x16x32_bf16 a[28:31],a[244:247],a[244:247],0
v_mul_f32 v176,v176,v248
v_mul_f32 v177,v177,v248
v_mul_f32 v178,v178,v248
v_mul_f32 v179,v179,v248
v_mul_f32 v180,v180,v248
v_mul_f32 v181,v181,v248
v_mul_f32 v182,v182,v248
v_mul_f32 v183,v183,v248
v_cvt_pk_bf16_f32 v184,v176,v177
v_cvt_pk_bf16_f32 v185,v178,v179
v_cvt_pk_bf16_f32 v186,v180,v181
v_cvt_pk_bf16_f32 v187,v182,v183
buffer_store_dwordx4 v[184:187],v255,s[40:43],s59 offen offset:0 sc1
v_accvgpr_read_b32 v192,a44
v_accvgpr_read_b32 v193,a45
v_accvgpr_read_b32 v194,a46
v_accvgpr_read_b32 v195,a47
v_mfma_f32_16x16x32_bf16 a[44:47],a[244:247],a[244:247],0
v_accvgpr_read_b32 v196,a60
v_accvgpr_read_b32 v197,a61
v_accvgpr_read_b32 v198,a62
v_accvgpr_read_b32 v199,a63
v_mfma_f32_16x16x32_bf16 a[60:63],a[244:247],a[244:247],0
v_mul_f32 v192,v192,v248
v_mul_f32 v193,v193,v248
v_mul_f32 v194,v194,v248
v_mul_f32 v195,v195,v248
v_mul_f32 v196,v196,v248
v_mul_f32 v197,v197,v248
v_mul_f32 v198,v198,v248
v_mul_f32 v199,v199,v248
v_cvt_pk_bf16_f32 v200,v192,v193
v_cvt_pk_bf16_f32 v201,v194,v195
v_cvt_pk_bf16_f32 v202,v196,v197
v_cvt_pk_bf16_f32 v203,v198,v199
buffer_store_dwordx4 v[200:203],v255,s[40:43],s59 offen offset:1024 sc1
v_accvgpr_read_b32 v176,a76
v_accvgpr_read_b32 v177,a77
v_accvgpr_read_b32 v178,a78
v_accvgpr_read_b32 v179,a79
v_mfma_f32_16x16x32_bf16 a[76:79],a[244:247],a[244:247],0
v_accvgpr_read_b32 v180,a92
v_accvgpr_read_b32 v181,a93
v_accvgpr_read_b32 v182,a94
v_accvgpr_read_b32 v183,a95
v_mfma_f32_16x16x32_bf16 a[92:95],a[244:247],a[244:247],0
v_mul_f32 v176,v176,v248
v_mul_f32 v177,v177,v248
v_mul_f32 v178,v178,v248
v_mul_f32 v179,v179,v248
v_mul_f32 v180,v180,v248
v_mul_f32 v181,v181,v248
v_mul_f32 v182,v182,v248
v_mul_f32 v183,v183,v248
v_cvt_pk_bf16_f32 v184,v176,v177
v_cvt_pk_bf16_f32 v185,v178,v179
v_cvt_pk_bf16_f32 v186,v180,v181
v_cvt_pk_bf16_f32 v187,v182,v183
buffer_store_dwordx4 v[184:187],v255,s[40:43],s58 offen offset:0 sc1
v_accvgpr_read_b32 v192,a108
v_accvgpr_read_b32 v193,a109
v_accvgpr_read_b32 v194,a110
v_accvgpr_read_b32 v195,a111
v_mfma_f32_16x16x32_bf16 a[108:111],a[244:247],a[244:247],0
v_accvgpr_read_b32 v196,a124
v_accvgpr_read_b32 v197,a125
v_accvgpr_read_b32 v198,a126
v_accvgpr_read_b32 v199,a127
v_mfma_f32_16x16x32_bf16 a[124:127],a[244:247],a[244:247],0
v_mul_f32 v192,v192,v248
v_mul_f32 v193,v193,v248
v_mul_f32 v194,v194,v248
v_mul_f32 v195,v195,v248
v_mul_f32 v196,v196,v248
v_mul_f32 v197,v197,v248
v_mul_f32 v198,v198,v248
v_mul_f32 v199,v199,v248
v_cvt_pk_bf16_f32 v200,v192,v193
v_cvt_pk_bf16_f32 v201,v194,v195
v_cvt_pk_bf16_f32 v202,v196,v197
v_cvt_pk_bf16_f32 v203,v198,v199
buffer_store_dwordx4 v[200:203],v255,s[40:43],s58 offen offset:1024 sc1
v_add_u32 v226,s83,v226
s_nop 0
s_cmp_eq_u32 s84,1
s_mov_b32 s84,0
s_cbranch_scc1 L_E2_%=
L_FINAL_%=:
ds_read_b128 v[176:179],v226 offset:4096
ds_read_b128 v[180:183],v226 offset:5120
ds_read_b128 v[184:187],v226 offset:6144
ds_read_b128 v[188:191],v226 offset:7168
ds_read_b128 v[192:195],v226 offset:8192
ds_read_b128 v[196:199],v226 offset:9216
ds_read_b128 v[200:203],v226 offset:10240
ds_read_b128 v[204:207],v226 offset:11264
ds_read_b128 v[144:147],v226 offset:12288
ds_read_b128 v[148:151],v226 offset:13312
ds_read_b128 v[152:155],v226 offset:14336
)"
R"(ds_read_b128 v[156:159],v226 offset:15360
v_exp_f32 v60,v60
v_exp_f32 v61,v61
v_exp_f32 v62,v62
v_exp_f32 v63,v63
s_lshl_b32 s58,s56,14
s_add_u32 s58,s58,s57
v_add_u32 v246,s58,v255
v_xor_b32 v247,0x4000,v246
v_xor_b32 v249,0x200,v234
v_cvt_pk_bf16_f32 v50,v52,v53
v_cvt_pk_bf16_f32 v51,v54,v55
v_cvt_pk_bf16_f32 v58,v60,v61
v_cvt_pk_bf16_f32 v59,v62,v63
s_nop 1
s_waitcnt lgkmcnt(12)
v_mfma_f32_16x16x32_bf16 a[8:11],v[160:163],v[16:19],a[8:11]
v_mfma_f32_16x16x32_bf16 a[8:11],v[164:167],v[48:51],a[8:11]
v_mfma_f32_16x16x32_bf16 a[24:27],v[168:171],v[16:19],a[24:27]
v_add_f32 v250,v250,v251
v_mfma_f32_16x16x32_bf16 a[24:27],v[172:175],v[48:51],a[24:27]
v_add_f32 v252,v252,v253
v_mfma_f32_16x16x32_bf16 a[12:15],v[160:163],v[24:27],a[12:15]
v_mfma_f32_16x16x32_bf16 a[12:15],v[164:167],v[56:59],a[12:15]
v_mov_b32 v251,v250
v_mfma_f32_16x16x32_bf16 a[28:31],v[168:171],v[24:27],a[28:31]
v_mov_b32 v253,v252
v_mfma_f32_16x16x32_bf16 a[28:31],v[172:175],v[56:59],a[28:31]
s_waitcnt lgkmcnt(8)
v_mfma_f32_16x16x32_bf16 a[40:43],v[176:179],v[16:19],a[40:43]
v_permlane32_swap_b32 v250,v251
v_mfma_f32_16x16x32_bf16 a[40:43],v[180:183],v[48:51],a[40:43]
v_permlane32_swap_b32 v252,v253
v_mfma_f32_16x16x32_bf16 a[56:59],v[184:187],v[16:19],a[56:59]
v_mfma_f32_16x16x32_bf16 a[56:59],v[188:191],v[48:51],a[56:59]
v_add_f32 v250,v250,v251
v_mfma_f32_16x16x32_bf16 a[44:47],v[176:179],v[24:27],a[44:47]
v_add_f32 v252,v252,v253
v_mfma_f32_16x16x32_bf16 a[44:47],v[180:183],v[56:59],a[44:47]
v_mfma_f32_16x16x32_bf16 a[60:63],v[184:187],v[24:27],a[60:63]
v_mov_b32 v251,v250
v_mfma_f32_16x16x32_bf16 a[60:63],v[188:191],v[56:59],a[60:63]
v_mov_b32 v253,v252
s_waitcnt lgkmcnt(4)
v_mfma_f32_16x16x32_bf16 a[72:75],v[192:195],v[16:19],a[72:75]
v_mfma_f32_16x16x32_bf16 a[72:75],v[196:199],v[48:51],a[72:75]
v_permlane16_swap_b32 v250,v251
v_mfma_f32_16x16x32_bf16 a[88:91],v[200:203],v[16:19],a[88:91]
v_permlane16_swap_b32 v252,v253
v_mfma_f32_16x16x32_bf16 a[88:91],v[204:207],v[48:51],a[88:91]
v_mfma_f32_16x16x32_bf16 a[76:79],v[192:195],v[24:27],a[76:79]
v_add_f32 v250,v250,v251
v_mfma_f32_16x16x32_bf16 a[76:79],v[196:199],v[56:59],a[76:79]
v_add_f32 v252,v252,v253
v_mfma_f32_16x16x32_bf16 a[92:95],v[200:203],v[24:27],a[92:95]
v_mfma_f32_16x16x32_bf16 a[92:95],v[204:207],v[56:59],a[92:95]
s_waitcnt lgkmcnt(0)
v_mfma_f32_16x16x32_bf16 a[104:107],v[144:147],v[16:19],a[104:107]
v_mfma_f32_16x16x32_bf16 a[104:107],v[148:151],v[48:51],a[104:107]
v_mfma_f32_16x16x32_bf16 a[120:123],v[152:155],v[16:19],a[120:123]
v_mfma_f32_16x16x32_bf16 a[120:123],v[156:159],v[48:51],a[120:123]
v_mfma_f32_16x16x32_bf16 a[108:111],v[144:147],v[24:27],a[108:111]
v_mfma_f32_16x16x32_bf16 a[108:111],v[148:151],v[56:59],a[108:111]
v_mfma_f32_16x16x32_bf16 a[124:127],v[152:155],v[24:27],a[124:127]
v_mfma_f32_16x16x32_bf16 a[124:127],v[156:159],v[56:59],a[124:127]
v_mfma_f32_16x16x32_bf16 a[136:139],a[240:243],v[16:19],a[136:139]
v_mfma_f32_16x16x32_bf16 a[136:139],a[240:243],v[48:51],a[136:139]
v_mfma_f32_16x16x32_bf16 a[140:143],a[240:243],v[24:27],a[140:143]
ds_write_b128 v246,a[8:11] offset:0
v_mfma_f32_16x16x32_bf16 a[140:143],a[240:243],v[56:59],a[140:143]
v_mfma_f32_16x16x32_bf16 a[0:3],v[160:163],v[0:3],a[0:3]
ds_write_b128 v246,a[12:15] offset:1024
v_mfma_f32_16x16x32_bf16 a[0:3],v[164:167],v[32:35],a[0:3]
v_mfma_f32_16x16x32_bf16 a[16:19],v[168:171],v[0:3],a[16:19]
ds_write_b128 v246,a[24:27] offset:2048
v_mfma_f32_16x16x32_bf16 a[16:19],v[172:175],v[32:35],a[16:19]
v_mfma_f32_16x16x32_bf16 a[4:7],v[160:163],v[8:11],a[4:7]
ds_write_b128 v246,a[28:31] offset:3072
v_mfma_f32_16x16x32_bf16 a[4:7],v[164:167],v[40:43],a[4:7]
ds_write_b32 v234,a136 offset:4096
v_mfma_f32_16x16x32_bf16 a[20:23],v[168:171],v[8:11],a[20:23]
ds_write_b128 v246,a[40:43] offset:4096
v_mfma_f32_16x16x32_bf16 a[20:23],v[172:175],v[40:43],a[20:23]
ds_write_b32 v234,a140 offset:4352
v_mfma_f32_16x16x32_bf16 a[32:35],v[176:179],v[0:3],a[32:35]
ds_write_b128 v246,a[44:47] offset:5120
v_mfma_f32_16x16x32_bf16 a[32:35],v[180:183],v[32:35],a[32:35]
v_mfma_f32_16x16x32_bf16 a[48:51],v[184:187],v[0:3],a[48:51]
ds_write_b128 v246,a[56:59] offset:6144
v_mfma_f32_16x16x32_bf16 a[48:51],v[188:191],v[32:35],a[48:51]
v_mfma_f32_16x16x32_bf16 a[36:39],v[176:179],v[8:11],a[36:39]
ds_write_b128 v246,a[60:63] offset:7168
v_mfma_f32_16x16x32_bf16 a[36:39],v[180:183],v[40:43],a[36:39]
v_mfma_f32_16x16x32_bf16 a[52:55],v[184:187],v[8:11],a[52:55]
ds_write_b128 v246,a[72:75] offset:8192
v_mfma_f32_16x16x32_bf16 a[52:55],v[188:191],v[40:43],a[52:55]
v_mfma_f32_16x16x32_bf16 a[64:67],v[192:195],v[0:3],a[64:67]
ds_write_b128 v246,a[76:79] offset:9216
v_mfma_f32_16x16x32_bf16 a[64:67],v[196:199],v[32:35],a[64:67]
v_mfma_f32_16x16x32_bf16 a[80:83],v[200:203],v[0:3],a[80:83]
ds_write_b128 v246,a[88:91] offset:10240
v_mfma_f32_16x16x32_bf16 a[80:83],v[204:207],v[32:35],a[80:83]
v_mfma_f32_16x16x32_bf16 a[68:71],v[192:195],v[8:11],a[68:71]
ds_write_b128 v246,a[92:95] offset:11264
v_mfma_f32_16x16x32_bf16 a[68:71],v[196:199],v[40:43],a[68:71]
v_mfma_f32_16x16x32_bf16 a[84:87],v[200:203],v[8:11],a[84:87]
ds_write_b128 v246,a[104:107] offset:12288
v_mfma_f32_16x16x32_bf16 a[84:87],v[204:207],v[40:43],a[84:87]
v_mfma_f32_16x16x32_bf16 a[96:99],v[144:147],v[0:3],a[96:99]
ds_write_b128 v246,a[108:111] offset:13312
v_mfma_f32_16x16x32_bf16 a[96:99],v[148:151],v[32:35],a[96:99]
v_mfma_f32_16x16x32_bf16 a[112:115],v[152:155],v[0:3],a[112:115]
ds_write_b128 v246,a[120:123] offset:14336
v_mfma_f32_16x16x32_bf16 a[112:115],v[156:159],v[32:35],a[112:115]
v_mfma_f32_16x16x32_bf16 a[100:103],v[144:147],v[8:11],a[100:103]
ds_write_b128 v246,a[124:127] offset:15360
v_mfma_f32_16x16x32_bf16 a[100:103],v[148:151],v[40:43],a[100:103]
v_mfma_f32_16x16x32_bf16 a[116:119],v[152:155],v[8:11],a[116:119]
v_mfma_f32_16x16x32_bf16 a[116:119],v[156:159],v[40:43],a[116:119]
s_waitcnt lgkmcnt(0)
s_barrier
ds_read_b32 v64,v249 offset:4096
ds_read_b128 v[0:3],v247 offset:0
ds_read_b128 v[8:11],v247 offset:2048
ds_read_b128 v[16:19],v247 offset:4096
ds_read_b128 v[24:27],v247 offset:6144
ds_read_b128 v[32:35],v247 offset:8192
ds_read_b128 v[40:43],v247 offset:10240
ds_read_b128 v[48:51],v247 offset:12288
ds_read_b128 v[56:59],v247 offset:14336
ds_read_b32 v65,v249 offset:4352
ds_read_b128 v[4:7],v247 offset:1024
ds_read_b128 v[12:15],v247 offset:3072
ds_read_b128 v[20:23],v247 offset:5120
ds_read_b128 v[28:31],v247 offset:7168
ds_read_b128 v[36:39],v247 offset:9216
ds_read_b128 v[44:47],v247 offset:11264
ds_read_b128 v[52:55],v247 offset:13312
ds_read_b128 v[60:63],v247 offset:15360
s_add_u32 s59,s86,0
s_add_u32 s58,s86,16384
s_waitcnt lgkmcnt(15)
v_add_f32 v246,v250,v64
v_cmp_nlt_f32 vcc,v246,v235
v_rcp_f32 v246,v246
s_nop 0
s_or_b64 s[90:91],s[90:91],vcc
v_accvgpr_read_b32 v176,a0
v_accvgpr_read_b32 v177,a1
v_accvgpr_read_b32 v178,a2
v_accvgpr_read_b32 v179,a3
v_accvgpr_read_b32 v180,a16
v_accvgpr_read_b32 v181,a17
v_accvgpr_read_b32 v182,a18
v_accvgpr_read_b32 v183,a19
v_pk_add_f32 v[176:177],v[176:177],v[0:1]
v_pk_add_f32 v[178:179],v[178:179],v[2:3]
v_pk_add_f32 v[180:181],v[180:181],v[8:9]
v_pk_add_f32 v[182:183],v[182:183],v[10:11]
v_pk_mul_f32 v[176:177],v[176:177],v[246:247] op_sel:[0,0] op_sel_hi:[1,0]
v_pk_mul_f32 v[178:179],v[178:179],v[246:247] op_sel:[0,0] op_sel_hi:[1,0]
v_pk_mul_f32 v[180:181],v[180:181],v[246:247] op_sel:[0,0] op_sel_hi:[1,0]
v_pk_mul_f32 v[182:183],v[182:183],v[246:247] op_sel:[0,0] op_sel_hi:[1,0]
v_cvt_pk_bf16_f32 v184,v176,v177
v_cvt_pk_bf16_f32 v185,v178,v179
v_cvt_pk_bf16_f32 v186,v180,v181
v_cvt_pk_bf16_f32 v187,v182,v183
buffer_store_dwordx4 v[184:187],v255,s[40:43],s59 offen offset:0 sc1
v_accvgpr_read_b32 v192,a32
v_accvgpr_read_b32 v193,a33
v_accvgpr_read_b32 v194,a34
v_accvgpr_read_b32 v195,a35
v_accvgpr_read_b32 v196,a48
v_accvgpr_read_b32 v197,a49
v_accvgpr_read_b32 v198,a50
v_accvgpr_read_b32 v199,a51
s_waitcnt lgkmcnt(14)
v_pk_add_f32 v[192:193],v[192:193],v[16:17]
v_pk_add_f32 v[194:195],v[194:195],v[18:19]
s_waitcnt lgkmcnt(13)
v_pk_add_f32 v[196:197],v[196:197],v[24:25]
v_pk_add_f32 v[198:199],v[198:199],v[26:27]
v_pk_mul_f32 v[192:193],v[192:193],v[246:247] op_sel:[0,0] op_sel_hi:[1,0]
v_pk_mul_f32 v[194:195],v[194:195],v[246:247] op_sel:[0,0] op_sel_hi:[1,0]
v_pk_mul_f32 v[196:197],v[196:197],v[246:247] op_sel:[0,0] op_sel_hi:[1,0]
v_pk_mul_f32 v[198:199],v[198:199],v[246:247] op_sel:[0,0] op_sel_hi:[1,0]
v_cvt_pk_bf16_f32 v200,v192,v193
v_cvt_pk_bf16_f32 v201,v194,v195
v_cvt_pk_bf16_f32 v202,v196,v197
v_cvt_pk_bf16_f32 v203,v198,v199
buffer_store_dwordx4 v[200:203],v255,s[40:43],s59 offen offset:1024 sc1
v_accvgpr_read_b32 v176,a64
v_accvgpr_read_b32 v177,a65
v_accvgpr_read_b32 v178,a66
v_accvgpr_read_b32 v179,a67
v_accvgpr_read_b32 v180,a80
v_accvgpr_read_b32 v181,a81
v_accvgpr_read_b32 v182,a82
v_accvgpr_read_b32 v183,a83
s_waitcnt lgkmcnt(12)
v_pk_add_f32 v[176:177],v[176:177],v[32:33]
v_pk_add_f32 v[178:179],v[178:179],v[34:35]
s_waitcnt lgkmcnt(11)
v_pk_add_f32 v[180:181],v[180:181],v[40:41]
v_pk_add_f32 v[182:183],v[182:183],v[42:43]
v_pk_mul_f32 v[176:177],v[176:177],v[246:247] op_sel:[0,0] op_sel_hi:[1,0]
v_pk_mul_f32 v[178:179],v[178:179],v[246:247] op_sel:[0,0] op_sel_hi:[1,0]
v_pk_mul_f32 v[180:181],v[180:181],v[246:247] op_sel:[0,0] op_sel_hi:[1,0]
v_pk_mul_f32 v[182:183],v[182:183],v[246:247] op_sel:[0,0] op_sel_hi:[1,0]
v_cvt_pk_bf16_f32 v184,v176,v177
v_cvt_pk_bf16_f32 v185,v178,v179
v_cvt_pk_bf16_f32 v186,v180,v181
v_cvt_pk_bf16_f32 v187,v182,v183
buffer_store_dwordx4 v[184:187],v255,s[40:43],s58 offen offset:0 sc1
v_accvgpr_read_b32 v192,a96
v_accvgpr_read_b32 v193,a97
v_accvgpr_read_b32 v194,a98
v_accvgpr_read_b32 v195,a99
v_accvgpr_read_b32 v196,a112
v_accvgpr_read_b32 v197,a113
v_accvgpr_read_b32 v198,a114
v_accvgpr_read_b32 v199,a115
s_waitcnt lgkmcnt(10)
v_pk_add_f32 v[192:193],v[192:193],v[48:49]
v_pk_add_f32 v[194:195],v[194:195],v[50:51]
s_waitcnt lgkmcnt(9)
v_pk_add_f32 v[196:197],v[196:197],v[56:57]
v_pk_add_f32 v[198:199],v[198:199],v[58:59]
v_pk_mul_f32 v[192:193],v[192:193],v[246:247] op_sel:[0,0] op_sel_hi:[1,0]
v_pk_mul_f32 v[194:195],v[194:195],v[246:247] op_sel:[0,0] op_sel_hi:[1,0]
v_pk_mul_f32 v[196:197],v[196:197],v[246:247] op_sel:[0,0] op_sel_hi:[1,0]
v_pk_mul_f32 v[198:199],v[198:199],v[246:247] op_sel:[0,0] op_sel_hi:[1,0]
v_cvt_pk_bf16_f32 v200,v192,v193
v_cvt_pk_bf16_f32 v201,v194,v195
v_cvt_pk_bf16_f32 v202,v196,v197
v_cvt_pk_bf16_f32 v203,v198,v199
buffer_store_dwordx4 v[200:203],v255,s[40:43],s58 offen offset:1024 sc1
s_add_u32 s59,s86,8192
s_add_u32 s58,s86,24576
s_waitcnt lgkmcnt(8)
v_add_f32 v248,v252,v65
v_cmp_nlt_f32 vcc,v248,v235
v_rcp_f32 v248,v248
s_nop 0
s_or_b64 s[90:91],s[90:91],vcc
v_accvgpr_read_b32 v176,a4
v_accvgpr_read_b32 v177,a5
v_accvgpr_read_b32 v178,a6
v_accvgpr_read_b32 v179,a7
v_accvgpr_read_b32 v180,a20
v_accvgpr_read_b32 v181,a21
v_accvgpr_read_b32 v182,a22
v_accvgpr_read_b32 v183,a23
s_waitcnt lgkmcnt(7)
v_pk_add_f32 v[176:177],v[176:177],v[4:5]
v_pk_add_f32 v[178:179],v[178:179],v[6:7]
s_waitcnt lgkmcnt(6)
v_pk_add_f32 v[180:181],v[180:181],v[12:13]
v_pk_add_f32 v[182:183],v[182:183],v[14:15]
v_pk_mul_f32 v[176:177],v[176:177],v[248:249] op_sel:[0,0] op_sel_hi:[1,0]
v_pk_mul_f32 v[178:179],v[178:179],v[248:249] op_sel:[0,0] op_sel_hi:[1,0]
v_pk_mul_f32 v[180:181],v[180:181],v[248:249] op_sel:[0,0] op_sel_hi:[1,0]
v_pk_mul_f32 v[182:183],v[182:183],v[248:249] op_sel:[0,0] op_sel_hi:[1,0]
v_cvt_pk_bf16_f32 v184,v176,v177
v_cvt_pk_bf16_f32 v185,v178,v179
v_cvt_pk_bf16_f32 v186,v180,v181
v_cvt_pk_bf16_f32 v187,v182,v183
buffer_store_dwordx4 v[184:187],v255,s[40:43],s59 offen offset:0 sc1
v_accvgpr_read_b32 v192,a36
v_accvgpr_read_b32 v193,a37
v_accvgpr_read_b32 v194,a38
v_accvgpr_read_b32 v195,a39
v_accvgpr_read_b32 v196,a52
v_accvgpr_read_b32 v197,a53
v_accvgpr_read_b32 v198,a54
v_accvgpr_read_b32 v199,a55
s_waitcnt lgkmcnt(5)
v_pk_add_f32 v[192:193],v[192:193],v[20:21]
v_pk_add_f32 v[194:195],v[194:195],v[22:23]
s_waitcnt lgkmcnt(4)
v_pk_add_f32 v[196:197],v[196:197],v[28:29]
v_pk_add_f32 v[198:199],v[198:199],v[30:31]
v_pk_mul_f32 v[192:193],v[192:193],v[248:249] op_sel:[0,0] op_sel_hi:[1,0]
v_pk_mul_f32 v[194:195],v[194:195],v[248:249] op_sel:[0,0] op_sel_hi:[1,0]
v_pk_mul_f32 v[196:197],v[196:197],v[248:249] op_sel:[0,0] op_sel_hi:[1,0]
v_pk_mul_f32 v[198:199],v[198:199],v[248:249] op_sel:[0,0] op_sel_hi:[1,0]
v_cvt_pk_bf16_f32 v200,v192,v193
v_cvt_pk_bf16_f32 v201,v194,v195
v_cvt_pk_bf16_f32 v202,v196,v197
v_cvt_pk_bf16_f32 v203,v198,v199
buffer_store_dwordx4 v[200:203],v255,s[40:43],s59 offen offset:1024 sc1
v_accvgpr_read_b32 v176,a68
v_accvgpr_read_b32 v177,a69
v_accvgpr_read_b32 v178,a70
v_accvgpr_read_b32 v179,a71
v_accvgpr_read_b32 v180,a84
v_accvgpr_read_b32 v181,a85
v_accvgpr_read_b32 v182,a86
v_accvgpr_read_b32 v183,a87
s_waitcnt lgkmcnt(3)
v_pk_add_f32 v[176:177],v[176:177],v[36:37]
v_pk_add_f32 v[178:179],v[178:179],v[38:39]
s_waitcnt lgkmcnt(2)
v_pk_add_f32 v[180:181],v[180:181],v[44:45]
v_pk_add_f32 v[182:183],v[182:183],v[46:47]
v_pk_mul_f32 v[176:177],v[176:177],v[248:249] op_sel:[0,0] op_sel_hi:[1,0]
v_pk_mul_f32 v[178:179],v[178:179],v[248:249] op_sel:[0,0] op_sel_hi:[1,0]
v_pk_mul_f32 v[180:181],v[180:181],v[248:249] op_sel:[0,0] op_sel_hi:[1,0]
v_pk_mul_f32 v[182:183],v[182:183],v[248:249] op_sel:[0,0] op_sel_hi:[1,0]
v_cvt_pk_bf16_f32 v184,v176,v177
v_cvt_pk_bf16_f32 v185,v178,v179
v_cvt_pk_bf16_f32 v186,v180,v181
v_cvt_pk_bf16_f32 v187,v182,v183
buffer_store_dwordx4 v[184:187],v255,s[40:43],s58 offen offset:0 sc1
v_accvgpr_read_b32 v192,a100
v_accvgpr_read_b32 v193,a101
v_accvgpr_read_b32 v194,a102
v_accvgpr_read_b32 v195,a103
v_accvgpr_read_b32 v196,a116
v_accvgpr_read_b32 v197,a117
v_accvgpr_read_b32 v198,a118
v_accvgpr_read_b32 v199,a119
s_waitcnt lgkmcnt(1)
v_pk_add_f32 v[192:193],v[192:193],v[52:53]
v_pk_add_f32 v[194:195],v[194:195],v[54:55]
s_waitcnt lgkmcnt(0)
v_pk_add_f32 v[196:197],v[196:197],v[60:61]
v_pk_add_f32 v[198:199],v[198:199],v[62:63]
v_pk_mul_f32 v[192:193],v[192:193],v[248:249] op_sel:[0,0] op_sel_hi:[1,0]
v_pk_mul_f32 v[194:195],v[194:195],v[248:249] op_sel:[0,0] op_sel_hi:[1,0]
v_pk_mul_f32 v[196:197],v[196:197],v[248:249] op_sel:[0,0] op_sel_hi:[1,0]
v_pk_mul_f32 v[198:199],v[198:199],v[248:249] op_sel:[0,0] op_sel_hi:[1,0]
v_cvt_pk_bf16_f32 v200,v192,v193
v_cvt_pk_bf16_f32 v201,v194,v195
v_cvt_pk_bf16_f32 v202,v196,v197
v_cvt_pk_bf16_f32 v203,v198,v199
buffer_store_dwordx4 v[200:203],v255,s[40:43],s58 offen offset:1024 sc1
s_or_b32 %0,s88,s89
s_or_b32 %1,s90,s91
s_waitcnt vmcnt(0)
L_CE_%=:
)"
    : "=&s"(fa), "=&s"(fb) : "s"(ka), "s"(bid), "s"(w), "s"(ldsb) : "v0","v1","v2","v3","v4","v5","v6","v7","v8","v9","v10","v11","v12","v13","v14","v15","v16","v17","v18","v19","v20","v21","v22","v23","v24","v25","v26","v27","v28","v29","v30","v31","v32","v33","v34","v35","v36","v37","v38","v39","v40","v41","v42","v43","v44","v45","v46","v47","v48","v49","v50","v51","v52","v53","v54","v55","v56","v57","v58","v59","v60","v61","v62","v63","v64","v65","v66","v67","v68","v69","v70","v71","v72","v73","v74","v75","v76","v77","v78","v79","v80","v81","v82","v83","v84","v85","v86","v87","v88","v89","v90","v91","v92","v93","v94","v95","v96","v97","v98","v99","v100","v101","v102","v103","v104","v105","v106","v107","v108","v109","v110","v111","v112","v113","v114","v115","v116","v117","v118","v119","v120","v121","v122","v123","v124","v125","v126","v127","v128","v129","v130","v131","v132","v133","v134","v135","v136","v137","v138","v139","v140","v141","v142","v143","v144","v145","v146","v147","v148","v149","v150","v151","v152","v153","v154","v155","v156","v157","v158","v159","v160","v161","v162","v163","v164","v165","v166","v167","v168","v169","v170","v171","v172","v173","v174","v175","v176","v177","v178","v179","v180","v181","v182","v183","v184","v185","v186","v187","v188","v189","v190","v191","v192","v193","v194","v195","v196","v197","v198","v199","v200","v201","v202","v203","v204","v205","v206","v207","v208","v209","v210","v211","v212","v213","v214","v215","v216","v217","v218","v219","v220","v221","v222","v223","v224","v225","v226","v227","v228","v229","v230","v231","v232","v233","v234","v235","v236","v237","v238","v239","v240","v241","v242","v243","v244","v245","v246","v247","v248","v249","v250","v251","v252","v253","v254","v255","a0","a1","a2","a3","a4","a5","a6","a7","a8","a9","a10","a11","a12","a13","a14","a15","a16","a17","a18","a19","a20","a21","a22","a23","a24","a25","a26","a27","a28","a29","a30","a31","a32","a33","a34","a35","a36","a37","a38","a39","a40","a41","a42","a43","a44","a45","a46","a47","a48","a49","a50","a51","a52","a53","a54","a55","a56","a57","a58","a59","a60","a61","a62","a63","a64","a65","a66","a67","a68","a69","a70","a71","a72","a73","a74","a75","a76","a77","a78","a79","a80","a81","a82","a83","a84","a85","a86","a87","a88","a89","a90","a91","a92","a93","a94","a95","a96","a97","a98","a99","a100","a101","a102","a103","a104","a105","a106","a107","a108","a109","a110","a111","a112","a113","a114","a115","a116","a117","a118","a119","a120","a121","a122","a123","a124","a125","a126","a127","a128","a129","a130","a131","a132","a133","a134","a135","a136","a137","a138","a139","a140","a141","a142","a143","a144","a145","a146","a147","a148","a149","a150","a151","a152","a153","a154","a155","a156","a157","a158","a159","a160","a161","a162","a163","a164","a165","a166","a167","a168","a169","a170","a171","a172","a173","a174","a175","a176","a177","a178","a179","a180","a181","a182","a183","a184","a185","a186","a187","a188","a189","a190","a191","a192","a193","a194","a195","a196","a197","a198","a199","a200","a201","a202","a203","a204","a205","a206","a207","a208","a209","a210","a211","a212","a213","a214","a215","a216","a217","a218","a219","a220","a221","a222","a223","a224","a225","a226","a227","a228","a229","a230","a231","a232","a233","a234","a235","a236","a237","a238","a239","a240","a241","a242","a243","a244","a245","a246","a247","a248","a249","a250","a251","a252","a253","a254","a255","s16","s17","s18","s19","s20","s21","s22","s23","s24","s25","s26","s27","s28","s29","s30","s31","s34","s35","s36","s37","s38","s39","s40","s41","s42","s43","s44","s45","s46","s47","s48","s49","s50","s51","s52","s53","s54","s55","s56","s57","s58","s59","s60","s61","s62","s63","s64","s65","s66","s67","s68","s69","s70","s71","s72","s73","s74","s75","s76","s77","s78","s79","s80","s81","s82","s83","s84","s85","s86","s87","s88","s89","s90","s91","s92","s93","s94","s95","s96","s97","s98","s99","vcc","scc","memory");
  if (__builtin_expect((fa | fb) != 0u, 0)) {
    const int lane = __builtin_amdgcn_mbcnt_hi(~0u, __builtin_amdgcn_mbcnt_lo(~0u, 0u));
    const int sl = bid >> 3, head = (bid & 7) * 4 + (sl >> 3), i = sl & 7;
    const char* KVh = ws + WS_KV + ((size_t)head << 20); char* ctx = ws + WS_CTX;
    for (int z = 0; z < 4; ++z) { const bool on = z < 2 ? fa != 0u : fb != 0u; const int qb = (z < 2 ? 8 * i : 8 * (15 - i)) + w + 4 * (z & 1);
      if (on) attn_slow16((const u16*)bx, (const u16*)wq, KVh, ctx, qb, head, lane); }
  }
}

__global__ __launch_bounds__(512, 2) void out_proj(char* ws, const char* wo, const char* cre, const char* cim, const char* ax, const char* dsk, char* out) {
  extern __shared__ __attribute__((aligned(16))) char lds[];
  const int bid = blockIdx.x; const int w = __builtin_amdgcn_readfirstlane(threadIdx.x >> 6); const unsigned ldsb = (unsigned)(size_t)lds;
  asm volatile(
R"(s_mov_b32 s56,%8
s_mov_b32 s57,%9
s_and_b32 s63,%7,7
s_lshr_b32 s64,%7,3
v_mbcnt_lo_u32_b32 v126,-1,0
v_mbcnt_hi_u32_b32 v126,-1,v126
v_lshlrev_b32 v127,4,v126
s_mul_i32 s58,s64,52
s_lshr_b32 s58,s58,8
s_mul_i32 s59,s58,5
s_sub_u32 s75,s64,s59
s_lshr_b32 s59,s63,1
s_lshl_b32 s59,s59,2
s_add_u32 s59,s59,s58
s_lshl_b32 s60,s63,1
s_add_u32 s60,s60,s58
s_sub_u32 s60,s60,4
s_and_b32 s76,s63,1
s_cmp_ge_u32 s64,20
s_cselect_b32 s74,s60,s59
s_cselect_b32 s76,2,s76
s_lshr_b32 s79,s56,2
s_and_b32 s56,s56,3
s_cmp_ge_u32 s64,30
s_cbranch_scc1 L_END_%=
s_cmp_eq_u32 s76,2
s_cbranch_scc1 L_Y_%=
s_cmp_eq_u32 s79,0
s_cbranch_scc1 L_OC_%=
s_mov_b64 s[36:37],%0
s_add_u32 s36,s36,34603008
s_addc_u32 s37,s37,0
s_mov_b32 s38,0x1000000
s_mov_b32 s39,0x20000
s_mov_b64 s[40:41],%1
s_mov_b32 s42,0x500000
s_mov_b32 s43,0x20000
v_and_b32 v118,7,v126
v_lshrrev_b32 v119,4,v126
v_lshrrev_b32 v125,3,v126
v_mul_u32_u24 v125,1280,v125
v_lshlrev_b32 v120,1,v119
v_xor_b32 v120,v120,v118
v_lshl_add_u32 v96,v120,4,v125
v_lshlrev_b32 v120,1,v119
v_xor_b32 v120,v120,v118
v_lshl_add_u32 v97,v120,4,v125
v_add_u32 v97,9216,v97
v_lshlrev_b32 v120,1,v119
v_xor_b32 v120,v120,v118
v_lshl_add_u32 v98,v120,4,v125
v_add_u32 v98,18432,v98
v_lshlrev_b32 v120,1,v119
v_xor_b32 v120,v120,v118
v_lshl_add_u32 v99,v120,4,v125
v_add_u32 v99,27648,v99
s_mul_i32 s66,s74,1048576
s_lshl_b32 s59,s56,12
s_add_u32 s66,s66,s59
s_lshl_b32 s58,s76,19
s_add_u32 s66,s66,s58
s_mul_i32 s68,s76,2621440
s_and_b32 s58,s56,1
s_mul_i32 s58,s58,40960
s_add_u32 s68,s68,s58
s_lshl_b32 s58,s75,8
s_add_u32 s68,s68,s58
s_lshr_b32 s58,s56,1
s_lshl_b32 s58,s58,7
s_add_u32 s68,s68,s58
s_lshl_b32 s58,s56,12
s_add_u32 s70,s57,s58
s_and_b32 s58,s74,3
s_mul_i32 s58,s58,0
s_mul_i32 s59,s75,0
s_add_u32 s58,s58,s59
s_and_b32 s67,s58,31
s_mov_b32 s69,s67
s_mov_b32 s80,s66
s_mov_b32 s81,s68
s_lshl_b32 s58,s67,14
s_add_u32 s66,s66,s58
s_mul_i32 s58,s67,81920
s_add_u32 s68,s68,s58
s_add_u32 m0,s70,16384
s_nop 0
buffer_load_dwordx4 v96,s[40:43],s68 offen offset:0 lds
buffer_load_dwordx4 v97,s[40:43],s68 offen offset:1024 lds
buffer_load_dwordx4 v98,s[40:43],s68 offen offset:2048 lds
buffer_load_dwordx4 v99,s[40:43],s68 offen offset:3072 lds
s_add_u32 s68,s68,81920
s_add_u32 s69,s69,1
s_and_b32 s69,s69,31
s_cselect_b32 s68,s68,s81
s_add_u32 m0,s70,0
s_nop 0
buffer_load_dwordx4 v127,s[36:39],s66 offen offset:0 lds
buffer_load_dwordx4 v127,s[36:39],s66 offen offset:1024 lds
buffer_load_dwordx4 v127,s[36:39],s66 offen offset:2048 lds
buffer_load_dwordx4 v127,s[36:39],s66 offen offset:3072 lds
s_add_u32 s66,s66,16384
s_add_u32 s67,s67,1
s_and_b32 s67,s67,31
s_cselect_b32 s66,s66,s80
s_add_u32 m0,s70,49152
s_nop 0
buffer_load_dwordx4 v96,s[40:43],s68 offen offset:0 lds
buffer_load_dwordx4 v97,s[40:43],s68 offen offset:1024 lds
buffer_load_dwordx4 v98,s[40:43],s68 offen offset:2048 lds
buffer_load_dwordx4 v99,s[40:43],s68 offen offset:3072 lds
s_add_u32 s68,s68,81920
s_add_u32 s69,s69,1
s_and_b32 s69,s69,31
s_cselect_b32 s68,s68,s81
s_add_u32 m0,s70,32768
s_nop 0
buffer_load_dwordx4 v127,s[36:39],s66 offen offset:0 lds
buffer_load_dwordx4 v127,s[36:39],s66 offen offset:1024 lds
buffer_load_dwordx4 v127,s[36:39],s66 offen offset:2048 lds
buffer_load_dwordx4 v127,s[36:39],s66 offen offset:3072 lds
s_add_u32 s66,s66,16384
s_add_u32 s67,s67,1
s_and_b32 s67,s67,31
s_cselect_b32 s66,s66,s80
s_add_u32 m0,s70,81920
s_nop 0
buffer_load_dwordx4 v96,s[40:43],s68 offen offset:0 lds
buffer_load_dwordx4 v97,s[40:43],s68 offen offset:1024 lds
buffer_load_dwordx4 v98,s[40:43],s68 offen offset:2048 lds
buffer_load_dwordx4 v99,s[40:43],s68 offen offset:3072 lds
s_add_u32 s68,s68,81920
s_add_u32 s69,s69,1
s_and_b32 s69,s69,31
s_cselect_b32 s68,s68,s81
s_add_u32 m0,s70,65536
s_nop 0
buffer_load_dwordx4 v127,s[36:39],s66 offen offset:0 lds
buffer_load_dwordx4 v127,s[36:39],s66 offen offset:1024 lds
buffer_load_dwordx4 v127,s[36:39],s66 offen offset:2048 lds
buffer_load_dwordx4 v127,s[36:39],s66 offen offset:3072 lds
s_add_u32 s66,s66,16384
s_add_u32 s67,s67,1
s_and_b32 s67,s67,31
s_cselect_b32 s66,s66,s80
s_add_u32 m0,s70,114688
s_nop 0
buffer_load_dwordx4 v96,s[40:43],s68 offen offset:0 lds
buffer_load_dwordx4 v97,s[40:43],s68 offen offset:1024 lds
buffer_load_dwordx4 v98,s[40:43],s68 offen offset:2048 lds
buffer_load_dwordx4 v99,s[40:43],s68 offen offset:3072 lds
s_add_u32 s68,s68,81920
s_add_u32 s69,s69,1
s_and_b32 s69,s69,31
s_cselect_b32 s68,s68,s81
s_add_u32 m0,s70,98304
s_nop 0
buffer_load_dwordx4 v127,s[36:39],s66 offen offset:0 lds
buffer_load_dwordx4 v127,s[36:39],s66 offen offset:1024 lds
buffer_load_dwordx4 v127,s[36:39],s66 offen offset:2048 lds
buffer_load_dwordx4 v127,s[36:39],s66 offen offset:3072 lds
s_add_u32 s66,s66,16384
s_add_u32 s67,s67,1
s_and_b32 s67,s67,31
s_cselect_b32 s66,s66,s80
s_add_u32 m0,s70,147456
s_nop 0
buffer_load_dwordx4 v96,s[40:43],s68 offen offset:0 lds
buffer_load_dwordx4 v97,s[40:43],s68 offen offset:1024 lds
buffer_load_dwordx4 v98,s[40:43],s68 offen offset:2048 lds
buffer_load_dwordx4 v99,s[40:43],s68 offen offset:3072 lds
s_add_u32 s68,s68,81920
s_add_u32 s69,s69,1
s_and_b32 s69,s69,31
s_cselect_b32 s68,s68,s81
s_add_u32 m0,s70,131072
s_nop 0
buffer_load_dwordx4 v127,s[36:39],s66 offen offset:0 lds
buffer_load_dwordx4 v127,s[36:39],s66 offen offset:1024 lds
buffer_load_dwordx4 v127,s[36:39],s66 offen offset:2048 lds
buffer_load_dwordx4 v127,s[36:39],s66 offen offset:3072 lds
s_add_u32 s66,s66,16384
s_add_u32 s67,s67,1
s_and_b32 s67,s67,31
s_cselect_b32 s66,s66,s80
s_waitcnt vmcnt(28) lgkmcnt(0)
s_barrier
s_barrier
s_mov_b32 s62,5
L_OJL_%=:
s_add_u32 m0,s70,16384
s_nop 0
buffer_load_dwordx4 v96,s[40:43],s68 offen offset:0 lds
buffer_load_dwordx4 v97,s[40:43],s68 offen offset:1024 lds
buffer_load_dwordx4 v98,s[40:43],s68 offen offset:2048 lds
buffer_load_dwordx4 v99,s[40:43],s68 offen offset:3072 lds
s_add_u32 s68,s68,81920
s_add_u32 s69,s69,1
s_and_b32 s69,s69,31
s_cselect_b32 s68,s68,s81
s_waitcnt vmcnt(24) lgkmcnt(0)
s_barrier
s_add_u32 m0,s70,0
s_nop 0
buffer_load_dwordx4 v127,s[36:39],s66 offen offset:0 lds
buffer_load_dwordx4 v127,s[36:39],s66 offen offset:1024 lds
buffer_load_dwordx4 v127,s[36:39],s66 offen offset:2048 lds
buffer_load_dwordx4 v127,s[36:39],s66 offen offset:3072 lds
s_add_u32 s66,s66,16384
s_add_u32 s67,s67,1
s_and_b32 s67,s67,31
s_cselect_b32 s66,s66,s80
s_add_u32 m0,s70,49152
s_nop 0
buffer_load_dwordx4 v96,s[40:43],s68 offen offset:0 lds
buffer_load_dwordx4 v97,s[40:43],s68 offen offset:1024 lds
buffer_load_dwordx4 v98,s[40:43],s68 offen offset:2048 lds
buffer_load_dwordx4 v99,s[40:43],s68 offen offset:3072 lds
s_add_u32 s68,s68,81920
s_add_u32 s69,s69,1
s_and_b32 s69,s69,31
s_cselect_b32 s68,s68,s81
s_waitcnt vmcnt(24) lgkmcnt(0)
s_barrier
s_add_u32 m0,s70,32768
s_nop 0
buffer_load_dwordx4 v127,s[36:39],s66 offen offset:0 lds
buffer_load_dwordx4 v127,s[36:39],s66 offen offset:1024 lds
buffer_load_dwordx4 v127,s[36:39],s66 offen offset:2048 lds
buffer_load_dwordx4 v127,s[36:39],s66 offen offset:3072 lds
s_add_u32 s66,s66,16384
s_add_u32 s67,s67,1
s_and_b32 s67,s67,31
s_cselect_b32 s66,s66,s80
s_add_u32 m0,s70,81920
s_nop 0
buffer_load_dwordx4 v96,s[40:43],s68 offen offset:0 lds
buffer_load_dwordx4 v97,s[40:43],s68 offen offset:1024 lds
buffer_load_dwordx4 v98,s[40:43],s68 offen offset:2048 lds
buffer_load_dwordx4 v99,s[40:43],s68 offen offset:3072 lds
s_add_u32 s68,s68,81920
s_add_u32 s69,s69,1
s_and_b32 s69,s69,31
s_cselect_b32 s68,s68,s81
s_waitcnt vmcnt(24) lgkmcnt(0)
s_barrier
s_add_u32 m0,s70,65536
s_nop 0
buffer_load_dwordx4 v127,s[36:39],s66 offen offset:0 lds
buffer_load_dwordx4 v127,s[36:39],s66 offen offset:1024 lds
buffer_load_dwordx4 v127,s[36:39],s66 offen offset:2048 lds
buffer_load_dwordx4 v127,s[36:39],s66 offen offset:3072 lds
s_add_u32 s66,s66,16384
s_add_u32 s67,s67,1
s_and_b32 s67,s67,31
s_cselect_b32 s66,s66,s80
s_add_u32 m0,s70,114688
s_nop 0
buffer_load_dwordx4 v96,s[40:43],s68 offen offset:0 lds
buffer_load_dwordx4 v97,s[40:43],s68 offen offset:1024 lds
buffer_load_dwordx4 v98,s[40:43],s68 offen offset:2048 lds
buffer_load_dwordx4 v99,s[40:43],s68 offen offset:3072 lds
s_add_u32 s68,s68,81920
s_add_u32 s69,s69,1
s_and_b32 s69,s69,31
s_cselect_b32 s68,s68,s81
s_waitcnt vmcnt(24) lgkmcnt(0)
s_barrier
s_add_u32 m0,s70,98304
s_nop 0
buffer_load_dwordx4 v127,s[36:39],s66 offen offset:0 lds
buffer_load_dwordx4 v127,s[36:39],s66 offen offset:1024 lds
buffer_load_dwordx4 v127,s[36:39],s66 offen offset:2048 lds
buffer_load_dwordx4 v127,s[36:39],s66 offen offset:3072 lds
s_add_u32 s66,s66,16384
s_add_u32 s67,s67,1
s_and_b32 s67,s67,31
s_cselect_b32 s66,s66,s80
s_add_u32 m0,s70,147456
s_nop 0
buffer_load_dwordx4 v96,s[40:43],s68 offen offset:0 lds
buffer_load_dwordx4 v97,s[40:43],s68 offen offset:1024 lds
buffer_load_dwordx4 v98,s[40:43],s68 offen offset:2048 lds
buffer_load_dwordx4 v99,s[40:43],s68 offen offset:3072 lds
s_add_u32 s68,s68,81920
s_add_u32 s69,s69,1
s_and_b32 s69,s69,31
s_cselect_b32 s68,s68,s81
s_waitcnt vmcnt(24) lgkmcnt(0)
s_barrier
s_add_u32 m0,s70,131072
s_nop 0
buffer_load_dwordx4 v127,s[36:39],s66 offen offset:0 lds
buffer_load_dwordx4 v127,s[36:39],s66 offen offset:1024 lds
buffer_load_dwordx4 v127,s[36:39],s66 offen offset:2048 lds
buffer_load_dwordx4 v127,s[36:39],s66 offen offset:3072 lds
s_add_u32 s66,s66,16384
s_add_u32 s67,s67,1
s_and_b32 s67,s67,31
s_cselect_b32 s66,s66,s80
s_sub_u32 s62,s62,1
s_cmp_lg_u32 s62,0
s_cbranch_scc1 L_OJL_%=
s_add_u32 m0,s70,16384
s_nop 0
buffer_load_dwordx4 v96,s[40:43],s68 offen offset:0 lds
buffer_load_dwordx4 v97,s[40:43],s68 offen offset:1024 lds
buffer_load_dwordx4 v98,s[40:43],s68 offen offset:2048 lds
buffer_load_dwordx4 v99,s[40:43],s68 offen offset:3072 lds
s_add_u32 s68,s68,81920
s_add_u32 s69,s69,1
s_and_b32 s69,s69,31
s_cselect_b32 s68,s68,s81
s_waitcnt vmcnt(24) lgkmcnt(0)
s_barrier
s_add_u32 m0,s70,0
s_nop 0
buffer_load_dwordx4 v127,s[36:39],s66 offen offset:0 lds
buffer_load_dwordx4 v127,s[36:39],s66 offen offset:1024 lds
buffer_load_dwordx4 v127,s[36:39],s66 offen offset:2048 lds
buffer_load_dwordx4 v127,s[36:39],s66 offen offset:3072 lds
s_add_u32 s66,s66,16384
s_add_u32 s67,s67,1
s_and_b32 s67,s67,31
s_cselect_b32 s66,s66,s80
s_add_u32 m0,s70,49152
s_nop 0
buffer_load_dwordx4 v96,s[40:43],s68 offen offset:0 lds
buffer_load_dwordx4 v97,s[40:43],s68 offen offset:1024 lds
buffer_load_dwordx4 v98,s[40:43],s68 offen offset:2048 lds
buffer_load_dwordx4 v99,s[40:43],s68 offen offset:3072 lds
s_add_u32 s68,s68,81920
s_add_u32 s69,s69,1
s_and_b32 s69,s69,31
s_cselect_b32 s68,s68,s81
s_waitcnt vmcnt(24) lgkmcnt(0)
s_barrier
s_add_u32 m0,s70,32768
s_nop 0
buffer_load_dwordx4 v127,s[36:39],s66 offen offset:0 lds
buffer_load_dwordx4 v127,s[36:39],s66 offen offset:1024 lds
buffer_load_dwordx4 v127,s[36:39],s66 offen offset:2048 lds
buffer_load_dwordx4 v127,s[36:39],s66 offen offset:3072 lds
s_add_u32 s66,s66,16384
s_add_u32 s67,s67,1
s_and_b32 s67,s67,31
s_cselect_b32 s66,s66,s80
s_waitcnt vmcnt(20) lgkmcnt(0)
s_barrier
s_waitcnt vmcnt(12) lgkmcnt(0)
s_barrier
s_waitcnt vmcnt(4) lgkmcnt(0)
s_barrier
s_waitcnt vmcnt(0) lgkmcnt(0)
s_barrier
s_branch L_END_%=
L_OC_%=:
s_and_b32 s58,s56,1
s_lshl_b32 s58,s58,13
s_add_u32 s58,s58,s57
s_add_u32 s58,s58,0x4000
v_lshrrev_b32 v118,4,v126
v_bfe_u32 v119,v126,2,2
v_and_b32 v120,3,v126
v_lshrrev_b32 v121,1,v119
v_and_b32 v122,1,v118
v_lshl_or_b32 v121,v122,1,v121
v_lshl_add_u32 v119,v118,2,v119
v_lshlrev_b32 v119,7,v119
v_lshl_add_u32 v119,v120,3,v119
v_add_u32 v119,s58,v119
v_xor_b32 v122,0,v121
v_lshl_add_u32 v100,v122,5,v119
v_xor_b32 v122,1,v121
v_lshl_add_u32 v101,v122,5,v119
v_xor_b32 v122,2,v121
v_lshl_add_u32 v102,v122,5,v119
v_xor_b32 v122,3,v121
v_lshl_add_u32 v103,v122,5,v119
s_lshl_b32 s59,s76,6
v_xor_b32 v100,s59,v100
v_add_u32 v104,0x10000,v100
v_add_u32 v108,0x20000,v100
v_xor_b32 v101,s59,v101
v_add_u32 v105,0x10000,v101
v_add_u32 v109,0x20000,v101
v_xor_b32 v102,s59,v102
v_add_u32 v106,0x10000,v102
v_add_u32 v110,0x20000,v102
v_xor_b32 v103,s59,v103
v_add_u32 v107,0x10000,v103
v_add_u32 v111,0x20000,v103
s_lshr_b32 s58,s56,1
s_lshl_b32 s58,s58,13
s_add_u32 s58,s58,s57
v_add_u32 v112,s58,v127
v_add_u32 v113,0x10000,v112
v_add_u32 v114,0x20000,v112
v_and_b32 v118,15,v126
v_mul_u32_u24 v118,5120,v118
v_lshrrev_b32 v119,4,v126
)"
R"(v_lshl_add_u32 v116,v119,4,v118
s_lshl_b32 s58,s74,7
s_lshr_b32 s59,s56,1
s_lshl_b32 s59,s59,6
s_add_u32 s58,s58,s59
s_mul_i32 s72,s58,5120
s_lshl_b32 s58,s75,9
s_add_u32 s72,s72,s58
s_and_b32 s58,s56,1
s_lshl_b32 s58,s58,8
s_add_u32 s72,s72,s58
s_add_u32 s72,s72,2560
s_lshl_b32 s58,s76,7
s_add_u32 s72,s72,s58
s_mov_b64 s[52:53],%6
s_mov_b32 s54,0xa00000
s_mov_b32 s55,0x20000
s_mul_i32 s58,s74,5
s_add_u32 s58,s58,s75
s_lshl_b32 s78,s58,6
s_add_u32 s78,s78,8192
s_lshl_b32 s58,s58,16
s_lshl_b32 s59,s76,15
s_add_u32 s58,s58,s59
s_lshl_b32 s59,s56,13
s_add_u32 s60,s58,s59
s_xor_b32 s61,s60,0x8000
s_mov_b64 s[48:49],%0
s_add_u32 s48,s48,58720256
s_addc_u32 s49,s49,0
s_mov_b32 s50,0x500000
s_mov_b32 s51,0x20000
s_waitcnt lgkmcnt(0)
s_barrier
ds_read_b64_tr_b16 v[0:1],v100 offset:0
ds_read_b64_tr_b16 v[2:3],v100 offset:2048
ds_read_b64_tr_b16 v[4:5],v100 offset:4096
ds_read_b64_tr_b16 v[6:7],v100 offset:6144
ds_read_b64_tr_b16 v[8:9],v101 offset:0
ds_read_b64_tr_b16 v[10:11],v101 offset:2048
ds_read_b64_tr_b16 v[12:13],v101 offset:4096
ds_read_b64_tr_b16 v[14:15],v101 offset:6144
ds_read_b64_tr_b16 v[16:17],v102 offset:0
ds_read_b64_tr_b16 v[18:19],v102 offset:2048
ds_read_b64_tr_b16 v[20:21],v102 offset:4096
ds_read_b64_tr_b16 v[22:23],v102 offset:6144
ds_read_b64_tr_b16 v[24:25],v103 offset:0
ds_read_b64_tr_b16 v[26:27],v103 offset:2048
ds_read_b64_tr_b16 v[28:29],v103 offset:4096
ds_read_b64_tr_b16 v[30:31],v103 offset:6144
ds_read_b128 v[64:67],v112 offset:0
ds_read_b128 v[68:71],v112 offset:1024
ds_read_b128 v[72:75],v112 offset:2048
ds_read_b128 v[76:79],v112 offset:3072
s_waitcnt lgkmcnt(0)
s_barrier
v_mfma_f32_16x16x32_bf16 a[0:3],v[0:3],v[64:67],0
v_mfma_f32_16x16x32_bf16 a[0:3],v[4:7],v[68:71],a[0:3]
ds_read_b128 v[80:83],v112 offset:4096
v_mfma_f32_16x16x32_bf16 a[16:19],v[8:11],v[64:67],0
ds_read_b128 v[84:87],v112 offset:5120
ds_read_b64_tr_b16 v[32:33],v100 offset:32768
v_mfma_f32_16x16x32_bf16 a[16:19],v[12:15],v[68:71],a[16:19]
ds_read_b64_tr_b16 v[34:35],v100 offset:34816
ds_read_b64_tr_b16 v[36:37],v100 offset:36864
v_mfma_f32_16x16x32_bf16 a[32:35],v[16:19],v[64:67],0
ds_read_b64_tr_b16 v[38:39],v100 offset:38912
ds_read_b64_tr_b16 v[40:41],v101 offset:32768
v_mfma_f32_16x16x32_bf16 a[32:35],v[20:23],v[68:71],a[32:35]
ds_read_b64_tr_b16 v[42:43],v101 offset:34816
ds_read_b64_tr_b16 v[44:45],v101 offset:36864
v_mfma_f32_16x16x32_bf16 a[48:51],v[24:27],v[64:67],0
ds_read_b64_tr_b16 v[46:47],v101 offset:38912
ds_read_b64_tr_b16 v[48:49],v102 offset:32768
ds_read_b64_tr_b16 v[54:55],v102 offset:38912
v_mfma_f32_16x16x32_bf16 a[48:51],v[28:31],v[68:71],a[48:51]
ds_read_b64_tr_b16 v[50:51],v102 offset:34816
ds_read_b64_tr_b16 v[52:53],v102 offset:36864
ds_read_b64_tr_b16 v[56:57],v103 offset:32768
ds_read_b64_tr_b16 v[60:61],v103 offset:36864
v_mfma_f32_16x16x32_bf16 a[4:7],v[0:3],v[72:75],0
ds_read_b128 v[88:91],v112 offset:6144
ds_read_b128 v[92:95],v112 offset:7168
ds_read_b64_tr_b16 v[58:59],v103 offset:34816
ds_read_b64_tr_b16 v[62:63],v103 offset:38912
v_mfma_f32_16x16x32_bf16 a[4:7],v[4:7],v[76:79],a[4:7]
v_mfma_f32_16x16x32_bf16 a[20:23],v[8:11],v[72:75],0
v_mfma_f32_16x16x32_bf16 a[20:23],v[12:15],v[76:79],a[20:23]
v_mfma_f32_16x16x32_bf16 a[36:39],v[16:19],v[72:75],0
v_mfma_f32_16x16x32_bf16 a[36:39],v[20:23],v[76:79],a[36:39]
v_mfma_f32_16x16x32_bf16 a[52:55],v[24:27],v[72:75],0
v_mfma_f32_16x16x32_bf16 a[52:55],v[28:31],v[76:79],a[52:55]
s_waitcnt lgkmcnt(0)
s_barrier
v_mfma_f32_16x16x32_bf16 a[8:11],v[0:3],v[80:83],0
v_mfma_f32_16x16x32_bf16 a[8:11],v[4:7],v[84:87],a[8:11]
ds_read_b128 v[64:67],v112 offset:32768
v_mfma_f32_16x16x32_bf16 a[24:27],v[8:11],v[80:83],0
ds_read_b128 v[68:71],v112 offset:33792
v_mfma_f32_16x16x32_bf16 a[24:27],v[12:15],v[84:87],a[24:27]
v_mfma_f32_16x16x32_bf16 a[40:43],v[16:19],v[80:83],0
v_mfma_f32_16x16x32_bf16 a[40:43],v[20:23],v[84:87],a[40:43]
v_mfma_f32_16x16x32_bf16 a[56:59],v[24:27],v[80:83],0
v_mfma_f32_16x16x32_bf16 a[56:59],v[28:31],v[84:87],a[56:59]
v_mfma_f32_16x16x32_bf16 a[12:15],v[0:3],v[88:91],0
v_mfma_f32_16x16x32_bf16 a[12:15],v[4:7],v[92:95],a[12:15]
ds_read_b128 v[72:75],v112 offset:34816
v_mfma_f32_16x16x32_bf16 a[28:31],v[8:11],v[88:91],0
ds_read_b128 v[76:79],v112 offset:35840
v_mfma_f32_16x16x32_bf16 a[28:31],v[12:15],v[92:95],a[28:31]
v_mfma_f32_16x16x32_bf16 a[44:47],v[16:19],v[88:91],0
v_mfma_f32_16x16x32_bf16 a[44:47],v[20:23],v[92:95],a[44:47]
v_mfma_f32_16x16x32_bf16 a[60:63],v[24:27],v[88:91],0
v_mfma_f32_16x16x32_bf16 a[60:63],v[28:31],v[92:95],a[60:63]
s_mov_b32 s62,3
L_OJC_%=:
s_waitcnt lgkmcnt(3)
v_mfma_f32_16x16x32_bf16 a[0:3],v[32:35],v[64:67],a[0:3]
s_waitcnt lgkmcnt(2)
v_mfma_f32_16x16x32_bf16 a[0:3],v[36:39],v[68:71],a[0:3]
ds_read_b128 v[80:83],v112 offset:36864
v_mfma_f32_16x16x32_bf16 a[16:19],v[40:43],v[64:67],a[16:19]
ds_read_b128 v[84:87],v112 offset:37888
ds_read_b64_tr_b16 v[0:1],v104 offset:0
v_mfma_f32_16x16x32_bf16 a[16:19],v[44:47],v[68:71],a[16:19]
ds_read_b64_tr_b16 v[2:3],v104 offset:2048
ds_read_b64_tr_b16 v[4:5],v104 offset:4096
v_mfma_f32_16x16x32_bf16 a[32:35],v[48:51],v[64:67],a[32:35]
ds_read_b64_tr_b16 v[6:7],v104 offset:6144
ds_read_b64_tr_b16 v[8:9],v105 offset:0
v_mfma_f32_16x16x32_bf16 a[32:35],v[52:55],v[68:71],a[32:35]
ds_read_b64_tr_b16 v[10:11],v105 offset:2048
ds_read_b64_tr_b16 v[12:13],v105 offset:4096
v_mfma_f32_16x16x32_bf16 a[48:51],v[56:59],v[64:67],a[48:51]
ds_read_b64_tr_b16 v[14:15],v105 offset:6144
ds_read_b64_tr_b16 v[16:17],v106 offset:0
ds_read_b64_tr_b16 v[22:23],v106 offset:6144
v_mfma_f32_16x16x32_bf16 a[48:51],v[60:63],v[68:71],a[48:51]
ds_read_b64_tr_b16 v[18:19],v106 offset:2048
ds_read_b64_tr_b16 v[20:21],v106 offset:4096
ds_read_b64_tr_b16 v[24:25],v107 offset:0
ds_read_b64_tr_b16 v[28:29],v107 offset:4096
s_waitcnt lgkmcnt(15)
v_mfma_f32_16x16x32_bf16 a[4:7],v[32:35],v[72:75],a[4:7]
ds_read_b128 v[88:91],v112 offset:38912
ds_read_b128 v[92:95],v112 offset:39936
ds_read_b64_tr_b16 v[26:27],v107 offset:2048
ds_read_b64_tr_b16 v[30:31],v107 offset:6144
v_mfma_f32_16x16x32_bf16 a[4:7],v[36:39],v[76:79],a[4:7]
v_mfma_f32_16x16x32_bf16 a[20:23],v[40:43],v[72:75],a[20:23]
v_mfma_f32_16x16x32_bf16 a[20:23],v[44:47],v[76:79],a[20:23]
v_mfma_f32_16x16x32_bf16 a[36:39],v[48:51],v[72:75],a[36:39]
v_mfma_f32_16x16x32_bf16 a[36:39],v[52:55],v[76:79],a[36:39]
v_mfma_f32_16x16x32_bf16 a[52:55],v[56:59],v[72:75],a[52:55]
v_mfma_f32_16x16x32_bf16 a[52:55],v[60:63],v[76:79],a[52:55]
s_waitcnt lgkmcnt(0)
s_barrier
v_mfma_f32_16x16x32_bf16 a[8:11],v[32:35],v[80:83],a[8:11]
v_mfma_f32_16x16x32_bf16 a[8:11],v[36:39],v[84:87],a[8:11]
ds_read_b128 v[64:67],v113 offset:0
v_mfma_f32_16x16x32_bf16 a[24:27],v[40:43],v[80:83],a[24:27]
ds_read_b128 v[68:71],v113 offset:1024
v_mfma_f32_16x16x32_bf16 a[24:27],v[44:47],v[84:87],a[24:27]
v_mfma_f32_16x16x32_bf16 a[40:43],v[48:51],v[80:83],a[40:43]
v_mfma_f32_16x16x32_bf16 a[40:43],v[52:55],v[84:87],a[40:43]
v_mfma_f32_16x16x32_bf16 a[56:59],v[56:59],v[80:83],a[56:59]
v_mfma_f32_16x16x32_bf16 a[56:59],v[60:63],v[84:87],a[56:59]
v_mfma_f32_16x16x32_bf16 a[12:15],v[32:35],v[88:91],a[12:15]
v_mfma_f32_16x16x32_bf16 a[12:15],v[36:39],v[92:95],a[12:15]
ds_read_b128 v[72:75],v113 offset:2048
v_mfma_f32_16x16x32_bf16 a[28:31],v[40:43],v[88:91],a[28:31]
ds_read_b128 v[76:79],v113 offset:3072
v_mfma_f32_16x16x32_bf16 a[28:31],v[44:47],v[92:95],a[28:31]
v_mfma_f32_16x16x32_bf16 a[44:47],v[48:51],v[88:91],a[44:47]
v_mfma_f32_16x16x32_bf16 a[44:47],v[52:55],v[92:95],a[44:47]
v_mfma_f32_16x16x32_bf16 a[60:63],v[56:59],v[88:91],a[60:63]
v_mfma_f32_16x16x32_bf16 a[60:63],v[60:63],v[92:95],a[60:63]
s_waitcnt lgkmcnt(3)
v_mfma_f32_16x16x32_bf16 a[0:3],v[0:3],v[64:67],a[0:3]
s_waitcnt lgkmcnt(2)
v_mfma_f32_16x16x32_bf16 a[0:3],v[4:7],v[68:71],a[0:3]
ds_read_b128 v[80:83],v113 offset:4096
v_mfma_f32_16x16x32_bf16 a[16:19],v[8:11],v[64:67],a[16:19]
ds_read_b128 v[84:87],v113 offset:5120
ds_read_b64_tr_b16 v[32:33],v104 offset:32768
v_mfma_f32_16x16x32_bf16 a[16:19],v[12:15],v[68:71],a[16:19]
ds_read_b64_tr_b16 v[34:35],v104 offset:34816
ds_read_b64_tr_b16 v[36:37],v104 offset:36864
v_mfma_f32_16x16x32_bf16 a[32:35],v[16:19],v[64:67],a[32:35]
ds_read_b64_tr_b16 v[38:39],v104 offset:38912
ds_read_b64_tr_b16 v[40:41],v105 offset:32768
v_mfma_f32_16x16x32_bf16 a[32:35],v[20:23],v[68:71],a[32:35]
ds_read_b64_tr_b16 v[42:43],v105 offset:34816
ds_read_b64_tr_b16 v[44:45],v105 offset:36864
v_mfma_f32_16x16x32_bf16 a[48:51],v[24:27],v[64:67],a[48:51]
ds_read_b64_tr_b16 v[46:47],v105 offset:38912
ds_read_b64_tr_b16 v[48:49],v106 offset:32768
ds_read_b64_tr_b16 v[54:55],v106 offset:38912
v_mfma_f32_16x16x32_bf16 a[48:51],v[28:31],v[68:71],a[48:51]
ds_read_b64_tr_b16 v[50:51],v106 offset:34816
ds_read_b64_tr_b16 v[52:53],v106 offset:36864
ds_read_b64_tr_b16 v[56:57],v107 offset:32768
ds_read_b64_tr_b16 v[60:61],v107 offset:36864
s_waitcnt lgkmcnt(15)
v_mfma_f32_16x16x32_bf16 a[4:7],v[0:3],v[72:75],a[4:7]
ds_read_b128 v[88:91],v113 offset:6144
ds_read_b128 v[92:95],v113 offset:7168
ds_read_b64_tr_b16 v[58:59],v107 offset:34816
ds_read_b64_tr_b16 v[62:63],v107 offset:38912
v_mfma_f32_16x16x32_bf16 a[4:7],v[4:7],v[76:79],a[4:7]
v_mfma_f32_16x16x32_bf16 a[20:23],v[8:11],v[72:75],a[20:23]
v_mfma_f32_16x16x32_bf16 a[20:23],v[12:15],v[76:79],a[20:23]
v_mfma_f32_16x16x32_bf16 a[36:39],v[16:19],v[72:75],a[36:39]
v_mfma_f32_16x16x32_bf16 a[36:39],v[20:23],v[76:79],a[36:39]
v_mfma_f32_16x16x32_bf16 a[52:55],v[24:27],v[72:75],a[52:55]
v_mfma_f32_16x16x32_bf16 a[52:55],v[28:31],v[76:79],a[52:55]
s_waitcnt lgkmcnt(0)
s_barrier
v_mfma_f32_16x16x32_bf16 a[8:11],v[0:3],v[80:83],a[8:11]
v_mfma_f32_16x16x32_bf16 a[8:11],v[4:7],v[84:87],a[8:11]
ds_read_b128 v[64:67],v113 offset:32768
v_mfma_f32_16x16x32_bf16 a[24:27],v[8:11],v[80:83],a[24:27]
ds_read_b128 v[68:71],v113 offset:33792
v_mfma_f32_16x16x32_bf16 a[24:27],v[12:15],v[84:87],a[24:27]
v_mfma_f32_16x16x32_bf16 a[40:43],v[16:19],v[80:83],a[40:43]
v_mfma_f32_16x16x32_bf16 a[40:43],v[20:23],v[84:87],a[40:43]
v_mfma_f32_16x16x32_bf16 a[56:59],v[24:27],v[80:83],a[56:59]
v_mfma_f32_16x16x32_bf16 a[56:59],v[28:31],v[84:87],a[56:59]
v_mfma_f32_16x16x32_bf16 a[12:15],v[0:3],v[88:91],a[12:15]
v_mfma_f32_16x16x32_bf16 a[12:15],v[4:7],v[92:95],a[12:15]
ds_read_b128 v[72:75],v113 offset:34816
v_mfma_f32_16x16x32_bf16 a[28:31],v[8:11],v[88:91],a[28:31]
ds_read_b128 v[76:79],v113 offset:35840
v_mfma_f32_16x16x32_bf16 a[28:31],v[12:15],v[92:95],a[28:31]
v_mfma_f32_16x16x32_bf16 a[44:47],v[16:19],v[88:91],a[44:47]
v_mfma_f32_16x16x32_bf16 a[44:47],v[20:23],v[92:95],a[44:47]
v_mfma_f32_16x16x32_bf16 a[60:63],v[24:27],v[88:91],a[60:63]
v_mfma_f32_16x16x32_bf16 a[60:63],v[28:31],v[92:95],a[60:63]
s_waitcnt lgkmcnt(3)
v_mfma_f32_16x16x32_bf16 a[0:3],v[32:35],v[64:67],a[0:3]
s_waitcnt lgkmcnt(2)
v_mfma_f32_16x16x32_bf16 a[0:3],v[36:39],v[68:71],a[0:3]
ds_read_b128 v[80:83],v113 offset:36864
v_mfma_f32_16x16x32_bf16 a[16:19],v[40:43],v[64:67],a[16:19]
ds_read_b128 v[84:87],v113 offset:37888
ds_read_b64_tr_b16 v[0:1],v108 offset:0
v_mfma_f32_16x16x32_bf16 a[16:19],v[44:47],v[68:71],a[16:19]
ds_read_b64_tr_b16 v[2:3],v108 offset:2048
ds_read_b64_tr_b16 v[4:5],v108 offset:4096
v_mfma_f32_16x16x32_bf16 a[32:35],v[48:51],v[64:67],a[32:35]
ds_read_b64_tr_b16 v[6:7],v108 offset:6144
ds_read_b64_tr_b16 v[8:9],v109 offset:0
v_mfma_f32_16x16x32_bf16 a[32:35],v[52:55],v[68:71],a[32:35]
ds_read_b64_tr_b16 v[10:11],v109 offset:2048
ds_read_b64_tr_b16 v[12:13],v109 offset:4096
v_mfma_f32_16x16x32_bf16 a[48:51],v[56:59],v[64:67],a[48:51]
ds_read_b64_tr_b16 v[14:15],v109 offset:6144
ds_read_b64_tr_b16 v[16:17],v110 offset:0
ds_read_b64_tr_b16 v[22:23],v110 offset:6144
v_mfma_f32_16x16x32_bf16 a[48:51],v[60:63],v[68:71],a[48:51]
ds_read_b64_tr_b16 v[18:19],v110 offset:2048
ds_read_b64_tr_b16 v[20:21],v110 offset:4096
ds_read_b64_tr_b16 v[24:25],v111 offset:0
ds_read_b64_tr_b16 v[28:29],v111 offset:4096
s_waitcnt lgkmcnt(15)
v_mfma_f32_16x16x32_bf16 a[4:7],v[32:35],v[72:75],a[4:7]
ds_read_b128 v[88:91],v113 offset:38912
ds_read_b128 v[92:95],v113 offset:39936
ds_read_b64_tr_b16 v[26:27],v111 offset:2048
ds_read_b64_tr_b16 v[30:31],v111 offset:6144
v_mfma_f32_16x16x32_bf16 a[4:7],v[36:39],v[76:79],a[4:7]
v_mfma_f32_16x16x32_bf16 a[20:23],v[40:43],v[72:75],a[20:23]
v_mfma_f32_16x16x32_bf16 a[20:23],v[44:47],v[76:79],a[20:23]
v_mfma_f32_16x16x32_bf16 a[36:39],v[48:51],v[72:75],a[36:39]
v_mfma_f32_16x16x32_bf16 a[36:39],v[52:55],v[76:79],a[36:39]
v_mfma_f32_16x16x32_bf16 a[52:55],v[56:59],v[72:75],a[52:55]
v_mfma_f32_16x16x32_bf16 a[52:55],v[60:63],v[76:79],a[52:55]
s_waitcnt lgkmcnt(0)
s_barrier
v_mfma_f32_16x16x32_bf16 a[8:11],v[32:35],v[80:83],a[8:11]
v_mfma_f32_16x16x32_bf16 a[8:11],v[36:39],v[84:87],a[8:11]
ds_read_b128 v[64:67],v114 offset:0
v_mfma_f32_16x16x32_bf16 a[24:27],v[40:43],v[80:83],a[24:27]
ds_read_b128 v[68:71],v114 offset:1024
v_mfma_f32_16x16x32_bf16 a[24:27],v[44:47],v[84:87],a[24:27]
v_mfma_f32_16x16x32_bf16 a[40:43],v[48:51],v[80:83],a[40:43]
v_mfma_f32_16x16x32_bf16 a[40:43],v[52:55],v[84:87],a[40:43]
v_mfma_f32_16x16x32_bf16 a[56:59],v[56:59],v[80:83],a[56:59]
v_mfma_f32_16x16x32_bf16 a[56:59],v[60:63],v[84:87],a[56:59]
v_mfma_f32_16x16x32_bf16 a[12:15],v[32:35],v[88:91],a[12:15]
v_mfma_f32_16x16x32_bf16 a[12:15],v[36:39],v[92:95],a[12:15]
ds_read_b128 v[72:75],v114 offset:2048
v_mfma_f32_16x16x32_bf16 a[28:31],v[40:43],v[88:91],a[28:31]
ds_read_b128 v[76:79],v114 offset:3072
v_mfma_f32_16x16x32_bf16 a[28:31],v[44:47],v[92:95],a[28:31]
v_mfma_f32_16x16x32_bf16 a[44:47],v[48:51],v[88:91],a[44:47]
v_mfma_f32_16x16x32_bf16 a[44:47],v[52:55],v[92:95],a[44:47]
v_mfma_f32_16x16x32_bf16 a[60:63],v[56:59],v[88:91],a[60:63]
v_mfma_f32_16x16x32_bf16 a[60:63],v[60:63],v[92:95],a[60:63]
s_waitcnt lgkmcnt(3)
v_mfma_f32_16x16x32_bf16 a[0:3],v[0:3],v[64:67],a[0:3]
s_waitcnt lgkmcnt(2)
v_mfma_f32_16x16x32_bf16 a[0:3],v[4:7],v[68:71],a[0:3]
ds_read_b128 v[80:83],v114 offset:4096
v_mfma_f32_16x16x32_bf16 a[16:19],v[8:11],v[64:67],a[16:19]
ds_read_b128 v[84:87],v114 offset:5120
ds_read_b64_tr_b16 v[32:33],v100 offset:0
v_mfma_f32_16x16x32_bf16 a[16:19],v[12:15],v[68:71],a[16:19]
ds_read_b64_tr_b16 v[34:35],v100 offset:2048
ds_read_b64_tr_b16 v[36:37],v100 offset:4096
v_mfma_f32_16x16x32_bf16 a[32:35],v[16:19],v[64:67],a[32:35]
ds_read_b64_tr_b16 v[38:39],v100 offset:6144
ds_read_b64_tr_b16 v[40:41],v101 offset:0
v_mfma_f32_16x16x32_bf16 a[32:35],v[20:23],v[68:71],a[32:35]
ds_read_b64_tr_b16 v[42:43],v101 offset:2048
ds_read_b64_tr_b16 v[44:45],v101 offset:4096
v_mfma_f32_16x16x32_bf16 a[48:51],v[24:27],v[64:67],a[48:51]
ds_read_b64_tr_b16 v[46:47],v101 offset:6144
ds_read_b64_tr_b16 v[48:49],v102 offset:0
ds_read_b64_tr_b16 v[54:55],v102 offset:6144
v_mfma_f32_16x16x32_bf16 a[48:51],v[28:31],v[68:71],a[48:51]
ds_read_b64_tr_b16 v[50:51],v102 offset:2048
ds_read_b64_tr_b16 v[52:53],v102 offset:4096
ds_read_b64_tr_b16 v[56:57],v103 offset:0
ds_read_b64_tr_b16 v[60:61],v103 offset:4096
s_waitcnt lgkmcnt(15)
v_mfma_f32_16x16x32_bf16 a[4:7],v[0:3],v[72:75],a[4:7]
ds_read_b128 v[88:91],v114 offset:6144
ds_read_b128 v[92:95],v114 offset:7168
ds_read_b64_tr_b16 v[58:59],v103 offset:2048
ds_read_b64_tr_b16 v[62:63],v103 offset:6144
v_mfma_f32_16x16x32_bf16 a[4:7],v[4:7],v[76:79],a[4:7]
v_mfma_f32_16x16x32_bf16 a[20:23],v[8:11],v[72:75],a[20:23]
v_mfma_f32_16x16x32_bf16 a[20:23],v[12:15],v[76:79],a[20:23]
v_mfma_f32_16x16x32_bf16 a[36:39],v[16:19],v[72:75],a[36:39]
v_mfma_f32_16x16x32_bf16 a[36:39],v[20:23],v[76:79],a[36:39]
v_mfma_f32_16x16x32_bf16 a[52:55],v[24:27],v[72:75],a[52:55]
v_mfma_f32_16x16x32_bf16 a[52:55],v[28:31],v[76:79],a[52:55]
s_waitcnt lgkmcnt(0)
s_barrier
v_mfma_f32_16x16x32_bf16 a[8:11],v[0:3],v[80:83],a[8:11]
v_mfma_f32_16x16x32_bf16 a[8:11],v[4:7],v[84:87],a[8:11]
ds_read_b128 v[64:67],v112 offset:0
v_mfma_f32_16x16x32_bf16 a[24:27],v[8:11],v[80:83],a[24:27]
ds_read_b128 v[68:71],v112 offset:1024
v_mfma_f32_16x16x32_bf16 a[24:27],v[12:15],v[84:87],a[24:27]
v_mfma_f32_16x16x32_bf16 a[40:43],v[16:19],v[80:83],a[40:43]
v_mfma_f32_16x16x32_bf16 a[40:43],v[20:23],v[84:87],a[40:43]
v_mfma_f32_16x16x32_bf16 a[56:59],v[24:27],v[80:83],a[56:59]
v_mfma_f32_16x16x32_bf16 a[56:59],v[28:31],v[84:87],a[56:59]
v_mfma_f32_16x16x32_bf16 a[12:15],v[0:3],v[88:91],a[12:15]
v_mfma_f32_16x16x32_bf16 a[12:15],v[4:7],v[92:95],a[12:15]
ds_read_b128 v[72:75],v112 offset:2048
v_mfma_f32_16x16x32_bf16 a[28:31],v[8:11],v[88:91],a[28:31]
ds_read_b128 v[76:79],v112 offset:3072
v_mfma_f32_16x16x32_bf16 a[28:31],v[12:15],v[92:95],a[28:31]
v_mfma_f32_16x16x32_bf16 a[44:47],v[16:19],v[88:91],a[44:47]
v_mfma_f32_16x16x32_bf16 a[44:47],v[20:23],v[92:95],a[44:47]
v_mfma_f32_16x16x32_bf16 a[60:63],v[24:27],v[88:91],a[60:63]
v_mfma_f32_16x16x32_bf16 a[60:63],v[28:31],v[92:95],a[60:63]
s_waitcnt lgkmcnt(3)
v_mfma_f32_16x16x32_bf16 a[0:3],v[32:35],v[64:67],a[0:3]
s_waitcnt lgkmcnt(2)
v_mfma_f32_16x16x32_bf16 a[0:3],v[36:39],v[68:71],a[0:3]
ds_read_b128 v[80:83],v112 offset:4096
v_mfma_f32_16x16x32_bf16 a[16:19],v[40:43],v[64:67],a[16:19]
ds_read_b128 v[84:87],v112 offset:5120
ds_read_b64_tr_b16 v[0:1],v100 offset:32768
v_mfma_f32_16x16x32_bf16 a[16:19],v[44:47],v[68:71],a[16:19]
ds_read_b64_tr_b16 v[2:3],v100 offset:34816
ds_read_b64_tr_b16 v[4:5],v100 offset:36864
v_mfma_f32_16x16x32_bf16 a[32:35],v[48:51],v[64:67],a[32:35]
ds_read_b64_tr_b16 v[6:7],v100 offset:38912
ds_read_b64_tr_b16 v[8:9],v101 offset:32768
v_mfma_f32_16x16x32_bf16 a[32:35],v[52:55],v[68:71],a[32:35]
ds_read_b64_tr_b16 v[10:11],v101 offset:34816
ds_read_b64_tr_b16 v[12:13],v101 offset:36864
v_mfma_f32_16x16x32_bf16 a[48:51],v[56:59],v[64:67],a[48:51]
ds_read_b64_tr_b16 v[14:15],v101 offset:38912
ds_read_b64_tr_b16 v[16:17],v102 offset:32768
ds_read_b64_tr_b16 v[22:23],v102 offset:38912
v_mfma_f32_16x16x32_bf16 a[48:51],v[60:63],v[68:71],a[48:51]
ds_read_b64_tr_b16 v[18:19],v102 offset:34816
ds_read_b64_tr_b16 v[20:21],v102 offset:36864
ds_read_b64_tr_b16 v[24:25],v103 offset:32768
ds_read_b64_tr_b16 v[28:29],v103 offset:36864
s_waitcnt lgkmcnt(15)
v_mfma_f32_16x16x32_bf16 a[4:7],v[32:35],v[72:75],a[4:7]
ds_read_b128 v[88:91],v112 offset:6144
ds_read_b128 v[92:95],v112 offset:7168
ds_read_b64_tr_b16 v[26:27],v103 offset:34816
ds_read_b64_tr_b16 v[30:31],v103 offset:38912
v_mfma_f32_16x16x32_bf16 a[4:7],v[36:39],v[76:79],a[4:7]
v_mfma_f32_16x16x32_bf16 a[20:23],v[40:43],v[72:75],a[20:23]
v_mfma_f32_16x16x32_bf16 a[20:23],v[44:47],v[76:79],a[20:23]
v_mfma_f32_16x16x32_bf16 a[36:39],v[48:51],v[72:75],a[36:39]
v_mfma_f32_16x16x32_bf16 a[36:39],v[52:55],v[76:79],a[36:39]
v_mfma_f32_16x16x32_bf16 a[52:55],v[56:59],v[72:75],a[52:55]
v_mfma_f32_16x16x32_bf16 a[52:55],v[60:63],v[76:79],a[52:55]
s_waitcnt lgkmcnt(0)
)"
R"(s_barrier
v_mfma_f32_16x16x32_bf16 a[8:11],v[32:35],v[80:83],a[8:11]
v_mfma_f32_16x16x32_bf16 a[8:11],v[36:39],v[84:87],a[8:11]
ds_read_b128 v[64:67],v112 offset:32768
v_mfma_f32_16x16x32_bf16 a[24:27],v[40:43],v[80:83],a[24:27]
ds_read_b128 v[68:71],v112 offset:33792
v_mfma_f32_16x16x32_bf16 a[24:27],v[44:47],v[84:87],a[24:27]
v_mfma_f32_16x16x32_bf16 a[40:43],v[48:51],v[80:83],a[40:43]
v_mfma_f32_16x16x32_bf16 a[40:43],v[52:55],v[84:87],a[40:43]
v_mfma_f32_16x16x32_bf16 a[56:59],v[56:59],v[80:83],a[56:59]
v_mfma_f32_16x16x32_bf16 a[56:59],v[60:63],v[84:87],a[56:59]
v_mfma_f32_16x16x32_bf16 a[12:15],v[32:35],v[88:91],a[12:15]
v_mfma_f32_16x16x32_bf16 a[12:15],v[36:39],v[92:95],a[12:15]
ds_read_b128 v[72:75],v112 offset:34816
v_mfma_f32_16x16x32_bf16 a[28:31],v[40:43],v[88:91],a[28:31]
ds_read_b128 v[76:79],v112 offset:35840
v_mfma_f32_16x16x32_bf16 a[28:31],v[44:47],v[92:95],a[28:31]
v_mfma_f32_16x16x32_bf16 a[44:47],v[48:51],v[88:91],a[44:47]
v_mfma_f32_16x16x32_bf16 a[44:47],v[52:55],v[92:95],a[44:47]
v_mfma_f32_16x16x32_bf16 a[60:63],v[56:59],v[88:91],a[60:63]
v_mfma_f32_16x16x32_bf16 a[60:63],v[60:63],v[92:95],a[60:63]
s_waitcnt lgkmcnt(3)
v_mfma_f32_16x16x32_bf16 a[0:3],v[0:3],v[64:67],a[0:3]
s_waitcnt lgkmcnt(2)
v_mfma_f32_16x16x32_bf16 a[0:3],v[4:7],v[68:71],a[0:3]
ds_read_b128 v[80:83],v112 offset:36864
v_mfma_f32_16x16x32_bf16 a[16:19],v[8:11],v[64:67],a[16:19]
ds_read_b128 v[84:87],v112 offset:37888
ds_read_b64_tr_b16 v[32:33],v104 offset:0
v_mfma_f32_16x16x32_bf16 a[16:19],v[12:15],v[68:71],a[16:19]
ds_read_b64_tr_b16 v[34:35],v104 offset:2048
ds_read_b64_tr_b16 v[36:37],v104 offset:4096
v_mfma_f32_16x16x32_bf16 a[32:35],v[16:19],v[64:67],a[32:35]
ds_read_b64_tr_b16 v[38:39],v104 offset:6144
ds_read_b64_tr_b16 v[40:41],v105 offset:0
v_mfma_f32_16x16x32_bf16 a[32:35],v[20:23],v[68:71],a[32:35]
ds_read_b64_tr_b16 v[42:43],v105 offset:2048
ds_read_b64_tr_b16 v[44:45],v105 offset:4096
v_mfma_f32_16x16x32_bf16 a[48:51],v[24:27],v[64:67],a[48:51]
ds_read_b64_tr_b16 v[46:47],v105 offset:6144
ds_read_b64_tr_b16 v[48:49],v106 offset:0
ds_read_b64_tr_b16 v[54:55],v106 offset:6144
v_mfma_f32_16x16x32_bf16 a[48:51],v[28:31],v[68:71],a[48:51]
ds_read_b64_tr_b16 v[50:51],v106 offset:2048
ds_read_b64_tr_b16 v[52:53],v106 offset:4096
ds_read_b64_tr_b16 v[56:57],v107 offset:0
ds_read_b64_tr_b16 v[60:61],v107 offset:4096
s_waitcnt lgkmcnt(15)
v_mfma_f32_16x16x32_bf16 a[4:7],v[0:3],v[72:75],a[4:7]
ds_read_b128 v[88:91],v112 offset:38912
ds_read_b128 v[92:95],v112 offset:39936
ds_read_b64_tr_b16 v[58:59],v107 offset:2048
ds_read_b64_tr_b16 v[62:63],v107 offset:6144
v_mfma_f32_16x16x32_bf16 a[4:7],v[4:7],v[76:79],a[4:7]
v_mfma_f32_16x16x32_bf16 a[20:23],v[8:11],v[72:75],a[20:23]
v_mfma_f32_16x16x32_bf16 a[20:23],v[12:15],v[76:79],a[20:23]
v_mfma_f32_16x16x32_bf16 a[36:39],v[16:19],v[72:75],a[36:39]
v_mfma_f32_16x16x32_bf16 a[36:39],v[20:23],v[76:79],a[36:39]
v_mfma_f32_16x16x32_bf16 a[52:55],v[24:27],v[72:75],a[52:55]
v_mfma_f32_16x16x32_bf16 a[52:55],v[28:31],v[76:79],a[52:55]
s_waitcnt lgkmcnt(0)
s_barrier
v_mfma_f32_16x16x32_bf16 a[8:11],v[0:3],v[80:83],a[8:11]
v_mfma_f32_16x16x32_bf16 a[8:11],v[4:7],v[84:87],a[8:11]
ds_read_b128 v[64:67],v113 offset:0
v_mfma_f32_16x16x32_bf16 a[24:27],v[8:11],v[80:83],a[24:27]
ds_read_b128 v[68:71],v113 offset:1024
v_mfma_f32_16x16x32_bf16 a[24:27],v[12:15],v[84:87],a[24:27]
v_mfma_f32_16x16x32_bf16 a[40:43],v[16:19],v[80:83],a[40:43]
v_mfma_f32_16x16x32_bf16 a[40:43],v[20:23],v[84:87],a[40:43]
v_mfma_f32_16x16x32_bf16 a[56:59],v[24:27],v[80:83],a[56:59]
v_mfma_f32_16x16x32_bf16 a[56:59],v[28:31],v[84:87],a[56:59]
v_mfma_f32_16x16x32_bf16 a[12:15],v[0:3],v[88:91],a[12:15]
v_mfma_f32_16x16x32_bf16 a[12:15],v[4:7],v[92:95],a[12:15]
ds_read_b128 v[72:75],v113 offset:2048
v_mfma_f32_16x16x32_bf16 a[28:31],v[8:11],v[88:91],a[28:31]
ds_read_b128 v[76:79],v113 offset:3072
v_mfma_f32_16x16x32_bf16 a[28:31],v[12:15],v[92:95],a[28:31]
v_mfma_f32_16x16x32_bf16 a[44:47],v[16:19],v[88:91],a[44:47]
v_mfma_f32_16x16x32_bf16 a[44:47],v[20:23],v[92:95],a[44:47]
v_mfma_f32_16x16x32_bf16 a[60:63],v[24:27],v[88:91],a[60:63]
v_mfma_f32_16x16x32_bf16 a[60:63],v[28:31],v[92:95],a[60:63]
s_waitcnt lgkmcnt(3)
v_mfma_f32_16x16x32_bf16 a[0:3],v[32:35],v[64:67],a[0:3]
s_waitcnt lgkmcnt(2)
v_mfma_f32_16x16x32_bf16 a[0:3],v[36:39],v[68:71],a[0:3]
ds_read_b128 v[80:83],v113 offset:4096
v_mfma_f32_16x16x32_bf16 a[16:19],v[40:43],v[64:67],a[16:19]
ds_read_b128 v[84:87],v113 offset:5120
ds_read_b64_tr_b16 v[0:1],v104 offset:32768
v_mfma_f32_16x16x32_bf16 a[16:19],v[44:47],v[68:71],a[16:19]
ds_read_b64_tr_b16 v[2:3],v104 offset:34816
ds_read_b64_tr_b16 v[4:5],v104 offset:36864
v_mfma_f32_16x16x32_bf16 a[32:35],v[48:51],v[64:67],a[32:35]
ds_read_b64_tr_b16 v[6:7],v104 offset:38912
ds_read_b64_tr_b16 v[8:9],v105 offset:32768
v_mfma_f32_16x16x32_bf16 a[32:35],v[52:55],v[68:71],a[32:35]
ds_read_b64_tr_b16 v[10:11],v105 offset:34816
ds_read_b64_tr_b16 v[12:13],v105 offset:36864
v_mfma_f32_16x16x32_bf16 a[48:51],v[56:59],v[64:67],a[48:51]
ds_read_b64_tr_b16 v[14:15],v105 offset:38912
ds_read_b64_tr_b16 v[16:17],v106 offset:32768
ds_read_b64_tr_b16 v[22:23],v106 offset:38912
v_mfma_f32_16x16x32_bf16 a[48:51],v[60:63],v[68:71],a[48:51]
ds_read_b64_tr_b16 v[18:19],v106 offset:34816
ds_read_b64_tr_b16 v[20:21],v106 offset:36864
ds_read_b64_tr_b16 v[24:25],v107 offset:32768
ds_read_b64_tr_b16 v[28:29],v107 offset:36864
s_waitcnt lgkmcnt(15)
v_mfma_f32_16x16x32_bf16 a[4:7],v[32:35],v[72:75],a[4:7]
ds_read_b128 v[88:91],v113 offset:6144
ds_read_b128 v[92:95],v113 offset:7168
ds_read_b64_tr_b16 v[26:27],v107 offset:34816
ds_read_b64_tr_b16 v[30:31],v107 offset:38912
v_mfma_f32_16x16x32_bf16 a[4:7],v[36:39],v[76:79],a[4:7]
v_mfma_f32_16x16x32_bf16 a[20:23],v[40:43],v[72:75],a[20:23]
v_mfma_f32_16x16x32_bf16 a[20:23],v[44:47],v[76:79],a[20:23]
v_mfma_f32_16x16x32_bf16 a[36:39],v[48:51],v[72:75],a[36:39]
v_mfma_f32_16x16x32_bf16 a[36:39],v[52:55],v[76:79],a[36:39]
v_mfma_f32_16x16x32_bf16 a[52:55],v[56:59],v[72:75],a[52:55]
v_mfma_f32_16x16x32_bf16 a[52:55],v[60:63],v[76:79],a[52:55]
s_waitcnt lgkmcnt(0)
s_barrier
v_mfma_f32_16x16x32_bf16 a[8:11],v[32:35],v[80:83],a[8:11]
v_mfma_f32_16x16x32_bf16 a[8:11],v[36:39],v[84:87],a[8:11]
ds_read_b128 v[64:67],v113 offset:32768
v_mfma_f32_16x16x32_bf16 a[24:27],v[40:43],v[80:83],a[24:27]
ds_read_b128 v[68:71],v113 offset:33792
v_mfma_f32_16x16x32_bf16 a[24:27],v[44:47],v[84:87],a[24:27]
v_mfma_f32_16x16x32_bf16 a[40:43],v[48:51],v[80:83],a[40:43]
v_mfma_f32_16x16x32_bf16 a[40:43],v[52:55],v[84:87],a[40:43]
v_mfma_f32_16x16x32_bf16 a[56:59],v[56:59],v[80:83],a[56:59]
v_mfma_f32_16x16x32_bf16 a[56:59],v[60:63],v[84:87],a[56:59]
v_mfma_f32_16x16x32_bf16 a[12:15],v[32:35],v[88:91],a[12:15]
v_mfma_f32_16x16x32_bf16 a[12:15],v[36:39],v[92:95],a[12:15]
ds_read_b128 v[72:75],v113 offset:34816
v_mfma_f32_16x16x32_bf16 a[28:31],v[40:43],v[88:91],a[28:31]
ds_read_b128 v[76:79],v113 offset:35840
v_mfma_f32_16x16x32_bf16 a[28:31],v[44:47],v[92:95],a[28:31]
v_mfma_f32_16x16x32_bf16 a[44:47],v[48:51],v[88:91],a[44:47]
v_mfma_f32_16x16x32_bf16 a[44:47],v[52:55],v[92:95],a[44:47]
v_mfma_f32_16x16x32_bf16 a[60:63],v[56:59],v[88:91],a[60:63]
v_mfma_f32_16x16x32_bf16 a[60:63],v[60:63],v[92:95],a[60:63]
s_waitcnt lgkmcnt(3)
v_mfma_f32_16x16x32_bf16 a[0:3],v[0:3],v[64:67],a[0:3]
s_waitcnt lgkmcnt(2)
v_mfma_f32_16x16x32_bf16 a[0:3],v[4:7],v[68:71],a[0:3]
ds_read_b128 v[80:83],v113 offset:36864
v_mfma_f32_16x16x32_bf16 a[16:19],v[8:11],v[64:67],a[16:19]
ds_read_b128 v[84:87],v113 offset:37888
ds_read_b64_tr_b16 v[32:33],v108 offset:0
v_mfma_f32_16x16x32_bf16 a[16:19],v[12:15],v[68:71],a[16:19]
ds_read_b64_tr_b16 v[34:35],v108 offset:2048
ds_read_b64_tr_b16 v[36:37],v108 offset:4096
v_mfma_f32_16x16x32_bf16 a[32:35],v[16:19],v[64:67],a[32:35]
ds_read_b64_tr_b16 v[38:39],v108 offset:6144
ds_read_b64_tr_b16 v[40:41],v109 offset:0
v_mfma_f32_16x16x32_bf16 a[32:35],v[20:23],v[68:71],a[32:35]
ds_read_b64_tr_b16 v[42:43],v109 offset:2048
ds_read_b64_tr_b16 v[44:45],v109 offset:4096
v_mfma_f32_16x16x32_bf16 a[48:51],v[24:27],v[64:67],a[48:51]
ds_read_b64_tr_b16 v[46:47],v109 offset:6144
ds_read_b64_tr_b16 v[48:49],v110 offset:0
ds_read_b64_tr_b16 v[54:55],v110 offset:6144
v_mfma_f32_16x16x32_bf16 a[48:51],v[28:31],v[68:71],a[48:51]
ds_read_b64_tr_b16 v[50:51],v110 offset:2048
ds_read_b64_tr_b16 v[52:53],v110 offset:4096
ds_read_b64_tr_b16 v[56:57],v111 offset:0
ds_read_b64_tr_b16 v[60:61],v111 offset:4096
s_waitcnt lgkmcnt(15)
v_mfma_f32_16x16x32_bf16 a[4:7],v[0:3],v[72:75],a[4:7]
ds_read_b128 v[88:91],v113 offset:38912
ds_read_b128 v[92:95],v113 offset:39936
ds_read_b64_tr_b16 v[58:59],v111 offset:2048
ds_read_b64_tr_b16 v[62:63],v111 offset:6144
v_mfma_f32_16x16x32_bf16 a[4:7],v[4:7],v[76:79],a[4:7]
v_mfma_f32_16x16x32_bf16 a[20:23],v[8:11],v[72:75],a[20:23]
v_mfma_f32_16x16x32_bf16 a[20:23],v[12:15],v[76:79],a[20:23]
v_mfma_f32_16x16x32_bf16 a[36:39],v[16:19],v[72:75],a[36:39]
v_mfma_f32_16x16x32_bf16 a[36:39],v[20:23],v[76:79],a[36:39]
v_mfma_f32_16x16x32_bf16 a[52:55],v[24:27],v[72:75],a[52:55]
v_mfma_f32_16x16x32_bf16 a[52:55],v[28:31],v[76:79],a[52:55]
s_waitcnt lgkmcnt(0)
s_barrier
v_mfma_f32_16x16x32_bf16 a[8:11],v[0:3],v[80:83],a[8:11]
v_mfma_f32_16x16x32_bf16 a[8:11],v[4:7],v[84:87],a[8:11]
ds_read_b128 v[64:67],v114 offset:0
v_mfma_f32_16x16x32_bf16 a[24:27],v[8:11],v[80:83],a[24:27]
ds_read_b128 v[68:71],v114 offset:1024
v_mfma_f32_16x16x32_bf16 a[24:27],v[12:15],v[84:87],a[24:27]
v_mfma_f32_16x16x32_bf16 a[40:43],v[16:19],v[80:83],a[40:43]
v_mfma_f32_16x16x32_bf16 a[40:43],v[20:23],v[84:87],a[40:43]
v_mfma_f32_16x16x32_bf16 a[56:59],v[24:27],v[80:83],a[56:59]
v_mfma_f32_16x16x32_bf16 a[56:59],v[28:31],v[84:87],a[56:59]
v_mfma_f32_16x16x32_bf16 a[12:15],v[0:3],v[88:91],a[12:15]
v_mfma_f32_16x16x32_bf16 a[12:15],v[4:7],v[92:95],a[12:15]
ds_read_b128 v[72:75],v114 offset:2048
v_mfma_f32_16x16x32_bf16 a[28:31],v[8:11],v[88:91],a[28:31]
ds_read_b128 v[76:79],v114 offset:3072
v_mfma_f32_16x16x32_bf16 a[28:31],v[12:15],v[92:95],a[28:31]
v_mfma_f32_16x16x32_bf16 a[44:47],v[16:19],v[88:91],a[44:47]
v_mfma_f32_16x16x32_bf16 a[44:47],v[20:23],v[92:95],a[44:47]
v_mfma_f32_16x16x32_bf16 a[60:63],v[24:27],v[88:91],a[60:63]
v_mfma_f32_16x16x32_bf16 a[60:63],v[28:31],v[92:95],a[60:63]
s_waitcnt lgkmcnt(3)
v_mfma_f32_16x16x32_bf16 a[0:3],v[32:35],v[64:67],a[0:3]
s_waitcnt lgkmcnt(2)
v_mfma_f32_16x16x32_bf16 a[0:3],v[36:39],v[68:71],a[0:3]
ds_read_b128 v[80:83],v114 offset:4096
v_mfma_f32_16x16x32_bf16 a[16:19],v[40:43],v[64:67],a[16:19]
ds_read_b128 v[84:87],v114 offset:5120
ds_read_b64_tr_b16 v[0:1],v100 offset:0
v_mfma_f32_16x16x32_bf16 a[16:19],v[44:47],v[68:71],a[16:19]
ds_read_b64_tr_b16 v[2:3],v100 offset:2048
ds_read_b64_tr_b16 v[4:5],v100 offset:4096
v_mfma_f32_16x16x32_bf16 a[32:35],v[48:51],v[64:67],a[32:35]
ds_read_b64_tr_b16 v[6:7],v100 offset:6144
ds_read_b64_tr_b16 v[8:9],v101 offset:0
v_mfma_f32_16x16x32_bf16 a[32:35],v[52:55],v[68:71],a[32:35]
ds_read_b64_tr_b16 v[10:11],v101 offset:2048
ds_read_b64_tr_b16 v[12:13],v101 offset:4096
v_mfma_f32_16x16x32_bf16 a[48:51],v[56:59],v[64:67],a[48:51]
ds_read_b64_tr_b16 v[14:15],v101 offset:6144
ds_read_b64_tr_b16 v[16:17],v102 offset:0
ds_read_b64_tr_b16 v[22:23],v102 offset:6144
v_mfma_f32_16x16x32_bf16 a[48:51],v[60:63],v[68:71],a[48:51]
ds_read_b64_tr_b16 v[18:19],v102 offset:2048
ds_read_b64_tr_b16 v[20:21],v102 offset:4096
ds_read_b64_tr_b16 v[24:25],v103 offset:0
ds_read_b64_tr_b16 v[28:29],v103 offset:4096
s_waitcnt lgkmcnt(15)
v_mfma_f32_16x16x32_bf16 a[4:7],v[32:35],v[72:75],a[4:7]
ds_read_b128 v[88:91],v114 offset:6144
ds_read_b128 v[92:95],v114 offset:7168
ds_read_b64_tr_b16 v[26:27],v103 offset:2048
ds_read_b64_tr_b16 v[30:31],v103 offset:6144
v_mfma_f32_16x16x32_bf16 a[4:7],v[36:39],v[76:79],a[4:7]
v_mfma_f32_16x16x32_bf16 a[20:23],v[40:43],v[72:75],a[20:23]
v_mfma_f32_16x16x32_bf16 a[20:23],v[44:47],v[76:79],a[20:23]
v_mfma_f32_16x16x32_bf16 a[36:39],v[48:51],v[72:75],a[36:39]
v_mfma_f32_16x16x32_bf16 a[36:39],v[52:55],v[76:79],a[36:39]
v_mfma_f32_16x16x32_bf16 a[52:55],v[56:59],v[72:75],a[52:55]
v_mfma_f32_16x16x32_bf16 a[52:55],v[60:63],v[76:79],a[52:55]
s_waitcnt lgkmcnt(0)
s_barrier
v_mfma_f32_16x16x32_bf16 a[8:11],v[32:35],v[80:83],a[8:11]
v_mfma_f32_16x16x32_bf16 a[8:11],v[36:39],v[84:87],a[8:11]
ds_read_b128 v[64:67],v112 offset:0
v_mfma_f32_16x16x32_bf16 a[24:27],v[40:43],v[80:83],a[24:27]
ds_read_b128 v[68:71],v112 offset:1024
v_mfma_f32_16x16x32_bf16 a[24:27],v[44:47],v[84:87],a[24:27]
v_mfma_f32_16x16x32_bf16 a[40:43],v[48:51],v[80:83],a[40:43]
v_mfma_f32_16x16x32_bf16 a[40:43],v[52:55],v[84:87],a[40:43]
v_mfma_f32_16x16x32_bf16 a[56:59],v[56:59],v[80:83],a[56:59]
v_mfma_f32_16x16x32_bf16 a[56:59],v[60:63],v[84:87],a[56:59]
v_mfma_f32_16x16x32_bf16 a[12:15],v[32:35],v[88:91],a[12:15]
v_mfma_f32_16x16x32_bf16 a[12:15],v[36:39],v[92:95],a[12:15]
ds_read_b128 v[72:75],v112 offset:2048
v_mfma_f32_16x16x32_bf16 a[28:31],v[40:43],v[88:91],a[28:31]
ds_read_b128 v[76:79],v112 offset:3072
v_mfma_f32_16x16x32_bf16 a[28:31],v[44:47],v[92:95],a[28:31]
v_mfma_f32_16x16x32_bf16 a[44:47],v[48:51],v[88:91],a[44:47]
v_mfma_f32_16x16x32_bf16 a[44:47],v[52:55],v[92:95],a[44:47]
v_mfma_f32_16x16x32_bf16 a[60:63],v[56:59],v[88:91],a[60:63]
v_mfma_f32_16x16x32_bf16 a[60:63],v[60:63],v[92:95],a[60:63]
s_waitcnt lgkmcnt(3)
v_mfma_f32_16x16x32_bf16 a[0:3],v[0:3],v[64:67],a[0:3]
s_waitcnt lgkmcnt(2)
v_mfma_f32_16x16x32_bf16 a[0:3],v[4:7],v[68:71],a[0:3]
ds_read_b128 v[80:83],v112 offset:4096
v_mfma_f32_16x16x32_bf16 a[16:19],v[8:11],v[64:67],a[16:19]
ds_read_b128 v[84:87],v112 offset:5120
ds_read_b64_tr_b16 v[32:33],v100 offset:32768
v_mfma_f32_16x16x32_bf16 a[16:19],v[12:15],v[68:71],a[16:19]
ds_read_b64_tr_b16 v[34:35],v100 offset:34816
ds_read_b64_tr_b16 v[36:37],v100 offset:36864
v_mfma_f32_16x16x32_bf16 a[32:35],v[16:19],v[64:67],a[32:35]
ds_read_b64_tr_b16 v[38:39],v100 offset:38912
ds_read_b64_tr_b16 v[40:41],v101 offset:32768
v_mfma_f32_16x16x32_bf16 a[32:35],v[20:23],v[68:71],a[32:35]
ds_read_b64_tr_b16 v[42:43],v101 offset:34816
ds_read_b64_tr_b16 v[44:45],v101 offset:36864
v_mfma_f32_16x16x32_bf16 a[48:51],v[24:27],v[64:67],a[48:51]
ds_read_b64_tr_b16 v[46:47],v101 offset:38912
ds_read_b64_tr_b16 v[48:49],v102 offset:32768
ds_read_b64_tr_b16 v[54:55],v102 offset:38912
v_mfma_f32_16x16x32_bf16 a[48:51],v[28:31],v[68:71],a[48:51]
ds_read_b64_tr_b16 v[50:51],v102 offset:34816
ds_read_b64_tr_b16 v[52:53],v102 offset:36864
ds_read_b64_tr_b16 v[56:57],v103 offset:32768
ds_read_b64_tr_b16 v[60:61],v103 offset:36864
s_waitcnt lgkmcnt(15)
v_mfma_f32_16x16x32_bf16 a[4:7],v[0:3],v[72:75],a[4:7]
ds_read_b128 v[88:91],v112 offset:6144
ds_read_b128 v[92:95],v112 offset:7168
ds_read_b64_tr_b16 v[58:59],v103 offset:34816
ds_read_b64_tr_b16 v[62:63],v103 offset:38912
v_mfma_f32_16x16x32_bf16 a[4:7],v[4:7],v[76:79],a[4:7]
v_mfma_f32_16x16x32_bf16 a[20:23],v[8:11],v[72:75],a[20:23]
v_mfma_f32_16x16x32_bf16 a[20:23],v[12:15],v[76:79],a[20:23]
v_mfma_f32_16x16x32_bf16 a[36:39],v[16:19],v[72:75],a[36:39]
v_mfma_f32_16x16x32_bf16 a[36:39],v[20:23],v[76:79],a[36:39]
v_mfma_f32_16x16x32_bf16 a[52:55],v[24:27],v[72:75],a[52:55]
v_mfma_f32_16x16x32_bf16 a[52:55],v[28:31],v[76:79],a[52:55]
s_waitcnt lgkmcnt(0)
s_barrier
v_mfma_f32_16x16x32_bf16 a[8:11],v[0:3],v[80:83],a[8:11]
v_mfma_f32_16x16x32_bf16 a[8:11],v[4:7],v[84:87],a[8:11]
ds_read_b128 v[64:67],v112 offset:32768
v_mfma_f32_16x16x32_bf16 a[24:27],v[8:11],v[80:83],a[24:27]
ds_read_b128 v[68:71],v112 offset:33792
v_mfma_f32_16x16x32_bf16 a[24:27],v[12:15],v[84:87],a[24:27]
v_mfma_f32_16x16x32_bf16 a[40:43],v[16:19],v[80:83],a[40:43]
v_mfma_f32_16x16x32_bf16 a[40:43],v[20:23],v[84:87],a[40:43]
v_mfma_f32_16x16x32_bf16 a[56:59],v[24:27],v[80:83],a[56:59]
v_mfma_f32_16x16x32_bf16 a[56:59],v[28:31],v[84:87],a[56:59]
v_mfma_f32_16x16x32_bf16 a[12:15],v[0:3],v[88:91],a[12:15]
v_mfma_f32_16x16x32_bf16 a[12:15],v[4:7],v[92:95],a[12:15]
ds_read_b128 v[72:75],v112 offset:34816
v_mfma_f32_16x16x32_bf16 a[28:31],v[8:11],v[88:91],a[28:31]
ds_read_b128 v[76:79],v112 offset:35840
v_mfma_f32_16x16x32_bf16 a[28:31],v[12:15],v[92:95],a[28:31]
v_mfma_f32_16x16x32_bf16 a[44:47],v[16:19],v[88:91],a[44:47]
v_mfma_f32_16x16x32_bf16 a[44:47],v[20:23],v[92:95],a[44:47]
v_mfma_f32_16x16x32_bf16 a[60:63],v[24:27],v[88:91],a[60:63]
v_mfma_f32_16x16x32_bf16 a[60:63],v[28:31],v[92:95],a[60:63]
s_sub_u32 s62,s62,1
s_cmp_lg_u32 s62,0
s_cbranch_scc1 L_OJC_%=
s_waitcnt lgkmcnt(3)
v_mfma_f32_16x16x32_bf16 a[0:3],v[32:35],v[64:67],a[0:3]
s_waitcnt lgkmcnt(2)
v_mfma_f32_16x16x32_bf16 a[0:3],v[36:39],v[68:71],a[0:3]
ds_read_b128 v[80:83],v112 offset:36864
v_mfma_f32_16x16x32_bf16 a[16:19],v[40:43],v[64:67],a[16:19]
ds_read_b128 v[84:87],v112 offset:37888
v_mfma_f32_16x16x32_bf16 a[16:19],v[44:47],v[68:71],a[16:19]
v_mfma_f32_16x16x32_bf16 a[32:35],v[48:51],v[64:67],a[32:35]
v_mfma_f32_16x16x32_bf16 a[32:35],v[52:55],v[68:71],a[32:35]
v_mfma_f32_16x16x32_bf16 a[48:51],v[56:59],v[64:67],a[48:51]
v_mfma_f32_16x16x32_bf16 a[48:51],v[60:63],v[68:71],a[48:51]
s_waitcnt lgkmcnt(3)
v_mfma_f32_16x16x32_bf16 a[4:7],v[32:35],v[72:75],a[4:7]
ds_read_b128 v[88:91],v112 offset:38912
ds_read_b128 v[92:95],v112 offset:39936
s_waitcnt lgkmcnt(4)
v_mfma_f32_16x16x32_bf16 a[4:7],v[36:39],v[76:79],a[4:7]
v_mfma_f32_16x16x32_bf16 a[20:23],v[40:43],v[72:75],a[20:23]
v_mfma_f32_16x16x32_bf16 a[20:23],v[44:47],v[76:79],a[20:23]
v_mfma_f32_16x16x32_bf16 a[36:39],v[48:51],v[72:75],a[36:39]
v_mfma_f32_16x16x32_bf16 a[36:39],v[52:55],v[76:79],a[36:39]
v_mfma_f32_16x16x32_bf16 a[52:55],v[56:59],v[72:75],a[52:55]
v_mfma_f32_16x16x32_bf16 a[52:55],v[60:63],v[76:79],a[52:55]
s_waitcnt lgkmcnt(3)
v_mfma_f32_16x16x32_bf16 a[8:11],v[32:35],v[80:83],a[8:11]
s_waitcnt lgkmcnt(2)
v_mfma_f32_16x16x32_bf16 a[8:11],v[36:39],v[84:87],a[8:11]
v_mfma_f32_16x16x32_bf16 a[24:27],v[40:43],v[80:83],a[24:27]
v_mfma_f32_16x16x32_bf16 a[24:27],v[44:47],v[84:87],a[24:27]
v_mfma_f32_16x16x32_bf16 a[40:43],v[48:51],v[80:83],a[40:43]
v_mfma_f32_16x16x32_bf16 a[40:43],v[52:55],v[84:87],a[40:43]
v_mfma_f32_16x16x32_bf16 a[56:59],v[56:59],v[80:83],a[56:59]
v_mfma_f32_16x16x32_bf16 a[56:59],v[60:63],v[84:87],a[56:59]
s_waitcnt lgkmcnt(1)
v_mfma_f32_16x16x32_bf16 a[12:15],v[32:35],v[88:91],a[12:15]
s_waitcnt lgkmcnt(0)
v_mfma_f32_16x16x32_bf16 a[12:15],v[36:39],v[92:95],a[12:15]
v_mfma_f32_16x16x32_bf16 a[28:31],v[40:43],v[88:91],a[28:31]
v_mfma_f32_16x16x32_bf16 a[28:31],v[44:47],v[92:95],a[28:31]
v_mfma_f32_16x16x32_bf16 a[44:47],v[48:51],v[88:91],a[44:47]
v_mfma_f32_16x16x32_bf16 a[44:47],v[52:55],v[92:95],a[44:47]
v_mfma_f32_16x16x32_bf16 a[60:63],v[56:59],v[88:91],a[60:63]
v_mfma_f32_16x16x32_bf16 a[60:63],v[60:63],v[92:95],a[60:63]
s_nop 7
s_add_u32 s58,s60,0x1000
buffer_store_dwordx4 a[32:35],v127,s[48:51],s60 offen offset:0 sc1
buffer_store_dwordx4 a[36:39],v127,s[48:51],s60 offen offset:1024 sc1
buffer_store_dwordx4 a[40:43],v127,s[48:51],s60 offen offset:2048 sc1
buffer_store_dwordx4 a[44:47],v127,s[48:51],s60 offen offset:3072 sc1
buffer_store_dwordx4 a[48:51],v127,s[48:51],s58 offen offset:0 sc1
buffer_store_dwordx4 a[52:55],v127,s[48:51],s58 offen offset:1024 sc1
buffer_store_dwordx4 a[56:59],v127,s[48:51],s58 offen offset:2048 sc1
buffer_store_dwordx4 a[60:63],v127,s[48:51],s58 offen offset:3072 sc1
v_accvgpr_read_b32 v32,a0
v_accvgpr_read_b32 v33,a1
v_accvgpr_read_b32 v34,a2
v_accvgpr_read_b32 v35,a3
v_accvgpr_read_b32 v36,a4
v_accvgpr_read_b32 v37,a5
v_accvgpr_read_b32 v38,a6
v_accvgpr_read_b32 v39,a7
v_accvgpr_read_b32 v40,a8
v_accvgpr_read_b32 v41,a9
v_accvgpr_read_b32 v42,a10
v_accvgpr_read_b32 v43,a11
v_accvgpr_read_b32 v44,a12
v_accvgpr_read_b32 v45,a13
v_accvgpr_read_b32 v46,a14
v_accvgpr_read_b32 v47,a15
v_accvgpr_read_b32 v48,a16
)"
R"(v_accvgpr_read_b32 v49,a17
v_accvgpr_read_b32 v50,a18
v_accvgpr_read_b32 v51,a19
v_accvgpr_read_b32 v52,a20
v_accvgpr_read_b32 v53,a21
v_accvgpr_read_b32 v54,a22
v_accvgpr_read_b32 v55,a23
v_accvgpr_read_b32 v56,a24
v_accvgpr_read_b32 v57,a25
v_accvgpr_read_b32 v58,a26
v_accvgpr_read_b32 v59,a27
v_accvgpr_read_b32 v60,a28
v_accvgpr_read_b32 v61,a29
v_accvgpr_read_b32 v62,a30
v_accvgpr_read_b32 v63,a31
v_mov_b32 v118,1
v_mov_b32 v119,s78
s_waitcnt vmcnt(0)
s_mov_b64 exec,1
global_atomic_add v119,v118,%0
s_mov_b64 exec,-1
s_cmp_lg_u32 s56,0
s_cbranch_scc1 L_OX_%=
s_mov_b32 s77,0
L_OP_%=:
global_load_dword v120,v119,%0 sc1
s_waitcnt vmcnt(0)
v_readfirstlane_b32 s58,v120
s_nop 3
s_cmp_ge_u32 s58,8
s_cbranch_scc1 L_OX_%=
s_add_u32 s77,s77,1
s_cmp_lt_u32 s77,0x40000
s_cbranch_scc1 L_OP_%=
L_OX_%=:
s_barrier
s_add_u32 s58,s61,0x1000
buffer_load_dwordx4 v[0:3],v127,s[48:51],s61 offen offset:0 sc1
buffer_load_dwordx4 v[4:7],v127,s[48:51],s61 offen offset:1024 sc1
buffer_load_dwordx4 v[8:11],v127,s[48:51],s61 offen offset:2048 sc1
buffer_load_dwordx4 v[12:15],v127,s[48:51],s61 offen offset:3072 sc1
buffer_load_dwordx4 v[16:19],v127,s[48:51],s58 offen offset:0 sc1
buffer_load_dwordx4 v[20:23],v127,s[48:51],s58 offen offset:1024 sc1
buffer_load_dwordx4 v[24:27],v127,s[48:51],s58 offen offset:2048 sc1
buffer_load_dwordx4 v[28:31],v127,s[48:51],s58 offen offset:3072 sc1
s_add_u32 s59,s72,0
s_waitcnt vmcnt(7)
v_pk_add_f32 v[0:1],v[0:1],v[32:33]
v_pk_add_f32 v[2:3],v[2:3],v[34:35]
buffer_store_dwordx4 v[0:3],v116,s[52:55],s59 offen offset:0 
s_add_u32 s59,s72,81920
s_waitcnt vmcnt(7)
v_pk_add_f32 v[4:5],v[4:5],v[36:37]
v_pk_add_f32 v[6:7],v[6:7],v[38:39]
buffer_store_dwordx4 v[4:7],v116,s[52:55],s59 offen offset:0 
s_add_u32 s59,s72,163840
s_waitcnt vmcnt(7)
v_pk_add_f32 v[8:9],v[8:9],v[40:41]
v_pk_add_f32 v[10:11],v[10:11],v[42:43]
buffer_store_dwordx4 v[8:11],v116,s[52:55],s59 offen offset:0 
s_add_u32 s59,s72,245760
s_waitcnt vmcnt(7)
v_pk_add_f32 v[12:13],v[12:13],v[44:45]
v_pk_add_f32 v[14:15],v[14:15],v[46:47]
buffer_store_dwordx4 v[12:15],v116,s[52:55],s59 offen offset:0 
s_add_u32 s59,s72,0
s_waitcnt vmcnt(7)
v_pk_add_f32 v[16:17],v[16:17],v[48:49]
v_pk_add_f32 v[18:19],v[18:19],v[50:51]
buffer_store_dwordx4 v[16:19],v116,s[52:55],s59 offen offset:64 
s_add_u32 s59,s72,81920
s_waitcnt vmcnt(7)
v_pk_add_f32 v[20:21],v[20:21],v[52:53]
v_pk_add_f32 v[22:23],v[22:23],v[54:55]
buffer_store_dwordx4 v[20:23],v116,s[52:55],s59 offen offset:64 
s_add_u32 s59,s72,163840
s_waitcnt vmcnt(7)
v_pk_add_f32 v[24:25],v[24:25],v[56:57]
v_pk_add_f32 v[26:27],v[26:27],v[58:59]
buffer_store_dwordx4 v[24:27],v116,s[52:55],s59 offen offset:64 
s_add_u32 s59,s72,245760
s_waitcnt vmcnt(7)
v_pk_add_f32 v[28:29],v[28:29],v[60:61]
v_pk_add_f32 v[30:31],v[30:31],v[62:63]
buffer_store_dwordx4 v[28:31],v116,s[52:55],s59 offen offset:64 
s_branch L_END_%=
L_Y_%=:
s_cmp_eq_u32 s79,0
s_cbranch_scc1 L_YC_%=
s_mov_b64 s[36:37],%0
s_add_u32 s36,s36,52428800
s_addc_u32 s37,s37,0
s_mov_b32 s38,0x500000
s_mov_b32 s39,0x20000
s_mov_b64 s[40:41],%2
s_mov_b32 s42,0xc8000
s_mov_b32 s43,0x20000
s_mov_b64 s[44:45],%3
s_mov_b32 s46,0xc8000
s_mov_b32 s47,0x20000
v_and_b32 v118,7,v126
v_lshrrev_b32 v119,4,v126
v_mov_b32 v120,v119
v_xor_b32 v120,v120,v118
v_lshlrev_b32 v120,4,v120
v_lshrrev_b32 v125,3,v126
v_mul_u32_u24 v125,1280,v125
v_add3_u32 v96,v125,v120,0
v_or_b32 v120,4,v119
v_xor_b32 v120,v120,v118
v_lshlrev_b32 v120,4,v120
v_lshrrev_b32 v125,3,v126
v_mul_u32_u24 v125,1280,v125
v_add3_u32 v97,v125,v120,0
v_add_u32 v97,9216,v97
v_mov_b32 v120,v119
v_xor_b32 v120,v120,v118
v_lshlrev_b32 v120,4,v120
v_lshrrev_b32 v125,3,v126
v_mul_u32_u24 v125,1280,v125
v_add3_u32 v98,v125,v120,0
v_add_u32 v98,18432,v98
v_or_b32 v120,4,v119
v_xor_b32 v120,v120,v118
v_lshlrev_b32 v120,4,v120
v_lshrrev_b32 v125,3,v126
v_mul_u32_u24 v125,1280,v125
v_add3_u32 v99,v125,v120,0
v_add_u32 v99,27648,v99
s_lshl_b32 s66,s74,14
s_lshl_b32 s58,s56,12
s_add_u32 s66,s66,s58
s_add_u32 s70,s57,s58
s_lshl_b32 s58,s75,7
s_lshl_b32 s59,s56,5
s_add_u32 s58,s58,s59
s_mul_i32 s68,s58,1280
s_add_u32 m0,s70,16384
s_nop 0
buffer_load_dwordx4 v96,s[40:43],s68 offen offset:0 lds
buffer_load_dwordx4 v97,s[40:43],s68 offen offset:1024 lds
buffer_load_dwordx4 v98,s[40:43],s68 offen offset:2048 lds
buffer_load_dwordx4 v99,s[40:43],s68 offen offset:3072 lds
s_add_u32 s68,s68,128
s_add_u32 m0,s70,0
s_nop 0
buffer_load_dwordx4 v127,s[36:39],s66 offen offset:0 lds
buffer_load_dwordx4 v127,s[36:39],s66 offen offset:1024 lds
buffer_load_dwordx4 v127,s[36:39],s66 offen offset:2048 lds
buffer_load_dwordx4 v127,s[36:39],s66 offen offset:3072 lds
s_add_u32 s66,s66,262144
s_add_u32 m0,s70,49152
s_nop 0
buffer_load_dwordx4 v96,s[40:43],s68 offen offset:0 lds
buffer_load_dwordx4 v97,s[40:43],s68 offen offset:1024 lds
buffer_load_dwordx4 v98,s[40:43],s68 offen offset:2048 lds
buffer_load_dwordx4 v99,s[40:43],s68 offen offset:3072 lds
s_add_u32 s68,s68,128
s_add_u32 m0,s70,32768
s_nop 0
buffer_load_dwordx4 v127,s[36:39],s66 offen offset:0 lds
buffer_load_dwordx4 v127,s[36:39],s66 offen offset:1024 lds
buffer_load_dwordx4 v127,s[36:39],s66 offen offset:2048 lds
buffer_load_dwordx4 v127,s[36:39],s66 offen offset:3072 lds
s_add_u32 s66,s66,262144
s_add_u32 m0,s70,81920
s_nop 0
buffer_load_dwordx4 v96,s[40:43],s68 offen offset:0 lds
buffer_load_dwordx4 v97,s[40:43],s68 offen offset:1024 lds
buffer_load_dwordx4 v98,s[40:43],s68 offen offset:2048 lds
buffer_load_dwordx4 v99,s[40:43],s68 offen offset:3072 lds
s_add_u32 s68,s68,128
s_add_u32 m0,s70,65536
s_nop 0
buffer_load_dwordx4 v127,s[36:39],s66 offen offset:0 lds
buffer_load_dwordx4 v127,s[36:39],s66 offen offset:1024 lds
buffer_load_dwordx4 v127,s[36:39],s66 offen offset:2048 lds
buffer_load_dwordx4 v127,s[36:39],s66 offen offset:3072 lds
s_add_u32 s66,s66,262144
s_add_u32 m0,s70,114688
s_nop 0
buffer_load_dwordx4 v96,s[40:43],s68 offen offset:0 lds
buffer_load_dwordx4 v97,s[40:43],s68 offen offset:1024 lds
buffer_load_dwordx4 v98,s[40:43],s68 offen offset:2048 lds
buffer_load_dwordx4 v99,s[40:43],s68 offen offset:3072 lds
s_add_u32 s68,s68,128
s_add_u32 m0,s70,98304
s_nop 0
buffer_load_dwordx4 v127,s[36:39],s66 offen offset:0 lds
buffer_load_dwordx4 v127,s[36:39],s66 offen offset:1024 lds
buffer_load_dwordx4 v127,s[36:39],s66 offen offset:2048 lds
buffer_load_dwordx4 v127,s[36:39],s66 offen offset:3072 lds
s_add_u32 s66,s66,262144
s_add_u32 m0,s70,147456
s_nop 0
buffer_load_dwordx4 v96,s[40:43],s68 offen offset:0 lds
buffer_load_dwordx4 v97,s[40:43],s68 offen offset:1024 lds
buffer_load_dwordx4 v98,s[40:43],s68 offen offset:2048 lds
buffer_load_dwordx4 v99,s[40:43],s68 offen offset:3072 lds
s_add_u32 s68,s68,128
s_add_u32 m0,s70,131072
s_nop 0
buffer_load_dwordx4 v127,s[36:39],s66 offen offset:0 lds
buffer_load_dwordx4 v127,s[36:39],s66 offen offset:1024 lds
buffer_load_dwordx4 v127,s[36:39],s66 offen offset:2048 lds
buffer_load_dwordx4 v127,s[36:39],s66 offen offset:3072 lds
s_add_u32 s66,s66,262144
s_waitcnt vmcnt(28) lgkmcnt(0)
s_barrier
s_barrier
s_add_u32 m0,s70,16384
s_nop 0
buffer_load_dwordx4 v96,s[40:43],s68 offen offset:0 lds
buffer_load_dwordx4 v97,s[40:43],s68 offen offset:1024 lds
buffer_load_dwordx4 v98,s[40:43],s68 offen offset:2048 lds
buffer_load_dwordx4 v99,s[40:43],s68 offen offset:3072 lds
s_add_u32 s68,s68,128
s_waitcnt vmcnt(24) lgkmcnt(0)
s_barrier
s_add_u32 m0,s70,0
s_nop 0
buffer_load_dwordx4 v127,s[36:39],s66 offen offset:0 lds
buffer_load_dwordx4 v127,s[36:39],s66 offen offset:1024 lds
buffer_load_dwordx4 v127,s[36:39],s66 offen offset:2048 lds
buffer_load_dwordx4 v127,s[36:39],s66 offen offset:3072 lds
s_add_u32 s66,s66,262144
s_add_u32 m0,s70,49152
s_nop 0
buffer_load_dwordx4 v96,s[40:43],s68 offen offset:0 lds
buffer_load_dwordx4 v97,s[40:43],s68 offen offset:1024 lds
buffer_load_dwordx4 v98,s[40:43],s68 offen offset:2048 lds
buffer_load_dwordx4 v99,s[40:43],s68 offen offset:3072 lds
s_add_u32 s68,s68,128
s_waitcnt vmcnt(24) lgkmcnt(0)
s_barrier
s_add_u32 m0,s70,32768
s_nop 0
buffer_load_dwordx4 v127,s[36:39],s66 offen offset:0 lds
buffer_load_dwordx4 v127,s[36:39],s66 offen offset:1024 lds
buffer_load_dwordx4 v127,s[36:39],s66 offen offset:2048 lds
buffer_load_dwordx4 v127,s[36:39],s66 offen offset:3072 lds
s_add_u32 s66,s66,262144
s_add_u32 m0,s70,81920
s_nop 0
buffer_load_dwordx4 v96,s[40:43],s68 offen offset:0 lds
buffer_load_dwordx4 v97,s[40:43],s68 offen offset:1024 lds
buffer_load_dwordx4 v98,s[40:43],s68 offen offset:2048 lds
buffer_load_dwordx4 v99,s[40:43],s68 offen offset:3072 lds
s_add_u32 s68,s68,128
s_waitcnt vmcnt(24) lgkmcnt(0)
s_barrier
s_add_u32 m0,s70,65536
s_nop 0
buffer_load_dwordx4 v127,s[36:39],s66 offen offset:0 lds
buffer_load_dwordx4 v127,s[36:39],s66 offen offset:1024 lds
buffer_load_dwordx4 v127,s[36:39],s66 offen offset:2048 lds
buffer_load_dwordx4 v127,s[36:39],s66 offen offset:3072 lds
s_add_u32 s66,s66,262144
s_add_u32 m0,s70,114688
s_nop 0
buffer_load_dwordx4 v96,s[40:43],s68 offen offset:0 lds
buffer_load_dwordx4 v97,s[40:43],s68 offen offset:1024 lds
buffer_load_dwordx4 v98,s[40:43],s68 offen offset:2048 lds
buffer_load_dwordx4 v99,s[40:43],s68 offen offset:3072 lds
s_add_u32 s68,s68,128
s_waitcnt vmcnt(24) lgkmcnt(0)
s_barrier
s_add_u32 m0,s70,98304
s_nop 0
buffer_load_dwordx4 v127,s[36:39],s66 offen offset:0 lds
buffer_load_dwordx4 v127,s[36:39],s66 offen offset:1024 lds
buffer_load_dwordx4 v127,s[36:39],s66 offen offset:2048 lds
buffer_load_dwordx4 v127,s[36:39],s66 offen offset:3072 lds
s_add_u32 s66,s66,262144
s_add_u32 m0,s70,147456
s_nop 0
buffer_load_dwordx4 v96,s[40:43],s68 offen offset:0 lds
buffer_load_dwordx4 v97,s[40:43],s68 offen offset:1024 lds
buffer_load_dwordx4 v98,s[40:43],s68 offen offset:2048 lds
buffer_load_dwordx4 v99,s[40:43],s68 offen offset:3072 lds
s_sub_u32 s68,s68,1152
s_waitcnt vmcnt(24) lgkmcnt(0)
s_barrier
s_add_u32 m0,s70,131072
s_nop 0
buffer_load_dwordx4 v127,s[36:39],s66 offen offset:0 lds
buffer_load_dwordx4 v127,s[36:39],s66 offen offset:1024 lds
buffer_load_dwordx4 v127,s[36:39],s66 offen offset:2048 lds
buffer_load_dwordx4 v127,s[36:39],s66 offen offset:3072 lds
s_add_u32 s66,s66,262144
s_mov_b32 s62,2
L_YJL_%=:
s_add_u32 m0,s70,16384
s_nop 0
buffer_load_dwordx4 v96,s[44:47],s68 offen offset:0 lds
buffer_load_dwordx4 v97,s[44:47],s68 offen offset:1024 lds
buffer_load_dwordx4 v98,s[44:47],s68 offen offset:2048 lds
buffer_load_dwordx4 v99,s[44:47],s68 offen offset:3072 lds
s_add_u32 s68,s68,128
s_waitcnt vmcnt(24) lgkmcnt(0)
s_barrier
s_add_u32 m0,s70,0
s_nop 0
buffer_load_dwordx4 v127,s[36:39],s66 offen offset:0 lds
buffer_load_dwordx4 v127,s[36:39],s66 offen offset:1024 lds
buffer_load_dwordx4 v127,s[36:39],s66 offen offset:2048 lds
buffer_load_dwordx4 v127,s[36:39],s66 offen offset:3072 lds
s_add_u32 s66,s66,262144
s_add_u32 m0,s70,49152
s_nop 0
buffer_load_dwordx4 v96,s[44:47],s68 offen offset:0 lds
buffer_load_dwordx4 v97,s[44:47],s68 offen offset:1024 lds
buffer_load_dwordx4 v98,s[44:47],s68 offen offset:2048 lds
buffer_load_dwordx4 v99,s[44:47],s68 offen offset:3072 lds
s_add_u32 s68,s68,128
s_waitcnt vmcnt(24) lgkmcnt(0)
s_barrier
s_add_u32 m0,s70,32768
s_nop 0
buffer_load_dwordx4 v127,s[36:39],s66 offen offset:0 lds
buffer_load_dwordx4 v127,s[36:39],s66 offen offset:1024 lds
buffer_load_dwordx4 v127,s[36:39],s66 offen offset:2048 lds
buffer_load_dwordx4 v127,s[36:39],s66 offen offset:3072 lds
s_add_u32 s66,s66,262144
s_add_u32 m0,s70,81920
s_nop 0
buffer_load_dwordx4 v96,s[44:47],s68 offen offset:0 lds
buffer_load_dwordx4 v97,s[44:47],s68 offen offset:1024 lds
buffer_load_dwordx4 v98,s[44:47],s68 offen offset:2048 lds
buffer_load_dwordx4 v99,s[44:47],s68 offen offset:3072 lds
s_add_u32 s68,s68,128
s_waitcnt vmcnt(24) lgkmcnt(0)
s_barrier
s_add_u32 m0,s70,65536
s_nop 0
buffer_load_dwordx4 v127,s[36:39],s66 offen offset:0 lds
buffer_load_dwordx4 v127,s[36:39],s66 offen offset:1024 lds
buffer_load_dwordx4 v127,s[36:39],s66 offen offset:2048 lds
buffer_load_dwordx4 v127,s[36:39],s66 offen offset:3072 lds
s_add_u32 s66,s66,262144
s_add_u32 m0,s70,114688
s_nop 0
buffer_load_dwordx4 v96,s[44:47],s68 offen offset:0 lds
buffer_load_dwordx4 v97,s[44:47],s68 offen offset:1024 lds
buffer_load_dwordx4 v98,s[44:47],s68 offen offset:2048 lds
buffer_load_dwordx4 v99,s[44:47],s68 offen offset:3072 lds
s_add_u32 s68,s68,128
s_waitcnt vmcnt(24) lgkmcnt(0)
s_barrier
s_add_u32 m0,s70,98304
s_nop 0
buffer_load_dwordx4 v127,s[36:39],s66 offen offset:0 lds
buffer_load_dwordx4 v127,s[36:39],s66 offen offset:1024 lds
buffer_load_dwordx4 v127,s[36:39],s66 offen offset:2048 lds
buffer_load_dwordx4 v127,s[36:39],s66 offen offset:3072 lds
s_add_u32 s66,s66,262144
s_add_u32 m0,s70,147456
s_nop 0
buffer_load_dwordx4 v96,s[44:47],s68 offen offset:0 lds
buffer_load_dwordx4 v97,s[44:47],s68 offen offset:1024 lds
buffer_load_dwordx4 v98,s[44:47],s68 offen offset:2048 lds
buffer_load_dwordx4 v99,s[44:47],s68 offen offset:3072 lds
s_add_u32 s68,s68,128
s_waitcnt vmcnt(24) lgkmcnt(0)
s_barrier
s_add_u32 m0,s70,131072
s_nop 0
buffer_load_dwordx4 v127,s[36:39],s66 offen offset:0 lds
buffer_load_dwordx4 v127,s[36:39],s66 offen offset:1024 lds
buffer_load_dwordx4 v127,s[36:39],s66 offen offset:2048 lds
buffer_load_dwordx4 v127,s[36:39],s66 offen offset:3072 lds
s_add_u32 s66,s66,262144
s_sub_u32 s62,s62,1
s_cmp_lg_u32 s62,0
s_cbranch_scc1 L_YJL_%=
s_waitcnt vmcnt(20) lgkmcnt(0)
s_barrier
s_waitcnt vmcnt(12) lgkmcnt(0)
s_barrier
s_waitcnt vmcnt(4) lgkmcnt(0)
s_barrier
s_waitcnt vmcnt(0) lgkmcnt(0)
s_barrier
s_branch L_END_%=
L_YC_%=:
s_mov_b64 s[48:49],%4
s_mov_b32 s50,0x280000
s_mov_b32 s51,0x20000
s_mov_b64 s[40:41],%5
s_mov_b32 s42,0x500
s_mov_b32 s43,0x20000
v_and_b32 v118,15,v126
v_mul_u32_u24 v118,1280,v118
v_lshrrev_b32 v119,4,v126
v_lshlrev_b32 v119,3,v119
v_add_u32 v118,v118,v119
s_lshl_b32 s58,s74,7
s_lshr_b32 s59,s56,1
s_lshl_b32 s59,s59,6
s_add_u32 s58,s58,s59
)"
R"(s_mul_i32 s58,s58,1280
s_lshl_b32 s60,s75,8
s_and_b32 s59,s56,1
s_lshl_b32 s59,s59,7
s_add_u32 s60,s60,s59
s_add_u32 s58,s58,s60
buffer_load_dwordx2 v[56:57],v119,s[40:43],s60 offen offset:0
buffer_load_dwordx2 v[58:59],v119,s[40:43],s60 offen offset:32
buffer_load_dwordx2 v[60:61],v119,s[40:43],s60 offen offset:64
buffer_load_dwordx2 v[62:63],v119,s[40:43],s60 offen offset:96
buffer_load_dwordx2 v[64:65],v118,s[48:51],s58 offen offset:0
buffer_load_dwordx2 v[72:73],v118,s[48:51],s58 offen offset:32
buffer_load_dwordx2 v[80:81],v118,s[48:51],s58 offen offset:64
buffer_load_dwordx2 v[88:89],v118,s[48:51],s58 offen offset:96
s_add_u32 s58,s58,20480
buffer_load_dwordx2 v[66:67],v118,s[48:51],s58 offen offset:0
buffer_load_dwordx2 v[74:75],v118,s[48:51],s58 offen offset:32
buffer_load_dwordx2 v[82:83],v118,s[48:51],s58 offen offset:64
buffer_load_dwordx2 v[90:91],v118,s[48:51],s58 offen offset:96
s_add_u32 s58,s58,20480
buffer_load_dwordx2 v[68:69],v118,s[48:51],s58 offen offset:0
buffer_load_dwordx2 v[76:77],v118,s[48:51],s58 offen offset:32
buffer_load_dwordx2 v[84:85],v118,s[48:51],s58 offen offset:64
buffer_load_dwordx2 v[92:93],v118,s[48:51],s58 offen offset:96
s_add_u32 s58,s58,20480
buffer_load_dwordx2 v[70:71],v118,s[48:51],s58 offen offset:0
buffer_load_dwordx2 v[78:79],v118,s[48:51],s58 offen offset:32
buffer_load_dwordx2 v[86:87],v118,s[48:51],s58 offen offset:64
buffer_load_dwordx2 v[94:95],v118,s[48:51],s58 offen offset:96
s_lshr_b32 s58,s56,1
s_lshl_b32 s58,s58,13
s_add_u32 s58,s58,s57
v_lshrrev_b32 v118,4,v126
v_bfe_u32 v119,v126,2,2
v_lshl_add_u32 v118,v118,3,v119
v_lshlrev_b32 v118,4,v118
v_bfe_u32 v119,v126,1,1
v_mul_u32_u24 v119,1088,v119
v_add_u32 v118,v118,v119
v_and_b32 v119,1,v126
v_lshl_add_u32 v118,v119,3,v118
v_xor_b32 v119,64,v118
v_add_u32 v100,s58,v118
v_add_u32 v101,s58,v119
v_add_u32 v104,0x10000,v100
v_add_u32 v108,0x20000,v100
v_add_u32 v105,0x10000,v101
v_add_u32 v109,0x20000,v101
s_and_b32 s58,s56,1
s_lshl_b32 s58,s58,13
s_add_u32 s58,s58,s57
s_add_u32 s58,s58,0x4000
v_lshrrev_b32 v118,4,v126
v_and_b32 v119,15,v126
v_lshrrev_b32 v120,1,v119
v_or_b32 v121,0,v118
v_xor_b32 v121,v121,v120
v_lshlrev_b32 v121,4,v121
v_lshl_add_u32 v121,v119,7,v121
v_add_u32 v112,s58,v121
v_or_b32 v121,4,v118
v_xor_b32 v121,v121,v120
v_lshlrev_b32 v121,4,v121
v_lshl_add_u32 v121,v119,7,v121
v_add_u32 v113,s58,v121
v_add_u32 v114,0x10000,v112
v_add_u32 v123,0x20000,v112
v_add_u32 v115,0x10000,v113
v_add_u32 v124,0x20000,v113
v_and_b32 v118,15,v126
v_mul_u32_u24 v118,5120,v118
v_lshrrev_b32 v119,4,v126
v_lshl_add_u32 v116,v119,4,v118
s_lshl_b32 s58,s74,7
s_lshr_b32 s59,s56,1
s_lshl_b32 s59,s59,6
s_add_u32 s58,s58,s59
s_mul_i32 s72,s58,5120
s_lshl_b32 s58,s75,9
s_add_u32 s72,s72,s58
s_and_b32 s58,s56,1
s_lshl_b32 s58,s58,8
s_add_u32 s72,s72,s58
s_mov_b64 s[52:53],%6
s_mov_b32 s54,0xa00000
s_mov_b32 s55,0x20000
s_waitcnt vmcnt(0)
v_lshlrev_b32 v32,16,v56
v_and_b32 v33,0xffff0000,v56
v_lshlrev_b32 v34,16,v57
v_and_b32 v35,0xffff0000,v57
v_lshlrev_b32 v36,16,v58
v_and_b32 v37,0xffff0000,v58
v_lshlrev_b32 v38,16,v59
v_and_b32 v39,0xffff0000,v59
v_lshlrev_b32 v40,16,v60
v_and_b32 v41,0xffff0000,v60
v_lshlrev_b32 v42,16,v61
v_and_b32 v43,0xffff0000,v61
v_lshlrev_b32 v44,16,v62
v_and_b32 v45,0xffff0000,v62
v_lshlrev_b32 v46,16,v63
v_and_b32 v47,0xffff0000,v63
v_lshlrev_b32 v0,16,v64
v_and_b32 v1,0xffff0000,v64
v_lshlrev_b32 v2,16,v65
v_and_b32 v3,0xffff0000,v65
v_pk_mul_f32 v[0:1],v[0:1],v[32:33]
v_pk_mul_f32 v[2:3],v[2:3],v[34:35]
v_cvt_pk_bf16_f32 v4,v0,v1
v_cvt_pk_bf16_f32 v5,v2,v3
v_lshlrev_b32 v0,16,v4
v_and_b32 v1,0xffff0000,v4
v_lshlrev_b32 v2,16,v5
v_and_b32 v3,0xffff0000,v5
v_accvgpr_write_b32 a0,v0
v_accvgpr_write_b32 a1,v1
v_accvgpr_write_b32 a2,v2
v_accvgpr_write_b32 a3,v3
v_lshlrev_b32 v8,16,v72
v_and_b32 v9,0xffff0000,v72
v_lshlrev_b32 v10,16,v73
v_and_b32 v11,0xffff0000,v73
v_pk_mul_f32 v[8:9],v[8:9],v[36:37]
v_pk_mul_f32 v[10:11],v[10:11],v[38:39]
v_cvt_pk_bf16_f32 v12,v8,v9
v_cvt_pk_bf16_f32 v13,v10,v11
v_lshlrev_b32 v8,16,v12
v_and_b32 v9,0xffff0000,v12
v_lshlrev_b32 v10,16,v13
v_and_b32 v11,0xffff0000,v13
v_accvgpr_write_b32 a16,v8
v_accvgpr_write_b32 a17,v9
v_accvgpr_write_b32 a18,v10
v_accvgpr_write_b32 a19,v11
v_lshlrev_b32 v16,16,v80
v_and_b32 v17,0xffff0000,v80
v_lshlrev_b32 v18,16,v81
v_and_b32 v19,0xffff0000,v81
v_pk_mul_f32 v[16:17],v[16:17],v[40:41]
v_pk_mul_f32 v[18:19],v[18:19],v[42:43]
v_cvt_pk_bf16_f32 v20,v16,v17
v_cvt_pk_bf16_f32 v21,v18,v19
v_lshlrev_b32 v16,16,v20
v_and_b32 v17,0xffff0000,v20
v_lshlrev_b32 v18,16,v21
v_and_b32 v19,0xffff0000,v21
v_accvgpr_write_b32 a32,v16
v_accvgpr_write_b32 a33,v17
v_accvgpr_write_b32 a34,v18
v_accvgpr_write_b32 a35,v19
v_lshlrev_b32 v24,16,v88
v_and_b32 v25,0xffff0000,v88
v_lshlrev_b32 v26,16,v89
v_and_b32 v27,0xffff0000,v89
v_pk_mul_f32 v[24:25],v[24:25],v[44:45]
v_pk_mul_f32 v[26:27],v[26:27],v[46:47]
v_cvt_pk_bf16_f32 v28,v24,v25
v_cvt_pk_bf16_f32 v29,v26,v27
v_lshlrev_b32 v24,16,v28
v_and_b32 v25,0xffff0000,v28
v_lshlrev_b32 v26,16,v29
v_and_b32 v27,0xffff0000,v29
v_accvgpr_write_b32 a48,v24
v_accvgpr_write_b32 a49,v25
v_accvgpr_write_b32 a50,v26
v_accvgpr_write_b32 a51,v27
v_lshlrev_b32 v0,16,v66
v_and_b32 v1,0xffff0000,v66
v_lshlrev_b32 v2,16,v67
v_and_b32 v3,0xffff0000,v67
v_pk_mul_f32 v[0:1],v[0:1],v[32:33]
v_pk_mul_f32 v[2:3],v[2:3],v[34:35]
v_cvt_pk_bf16_f32 v4,v0,v1
v_cvt_pk_bf16_f32 v5,v2,v3
v_lshlrev_b32 v0,16,v4
v_and_b32 v1,0xffff0000,v4
v_lshlrev_b32 v2,16,v5
v_and_b32 v3,0xffff0000,v5
v_accvgpr_write_b32 a4,v0
v_accvgpr_write_b32 a5,v1
v_accvgpr_write_b32 a6,v2
v_accvgpr_write_b32 a7,v3
v_lshlrev_b32 v8,16,v74
v_and_b32 v9,0xffff0000,v74
v_lshlrev_b32 v10,16,v75
v_and_b32 v11,0xffff0000,v75
v_pk_mul_f32 v[8:9],v[8:9],v[36:37]
v_pk_mul_f32 v[10:11],v[10:11],v[38:39]
v_cvt_pk_bf16_f32 v12,v8,v9
v_cvt_pk_bf16_f32 v13,v10,v11
v_lshlrev_b32 v8,16,v12
v_and_b32 v9,0xffff0000,v12
v_lshlrev_b32 v10,16,v13
v_and_b32 v11,0xffff0000,v13
v_accvgpr_write_b32 a20,v8
v_accvgpr_write_b32 a21,v9
v_accvgpr_write_b32 a22,v10
v_accvgpr_write_b32 a23,v11
v_lshlrev_b32 v16,16,v82
v_and_b32 v17,0xffff0000,v82
v_lshlrev_b32 v18,16,v83
v_and_b32 v19,0xffff0000,v83
v_pk_mul_f32 v[16:17],v[16:17],v[40:41]
v_pk_mul_f32 v[18:19],v[18:19],v[42:43]
v_cvt_pk_bf16_f32 v20,v16,v17
v_cvt_pk_bf16_f32 v21,v18,v19
v_lshlrev_b32 v16,16,v20
v_and_b32 v17,0xffff0000,v20
v_lshlrev_b32 v18,16,v21
v_and_b32 v19,0xffff0000,v21
v_accvgpr_write_b32 a36,v16
v_accvgpr_write_b32 a37,v17
v_accvgpr_write_b32 a38,v18
v_accvgpr_write_b32 a39,v19
v_lshlrev_b32 v24,16,v90
v_and_b32 v25,0xffff0000,v90
v_lshlrev_b32 v26,16,v91
v_and_b32 v27,0xffff0000,v91
v_pk_mul_f32 v[24:25],v[24:25],v[44:45]
v_pk_mul_f32 v[26:27],v[26:27],v[46:47]
v_cvt_pk_bf16_f32 v28,v24,v25
v_cvt_pk_bf16_f32 v29,v26,v27
v_lshlrev_b32 v24,16,v28
v_and_b32 v25,0xffff0000,v28
v_lshlrev_b32 v26,16,v29
v_and_b32 v27,0xffff0000,v29
v_accvgpr_write_b32 a52,v24
v_accvgpr_write_b32 a53,v25
v_accvgpr_write_b32 a54,v26
v_accvgpr_write_b32 a55,v27
v_lshlrev_b32 v0,16,v68
v_and_b32 v1,0xffff0000,v68
v_lshlrev_b32 v2,16,v69
v_and_b32 v3,0xffff0000,v69
v_pk_mul_f32 v[0:1],v[0:1],v[32:33]
v_pk_mul_f32 v[2:3],v[2:3],v[34:35]
v_cvt_pk_bf16_f32 v4,v0,v1
v_cvt_pk_bf16_f32 v5,v2,v3
v_lshlrev_b32 v0,16,v4
v_and_b32 v1,0xffff0000,v4
v_lshlrev_b32 v2,16,v5
v_and_b32 v3,0xffff0000,v5
v_accvgpr_write_b32 a8,v0
v_accvgpr_write_b32 a9,v1
v_accvgpr_write_b32 a10,v2
v_accvgpr_write_b32 a11,v3
v_lshlrev_b32 v8,16,v76
v_and_b32 v9,0xffff0000,v76
v_lshlrev_b32 v10,16,v77
v_and_b32 v11,0xffff0000,v77
v_pk_mul_f32 v[8:9],v[8:9],v[36:37]
v_pk_mul_f32 v[10:11],v[10:11],v[38:39]
v_cvt_pk_bf16_f32 v12,v8,v9
v_cvt_pk_bf16_f32 v13,v10,v11
v_lshlrev_b32 v8,16,v12
v_and_b32 v9,0xffff0000,v12
v_lshlrev_b32 v10,16,v13
v_and_b32 v11,0xffff0000,v13
v_accvgpr_write_b32 a24,v8
v_accvgpr_write_b32 a25,v9
v_accvgpr_write_b32 a26,v10
v_accvgpr_write_b32 a27,v11
v_lshlrev_b32 v16,16,v84
v_and_b32 v17,0xffff0000,v84
v_lshlrev_b32 v18,16,v85
v_and_b32 v19,0xffff0000,v85
v_pk_mul_f32 v[16:17],v[16:17],v[40:41]
v_pk_mul_f32 v[18:19],v[18:19],v[42:43]
v_cvt_pk_bf16_f32 v20,v16,v17
v_cvt_pk_bf16_f32 v21,v18,v19
v_lshlrev_b32 v16,16,v20
v_and_b32 v17,0xffff0000,v20
v_lshlrev_b32 v18,16,v21
v_and_b32 v19,0xffff0000,v21
v_accvgpr_write_b32 a40,v16
v_accvgpr_write_b32 a41,v17
v_accvgpr_write_b32 a42,v18
v_accvgpr_write_b32 a43,v19
v_lshlrev_b32 v24,16,v92
v_and_b32 v25,0xffff0000,v92
v_lshlrev_b32 v26,16,v93
v_and_b32 v27,0xffff0000,v93
v_pk_mul_f32 v[24:25],v[24:25],v[44:45]
v_pk_mul_f32 v[26:27],v[26:27],v[46:47]
v_cvt_pk_bf16_f32 v28,v24,v25
v_cvt_pk_bf16_f32 v29,v26,v27
v_lshlrev_b32 v24,16,v28
v_and_b32 v25,0xffff0000,v28
v_lshlrev_b32 v26,16,v29
v_and_b32 v27,0xffff0000,v29
v_accvgpr_write_b32 a56,v24
v_accvgpr_write_b32 a57,v25
v_accvgpr_write_b32 a58,v26
v_accvgpr_write_b32 a59,v27
v_lshlrev_b32 v0,16,v70
v_and_b32 v1,0xffff0000,v70
v_lshlrev_b32 v2,16,v71
v_and_b32 v3,0xffff0000,v71
v_pk_mul_f32 v[0:1],v[0:1],v[32:33]
v_pk_mul_f32 v[2:3],v[2:3],v[34:35]
v_cvt_pk_bf16_f32 v4,v0,v1
v_cvt_pk_bf16_f32 v5,v2,v3
v_lshlrev_b32 v0,16,v4
v_and_b32 v1,0xffff0000,v4
v_lshlrev_b32 v2,16,v5
v_and_b32 v3,0xffff0000,v5
v_accvgpr_write_b32 a12,v0
v_accvgpr_write_b32 a13,v1
v_accvgpr_write_b32 a14,v2
v_accvgpr_write_b32 a15,v3
v_lshlrev_b32 v8,16,v78
v_and_b32 v9,0xffff0000,v78
v_lshlrev_b32 v10,16,v79
v_and_b32 v11,0xffff0000,v79
v_pk_mul_f32 v[8:9],v[8:9],v[36:37]
v_pk_mul_f32 v[10:11],v[10:11],v[38:39]
v_cvt_pk_bf16_f32 v12,v8,v9
v_cvt_pk_bf16_f32 v13,v10,v11
v_lshlrev_b32 v8,16,v12
v_and_b32 v9,0xffff0000,v12
v_lshlrev_b32 v10,16,v13
v_and_b32 v11,0xffff0000,v13
v_accvgpr_write_b32 a28,v8
v_accvgpr_write_b32 a29,v9
v_accvgpr_write_b32 a30,v10
v_accvgpr_write_b32 a31,v11
v_lshlrev_b32 v16,16,v86
v_and_b32 v17,0xffff0000,v86
v_lshlrev_b32 v18,16,v87
v_and_b32 v19,0xffff0000,v87
v_pk_mul_f32 v[16:17],v[16:17],v[40:41]
v_pk_mul_f32 v[18:19],v[18:19],v[42:43]
v_cvt_pk_bf16_f32 v20,v16,v17
v_cvt_pk_bf16_f32 v21,v18,v19
v_lshlrev_b32 v16,16,v20
v_and_b32 v17,0xffff0000,v20
v_lshlrev_b32 v18,16,v21
v_and_b32 v19,0xffff0000,v21
v_accvgpr_write_b32 a44,v16
v_accvgpr_write_b32 a45,v17
v_accvgpr_write_b32 a46,v18
v_accvgpr_write_b32 a47,v19
v_lshlrev_b32 v24,16,v94
v_and_b32 v25,0xffff0000,v94
v_lshlrev_b32 v26,16,v95
v_and_b32 v27,0xffff0000,v95
v_pk_mul_f32 v[24:25],v[24:25],v[44:45]
v_pk_mul_f32 v[26:27],v[26:27],v[46:47]
v_cvt_pk_bf16_f32 v28,v24,v25
v_cvt_pk_bf16_f32 v29,v26,v27
v_lshlrev_b32 v24,16,v28
v_and_b32 v25,0xffff0000,v28
v_lshlrev_b32 v26,16,v29
v_and_b32 v27,0xffff0000,v29
v_accvgpr_write_b32 a60,v24
v_accvgpr_write_b32 a61,v25
v_accvgpr_write_b32 a62,v26
v_accvgpr_write_b32 a63,v27
s_waitcnt lgkmcnt(0)
s_barrier
ds_read_b128 v[0:3],v112 offset:0
ds_read_b128 v[4:7],v113 offset:0
ds_read_b128 v[8:11],v112 offset:2048
ds_read_b128 v[12:15],v113 offset:2048
ds_read_b128 v[16:19],v112 offset:4096
ds_read_b128 v[20:23],v113 offset:4096
ds_read_b128 v[24:27],v112 offset:6144
ds_read_b128 v[28:31],v113 offset:6144
ds_read_b64_tr_b16 v[64:65],v100 offset:0
ds_read_b64_tr_b16 v[66:67],v101 offset:0
ds_read_b64_tr_b16 v[68:69],v100 offset:512
ds_read_b64_tr_b16 v[70:71],v101 offset:512
ds_read_b64_tr_b16 v[72:73],v100 offset:2048
ds_read_b64_tr_b16 v[74:75],v101 offset:2048
ds_read_b64_tr_b16 v[76:77],v100 offset:2560
ds_read_b64_tr_b16 v[78:79],v101 offset:2560
s_waitcnt lgkmcnt(0)
s_barrier
v_mfma_f32_16x16x32_bf16 a[0:3],v[0:3],v[64:67],a[0:3]
v_mfma_f32_16x16x32_bf16 a[0:3],v[4:7],v[68:71],a[0:3]
ds_read_b64_tr_b16 v[80:81],v100 offset:4096
v_mfma_f32_16x16x32_bf16 a[16:19],v[8:11],v[64:67],a[16:19]
ds_read_b64_tr_b16 v[82:83],v101 offset:4096
ds_read_b128 v[32:35],v112 offset:32768
v_mfma_f32_16x16x32_bf16 a[16:19],v[12:15],v[68:71],a[16:19]
ds_read_b64_tr_b16 v[84:85],v100 offset:4608
ds_read_b128 v[36:39],v113 offset:32768
v_mfma_f32_16x16x32_bf16 a[32:35],v[16:19],v[64:67],a[32:35]
ds_read_b64_tr_b16 v[86:87],v101 offset:4608
ds_read_b128 v[40:43],v112 offset:34816
v_mfma_f32_16x16x32_bf16 a[32:35],v[20:23],v[68:71],a[32:35]
ds_read_b128 v[44:47],v113 offset:34816
ds_read_b128 v[48:51],v112 offset:36864
v_mfma_f32_16x16x32_bf16 a[48:51],v[24:27],v[64:67],a[48:51]
ds_read_b128 v[52:55],v113 offset:36864
ds_read_b128 v[56:59],v112 offset:38912
v_mfma_f32_16x16x32_bf16 a[48:51],v[28:31],v[68:71],a[48:51]
ds_read_b128 v[60:63],v113 offset:38912
v_mfma_f32_16x16x32_bf16 a[4:7],v[0:3],v[72:75],a[4:7]
)"
R"(ds_read_b64_tr_b16 v[88:89],v100 offset:6144
ds_read_b64_tr_b16 v[90:91],v101 offset:6144
v_mfma_f32_16x16x32_bf16 a[4:7],v[4:7],v[76:79],a[4:7]
ds_read_b64_tr_b16 v[92:93],v100 offset:6656
ds_read_b64_tr_b16 v[94:95],v101 offset:6656
v_mfma_f32_16x16x32_bf16 a[20:23],v[8:11],v[72:75],a[20:23]
v_mfma_f32_16x16x32_bf16 a[20:23],v[12:15],v[76:79],a[20:23]
v_mfma_f32_16x16x32_bf16 a[36:39],v[16:19],v[72:75],a[36:39]
v_mfma_f32_16x16x32_bf16 a[36:39],v[20:23],v[76:79],a[36:39]
v_mfma_f32_16x16x32_bf16 a[52:55],v[24:27],v[72:75],a[52:55]
v_mfma_f32_16x16x32_bf16 a[52:55],v[28:31],v[76:79],a[52:55]
s_waitcnt lgkmcnt(0)
s_barrier
v_mfma_f32_16x16x32_bf16 a[8:11],v[0:3],v[80:83],a[8:11]
v_mfma_f32_16x16x32_bf16 a[8:11],v[4:7],v[84:87],a[8:11]
ds_read_b64_tr_b16 v[64:65],v100 offset:32768
v_mfma_f32_16x16x32_bf16 a[24:27],v[8:11],v[80:83],a[24:27]
ds_read_b64_tr_b16 v[66:67],v101 offset:32768
v_mfma_f32_16x16x32_bf16 a[24:27],v[12:15],v[84:87],a[24:27]
ds_read_b64_tr_b16 v[68:69],v100 offset:33280
v_mfma_f32_16x16x32_bf16 a[40:43],v[16:19],v[80:83],a[40:43]
ds_read_b64_tr_b16 v[70:71],v101 offset:33280
v_mfma_f32_16x16x32_bf16 a[40:43],v[20:23],v[84:87],a[40:43]
v_mfma_f32_16x16x32_bf16 a[56:59],v[24:27],v[80:83],a[56:59]
v_mfma_f32_16x16x32_bf16 a[56:59],v[28:31],v[84:87],a[56:59]
v_mfma_f32_16x16x32_bf16 a[12:15],v[0:3],v[88:91],a[12:15]
v_mfma_f32_16x16x32_bf16 a[12:15],v[4:7],v[92:95],a[12:15]
ds_read_b64_tr_b16 v[72:73],v100 offset:34816
v_mfma_f32_16x16x32_bf16 a[28:31],v[8:11],v[88:91],a[28:31]
ds_read_b64_tr_b16 v[74:75],v101 offset:34816
v_mfma_f32_16x16x32_bf16 a[28:31],v[12:15],v[92:95],a[28:31]
ds_read_b64_tr_b16 v[76:77],v100 offset:35328
v_mfma_f32_16x16x32_bf16 a[44:47],v[16:19],v[88:91],a[44:47]
ds_read_b64_tr_b16 v[78:79],v101 offset:35328
v_mfma_f32_16x16x32_bf16 a[44:47],v[20:23],v[92:95],a[44:47]
v_mfma_f32_16x16x32_bf16 a[60:63],v[24:27],v[88:91],a[60:63]
v_mfma_f32_16x16x32_bf16 a[60:63],v[28:31],v[92:95],a[60:63]
s_waitcnt lgkmcnt(6)
v_mfma_f32_16x16x32_bf16 a[0:3],v[32:35],v[64:67],a[0:3]
s_waitcnt lgkmcnt(4)
v_mfma_f32_16x16x32_bf16 a[0:3],v[36:39],v[68:71],a[0:3]
ds_read_b64_tr_b16 v[80:81],v100 offset:36864
v_mfma_f32_16x16x32_bf16 a[16:19],v[40:43],v[64:67],a[16:19]
ds_read_b64_tr_b16 v[82:83],v101 offset:36864
ds_read_b128 v[0:3],v114 offset:0
v_mfma_f32_16x16x32_bf16 a[16:19],v[44:47],v[68:71],a[16:19]
ds_read_b64_tr_b16 v[84:85],v100 offset:37376
ds_read_b128 v[4:7],v115 offset:0
v_mfma_f32_16x16x32_bf16 a[32:35],v[48:51],v[64:67],a[32:35]
ds_read_b64_tr_b16 v[86:87],v101 offset:37376
ds_read_b128 v[8:11],v114 offset:2048
v_mfma_f32_16x16x32_bf16 a[32:35],v[52:55],v[68:71],a[32:35]
ds_read_b128 v[12:15],v115 offset:2048
ds_read_b128 v[16:19],v114 offset:4096
v_mfma_f32_16x16x32_bf16 a[48:51],v[56:59],v[64:67],a[48:51]
ds_read_b128 v[20:23],v115 offset:4096
ds_read_b128 v[24:27],v114 offset:6144
v_mfma_f32_16x16x32_bf16 a[48:51],v[60:63],v[68:71],a[48:51]
ds_read_b128 v[28:31],v115 offset:6144
s_waitcnt lgkmcnt(14)
v_mfma_f32_16x16x32_bf16 a[4:7],v[32:35],v[72:75],a[4:7]
ds_read_b64_tr_b16 v[88:89],v100 offset:38912
ds_read_b64_tr_b16 v[90:91],v101 offset:38912
s_waitcnt lgkmcnt(14)
v_mfma_f32_16x16x32_bf16 a[4:7],v[36:39],v[76:79],a[4:7]
ds_read_b64_tr_b16 v[92:93],v100 offset:39424
ds_read_b64_tr_b16 v[94:95],v101 offset:39424
v_mfma_f32_16x16x32_bf16 a[20:23],v[40:43],v[72:75],a[20:23]
v_mfma_f32_16x16x32_bf16 a[20:23],v[44:47],v[76:79],a[20:23]
v_mfma_f32_16x16x32_bf16 a[36:39],v[48:51],v[72:75],a[36:39]
v_mfma_f32_16x16x32_bf16 a[36:39],v[52:55],v[76:79],a[36:39]
v_mfma_f32_16x16x32_bf16 a[52:55],v[56:59],v[72:75],a[52:55]
v_mfma_f32_16x16x32_bf16 a[52:55],v[60:63],v[76:79],a[52:55]
s_waitcnt lgkmcnt(0)
s_barrier
v_mfma_f32_16x16x32_bf16 a[8:11],v[32:35],v[80:83],a[8:11]
v_mfma_f32_16x16x32_bf16 a[8:11],v[36:39],v[84:87],a[8:11]
ds_read_b64_tr_b16 v[64:65],v104 offset:0
v_mfma_f32_16x16x32_bf16 a[24:27],v[40:43],v[80:83],a[24:27]
ds_read_b64_tr_b16 v[66:67],v105 offset:0
v_mfma_f32_16x16x32_bf16 a[24:27],v[44:47],v[84:87],a[24:27]
ds_read_b64_tr_b16 v[68:69],v104 offset:512
v_mfma_f32_16x16x32_bf16 a[40:43],v[48:51],v[80:83],a[40:43]
ds_read_b64_tr_b16 v[70:71],v105 offset:512
v_mfma_f32_16x16x32_bf16 a[40:43],v[52:55],v[84:87],a[40:43]
v_mfma_f32_16x16x32_bf16 a[56:59],v[56:59],v[80:83],a[56:59]
v_mfma_f32_16x16x32_bf16 a[56:59],v[60:63],v[84:87],a[56:59]
v_mfma_f32_16x16x32_bf16 a[12:15],v[32:35],v[88:91],a[12:15]
v_mfma_f32_16x16x32_bf16 a[12:15],v[36:39],v[92:95],a[12:15]
ds_read_b64_tr_b16 v[72:73],v104 offset:2048
v_mfma_f32_16x16x32_bf16 a[28:31],v[40:43],v[88:91],a[28:31]
ds_read_b64_tr_b16 v[74:75],v105 offset:2048
v_mfma_f32_16x16x32_bf16 a[28:31],v[44:47],v[92:95],a[28:31]
ds_read_b64_tr_b16 v[76:77],v104 offset:2560
v_mfma_f32_16x16x32_bf16 a[44:47],v[48:51],v[88:91],a[44:47]
ds_read_b64_tr_b16 v[78:79],v105 offset:2560
v_mfma_f32_16x16x32_bf16 a[44:47],v[52:55],v[92:95],a[44:47]
v_mfma_f32_16x16x32_bf16 a[60:63],v[56:59],v[88:91],a[60:63]
v_mfma_f32_16x16x32_bf16 a[60:63],v[60:63],v[92:95],a[60:63]
s_waitcnt lgkmcnt(6)
v_mfma_f32_16x16x32_bf16 a[0:3],v[0:3],v[64:67],a[0:3]
s_waitcnt lgkmcnt(4)
v_mfma_f32_16x16x32_bf16 a[0:3],v[4:7],v[68:71],a[0:3]
ds_read_b64_tr_b16 v[80:81],v104 offset:4096
v_mfma_f32_16x16x32_bf16 a[16:19],v[8:11],v[64:67],a[16:19]
ds_read_b64_tr_b16 v[82:83],v105 offset:4096
ds_read_b128 v[32:35],v114 offset:32768
v_mfma_f32_16x16x32_bf16 a[16:19],v[12:15],v[68:71],a[16:19]
ds_read_b64_tr_b16 v[84:85],v104 offset:4608
ds_read_b128 v[36:39],v115 offset:32768
v_mfma_f32_16x16x32_bf16 a[32:35],v[16:19],v[64:67],a[32:35]
ds_read_b64_tr_b16 v[86:87],v105 offset:4608
ds_read_b128 v[40:43],v114 offset:34816
v_mfma_f32_16x16x32_bf16 a[32:35],v[20:23],v[68:71],a[32:35]
ds_read_b128 v[44:47],v115 offset:34816
ds_read_b128 v[48:51],v114 offset:36864
v_mfma_f32_16x16x32_bf16 a[48:51],v[24:27],v[64:67],a[48:51]
ds_read_b128 v[52:55],v115 offset:36864
ds_read_b128 v[56:59],v114 offset:38912
v_mfma_f32_16x16x32_bf16 a[48:51],v[28:31],v[68:71],a[48:51]
ds_read_b128 v[60:63],v115 offset:38912
s_waitcnt lgkmcnt(14)
v_mfma_f32_16x16x32_bf16 a[4:7],v[0:3],v[72:75],a[4:7]
ds_read_b64_tr_b16 v[88:89],v104 offset:6144
ds_read_b64_tr_b16 v[90:91],v105 offset:6144
s_waitcnt lgkmcnt(14)
v_mfma_f32_16x16x32_bf16 a[4:7],v[4:7],v[76:79],a[4:7]
ds_read_b64_tr_b16 v[92:93],v104 offset:6656
ds_read_b64_tr_b16 v[94:95],v105 offset:6656
v_mfma_f32_16x16x32_bf16 a[20:23],v[8:11],v[72:75],a[20:23]
v_mfma_f32_16x16x32_bf16 a[20:23],v[12:15],v[76:79],a[20:23]
v_mfma_f32_16x16x32_bf16 a[36:39],v[16:19],v[72:75],a[36:39]
v_mfma_f32_16x16x32_bf16 a[36:39],v[20:23],v[76:79],a[36:39]
v_mfma_f32_16x16x32_bf16 a[52:55],v[24:27],v[72:75],a[52:55]
v_mfma_f32_16x16x32_bf16 a[52:55],v[28:31],v[76:79],a[52:55]
s_waitcnt lgkmcnt(0)
s_barrier
v_mfma_f32_16x16x32_bf16 a[8:11],v[0:3],v[80:83],a[8:11]
v_mfma_f32_16x16x32_bf16 a[8:11],v[4:7],v[84:87],a[8:11]
ds_read_b64_tr_b16 v[64:65],v104 offset:32768
v_mfma_f32_16x16x32_bf16 a[24:27],v[8:11],v[80:83],a[24:27]
ds_read_b64_tr_b16 v[66:67],v105 offset:32768
v_mfma_f32_16x16x32_bf16 a[24:27],v[12:15],v[84:87],a[24:27]
ds_read_b64_tr_b16 v[68:69],v104 offset:33280
v_mfma_f32_16x16x32_bf16 a[40:43],v[16:19],v[80:83],a[40:43]
ds_read_b64_tr_b16 v[70:71],v105 offset:33280
v_mfma_f32_16x16x32_bf16 a[40:43],v[20:23],v[84:87],a[40:43]
v_mfma_f32_16x16x32_bf16 a[56:59],v[24:27],v[80:83],a[56:59]
v_mfma_f32_16x16x32_bf16 a[56:59],v[28:31],v[84:87],a[56:59]
v_mfma_f32_16x16x32_bf16 a[12:15],v[0:3],v[88:91],a[12:15]
v_mfma_f32_16x16x32_bf16 a[12:15],v[4:7],v[92:95],a[12:15]
ds_read_b64_tr_b16 v[72:73],v104 offset:34816
v_mfma_f32_16x16x32_bf16 a[28:31],v[8:11],v[88:91],a[28:31]
ds_read_b64_tr_b16 v[74:75],v105 offset:34816
v_mfma_f32_16x16x32_bf16 a[28:31],v[12:15],v[92:95],a[28:31]
ds_read_b64_tr_b16 v[76:77],v104 offset:35328
v_mfma_f32_16x16x32_bf16 a[44:47],v[16:19],v[88:91],a[44:47]
ds_read_b64_tr_b16 v[78:79],v105 offset:35328
v_mfma_f32_16x16x32_bf16 a[44:47],v[20:23],v[92:95],a[44:47]
v_mfma_f32_16x16x32_bf16 a[60:63],v[24:27],v[88:91],a[60:63]
v_mfma_f32_16x16x32_bf16 a[60:63],v[28:31],v[92:95],a[60:63]
s_waitcnt lgkmcnt(6)
v_mfma_f32_16x16x32_bf16 a[0:3],v[32:35],v[64:67],a[0:3]
s_waitcnt lgkmcnt(4)
v_mfma_f32_16x16x32_bf16 a[0:3],v[36:39],v[68:71],a[0:3]
ds_read_b64_tr_b16 v[80:81],v104 offset:36864
v_mfma_f32_16x16x32_bf16 a[16:19],v[40:43],v[64:67],a[16:19]
ds_read_b64_tr_b16 v[82:83],v105 offset:36864
ds_read_b128 v[0:3],v123 offset:0
v_mfma_f32_16x16x32_bf16 a[16:19],v[44:47],v[68:71],a[16:19]
ds_read_b64_tr_b16 v[84:85],v104 offset:37376
ds_read_b128 v[4:7],v124 offset:0
v_mfma_f32_16x16x32_bf16 a[32:35],v[48:51],v[64:67],a[32:35]
ds_read_b64_tr_b16 v[86:87],v105 offset:37376
ds_read_b128 v[8:11],v123 offset:2048
v_mfma_f32_16x16x32_bf16 a[32:35],v[52:55],v[68:71],a[32:35]
ds_read_b128 v[12:15],v124 offset:2048
ds_read_b128 v[16:19],v123 offset:4096
v_mfma_f32_16x16x32_bf16 a[48:51],v[56:59],v[64:67],a[48:51]
ds_read_b128 v[20:23],v124 offset:4096
ds_read_b128 v[24:27],v123 offset:6144
v_mfma_f32_16x16x32_bf16 a[48:51],v[60:63],v[68:71],a[48:51]
ds_read_b128 v[28:31],v124 offset:6144
s_waitcnt lgkmcnt(14)
v_mfma_f32_16x16x32_bf16 a[4:7],v[32:35],v[72:75],a[4:7]
ds_read_b64_tr_b16 v[88:89],v104 offset:38912
ds_read_b64_tr_b16 v[90:91],v105 offset:38912
s_waitcnt lgkmcnt(14)
v_mfma_f32_16x16x32_bf16 a[4:7],v[36:39],v[76:79],a[4:7]
ds_read_b64_tr_b16 v[92:93],v104 offset:39424
ds_read_b64_tr_b16 v[94:95],v105 offset:39424
v_mfma_f32_16x16x32_bf16 a[20:23],v[40:43],v[72:75],a[20:23]
v_mfma_f32_16x16x32_bf16 a[20:23],v[44:47],v[76:79],a[20:23]
v_mfma_f32_16x16x32_bf16 a[36:39],v[48:51],v[72:75],a[36:39]
v_mfma_f32_16x16x32_bf16 a[36:39],v[52:55],v[76:79],a[36:39]
v_mfma_f32_16x16x32_bf16 a[52:55],v[56:59],v[72:75],a[52:55]
v_mfma_f32_16x16x32_bf16 a[52:55],v[60:63],v[76:79],a[52:55]
s_waitcnt lgkmcnt(0)
s_barrier
v_mfma_f32_16x16x32_bf16 a[8:11],v[32:35],v[80:83],a[8:11]
v_mfma_f32_16x16x32_bf16 a[8:11],v[36:39],v[84:87],a[8:11]
ds_read_b64_tr_b16 v[64:65],v108 offset:0
v_mfma_f32_16x16x32_bf16 a[24:27],v[40:43],v[80:83],a[24:27]
ds_read_b64_tr_b16 v[66:67],v109 offset:0
v_mfma_f32_16x16x32_bf16 a[24:27],v[44:47],v[84:87],a[24:27]
ds_read_b64_tr_b16 v[68:69],v108 offset:512
v_mfma_f32_16x16x32_bf16 a[40:43],v[48:51],v[80:83],a[40:43]
ds_read_b64_tr_b16 v[70:71],v109 offset:512
v_mfma_f32_16x16x32_bf16 a[40:43],v[52:55],v[84:87],a[40:43]
v_mfma_f32_16x16x32_bf16 a[56:59],v[56:59],v[80:83],a[56:59]
v_mfma_f32_16x16x32_bf16 a[56:59],v[60:63],v[84:87],a[56:59]
v_mfma_f32_16x16x32_bf16 a[12:15],v[32:35],v[88:91],a[12:15]
v_mfma_f32_16x16x32_bf16 a[12:15],v[36:39],v[92:95],a[12:15]
ds_read_b64_tr_b16 v[72:73],v108 offset:2048
v_mfma_f32_16x16x32_bf16 a[28:31],v[40:43],v[88:91],a[28:31]
ds_read_b64_tr_b16 v[74:75],v109 offset:2048
v_mfma_f32_16x16x32_bf16 a[28:31],v[44:47],v[92:95],a[28:31]
ds_read_b64_tr_b16 v[76:77],v108 offset:2560
v_mfma_f32_16x16x32_bf16 a[44:47],v[48:51],v[88:91],a[44:47]
ds_read_b64_tr_b16 v[78:79],v109 offset:2560
v_mfma_f32_16x16x32_bf16 a[44:47],v[52:55],v[92:95],a[44:47]
v_mfma_f32_16x16x32_bf16 a[60:63],v[56:59],v[88:91],a[60:63]
v_mfma_f32_16x16x32_bf16 a[60:63],v[60:63],v[92:95],a[60:63]
s_waitcnt lgkmcnt(6)
v_mfma_f32_16x16x32_bf16 a[0:3],v[0:3],v[64:67],a[0:3]
s_waitcnt lgkmcnt(4)
v_mfma_f32_16x16x32_bf16 a[0:3],v[4:7],v[68:71],a[0:3]
ds_read_b64_tr_b16 v[80:81],v108 offset:4096
v_mfma_f32_16x16x32_bf16 a[16:19],v[8:11],v[64:67],a[16:19]
ds_read_b64_tr_b16 v[82:83],v109 offset:4096
ds_read_b128 v[32:35],v112 offset:0
v_mfma_f32_16x16x32_bf16 a[16:19],v[12:15],v[68:71],a[16:19]
ds_read_b64_tr_b16 v[84:85],v108 offset:4608
ds_read_b128 v[36:39],v113 offset:0
v_mfma_f32_16x16x32_bf16 a[32:35],v[16:19],v[64:67],a[32:35]
ds_read_b64_tr_b16 v[86:87],v109 offset:4608
ds_read_b128 v[40:43],v112 offset:2048
v_mfma_f32_16x16x32_bf16 a[32:35],v[20:23],v[68:71],a[32:35]
ds_read_b128 v[44:47],v113 offset:2048
ds_read_b128 v[48:51],v112 offset:4096
v_mfma_f32_16x16x32_bf16 a[48:51],v[24:27],v[64:67],a[48:51]
ds_read_b128 v[52:55],v113 offset:4096
ds_read_b128 v[56:59],v112 offset:6144
v_mfma_f32_16x16x32_bf16 a[48:51],v[28:31],v[68:71],a[48:51]
ds_read_b128 v[60:63],v113 offset:6144
s_waitcnt lgkmcnt(14)
v_mfma_f32_16x16x32_bf16 a[4:7],v[0:3],v[72:75],a[4:7]
ds_read_b64_tr_b16 v[88:89],v108 offset:6144
ds_read_b64_tr_b16 v[90:91],v109 offset:6144
s_waitcnt lgkmcnt(14)
v_mfma_f32_16x16x32_bf16 a[4:7],v[4:7],v[76:79],a[4:7]
ds_read_b64_tr_b16 v[92:93],v108 offset:6656
ds_read_b64_tr_b16 v[94:95],v109 offset:6656
v_mfma_f32_16x16x32_bf16 a[20:23],v[8:11],v[72:75],a[20:23]
v_mfma_f32_16x16x32_bf16 a[20:23],v[12:15],v[76:79],a[20:23]
v_mfma_f32_16x16x32_bf16 a[36:39],v[16:19],v[72:75],a[36:39]
v_mfma_f32_16x16x32_bf16 a[36:39],v[20:23],v[76:79],a[36:39]
v_mfma_f32_16x16x32_bf16 a[52:55],v[24:27],v[72:75],a[52:55]
v_mfma_f32_16x16x32_bf16 a[52:55],v[28:31],v[76:79],a[52:55]
s_waitcnt lgkmcnt(0)
s_barrier
v_mfma_f32_16x16x32_bf16 a[8:11],v[0:3],v[80:83],a[8:11]
v_mfma_f32_16x16x32_bf16 a[8:11],v[4:7],v[84:87],a[8:11]
ds_read_b64_tr_b16 v[64:65],v100 offset:0
v_mfma_f32_16x16x32_bf16 a[24:27],v[8:11],v[80:83],a[24:27]
ds_read_b64_tr_b16 v[66:67],v101 offset:0
v_mfma_f32_16x16x32_bf16 a[24:27],v[12:15],v[84:87],a[24:27]
ds_read_b64_tr_b16 v[68:69],v100 offset:512
v_mfma_f32_16x16x32_bf16 a[40:43],v[16:19],v[80:83],a[40:43]
ds_read_b64_tr_b16 v[70:71],v101 offset:512
v_mfma_f32_16x16x32_bf16 a[40:43],v[20:23],v[84:87],a[40:43]
v_mfma_f32_16x16x32_bf16 a[56:59],v[24:27],v[80:83],a[56:59]
v_mfma_f32_16x16x32_bf16 a[56:59],v[28:31],v[84:87],a[56:59]
v_mfma_f32_16x16x32_bf16 a[12:15],v[0:3],v[88:91],a[12:15]
v_mfma_f32_16x16x32_bf16 a[12:15],v[4:7],v[92:95],a[12:15]
ds_read_b64_tr_b16 v[72:73],v100 offset:2048
v_mfma_f32_16x16x32_bf16 a[28:31],v[8:11],v[88:91],a[28:31]
ds_read_b64_tr_b16 v[74:75],v101 offset:2048
v_mfma_f32_16x16x32_bf16 a[28:31],v[12:15],v[92:95],a[28:31]
ds_read_b64_tr_b16 v[76:77],v100 offset:2560
v_mfma_f32_16x16x32_bf16 a[44:47],v[16:19],v[88:91],a[44:47]
ds_read_b64_tr_b16 v[78:79],v101 offset:2560
v_mfma_f32_16x16x32_bf16 a[44:47],v[20:23],v[92:95],a[44:47]
v_mfma_f32_16x16x32_bf16 a[60:63],v[24:27],v[88:91],a[60:63]
v_mfma_f32_16x16x32_bf16 a[60:63],v[28:31],v[92:95],a[60:63]
s_waitcnt lgkmcnt(6)
v_mfma_f32_16x16x32_bf16 a[0:3],v[32:35],v[64:67],a[0:3]
s_waitcnt lgkmcnt(4)
v_mfma_f32_16x16x32_bf16 a[0:3],v[36:39],v[68:71],a[0:3]
ds_read_b64_tr_b16 v[80:81],v100 offset:4096
v_mfma_f32_16x16x32_bf16 a[16:19],v[40:43],v[64:67],a[16:19]
ds_read_b64_tr_b16 v[82:83],v101 offset:4096
ds_read_b128 v[0:3],v112 offset:32768
v_mfma_f32_16x16x32_bf16 a[16:19],v[44:47],v[68:71],a[16:19]
ds_read_b64_tr_b16 v[84:85],v100 offset:4608
ds_read_b128 v[4:7],v113 offset:32768
v_mfma_f32_16x16x32_bf16 a[32:35],v[48:51],v[64:67],a[32:35]
ds_read_b64_tr_b16 v[86:87],v101 offset:4608
ds_read_b128 v[8:11],v112 offset:34816
v_mfma_f32_16x16x32_bf16 a[32:35],v[52:55],v[68:71],a[32:35]
ds_read_b128 v[12:15],v113 offset:34816
ds_read_b128 v[16:19],v112 offset:36864
v_mfma_f32_16x16x32_bf16 a[48:51],v[56:59],v[64:67],a[48:51]
ds_read_b128 v[20:23],v113 offset:36864
ds_read_b128 v[24:27],v112 offset:38912
v_mfma_f32_16x16x32_bf16 a[48:51],v[60:63],v[68:71],a[48:51]
ds_read_b128 v[28:31],v113 offset:38912
s_waitcnt lgkmcnt(14)
v_mfma_f32_16x16x32_bf16 a[4:7],v[32:35],v[72:75],a[4:7]
ds_read_b64_tr_b16 v[88:89],v100 offset:6144
ds_read_b64_tr_b16 v[90:91],v101 offset:6144
s_waitcnt lgkmcnt(14)
v_mfma_f32_16x16x32_bf16 a[4:7],v[36:39],v[76:79],a[4:7]
ds_read_b64_tr_b16 v[92:93],v100 offset:6656
ds_read_b64_tr_b16 v[94:95],v101 offset:6656
v_mfma_f32_16x16x32_bf16 a[20:23],v[40:43],v[72:75],a[20:23]
v_mfma_f32_16x16x32_bf16 a[20:23],v[44:47],v[76:79],a[20:23]
v_mfma_f32_16x16x32_bf16 a[36:39],v[48:51],v[72:75],a[36:39]
v_mfma_f32_16x16x32_bf16 a[36:39],v[52:55],v[76:79],a[36:39]
v_mfma_f32_16x16x32_bf16 a[52:55],v[56:59],v[72:75],a[52:55]
v_mfma_f32_16x16x32_bf16 a[52:55],v[60:63],v[76:79],a[52:55]
s_waitcnt lgkmcnt(0)
s_barrier
v_mfma_f32_16x16x32_bf16 a[8:11],v[32:35],v[80:83],a[8:11]
v_mfma_f32_16x16x32_bf16 a[8:11],v[36:39],v[84:87],a[8:11]
ds_read_b64_tr_b16 v[64:65],v100 offset:32768
v_mfma_f32_16x16x32_bf16 a[24:27],v[40:43],v[80:83],a[24:27]
ds_read_b64_tr_b16 v[66:67],v101 offset:32768
v_mfma_f32_16x16x32_bf16 a[24:27],v[44:47],v[84:87],a[24:27]
ds_read_b64_tr_b16 v[68:69],v100 offset:33280
v_mfma_f32_16x16x32_bf16 a[40:43],v[48:51],v[80:83],a[40:43]
ds_read_b64_tr_b16 v[70:71],v101 offset:33280
v_mfma_f32_16x16x32_bf16 a[40:43],v[52:55],v[84:87],a[40:43]
v_mfma_f32_16x16x32_bf16 a[56:59],v[56:59],v[80:83],a[56:59]
v_mfma_f32_16x16x32_bf16 a[56:59],v[60:63],v[84:87],a[56:59]
v_mfma_f32_16x16x32_bf16 a[12:15],v[32:35],v[88:91],a[12:15]
v_mfma_f32_16x16x32_bf16 a[12:15],v[36:39],v[92:95],a[12:15]
ds_read_b64_tr_b16 v[72:73],v100 offset:34816
v_mfma_f32_16x16x32_bf16 a[28:31],v[40:43],v[88:91],a[28:31]
ds_read_b64_tr_b16 v[74:75],v101 offset:34816
v_mfma_f32_16x16x32_bf16 a[28:31],v[44:47],v[92:95],a[28:31]
ds_read_b64_tr_b16 v[76:77],v100 offset:35328
v_mfma_f32_16x16x32_bf16 a[44:47],v[48:51],v[88:91],a[44:47]
ds_read_b64_tr_b16 v[78:79],v101 offset:35328
v_mfma_f32_16x16x32_bf16 a[44:47],v[52:55],v[92:95],a[44:47]
v_mfma_f32_16x16x32_bf16 a[60:63],v[56:59],v[88:91],a[60:63]
v_mfma_f32_16x16x32_bf16 a[60:63],v[60:63],v[92:95],a[60:63]
s_waitcnt lgkmcnt(6)
v_mfma_f32_16x16x32_bf16 a[0:3],v[0:3],v[64:67],a[0:3]
s_waitcnt lgkmcnt(4)
v_mfma_f32_16x16x32_bf16 a[0:3],v[4:7],v[68:71],a[0:3]
ds_read_b64_tr_b16 v[80:81],v100 offset:36864
v_mfma_f32_16x16x32_bf16 a[16:19],v[8:11],v[64:67],a[16:19]
ds_read_b64_tr_b16 v[82:83],v101 offset:36864
ds_read_b128 v[32:35],v114 offset:0
v_mfma_f32_16x16x32_bf16 a[16:19],v[12:15],v[68:71],a[16:19]
ds_read_b64_tr_b16 v[84:85],v100 offset:37376
ds_read_b128 v[36:39],v115 offset:0
v_mfma_f32_16x16x32_bf16 a[32:35],v[16:19],v[64:67],a[32:35]
ds_read_b64_tr_b16 v[86:87],v101 offset:37376
ds_read_b128 v[40:43],v114 offset:2048
v_mfma_f32_16x16x32_bf16 a[32:35],v[20:23],v[68:71],a[32:35]
ds_read_b128 v[44:47],v115 offset:2048
ds_read_b128 v[48:51],v114 offset:4096
v_mfma_f32_16x16x32_bf16 a[48:51],v[24:27],v[64:67],a[48:51]
ds_read_b128 v[52:55],v115 offset:4096
ds_read_b128 v[56:59],v114 offset:6144
v_mfma_f32_16x16x32_bf16 a[48:51],v[28:31],v[68:71],a[48:51]
ds_read_b128 v[60:63],v115 offset:6144
s_waitcnt lgkmcnt(14)
v_mfma_f32_16x16x32_bf16 a[4:7],v[0:3],v[72:75],a[4:7]
ds_read_b64_tr_b16 v[88:89],v100 offset:38912
ds_read_b64_tr_b16 v[90:91],v101 offset:38912
s_waitcnt lgkmcnt(14)
v_mfma_f32_16x16x32_bf16 a[4:7],v[4:7],v[76:79],a[4:7]
ds_read_b64_tr_b16 v[92:93],v100 offset:39424
ds_read_b64_tr_b16 v[94:95],v101 offset:39424
v_mfma_f32_16x16x32_bf16 a[20:23],v[8:11],v[72:75],a[20:23]
v_mfma_f32_16x16x32_bf16 a[20:23],v[12:15],v[76:79],a[20:23]
v_mfma_f32_16x16x32_bf16 a[36:39],v[16:19],v[72:75],a[36:39]
v_mfma_f32_16x16x32_bf16 a[36:39],v[20:23],v[76:79],a[36:39]
v_mfma_f32_16x16x32_bf16 a[52:55],v[24:27],v[72:75],a[52:55]
v_mfma_f32_16x16x32_bf16 a[52:55],v[28:31],v[76:79],a[52:55]
s_waitcnt lgkmcnt(0)
s_barrier
v_mfma_f32_16x16x32_bf16 a[8:11],v[0:3],v[80:83],a[8:11]
v_mfma_f32_16x16x32_bf16 a[8:11],v[4:7],v[84:87],a[8:11]
ds_read_b64_tr_b16 v[64:65],v104 offset:0
v_mfma_f32_16x16x32_bf16 a[24:27],v[8:11],v[80:83],a[24:27]
ds_read_b64_tr_b16 v[66:67],v105 offset:0
v_mfma_f32_16x16x32_bf16 a[24:27],v[12:15],v[84:87],a[24:27]
ds_read_b64_tr_b16 v[68:69],v104 offset:512
v_mfma_f32_16x16x32_bf16 a[40:43],v[16:19],v[80:83],a[40:43]
ds_read_b64_tr_b16 v[70:71],v105 offset:512
v_mfma_f32_16x16x32_bf16 a[40:43],v[20:23],v[84:87],a[40:43]
v_mfma_f32_16x16x32_bf16 a[56:59],v[24:27],v[80:83],a[56:59]
v_mfma_f32_16x16x32_bf16 a[56:59],v[28:31],v[84:87],a[56:59]
v_mfma_f32_16x16x32_bf16 a[12:15],v[0:3],v[88:91],a[12:15]
v_mfma_f32_16x16x32_bf16 a[12:15],v[4:7],v[92:95],a[12:15]
ds_read_b64_tr_b16 v[72:73],v104 offset:2048
)"
R"(v_mfma_f32_16x16x32_bf16 a[28:31],v[8:11],v[88:91],a[28:31]
ds_read_b64_tr_b16 v[74:75],v105 offset:2048
v_mfma_f32_16x16x32_bf16 a[28:31],v[12:15],v[92:95],a[28:31]
ds_read_b64_tr_b16 v[76:77],v104 offset:2560
v_mfma_f32_16x16x32_bf16 a[44:47],v[16:19],v[88:91],a[44:47]
ds_read_b64_tr_b16 v[78:79],v105 offset:2560
v_mfma_f32_16x16x32_bf16 a[44:47],v[20:23],v[92:95],a[44:47]
v_mfma_f32_16x16x32_bf16 a[60:63],v[24:27],v[88:91],a[60:63]
v_mfma_f32_16x16x32_bf16 a[60:63],v[28:31],v[92:95],a[60:63]
s_waitcnt lgkmcnt(6)
v_mfma_f32_16x16x32_bf16 a[0:3],v[32:35],v[64:67],a[0:3]
s_waitcnt lgkmcnt(4)
v_mfma_f32_16x16x32_bf16 a[0:3],v[36:39],v[68:71],a[0:3]
ds_read_b64_tr_b16 v[80:81],v104 offset:4096
v_mfma_f32_16x16x32_bf16 a[16:19],v[40:43],v[64:67],a[16:19]
ds_read_b64_tr_b16 v[82:83],v105 offset:4096
ds_read_b128 v[0:3],v114 offset:32768
v_mfma_f32_16x16x32_bf16 a[16:19],v[44:47],v[68:71],a[16:19]
ds_read_b64_tr_b16 v[84:85],v104 offset:4608
ds_read_b128 v[4:7],v115 offset:32768
v_mfma_f32_16x16x32_bf16 a[32:35],v[48:51],v[64:67],a[32:35]
ds_read_b64_tr_b16 v[86:87],v105 offset:4608
ds_read_b128 v[8:11],v114 offset:34816
v_mfma_f32_16x16x32_bf16 a[32:35],v[52:55],v[68:71],a[32:35]
ds_read_b128 v[12:15],v115 offset:34816
ds_read_b128 v[16:19],v114 offset:36864
v_mfma_f32_16x16x32_bf16 a[48:51],v[56:59],v[64:67],a[48:51]
ds_read_b128 v[20:23],v115 offset:36864
ds_read_b128 v[24:27],v114 offset:38912
v_mfma_f32_16x16x32_bf16 a[48:51],v[60:63],v[68:71],a[48:51]
ds_read_b128 v[28:31],v115 offset:38912
s_waitcnt lgkmcnt(14)
v_mfma_f32_16x16x32_bf16 a[4:7],v[32:35],v[72:75],a[4:7]
ds_read_b64_tr_b16 v[88:89],v104 offset:6144
ds_read_b64_tr_b16 v[90:91],v105 offset:6144
s_waitcnt lgkmcnt(14)
v_mfma_f32_16x16x32_bf16 a[4:7],v[36:39],v[76:79],a[4:7]
ds_read_b64_tr_b16 v[92:93],v104 offset:6656
ds_read_b64_tr_b16 v[94:95],v105 offset:6656
v_mfma_f32_16x16x32_bf16 a[20:23],v[40:43],v[72:75],a[20:23]
v_mfma_f32_16x16x32_bf16 a[20:23],v[44:47],v[76:79],a[20:23]
v_mfma_f32_16x16x32_bf16 a[36:39],v[48:51],v[72:75],a[36:39]
v_mfma_f32_16x16x32_bf16 a[36:39],v[52:55],v[76:79],a[36:39]
v_mfma_f32_16x16x32_bf16 a[52:55],v[56:59],v[72:75],a[52:55]
v_mfma_f32_16x16x32_bf16 a[52:55],v[60:63],v[76:79],a[52:55]
s_waitcnt lgkmcnt(0)
s_barrier
v_mfma_f32_16x16x32_bf16 a[8:11],v[32:35],v[80:83],a[8:11]
v_mfma_f32_16x16x32_bf16 a[8:11],v[36:39],v[84:87],a[8:11]
ds_read_b64_tr_b16 v[64:65],v104 offset:32768
v_mfma_f32_16x16x32_bf16 a[24:27],v[40:43],v[80:83],a[24:27]
ds_read_b64_tr_b16 v[66:67],v105 offset:32768
v_mfma_f32_16x16x32_bf16 a[24:27],v[44:47],v[84:87],a[24:27]
ds_read_b64_tr_b16 v[68:69],v104 offset:33280
v_mfma_f32_16x16x32_bf16 a[40:43],v[48:51],v[80:83],a[40:43]
ds_read_b64_tr_b16 v[70:71],v105 offset:33280
v_mfma_f32_16x16x32_bf16 a[40:43],v[52:55],v[84:87],a[40:43]
v_mfma_f32_16x16x32_bf16 a[56:59],v[56:59],v[80:83],a[56:59]
v_mfma_f32_16x16x32_bf16 a[56:59],v[60:63],v[84:87],a[56:59]
v_mfma_f32_16x16x32_bf16 a[12:15],v[32:35],v[88:91],a[12:15]
v_mfma_f32_16x16x32_bf16 a[12:15],v[36:39],v[92:95],a[12:15]
ds_read_b64_tr_b16 v[72:73],v104 offset:34816
v_mfma_f32_16x16x32_bf16 a[28:31],v[40:43],v[88:91],a[28:31]
ds_read_b64_tr_b16 v[74:75],v105 offset:34816
v_mfma_f32_16x16x32_bf16 a[28:31],v[44:47],v[92:95],a[28:31]
ds_read_b64_tr_b16 v[76:77],v104 offset:35328
v_mfma_f32_16x16x32_bf16 a[44:47],v[48:51],v[88:91],a[44:47]
ds_read_b64_tr_b16 v[78:79],v105 offset:35328
v_mfma_f32_16x16x32_bf16 a[44:47],v[52:55],v[92:95],a[44:47]
v_mfma_f32_16x16x32_bf16 a[60:63],v[56:59],v[88:91],a[60:63]
v_mfma_f32_16x16x32_bf16 a[60:63],v[60:63],v[92:95],a[60:63]
s_waitcnt lgkmcnt(6)
v_mfma_f32_16x16x32_bf16 a[0:3],v[0:3],v[64:67],a[0:3]
s_waitcnt lgkmcnt(4)
v_mfma_f32_16x16x32_bf16 a[0:3],v[4:7],v[68:71],a[0:3]
ds_read_b64_tr_b16 v[80:81],v104 offset:36864
v_mfma_f32_16x16x32_bf16 a[16:19],v[8:11],v[64:67],a[16:19]
ds_read_b64_tr_b16 v[82:83],v105 offset:36864
ds_read_b128 v[32:35],v123 offset:0
v_mfma_f32_16x16x32_bf16 a[16:19],v[12:15],v[68:71],a[16:19]
ds_read_b64_tr_b16 v[84:85],v104 offset:37376
ds_read_b128 v[36:39],v124 offset:0
v_mfma_f32_16x16x32_bf16 a[32:35],v[16:19],v[64:67],a[32:35]
ds_read_b64_tr_b16 v[86:87],v105 offset:37376
ds_read_b128 v[40:43],v123 offset:2048
v_mfma_f32_16x16x32_bf16 a[32:35],v[20:23],v[68:71],a[32:35]
ds_read_b128 v[44:47],v124 offset:2048
ds_read_b128 v[48:51],v123 offset:4096
v_mfma_f32_16x16x32_bf16 a[48:51],v[24:27],v[64:67],a[48:51]
ds_read_b128 v[52:55],v124 offset:4096
ds_read_b128 v[56:59],v123 offset:6144
v_mfma_f32_16x16x32_bf16 a[48:51],v[28:31],v[68:71],a[48:51]
ds_read_b128 v[60:63],v124 offset:6144
s_waitcnt lgkmcnt(14)
v_mfma_f32_16x16x32_bf16 a[4:7],v[0:3],v[72:75],a[4:7]
ds_read_b64_tr_b16 v[88:89],v104 offset:38912
ds_read_b64_tr_b16 v[90:91],v105 offset:38912
s_waitcnt lgkmcnt(14)
v_mfma_f32_16x16x32_bf16 a[4:7],v[4:7],v[76:79],a[4:7]
ds_read_b64_tr_b16 v[92:93],v104 offset:39424
ds_read_b64_tr_b16 v[94:95],v105 offset:39424
v_mfma_f32_16x16x32_bf16 a[20:23],v[8:11],v[72:75],a[20:23]
v_mfma_f32_16x16x32_bf16 a[20:23],v[12:15],v[76:79],a[20:23]
v_mfma_f32_16x16x32_bf16 a[36:39],v[16:19],v[72:75],a[36:39]
v_mfma_f32_16x16x32_bf16 a[36:39],v[20:23],v[76:79],a[36:39]
v_mfma_f32_16x16x32_bf16 a[52:55],v[24:27],v[72:75],a[52:55]
v_mfma_f32_16x16x32_bf16 a[52:55],v[28:31],v[76:79],a[52:55]
s_waitcnt lgkmcnt(0)
s_barrier
v_mfma_f32_16x16x32_bf16 a[8:11],v[0:3],v[80:83],a[8:11]
v_mfma_f32_16x16x32_bf16 a[8:11],v[4:7],v[84:87],a[8:11]
ds_read_b64_tr_b16 v[64:65],v108 offset:0
v_mfma_f32_16x16x32_bf16 a[24:27],v[8:11],v[80:83],a[24:27]
ds_read_b64_tr_b16 v[66:67],v109 offset:0
v_mfma_f32_16x16x32_bf16 a[24:27],v[12:15],v[84:87],a[24:27]
ds_read_b64_tr_b16 v[68:69],v108 offset:512
v_mfma_f32_16x16x32_bf16 a[40:43],v[16:19],v[80:83],a[40:43]
ds_read_b64_tr_b16 v[70:71],v109 offset:512
v_mfma_f32_16x16x32_bf16 a[40:43],v[20:23],v[84:87],a[40:43]
v_mfma_f32_16x16x32_bf16 a[56:59],v[24:27],v[80:83],a[56:59]
v_mfma_f32_16x16x32_bf16 a[56:59],v[28:31],v[84:87],a[56:59]
v_mfma_f32_16x16x32_bf16 a[12:15],v[0:3],v[88:91],a[12:15]
v_mfma_f32_16x16x32_bf16 a[12:15],v[4:7],v[92:95],a[12:15]
ds_read_b64_tr_b16 v[72:73],v108 offset:2048
v_mfma_f32_16x16x32_bf16 a[28:31],v[8:11],v[88:91],a[28:31]
ds_read_b64_tr_b16 v[74:75],v109 offset:2048
v_mfma_f32_16x16x32_bf16 a[28:31],v[12:15],v[92:95],a[28:31]
ds_read_b64_tr_b16 v[76:77],v108 offset:2560
v_mfma_f32_16x16x32_bf16 a[44:47],v[16:19],v[88:91],a[44:47]
ds_read_b64_tr_b16 v[78:79],v109 offset:2560
v_mfma_f32_16x16x32_bf16 a[44:47],v[20:23],v[92:95],a[44:47]
v_mfma_f32_16x16x32_bf16 a[60:63],v[24:27],v[88:91],a[60:63]
v_mfma_f32_16x16x32_bf16 a[60:63],v[28:31],v[92:95],a[60:63]
s_waitcnt lgkmcnt(6)
v_mfma_f32_16x16x32_bf16 a[0:3],v[32:35],v[64:67],a[0:3]
s_waitcnt lgkmcnt(4)
v_mfma_f32_16x16x32_bf16 a[0:3],v[36:39],v[68:71],a[0:3]
ds_read_b64_tr_b16 v[80:81],v108 offset:4096
v_mfma_f32_16x16x32_bf16 a[16:19],v[40:43],v[64:67],a[16:19]
ds_read_b64_tr_b16 v[82:83],v109 offset:4096
ds_read_b128 v[0:3],v112 offset:0
v_mfma_f32_16x16x32_bf16 a[16:19],v[44:47],v[68:71],a[16:19]
ds_read_b64_tr_b16 v[84:85],v108 offset:4608
ds_read_b128 v[4:7],v113 offset:0
v_mfma_f32_16x16x32_bf16 a[32:35],v[48:51],v[64:67],a[32:35]
ds_read_b64_tr_b16 v[86:87],v109 offset:4608
ds_read_b128 v[8:11],v112 offset:2048
v_mfma_f32_16x16x32_bf16 a[32:35],v[52:55],v[68:71],a[32:35]
ds_read_b128 v[12:15],v113 offset:2048
ds_read_b128 v[16:19],v112 offset:4096
v_mfma_f32_16x16x32_bf16 a[48:51],v[56:59],v[64:67],a[48:51]
ds_read_b128 v[20:23],v113 offset:4096
ds_read_b128 v[24:27],v112 offset:6144
v_mfma_f32_16x16x32_bf16 a[48:51],v[60:63],v[68:71],a[48:51]
ds_read_b128 v[28:31],v113 offset:6144
s_waitcnt lgkmcnt(14)
v_mfma_f32_16x16x32_bf16 a[4:7],v[32:35],v[72:75],a[4:7]
ds_read_b64_tr_b16 v[88:89],v108 offset:6144
ds_read_b64_tr_b16 v[90:91],v109 offset:6144
s_waitcnt lgkmcnt(14)
v_mfma_f32_16x16x32_bf16 a[4:7],v[36:39],v[76:79],a[4:7]
ds_read_b64_tr_b16 v[92:93],v108 offset:6656
ds_read_b64_tr_b16 v[94:95],v109 offset:6656
v_mfma_f32_16x16x32_bf16 a[20:23],v[40:43],v[72:75],a[20:23]
v_mfma_f32_16x16x32_bf16 a[20:23],v[44:47],v[76:79],a[20:23]
v_mfma_f32_16x16x32_bf16 a[36:39],v[48:51],v[72:75],a[36:39]
v_mfma_f32_16x16x32_bf16 a[36:39],v[52:55],v[76:79],a[36:39]
v_mfma_f32_16x16x32_bf16 a[52:55],v[56:59],v[72:75],a[52:55]
v_mfma_f32_16x16x32_bf16 a[52:55],v[60:63],v[76:79],a[52:55]
s_waitcnt lgkmcnt(0)
s_barrier
v_mfma_f32_16x16x32_bf16 a[8:11],v[32:35],v[80:83],a[8:11]
v_mfma_f32_16x16x32_bf16 a[8:11],v[36:39],v[84:87],a[8:11]
ds_read_b64_tr_b16 v[64:65],v100 offset:0
v_mfma_f32_16x16x32_bf16 a[24:27],v[40:43],v[80:83],a[24:27]
ds_read_b64_tr_b16 v[66:67],v101 offset:0
v_mfma_f32_16x16x32_bf16 a[24:27],v[44:47],v[84:87],a[24:27]
ds_read_b64_tr_b16 v[68:69],v100 offset:512
v_mfma_f32_16x16x32_bf16 a[40:43],v[48:51],v[80:83],a[40:43]
ds_read_b64_tr_b16 v[70:71],v101 offset:512
v_mfma_f32_16x16x32_bf16 a[40:43],v[52:55],v[84:87],a[40:43]
v_mfma_f32_16x16x32_bf16 a[56:59],v[56:59],v[80:83],a[56:59]
v_mfma_f32_16x16x32_bf16 a[56:59],v[60:63],v[84:87],a[56:59]
v_mfma_f32_16x16x32_bf16 a[12:15],v[32:35],v[88:91],a[12:15]
v_mfma_f32_16x16x32_bf16 a[12:15],v[36:39],v[92:95],a[12:15]
ds_read_b64_tr_b16 v[72:73],v100 offset:2048
v_mfma_f32_16x16x32_bf16 a[28:31],v[40:43],v[88:91],a[28:31]
ds_read_b64_tr_b16 v[74:75],v101 offset:2048
v_mfma_f32_16x16x32_bf16 a[28:31],v[44:47],v[92:95],a[28:31]
ds_read_b64_tr_b16 v[76:77],v100 offset:2560
v_mfma_f32_16x16x32_bf16 a[44:47],v[48:51],v[88:91],a[44:47]
ds_read_b64_tr_b16 v[78:79],v101 offset:2560
v_mfma_f32_16x16x32_bf16 a[44:47],v[52:55],v[92:95],a[44:47]
v_mfma_f32_16x16x32_bf16 a[60:63],v[56:59],v[88:91],a[60:63]
v_mfma_f32_16x16x32_bf16 a[60:63],v[60:63],v[92:95],a[60:63]
s_waitcnt lgkmcnt(6)
v_mfma_f32_16x16x32_bf16 a[0:3],v[0:3],v[64:67],a[0:3]
s_waitcnt lgkmcnt(4)
v_mfma_f32_16x16x32_bf16 a[0:3],v[4:7],v[68:71],a[0:3]
ds_read_b64_tr_b16 v[80:81],v100 offset:4096
v_mfma_f32_16x16x32_bf16 a[16:19],v[8:11],v[64:67],a[16:19]
ds_read_b64_tr_b16 v[82:83],v101 offset:4096
ds_read_b128 v[32:35],v112 offset:32768
v_mfma_f32_16x16x32_bf16 a[16:19],v[12:15],v[68:71],a[16:19]
ds_read_b64_tr_b16 v[84:85],v100 offset:4608
ds_read_b128 v[36:39],v113 offset:32768
v_mfma_f32_16x16x32_bf16 a[32:35],v[16:19],v[64:67],a[32:35]
ds_read_b64_tr_b16 v[86:87],v101 offset:4608
ds_read_b128 v[40:43],v112 offset:34816
v_mfma_f32_16x16x32_bf16 a[32:35],v[20:23],v[68:71],a[32:35]
ds_read_b128 v[44:47],v113 offset:34816
ds_read_b128 v[48:51],v112 offset:36864
v_mfma_f32_16x16x32_bf16 a[48:51],v[24:27],v[64:67],a[48:51]
ds_read_b128 v[52:55],v113 offset:36864
ds_read_b128 v[56:59],v112 offset:38912
v_mfma_f32_16x16x32_bf16 a[48:51],v[28:31],v[68:71],a[48:51]
ds_read_b128 v[60:63],v113 offset:38912
s_waitcnt lgkmcnt(14)
v_mfma_f32_16x16x32_bf16 a[4:7],v[0:3],v[72:75],a[4:7]
ds_read_b64_tr_b16 v[88:89],v100 offset:6144
ds_read_b64_tr_b16 v[90:91],v101 offset:6144
s_waitcnt lgkmcnt(14)
v_mfma_f32_16x16x32_bf16 a[4:7],v[4:7],v[76:79],a[4:7]
ds_read_b64_tr_b16 v[92:93],v100 offset:6656
ds_read_b64_tr_b16 v[94:95],v101 offset:6656
v_mfma_f32_16x16x32_bf16 a[20:23],v[8:11],v[72:75],a[20:23]
v_mfma_f32_16x16x32_bf16 a[20:23],v[12:15],v[76:79],a[20:23]
v_mfma_f32_16x16x32_bf16 a[36:39],v[16:19],v[72:75],a[36:39]
v_mfma_f32_16x16x32_bf16 a[36:39],v[20:23],v[76:79],a[36:39]
v_mfma_f32_16x16x32_bf16 a[52:55],v[24:27],v[72:75],a[52:55]
v_mfma_f32_16x16x32_bf16 a[52:55],v[28:31],v[76:79],a[52:55]
s_waitcnt lgkmcnt(0)
s_barrier
v_mfma_f32_16x16x32_bf16 a[8:11],v[0:3],v[80:83],a[8:11]
v_mfma_f32_16x16x32_bf16 a[8:11],v[4:7],v[84:87],a[8:11]
ds_read_b64_tr_b16 v[64:65],v100 offset:32768
v_mfma_f32_16x16x32_bf16 a[24:27],v[8:11],v[80:83],a[24:27]
ds_read_b64_tr_b16 v[66:67],v101 offset:32768
v_mfma_f32_16x16x32_bf16 a[24:27],v[12:15],v[84:87],a[24:27]
ds_read_b64_tr_b16 v[68:69],v100 offset:33280
v_mfma_f32_16x16x32_bf16 a[40:43],v[16:19],v[80:83],a[40:43]
ds_read_b64_tr_b16 v[70:71],v101 offset:33280
v_mfma_f32_16x16x32_bf16 a[40:43],v[20:23],v[84:87],a[40:43]
v_mfma_f32_16x16x32_bf16 a[56:59],v[24:27],v[80:83],a[56:59]
v_mfma_f32_16x16x32_bf16 a[56:59],v[28:31],v[84:87],a[56:59]
v_mfma_f32_16x16x32_bf16 a[12:15],v[0:3],v[88:91],a[12:15]
v_mfma_f32_16x16x32_bf16 a[12:15],v[4:7],v[92:95],a[12:15]
ds_read_b64_tr_b16 v[72:73],v100 offset:34816
v_mfma_f32_16x16x32_bf16 a[28:31],v[8:11],v[88:91],a[28:31]
ds_read_b64_tr_b16 v[74:75],v101 offset:34816
v_mfma_f32_16x16x32_bf16 a[28:31],v[12:15],v[92:95],a[28:31]
ds_read_b64_tr_b16 v[76:77],v100 offset:35328
v_mfma_f32_16x16x32_bf16 a[44:47],v[16:19],v[88:91],a[44:47]
ds_read_b64_tr_b16 v[78:79],v101 offset:35328
v_mfma_f32_16x16x32_bf16 a[44:47],v[20:23],v[92:95],a[44:47]
v_mfma_f32_16x16x32_bf16 a[60:63],v[24:27],v[88:91],a[60:63]
v_mfma_f32_16x16x32_bf16 a[60:63],v[28:31],v[92:95],a[60:63]
s_waitcnt lgkmcnt(6)
v_mfma_f32_16x16x32_bf16 a[0:3],v[32:35],v[64:67],a[0:3]
s_waitcnt lgkmcnt(4)
v_mfma_f32_16x16x32_bf16 a[0:3],v[36:39],v[68:71],a[0:3]
ds_read_b64_tr_b16 v[80:81],v100 offset:36864
v_mfma_f32_16x16x32_bf16 a[16:19],v[40:43],v[64:67],a[16:19]
ds_read_b64_tr_b16 v[82:83],v101 offset:36864
ds_read_b128 v[0:3],v114 offset:0
v_mfma_f32_16x16x32_bf16 a[16:19],v[44:47],v[68:71],a[16:19]
ds_read_b64_tr_b16 v[84:85],v100 offset:37376
ds_read_b128 v[4:7],v115 offset:0
v_mfma_f32_16x16x32_bf16 a[32:35],v[48:51],v[64:67],a[32:35]
ds_read_b64_tr_b16 v[86:87],v101 offset:37376
ds_read_b128 v[8:11],v114 offset:2048
v_mfma_f32_16x16x32_bf16 a[32:35],v[52:55],v[68:71],a[32:35]
ds_read_b128 v[12:15],v115 offset:2048
ds_read_b128 v[16:19],v114 offset:4096
v_mfma_f32_16x16x32_bf16 a[48:51],v[56:59],v[64:67],a[48:51]
ds_read_b128 v[20:23],v115 offset:4096
ds_read_b128 v[24:27],v114 offset:6144
v_mfma_f32_16x16x32_bf16 a[48:51],v[60:63],v[68:71],a[48:51]
ds_read_b128 v[28:31],v115 offset:6144
s_waitcnt lgkmcnt(14)
v_mfma_f32_16x16x32_bf16 a[4:7],v[32:35],v[72:75],a[4:7]
ds_read_b64_tr_b16 v[88:89],v100 offset:38912
ds_read_b64_tr_b16 v[90:91],v101 offset:38912
s_waitcnt lgkmcnt(14)
v_mfma_f32_16x16x32_bf16 a[4:7],v[36:39],v[76:79],a[4:7]
ds_read_b64_tr_b16 v[92:93],v100 offset:39424
ds_read_b64_tr_b16 v[94:95],v101 offset:39424
v_mfma_f32_16x16x32_bf16 a[20:23],v[40:43],v[72:75],a[20:23]
v_mfma_f32_16x16x32_bf16 a[20:23],v[44:47],v[76:79],a[20:23]
v_mfma_f32_16x16x32_bf16 a[36:39],v[48:51],v[72:75],a[36:39]
v_mfma_f32_16x16x32_bf16 a[36:39],v[52:55],v[76:79],a[36:39]
v_mfma_f32_16x16x32_bf16 a[52:55],v[56:59],v[72:75],a[52:55]
v_mfma_f32_16x16x32_bf16 a[52:55],v[60:63],v[76:79],a[52:55]
s_waitcnt lgkmcnt(0)
s_barrier
v_mfma_f32_16x16x32_bf16 a[8:11],v[32:35],v[80:83],a[8:11]
v_mfma_f32_16x16x32_bf16 a[8:11],v[36:39],v[84:87],a[8:11]
ds_read_b64_tr_b16 v[64:65],v104 offset:0
v_mfma_f32_16x16x32_bf16 a[24:27],v[40:43],v[80:83],a[24:27]
ds_read_b64_tr_b16 v[66:67],v105 offset:0
v_mfma_f32_16x16x32_bf16 a[24:27],v[44:47],v[84:87],a[24:27]
ds_read_b64_tr_b16 v[68:69],v104 offset:512
v_mfma_f32_16x16x32_bf16 a[40:43],v[48:51],v[80:83],a[40:43]
ds_read_b64_tr_b16 v[70:71],v105 offset:512
v_mfma_f32_16x16x32_bf16 a[40:43],v[52:55],v[84:87],a[40:43]
v_mfma_f32_16x16x32_bf16 a[56:59],v[56:59],v[80:83],a[56:59]
v_mfma_f32_16x16x32_bf16 a[56:59],v[60:63],v[84:87],a[56:59]
v_mfma_f32_16x16x32_bf16 a[12:15],v[32:35],v[88:91],a[12:15]
v_mfma_f32_16x16x32_bf16 a[12:15],v[36:39],v[92:95],a[12:15]
ds_read_b64_tr_b16 v[72:73],v104 offset:2048
v_mfma_f32_16x16x32_bf16 a[28:31],v[40:43],v[88:91],a[28:31]
ds_read_b64_tr_b16 v[74:75],v105 offset:2048
v_mfma_f32_16x16x32_bf16 a[28:31],v[44:47],v[92:95],a[28:31]
ds_read_b64_tr_b16 v[76:77],v104 offset:2560
v_mfma_f32_16x16x32_bf16 a[44:47],v[48:51],v[88:91],a[44:47]
ds_read_b64_tr_b16 v[78:79],v105 offset:2560
v_mfma_f32_16x16x32_bf16 a[44:47],v[52:55],v[92:95],a[44:47]
v_mfma_f32_16x16x32_bf16 a[60:63],v[56:59],v[88:91],a[60:63]
v_mfma_f32_16x16x32_bf16 a[60:63],v[60:63],v[92:95],a[60:63]
s_waitcnt lgkmcnt(6)
v_mfma_f32_16x16x32_bf16 a[0:3],v[0:3],v[64:67],a[0:3]
s_waitcnt lgkmcnt(4)
v_mfma_f32_16x16x32_bf16 a[0:3],v[4:7],v[68:71],a[0:3]
ds_read_b64_tr_b16 v[80:81],v104 offset:4096
v_mfma_f32_16x16x32_bf16 a[16:19],v[8:11],v[64:67],a[16:19]
ds_read_b64_tr_b16 v[82:83],v105 offset:4096
ds_read_b128 v[32:35],v114 offset:32768
v_mfma_f32_16x16x32_bf16 a[16:19],v[12:15],v[68:71],a[16:19]
ds_read_b64_tr_b16 v[84:85],v104 offset:4608
ds_read_b128 v[36:39],v115 offset:32768
v_mfma_f32_16x16x32_bf16 a[32:35],v[16:19],v[64:67],a[32:35]
ds_read_b64_tr_b16 v[86:87],v105 offset:4608
ds_read_b128 v[40:43],v114 offset:34816
v_mfma_f32_16x16x32_bf16 a[32:35],v[20:23],v[68:71],a[32:35]
ds_read_b128 v[44:47],v115 offset:34816
ds_read_b128 v[48:51],v114 offset:36864
v_mfma_f32_16x16x32_bf16 a[48:51],v[24:27],v[64:67],a[48:51]
ds_read_b128 v[52:55],v115 offset:36864
ds_read_b128 v[56:59],v114 offset:38912
v_mfma_f32_16x16x32_bf16 a[48:51],v[28:31],v[68:71],a[48:51]
ds_read_b128 v[60:63],v115 offset:38912
s_waitcnt lgkmcnt(14)
v_mfma_f32_16x16x32_bf16 a[4:7],v[0:3],v[72:75],a[4:7]
ds_read_b64_tr_b16 v[88:89],v104 offset:6144
ds_read_b64_tr_b16 v[90:91],v105 offset:6144
s_waitcnt lgkmcnt(14)
v_mfma_f32_16x16x32_bf16 a[4:7],v[4:7],v[76:79],a[4:7]
ds_read_b64_tr_b16 v[92:93],v104 offset:6656
ds_read_b64_tr_b16 v[94:95],v105 offset:6656
v_mfma_f32_16x16x32_bf16 a[20:23],v[8:11],v[72:75],a[20:23]
v_mfma_f32_16x16x32_bf16 a[20:23],v[12:15],v[76:79],a[20:23]
v_mfma_f32_16x16x32_bf16 a[36:39],v[16:19],v[72:75],a[36:39]
v_mfma_f32_16x16x32_bf16 a[36:39],v[20:23],v[76:79],a[36:39]
v_mfma_f32_16x16x32_bf16 a[52:55],v[24:27],v[72:75],a[52:55]
v_mfma_f32_16x16x32_bf16 a[52:55],v[28:31],v[76:79],a[52:55]
s_waitcnt lgkmcnt(0)
s_barrier
v_mfma_f32_16x16x32_bf16 a[8:11],v[0:3],v[80:83],a[8:11]
v_mfma_f32_16x16x32_bf16 a[8:11],v[4:7],v[84:87],a[8:11]
ds_read_b64_tr_b16 v[64:65],v104 offset:32768
v_mfma_f32_16x16x32_bf16 a[24:27],v[8:11],v[80:83],a[24:27]
ds_read_b64_tr_b16 v[66:67],v105 offset:32768
v_mfma_f32_16x16x32_bf16 a[24:27],v[12:15],v[84:87],a[24:27]
ds_read_b64_tr_b16 v[68:69],v104 offset:33280
v_mfma_f32_16x16x32_bf16 a[40:43],v[16:19],v[80:83],a[40:43]
ds_read_b64_tr_b16 v[70:71],v105 offset:33280
v_mfma_f32_16x16x32_bf16 a[40:43],v[20:23],v[84:87],a[40:43]
v_mfma_f32_16x16x32_bf16 a[56:59],v[24:27],v[80:83],a[56:59]
v_mfma_f32_16x16x32_bf16 a[56:59],v[28:31],v[84:87],a[56:59]
v_mfma_f32_16x16x32_bf16 a[12:15],v[0:3],v[88:91],a[12:15]
v_mfma_f32_16x16x32_bf16 a[12:15],v[4:7],v[92:95],a[12:15]
ds_read_b64_tr_b16 v[72:73],v104 offset:34816
v_mfma_f32_16x16x32_bf16 a[28:31],v[8:11],v[88:91],a[28:31]
ds_read_b64_tr_b16 v[74:75],v105 offset:34816
v_mfma_f32_16x16x32_bf16 a[28:31],v[12:15],v[92:95],a[28:31]
ds_read_b64_tr_b16 v[76:77],v104 offset:35328
v_mfma_f32_16x16x32_bf16 a[44:47],v[16:19],v[88:91],a[44:47]
ds_read_b64_tr_b16 v[78:79],v105 offset:35328
v_mfma_f32_16x16x32_bf16 a[44:47],v[20:23],v[92:95],a[44:47]
v_mfma_f32_16x16x32_bf16 a[60:63],v[24:27],v[88:91],a[60:63]
v_mfma_f32_16x16x32_bf16 a[60:63],v[28:31],v[92:95],a[60:63]
s_waitcnt lgkmcnt(6)
v_mfma_f32_16x16x32_bf16 a[0:3],v[32:35],v[64:67],a[0:3]
s_waitcnt lgkmcnt(4)
v_mfma_f32_16x16x32_bf16 a[0:3],v[36:39],v[68:71],a[0:3]
ds_read_b64_tr_b16 v[80:81],v104 offset:36864
v_mfma_f32_16x16x32_bf16 a[16:19],v[40:43],v[64:67],a[16:19]
ds_read_b64_tr_b16 v[82:83],v105 offset:36864
ds_read_b128 v[0:3],v123 offset:0
v_mfma_f32_16x16x32_bf16 a[16:19],v[44:47],v[68:71],a[16:19]
ds_read_b64_tr_b16 v[84:85],v104 offset:37376
ds_read_b128 v[4:7],v124 offset:0
v_mfma_f32_16x16x32_bf16 a[32:35],v[48:51],v[64:67],a[32:35]
ds_read_b64_tr_b16 v[86:87],v105 offset:37376
ds_read_b128 v[8:11],v123 offset:2048
v_mfma_f32_16x16x32_bf16 a[32:35],v[52:55],v[68:71],a[32:35]
ds_read_b128 v[12:15],v124 offset:2048
ds_read_b128 v[16:19],v123 offset:4096
v_mfma_f32_16x16x32_bf16 a[48:51],v[56:59],v[64:67],a[48:51]
ds_read_b128 v[20:23],v124 offset:4096
)"
R"(ds_read_b128 v[24:27],v123 offset:6144
v_mfma_f32_16x16x32_bf16 a[48:51],v[60:63],v[68:71],a[48:51]
ds_read_b128 v[28:31],v124 offset:6144
s_waitcnt lgkmcnt(14)
v_mfma_f32_16x16x32_bf16 a[4:7],v[32:35],v[72:75],a[4:7]
ds_read_b64_tr_b16 v[88:89],v104 offset:38912
ds_read_b64_tr_b16 v[90:91],v105 offset:38912
s_waitcnt lgkmcnt(14)
v_mfma_f32_16x16x32_bf16 a[4:7],v[36:39],v[76:79],a[4:7]
ds_read_b64_tr_b16 v[92:93],v104 offset:39424
ds_read_b64_tr_b16 v[94:95],v105 offset:39424
v_mfma_f32_16x16x32_bf16 a[20:23],v[40:43],v[72:75],a[20:23]
v_mfma_f32_16x16x32_bf16 a[20:23],v[44:47],v[76:79],a[20:23]
v_mfma_f32_16x16x32_bf16 a[36:39],v[48:51],v[72:75],a[36:39]
v_mfma_f32_16x16x32_bf16 a[36:39],v[52:55],v[76:79],a[36:39]
v_mfma_f32_16x16x32_bf16 a[52:55],v[56:59],v[72:75],a[52:55]
v_mfma_f32_16x16x32_bf16 a[52:55],v[60:63],v[76:79],a[52:55]
s_waitcnt lgkmcnt(0)
s_barrier
v_mfma_f32_16x16x32_bf16 a[8:11],v[32:35],v[80:83],a[8:11]
v_mfma_f32_16x16x32_bf16 a[8:11],v[36:39],v[84:87],a[8:11]
ds_read_b64_tr_b16 v[64:65],v108 offset:0
v_mfma_f32_16x16x32_bf16 a[24:27],v[40:43],v[80:83],a[24:27]
ds_read_b64_tr_b16 v[66:67],v109 offset:0
v_mfma_f32_16x16x32_bf16 a[24:27],v[44:47],v[84:87],a[24:27]
ds_read_b64_tr_b16 v[68:69],v108 offset:512
v_mfma_f32_16x16x32_bf16 a[40:43],v[48:51],v[80:83],a[40:43]
ds_read_b64_tr_b16 v[70:71],v109 offset:512
v_mfma_f32_16x16x32_bf16 a[40:43],v[52:55],v[84:87],a[40:43]
v_mfma_f32_16x16x32_bf16 a[56:59],v[56:59],v[80:83],a[56:59]
v_mfma_f32_16x16x32_bf16 a[56:59],v[60:63],v[84:87],a[56:59]
v_mfma_f32_16x16x32_bf16 a[12:15],v[32:35],v[88:91],a[12:15]
v_mfma_f32_16x16x32_bf16 a[12:15],v[36:39],v[92:95],a[12:15]
ds_read_b64_tr_b16 v[72:73],v108 offset:2048
v_mfma_f32_16x16x32_bf16 a[28:31],v[40:43],v[88:91],a[28:31]
ds_read_b64_tr_b16 v[74:75],v109 offset:2048
v_mfma_f32_16x16x32_bf16 a[28:31],v[44:47],v[92:95],a[28:31]
ds_read_b64_tr_b16 v[76:77],v108 offset:2560
v_mfma_f32_16x16x32_bf16 a[44:47],v[48:51],v[88:91],a[44:47]
ds_read_b64_tr_b16 v[78:79],v109 offset:2560
v_mfma_f32_16x16x32_bf16 a[44:47],v[52:55],v[92:95],a[44:47]
v_mfma_f32_16x16x32_bf16 a[60:63],v[56:59],v[88:91],a[60:63]
v_mfma_f32_16x16x32_bf16 a[60:63],v[60:63],v[92:95],a[60:63]
s_waitcnt lgkmcnt(6)
v_mfma_f32_16x16x32_bf16 a[0:3],v[0:3],v[64:67],a[0:3]
s_waitcnt lgkmcnt(4)
v_mfma_f32_16x16x32_bf16 a[0:3],v[4:7],v[68:71],a[0:3]
ds_read_b64_tr_b16 v[80:81],v108 offset:4096
v_mfma_f32_16x16x32_bf16 a[16:19],v[8:11],v[64:67],a[16:19]
ds_read_b64_tr_b16 v[82:83],v109 offset:4096
ds_read_b128 v[32:35],v112 offset:0
v_mfma_f32_16x16x32_bf16 a[16:19],v[12:15],v[68:71],a[16:19]
ds_read_b64_tr_b16 v[84:85],v108 offset:4608
ds_read_b128 v[36:39],v113 offset:0
v_mfma_f32_16x16x32_bf16 a[32:35],v[16:19],v[64:67],a[32:35]
ds_read_b64_tr_b16 v[86:87],v109 offset:4608
ds_read_b128 v[40:43],v112 offset:2048
v_mfma_f32_16x16x32_bf16 a[32:35],v[20:23],v[68:71],a[32:35]
ds_read_b128 v[44:47],v113 offset:2048
ds_read_b128 v[48:51],v112 offset:4096
v_mfma_f32_16x16x32_bf16 a[48:51],v[24:27],v[64:67],a[48:51]
ds_read_b128 v[52:55],v113 offset:4096
ds_read_b128 v[56:59],v112 offset:6144
v_mfma_f32_16x16x32_bf16 a[48:51],v[28:31],v[68:71],a[48:51]
ds_read_b128 v[60:63],v113 offset:6144
s_waitcnt lgkmcnt(14)
v_mfma_f32_16x16x32_bf16 a[4:7],v[0:3],v[72:75],a[4:7]
ds_read_b64_tr_b16 v[88:89],v108 offset:6144
ds_read_b64_tr_b16 v[90:91],v109 offset:6144
s_waitcnt lgkmcnt(14)
v_mfma_f32_16x16x32_bf16 a[4:7],v[4:7],v[76:79],a[4:7]
ds_read_b64_tr_b16 v[92:93],v108 offset:6656
ds_read_b64_tr_b16 v[94:95],v109 offset:6656
v_mfma_f32_16x16x32_bf16 a[20:23],v[8:11],v[72:75],a[20:23]
v_mfma_f32_16x16x32_bf16 a[20:23],v[12:15],v[76:79],a[20:23]
v_mfma_f32_16x16x32_bf16 a[36:39],v[16:19],v[72:75],a[36:39]
v_mfma_f32_16x16x32_bf16 a[36:39],v[20:23],v[76:79],a[36:39]
v_mfma_f32_16x16x32_bf16 a[52:55],v[24:27],v[72:75],a[52:55]
v_mfma_f32_16x16x32_bf16 a[52:55],v[28:31],v[76:79],a[52:55]
s_waitcnt lgkmcnt(0)
s_barrier
v_mfma_f32_16x16x32_bf16 a[8:11],v[0:3],v[80:83],a[8:11]
v_mfma_f32_16x16x32_bf16 a[8:11],v[4:7],v[84:87],a[8:11]
ds_read_b64_tr_b16 v[64:65],v100 offset:0
v_mfma_f32_16x16x32_bf16 a[24:27],v[8:11],v[80:83],a[24:27]
ds_read_b64_tr_b16 v[66:67],v101 offset:0
v_mfma_f32_16x16x32_bf16 a[24:27],v[12:15],v[84:87],a[24:27]
ds_read_b64_tr_b16 v[68:69],v100 offset:512
v_mfma_f32_16x16x32_bf16 a[40:43],v[16:19],v[80:83],a[40:43]
ds_read_b64_tr_b16 v[70:71],v101 offset:512
v_mfma_f32_16x16x32_bf16 a[40:43],v[20:23],v[84:87],a[40:43]
v_mfma_f32_16x16x32_bf16 a[56:59],v[24:27],v[80:83],a[56:59]
v_mfma_f32_16x16x32_bf16 a[56:59],v[28:31],v[84:87],a[56:59]
v_mfma_f32_16x16x32_bf16 a[12:15],v[0:3],v[88:91],a[12:15]
v_mfma_f32_16x16x32_bf16 a[12:15],v[4:7],v[92:95],a[12:15]
ds_read_b64_tr_b16 v[72:73],v100 offset:2048
v_mfma_f32_16x16x32_bf16 a[28:31],v[8:11],v[88:91],a[28:31]
ds_read_b64_tr_b16 v[74:75],v101 offset:2048
v_mfma_f32_16x16x32_bf16 a[28:31],v[12:15],v[92:95],a[28:31]
ds_read_b64_tr_b16 v[76:77],v100 offset:2560
v_mfma_f32_16x16x32_bf16 a[44:47],v[16:19],v[88:91],a[44:47]
ds_read_b64_tr_b16 v[78:79],v101 offset:2560
v_mfma_f32_16x16x32_bf16 a[44:47],v[20:23],v[92:95],a[44:47]
v_mfma_f32_16x16x32_bf16 a[60:63],v[24:27],v[88:91],a[60:63]
v_mfma_f32_16x16x32_bf16 a[60:63],v[28:31],v[92:95],a[60:63]
s_waitcnt lgkmcnt(6)
v_mfma_f32_16x16x32_bf16 a[0:3],v[32:35],v[64:67],a[0:3]
s_waitcnt lgkmcnt(4)
v_mfma_f32_16x16x32_bf16 a[0:3],v[36:39],v[68:71],a[0:3]
ds_read_b64_tr_b16 v[80:81],v100 offset:4096
v_mfma_f32_16x16x32_bf16 a[16:19],v[40:43],v[64:67],a[16:19]
ds_read_b64_tr_b16 v[82:83],v101 offset:4096
ds_read_b128 v[0:3],v112 offset:32768
v_mfma_f32_16x16x32_bf16 a[16:19],v[44:47],v[68:71],a[16:19]
ds_read_b64_tr_b16 v[84:85],v100 offset:4608
ds_read_b128 v[4:7],v113 offset:32768
v_mfma_f32_16x16x32_bf16 a[32:35],v[48:51],v[64:67],a[32:35]
ds_read_b64_tr_b16 v[86:87],v101 offset:4608
ds_read_b128 v[8:11],v112 offset:34816
v_mfma_f32_16x16x32_bf16 a[32:35],v[52:55],v[68:71],a[32:35]
ds_read_b128 v[12:15],v113 offset:34816
ds_read_b128 v[16:19],v112 offset:36864
v_mfma_f32_16x16x32_bf16 a[48:51],v[56:59],v[64:67],a[48:51]
ds_read_b128 v[20:23],v113 offset:36864
ds_read_b128 v[24:27],v112 offset:38912
v_mfma_f32_16x16x32_bf16 a[48:51],v[60:63],v[68:71],a[48:51]
ds_read_b128 v[28:31],v113 offset:38912
s_waitcnt lgkmcnt(14)
v_mfma_f32_16x16x32_bf16 a[4:7],v[32:35],v[72:75],a[4:7]
ds_read_b64_tr_b16 v[88:89],v100 offset:6144
ds_read_b64_tr_b16 v[90:91],v101 offset:6144
s_waitcnt lgkmcnt(14)
v_mfma_f32_16x16x32_bf16 a[4:7],v[36:39],v[76:79],a[4:7]
ds_read_b64_tr_b16 v[92:93],v100 offset:6656
ds_read_b64_tr_b16 v[94:95],v101 offset:6656
v_mfma_f32_16x16x32_bf16 a[20:23],v[40:43],v[72:75],a[20:23]
v_mfma_f32_16x16x32_bf16 a[20:23],v[44:47],v[76:79],a[20:23]
v_mfma_f32_16x16x32_bf16 a[36:39],v[48:51],v[72:75],a[36:39]
v_mfma_f32_16x16x32_bf16 a[36:39],v[52:55],v[76:79],a[36:39]
v_mfma_f32_16x16x32_bf16 a[52:55],v[56:59],v[72:75],a[52:55]
v_mfma_f32_16x16x32_bf16 a[52:55],v[60:63],v[76:79],a[52:55]
s_waitcnt lgkmcnt(0)
s_barrier
v_mfma_f32_16x16x32_bf16 a[8:11],v[32:35],v[80:83],a[8:11]
v_mfma_f32_16x16x32_bf16 a[8:11],v[36:39],v[84:87],a[8:11]
ds_read_b64_tr_b16 v[64:65],v100 offset:32768
v_mfma_f32_16x16x32_bf16 a[24:27],v[40:43],v[80:83],a[24:27]
ds_read_b64_tr_b16 v[66:67],v101 offset:32768
v_mfma_f32_16x16x32_bf16 a[24:27],v[44:47],v[84:87],a[24:27]
ds_read_b64_tr_b16 v[68:69],v100 offset:33280
v_mfma_f32_16x16x32_bf16 a[40:43],v[48:51],v[80:83],a[40:43]
ds_read_b64_tr_b16 v[70:71],v101 offset:33280
v_mfma_f32_16x16x32_bf16 a[40:43],v[52:55],v[84:87],a[40:43]
v_mfma_f32_16x16x32_bf16 a[56:59],v[56:59],v[80:83],a[56:59]
v_mfma_f32_16x16x32_bf16 a[56:59],v[60:63],v[84:87],a[56:59]
v_mfma_f32_16x16x32_bf16 a[12:15],v[32:35],v[88:91],a[12:15]
v_mfma_f32_16x16x32_bf16 a[12:15],v[36:39],v[92:95],a[12:15]
ds_read_b64_tr_b16 v[72:73],v100 offset:34816
v_mfma_f32_16x16x32_bf16 a[28:31],v[40:43],v[88:91],a[28:31]
ds_read_b64_tr_b16 v[74:75],v101 offset:34816
v_mfma_f32_16x16x32_bf16 a[28:31],v[44:47],v[92:95],a[28:31]
ds_read_b64_tr_b16 v[76:77],v100 offset:35328
v_mfma_f32_16x16x32_bf16 a[44:47],v[48:51],v[88:91],a[44:47]
ds_read_b64_tr_b16 v[78:79],v101 offset:35328
v_mfma_f32_16x16x32_bf16 a[44:47],v[52:55],v[92:95],a[44:47]
v_mfma_f32_16x16x32_bf16 a[60:63],v[56:59],v[88:91],a[60:63]
v_mfma_f32_16x16x32_bf16 a[60:63],v[60:63],v[92:95],a[60:63]
s_waitcnt lgkmcnt(6)
v_mfma_f32_16x16x32_bf16 a[0:3],v[0:3],v[64:67],a[0:3]
s_waitcnt lgkmcnt(4)
v_mfma_f32_16x16x32_bf16 a[0:3],v[4:7],v[68:71],a[0:3]
ds_read_b64_tr_b16 v[80:81],v100 offset:36864
v_mfma_f32_16x16x32_bf16 a[16:19],v[8:11],v[64:67],a[16:19]
ds_read_b64_tr_b16 v[82:83],v101 offset:36864
ds_read_b128 v[32:35],v114 offset:0
v_mfma_f32_16x16x32_bf16 a[16:19],v[12:15],v[68:71],a[16:19]
ds_read_b64_tr_b16 v[84:85],v100 offset:37376
ds_read_b128 v[36:39],v115 offset:0
v_mfma_f32_16x16x32_bf16 a[32:35],v[16:19],v[64:67],a[32:35]
ds_read_b64_tr_b16 v[86:87],v101 offset:37376
ds_read_b128 v[40:43],v114 offset:2048
v_mfma_f32_16x16x32_bf16 a[32:35],v[20:23],v[68:71],a[32:35]
ds_read_b128 v[44:47],v115 offset:2048
ds_read_b128 v[48:51],v114 offset:4096
v_mfma_f32_16x16x32_bf16 a[48:51],v[24:27],v[64:67],a[48:51]
ds_read_b128 v[52:55],v115 offset:4096
ds_read_b128 v[56:59],v114 offset:6144
v_mfma_f32_16x16x32_bf16 a[48:51],v[28:31],v[68:71],a[48:51]
ds_read_b128 v[60:63],v115 offset:6144
s_waitcnt lgkmcnt(14)
v_mfma_f32_16x16x32_bf16 a[4:7],v[0:3],v[72:75],a[4:7]
ds_read_b64_tr_b16 v[88:89],v100 offset:38912
ds_read_b64_tr_b16 v[90:91],v101 offset:38912
s_waitcnt lgkmcnt(14)
v_mfma_f32_16x16x32_bf16 a[4:7],v[4:7],v[76:79],a[4:7]
ds_read_b64_tr_b16 v[92:93],v100 offset:39424
ds_read_b64_tr_b16 v[94:95],v101 offset:39424
v_mfma_f32_16x16x32_bf16 a[20:23],v[8:11],v[72:75],a[20:23]
v_mfma_f32_16x16x32_bf16 a[20:23],v[12:15],v[76:79],a[20:23]
v_mfma_f32_16x16x32_bf16 a[36:39],v[16:19],v[72:75],a[36:39]
v_mfma_f32_16x16x32_bf16 a[36:39],v[20:23],v[76:79],a[36:39]
v_mfma_f32_16x16x32_bf16 a[52:55],v[24:27],v[72:75],a[52:55]
v_mfma_f32_16x16x32_bf16 a[52:55],v[28:31],v[76:79],a[52:55]
s_waitcnt lgkmcnt(0)
s_barrier
v_mfma_f32_16x16x32_bf16 a[8:11],v[0:3],v[80:83],a[8:11]
v_mfma_f32_16x16x32_bf16 a[8:11],v[4:7],v[84:87],a[8:11]
ds_read_b64_tr_b16 v[64:65],v104 offset:0
v_mfma_f32_16x16x32_bf16 a[24:27],v[8:11],v[80:83],a[24:27]
ds_read_b64_tr_b16 v[66:67],v105 offset:0
v_mfma_f32_16x16x32_bf16 a[24:27],v[12:15],v[84:87],a[24:27]
ds_read_b64_tr_b16 v[68:69],v104 offset:512
v_mfma_f32_16x16x32_bf16 a[40:43],v[16:19],v[80:83],a[40:43]
ds_read_b64_tr_b16 v[70:71],v105 offset:512
v_mfma_f32_16x16x32_bf16 a[40:43],v[20:23],v[84:87],a[40:43]
v_mfma_f32_16x16x32_bf16 a[56:59],v[24:27],v[80:83],a[56:59]
v_mfma_f32_16x16x32_bf16 a[56:59],v[28:31],v[84:87],a[56:59]
v_mfma_f32_16x16x32_bf16 a[12:15],v[0:3],v[88:91],a[12:15]
v_mfma_f32_16x16x32_bf16 a[12:15],v[4:7],v[92:95],a[12:15]
ds_read_b64_tr_b16 v[72:73],v104 offset:2048
v_mfma_f32_16x16x32_bf16 a[28:31],v[8:11],v[88:91],a[28:31]
ds_read_b64_tr_b16 v[74:75],v105 offset:2048
v_mfma_f32_16x16x32_bf16 a[28:31],v[12:15],v[92:95],a[28:31]
ds_read_b64_tr_b16 v[76:77],v104 offset:2560
v_mfma_f32_16x16x32_bf16 a[44:47],v[16:19],v[88:91],a[44:47]
ds_read_b64_tr_b16 v[78:79],v105 offset:2560
v_mfma_f32_16x16x32_bf16 a[44:47],v[20:23],v[92:95],a[44:47]
v_mfma_f32_16x16x32_bf16 a[60:63],v[24:27],v[88:91],a[60:63]
v_mfma_f32_16x16x32_bf16 a[60:63],v[28:31],v[92:95],a[60:63]
s_waitcnt lgkmcnt(6)
v_mfma_f32_16x16x32_bf16 a[0:3],v[32:35],v[64:67],a[0:3]
s_waitcnt lgkmcnt(4)
v_mfma_f32_16x16x32_bf16 a[0:3],v[36:39],v[68:71],a[0:3]
ds_read_b64_tr_b16 v[80:81],v104 offset:4096
v_mfma_f32_16x16x32_bf16 a[16:19],v[40:43],v[64:67],a[16:19]
ds_read_b64_tr_b16 v[82:83],v105 offset:4096
ds_read_b128 v[0:3],v114 offset:32768
v_mfma_f32_16x16x32_bf16 a[16:19],v[44:47],v[68:71],a[16:19]
ds_read_b64_tr_b16 v[84:85],v104 offset:4608
ds_read_b128 v[4:7],v115 offset:32768
v_mfma_f32_16x16x32_bf16 a[32:35],v[48:51],v[64:67],a[32:35]
ds_read_b64_tr_b16 v[86:87],v105 offset:4608
ds_read_b128 v[8:11],v114 offset:34816
v_mfma_f32_16x16x32_bf16 a[32:35],v[52:55],v[68:71],a[32:35]
ds_read_b128 v[12:15],v115 offset:34816
ds_read_b128 v[16:19],v114 offset:36864
v_mfma_f32_16x16x32_bf16 a[48:51],v[56:59],v[64:67],a[48:51]
ds_read_b128 v[20:23],v115 offset:36864
ds_read_b128 v[24:27],v114 offset:38912
v_mfma_f32_16x16x32_bf16 a[48:51],v[60:63],v[68:71],a[48:51]
ds_read_b128 v[28:31],v115 offset:38912
s_waitcnt lgkmcnt(14)
v_mfma_f32_16x16x32_bf16 a[4:7],v[32:35],v[72:75],a[4:7]
ds_read_b64_tr_b16 v[88:89],v104 offset:6144
ds_read_b64_tr_b16 v[90:91],v105 offset:6144
s_waitcnt lgkmcnt(14)
v_mfma_f32_16x16x32_bf16 a[4:7],v[36:39],v[76:79],a[4:7]
ds_read_b64_tr_b16 v[92:93],v104 offset:6656
ds_read_b64_tr_b16 v[94:95],v105 offset:6656
v_mfma_f32_16x16x32_bf16 a[20:23],v[40:43],v[72:75],a[20:23]
v_mfma_f32_16x16x32_bf16 a[20:23],v[44:47],v[76:79],a[20:23]
v_mfma_f32_16x16x32_bf16 a[36:39],v[48:51],v[72:75],a[36:39]
v_mfma_f32_16x16x32_bf16 a[36:39],v[52:55],v[76:79],a[36:39]
v_mfma_f32_16x16x32_bf16 a[52:55],v[56:59],v[72:75],a[52:55]
v_mfma_f32_16x16x32_bf16 a[52:55],v[60:63],v[76:79],a[52:55]
s_waitcnt lgkmcnt(0)
s_barrier
v_mfma_f32_16x16x32_bf16 a[8:11],v[32:35],v[80:83],a[8:11]
v_mfma_f32_16x16x32_bf16 a[8:11],v[36:39],v[84:87],a[8:11]
ds_read_b64_tr_b16 v[64:65],v104 offset:32768
v_mfma_f32_16x16x32_bf16 a[24:27],v[40:43],v[80:83],a[24:27]
ds_read_b64_tr_b16 v[66:67],v105 offset:32768
v_mfma_f32_16x16x32_bf16 a[24:27],v[44:47],v[84:87],a[24:27]
ds_read_b64_tr_b16 v[68:69],v104 offset:33280
v_mfma_f32_16x16x32_bf16 a[40:43],v[48:51],v[80:83],a[40:43]
ds_read_b64_tr_b16 v[70:71],v105 offset:33280
v_mfma_f32_16x16x32_bf16 a[40:43],v[52:55],v[84:87],a[40:43]
v_mfma_f32_16x16x32_bf16 a[56:59],v[56:59],v[80:83],a[56:59]
v_mfma_f32_16x16x32_bf16 a[56:59],v[60:63],v[84:87],a[56:59]
v_mfma_f32_16x16x32_bf16 a[12:15],v[32:35],v[88:91],a[12:15]
v_mfma_f32_16x16x32_bf16 a[12:15],v[36:39],v[92:95],a[12:15]
ds_read_b64_tr_b16 v[72:73],v104 offset:34816
v_mfma_f32_16x16x32_bf16 a[28:31],v[40:43],v[88:91],a[28:31]
ds_read_b64_tr_b16 v[74:75],v105 offset:34816
v_mfma_f32_16x16x32_bf16 a[28:31],v[44:47],v[92:95],a[28:31]
ds_read_b64_tr_b16 v[76:77],v104 offset:35328
v_mfma_f32_16x16x32_bf16 a[44:47],v[48:51],v[88:91],a[44:47]
ds_read_b64_tr_b16 v[78:79],v105 offset:35328
v_mfma_f32_16x16x32_bf16 a[44:47],v[52:55],v[92:95],a[44:47]
v_mfma_f32_16x16x32_bf16 a[60:63],v[56:59],v[88:91],a[60:63]
v_mfma_f32_16x16x32_bf16 a[60:63],v[60:63],v[92:95],a[60:63]
s_waitcnt lgkmcnt(6)
v_mfma_f32_16x16x32_bf16 a[0:3],v[0:3],v[64:67],a[0:3]
s_waitcnt lgkmcnt(4)
v_mfma_f32_16x16x32_bf16 a[0:3],v[4:7],v[68:71],a[0:3]
ds_read_b64_tr_b16 v[80:81],v104 offset:36864
v_mfma_f32_16x16x32_bf16 a[16:19],v[8:11],v[64:67],a[16:19]
ds_read_b64_tr_b16 v[82:83],v105 offset:36864
ds_read_b128 v[32:35],v123 offset:0
v_mfma_f32_16x16x32_bf16 a[16:19],v[12:15],v[68:71],a[16:19]
ds_read_b64_tr_b16 v[84:85],v104 offset:37376
ds_read_b128 v[36:39],v124 offset:0
v_mfma_f32_16x16x32_bf16 a[32:35],v[16:19],v[64:67],a[32:35]
ds_read_b64_tr_b16 v[86:87],v105 offset:37376
ds_read_b128 v[40:43],v123 offset:2048
v_mfma_f32_16x16x32_bf16 a[32:35],v[20:23],v[68:71],a[32:35]
ds_read_b128 v[44:47],v124 offset:2048
ds_read_b128 v[48:51],v123 offset:4096
v_mfma_f32_16x16x32_bf16 a[48:51],v[24:27],v[64:67],a[48:51]
ds_read_b128 v[52:55],v124 offset:4096
ds_read_b128 v[56:59],v123 offset:6144
v_mfma_f32_16x16x32_bf16 a[48:51],v[28:31],v[68:71],a[48:51]
ds_read_b128 v[60:63],v124 offset:6144
s_waitcnt lgkmcnt(14)
v_mfma_f32_16x16x32_bf16 a[4:7],v[0:3],v[72:75],a[4:7]
ds_read_b64_tr_b16 v[88:89],v104 offset:38912
ds_read_b64_tr_b16 v[90:91],v105 offset:38912
s_waitcnt lgkmcnt(14)
v_mfma_f32_16x16x32_bf16 a[4:7],v[4:7],v[76:79],a[4:7]
ds_read_b64_tr_b16 v[92:93],v104 offset:39424
ds_read_b64_tr_b16 v[94:95],v105 offset:39424
v_mfma_f32_16x16x32_bf16 a[20:23],v[8:11],v[72:75],a[20:23]
v_mfma_f32_16x16x32_bf16 a[20:23],v[12:15],v[76:79],a[20:23]
v_mfma_f32_16x16x32_bf16 a[36:39],v[16:19],v[72:75],a[36:39]
v_mfma_f32_16x16x32_bf16 a[36:39],v[20:23],v[76:79],a[36:39]
v_mfma_f32_16x16x32_bf16 a[52:55],v[24:27],v[72:75],a[52:55]
v_mfma_f32_16x16x32_bf16 a[52:55],v[28:31],v[76:79],a[52:55]
s_waitcnt lgkmcnt(0)
s_barrier
v_mfma_f32_16x16x32_bf16 a[8:11],v[0:3],v[80:83],a[8:11]
v_mfma_f32_16x16x32_bf16 a[8:11],v[4:7],v[84:87],a[8:11]
ds_read_b64_tr_b16 v[64:65],v108 offset:0
v_mfma_f32_16x16x32_bf16 a[24:27],v[8:11],v[80:83],a[24:27]
ds_read_b64_tr_b16 v[66:67],v109 offset:0
v_mfma_f32_16x16x32_bf16 a[24:27],v[12:15],v[84:87],a[24:27]
ds_read_b64_tr_b16 v[68:69],v108 offset:512
v_mfma_f32_16x16x32_bf16 a[40:43],v[16:19],v[80:83],a[40:43]
ds_read_b64_tr_b16 v[70:71],v109 offset:512
v_mfma_f32_16x16x32_bf16 a[40:43],v[20:23],v[84:87],a[40:43]
v_mfma_f32_16x16x32_bf16 a[56:59],v[24:27],v[80:83],a[56:59]
v_mfma_f32_16x16x32_bf16 a[56:59],v[28:31],v[84:87],a[56:59]
v_mfma_f32_16x16x32_bf16 a[12:15],v[0:3],v[88:91],a[12:15]
v_mfma_f32_16x16x32_bf16 a[12:15],v[4:7],v[92:95],a[12:15]
ds_read_b64_tr_b16 v[72:73],v108 offset:2048
v_mfma_f32_16x16x32_bf16 a[28:31],v[8:11],v[88:91],a[28:31]
ds_read_b64_tr_b16 v[74:75],v109 offset:2048
v_mfma_f32_16x16x32_bf16 a[28:31],v[12:15],v[92:95],a[28:31]
ds_read_b64_tr_b16 v[76:77],v108 offset:2560
v_mfma_f32_16x16x32_bf16 a[44:47],v[16:19],v[88:91],a[44:47]
ds_read_b64_tr_b16 v[78:79],v109 offset:2560
v_mfma_f32_16x16x32_bf16 a[44:47],v[20:23],v[92:95],a[44:47]
v_mfma_f32_16x16x32_bf16 a[60:63],v[24:27],v[88:91],a[60:63]
v_mfma_f32_16x16x32_bf16 a[60:63],v[28:31],v[92:95],a[60:63]
s_waitcnt lgkmcnt(6)
v_mfma_f32_16x16x32_bf16 a[0:3],v[32:35],v[64:67],a[0:3]
s_waitcnt lgkmcnt(4)
v_mfma_f32_16x16x32_bf16 a[0:3],v[36:39],v[68:71],a[0:3]
ds_read_b64_tr_b16 v[80:81],v108 offset:4096
v_mfma_f32_16x16x32_bf16 a[16:19],v[40:43],v[64:67],a[16:19]
ds_read_b64_tr_b16 v[82:83],v109 offset:4096
v_mfma_f32_16x16x32_bf16 a[16:19],v[44:47],v[68:71],a[16:19]
ds_read_b64_tr_b16 v[84:85],v108 offset:4608
v_mfma_f32_16x16x32_bf16 a[32:35],v[48:51],v[64:67],a[32:35]
ds_read_b64_tr_b16 v[86:87],v109 offset:4608
v_mfma_f32_16x16x32_bf16 a[32:35],v[52:55],v[68:71],a[32:35]
v_mfma_f32_16x16x32_bf16 a[48:51],v[56:59],v[64:67],a[48:51]
v_mfma_f32_16x16x32_bf16 a[48:51],v[60:63],v[68:71],a[48:51]
s_waitcnt lgkmcnt(6)
v_mfma_f32_16x16x32_bf16 a[4:7],v[32:35],v[72:75],a[4:7]
ds_read_b64_tr_b16 v[88:89],v108 offset:6144
ds_read_b64_tr_b16 v[90:91],v109 offset:6144
s_waitcnt lgkmcnt(6)
v_mfma_f32_16x16x32_bf16 a[4:7],v[36:39],v[76:79],a[4:7]
ds_read_b64_tr_b16 v[92:93],v108 offset:6656
ds_read_b64_tr_b16 v[94:95],v109 offset:6656
v_mfma_f32_16x16x32_bf16 a[20:23],v[40:43],v[72:75],a[20:23]
v_mfma_f32_16x16x32_bf16 a[20:23],v[44:47],v[76:79],a[20:23]
v_mfma_f32_16x16x32_bf16 a[36:39],v[48:51],v[72:75],a[36:39]
v_mfma_f32_16x16x32_bf16 a[36:39],v[52:55],v[76:79],a[36:39]
v_mfma_f32_16x16x32_bf16 a[52:55],v[56:59],v[72:75],a[52:55]
v_mfma_f32_16x16x32_bf16 a[52:55],v[60:63],v[76:79],a[52:55]
s_waitcnt lgkmcnt(6)
v_mfma_f32_16x16x32_bf16 a[8:11],v[32:35],v[80:83],a[8:11]
s_waitcnt lgkmcnt(4)
v_mfma_f32_16x16x32_bf16 a[8:11],v[36:39],v[84:87],a[8:11]
v_mfma_f32_16x16x32_bf16 a[24:27],v[40:43],v[80:83],a[24:27]
v_mfma_f32_16x16x32_bf16 a[24:27],v[44:47],v[84:87],a[24:27]
v_mfma_f32_16x16x32_bf16 a[40:43],v[48:51],v[80:83],a[40:43]
v_mfma_f32_16x16x32_bf16 a[40:43],v[52:55],v[84:87],a[40:43]
v_mfma_f32_16x16x32_bf16 a[56:59],v[56:59],v[80:83],a[56:59]
v_mfma_f32_16x16x32_bf16 a[56:59],v[60:63],v[84:87],a[56:59]
s_waitcnt lgkmcnt(2)
v_mfma_f32_16x16x32_bf16 a[12:15],v[32:35],v[88:91],a[12:15]
s_waitcnt lgkmcnt(0)
v_mfma_f32_16x16x32_bf16 a[12:15],v[36:39],v[92:95],a[12:15]
v_mfma_f32_16x16x32_bf16 a[28:31],v[40:43],v[88:91],a[28:31]
v_mfma_f32_16x16x32_bf16 a[28:31],v[44:47],v[92:95],a[28:31]
v_mfma_f32_16x16x32_bf16 a[44:47],v[48:51],v[88:91],a[44:47]
v_mfma_f32_16x16x32_bf16 a[44:47],v[52:55],v[92:95],a[44:47]
v_mfma_f32_16x16x32_bf16 a[60:63],v[56:59],v[88:91],a[60:63]
)"
R"(v_mfma_f32_16x16x32_bf16 a[60:63],v[60:63],v[92:95],a[60:63]
s_nop 7
s_add_u32 s59,s72,0
buffer_store_dwordx4 a[0:3],v116,s[52:55],s59 offen offset:0 
buffer_store_dwordx4 a[16:19],v116,s[52:55],s59 offen offset:64 
buffer_store_dwordx4 a[32:35],v116,s[52:55],s59 offen offset:128 
buffer_store_dwordx4 a[48:51],v116,s[52:55],s59 offen offset:192 
s_add_u32 s59,s72,81920
buffer_store_dwordx4 a[4:7],v116,s[52:55],s59 offen offset:0 
buffer_store_dwordx4 a[20:23],v116,s[52:55],s59 offen offset:64 
buffer_store_dwordx4 a[36:39],v116,s[52:55],s59 offen offset:128 
buffer_store_dwordx4 a[52:55],v116,s[52:55],s59 offen offset:192 
s_add_u32 s59,s72,163840
buffer_store_dwordx4 a[8:11],v116,s[52:55],s59 offen offset:0 
buffer_store_dwordx4 a[24:27],v116,s[52:55],s59 offen offset:64 
buffer_store_dwordx4 a[40:43],v116,s[52:55],s59 offen offset:128 
buffer_store_dwordx4 a[56:59],v116,s[52:55],s59 offen offset:192 
s_add_u32 s59,s72,245760
buffer_store_dwordx4 a[12:15],v116,s[52:55],s59 offen offset:0 
buffer_store_dwordx4 a[28:31],v116,s[52:55],s59 offen offset:64 
buffer_store_dwordx4 a[44:47],v116,s[52:55],s59 offen offset:128 
buffer_store_dwordx4 a[60:63],v116,s[52:55],s59 offen offset:192 
s_branch L_END_%=
L_END_%=:
s_waitcnt vmcnt(0)
)"
    : : "s"(ws), "s"(wo), "s"(cre), "s"(cim), "s"(ax), "s"(dsk), "s"(out), "s"(bid), "s"(w), "s"(ldsb) : "v0","v1","v2","v3","v4","v5","v6","v7","v8","v9","v10","v11","v12","v13","v14","v15","v16","v17","v18","v19","v20","v21","v22","v23","v24","v25","v26","v27","v28","v29","v30","v31","v32","v33","v34","v35","v36","v37","v38","v39","v40","v41","v42","v43","v44","v45","v46","v47","v48","v49","v50","v51","v52","v53","v54","v55","v56","v57","v58","v59","v60","v61","v62","v63","v64","v65","v66","v67","v68","v69","v70","v71","v72","v73","v74","v75","v76","v77","v78","v79","v80","v81","v82","v83","v84","v85","v86","v87","v88","v89","v90","v91","v92","v93","v94","v95","v96","v97","v98","v99","v100","v101","v102","v103","v104","v105","v106","v107","v108","v109","v110","v111","v112","v113","v114","v115","v116","v117","v118","v119","v120","v121","v122","v123","v124","v125","v126","v127","a0","a1","a2","a3","a4","a5","a6","a7","a8","a9","a10","a11","a12","a13","a14","a15","a16","a17","a18","a19","a20","a21","a22","a23","a24","a25","a26","a27","a28","a29","a30","a31","a32","a33","a34","a35","a36","a37","a38","a39","a40","a41","a42","a43","a44","a45","a46","a47","a48","a49","a50","a51","a52","a53","a54","a55","a56","a57","a58","a59","a60","a61","a62","a63","s36","s37","s38","s39","s40","s41","s42","s43","s44","s45","s46","s47","s48","s49","s50","s51","s52","s53","s54","s55","s56","s57","s58","s59","s60","s61","s62","s63","s64","s65","s66","s67","s68","s69","s70","s71","s72","s73","s74","s75","s76","s77","s78","s79","s80","s81","s82","s83","s84","s85","s86","s87","s88","s89","s90","s91","s92","s93","s94","s95","s96","s97","s98","s99","vcc","scc","memory");
}

extern "C" void kernel_launch(void* const* d_in, const int* in_sizes, int n_in, void* d_out, int out_size, void* d_ws, size_t ws_size, hipStream_t stream) {
  (void)hipFuncSetAttribute((const void*)fused_kv_q_attn, hipFuncAttributeMaxDynamicSharedMemorySize, 163840);
  (void)hipMemsetAsync(d_ws, 0, 16384, stream);
  fused_kv_q_attn<<<256, 256, 163840, stream>>>((const char*)d_in[10], (const char*)d_in[12], (const char*)d_in[13], (char*)d_ws, (const char*)d_in[9], (const char*)d_in[11],
      (const char*)d_in[0], (const char*)d_in[3], (const char*)d_in[4], (const u16*)d_in[1], (const u16*)d_in[2], (const u16*)d_in[7], (const char*)d_in[14], (const char*)d_in[5], (const char*)d_in[6]);
  (void)hipFuncSetAttribute((const void*)out_proj, hipFuncAttributeMaxDynamicSharedMemorySize, 163840);
  out_proj<<<256, 512, 163840, stream>>>((char*)d_ws, (const char*)d_in[14], (const char*)d_in[5], (const char*)d_in[6], (const char*)d_in[0], (const char*)d_in[8], (char*)d_out);
}
```
